```python
import torch

def _softmax(x, axis=-1):
    return torch.softmax(x, dim=axis)

def _gelu(x):
    return torch.nn.functional.gelu(x, approximate='tanh')

def _silu(x):
    return torch.nn.functional.silu(x)

def _sigmoid(x):
    return torch.sigmoid(x)

def _layernorm(x, axis=-1, eps=1e-05):
    m = x.mean(dim=axis, keepdim=True)
    v = x.var(dim=axis, keepdim=True, unbiased=False)
    return (x - m) / torch.sqrt(v + eps)

def _scatter_add(tgt, idx, src, axis=0):
    return tgt.clone().index_add_(axis, idx, src)

def _segment_sum(src, seg, n):
    out = torch.zeros((n,) + src.shape[1:], dtype=src.dtype)
    return out.index_add_(0, seg, src)

def _pad(x, pw):
    flat = tuple((v for pair in reversed(pw) for v in pair))
    return torch.nn.functional.pad(x, flat)
kernel_name = 'gated_mix__mha_block__mixer_gmlp__prompt'

def setup_inputs():

    _w = lambda a: (lambda t: t.to(torch.bfloat16) if t.is_floating_point() else t)(torch.as_tensor(a))

    def _A_setup():
        B, S, D, H = (1, 2048, 1024, 32)
        return {'x': _w((lambda *s: torch.randn(*s, dtype=torch.bfloat16) * 0.05)(B, S, D)), 'wqkv': _w((lambda *s: torch.randn(*s, dtype=torch.bfloat16) * 0.8)(D, 3 * D)), 'wo': _w((lambda *s: torch.randn(*s, dtype=torch.bfloat16) * 0.1)(D, D))}
    _A_in = _A_setup()

    def _B_setup():
        B, S, D, F = (1, 2048, 1024, 3584)
        Fh = F // 2
        return {'x': _w((lambda *s: torch.randn(*s, dtype=torch.bfloat16) * 0.05)(B, S, D)), 'w_in': _w((lambda *s: torch.randn(*s, dtype=torch.bfloat16) * 0.4)(D, 2 * Fh)), 'w_sp': _w(0.01 * (lambda *s: torch.randn(*s, dtype=torch.bfloat16) * 0.4)(S, S)), 'b_sp': _w(1.0 + 0.01 * (lambda *s: torch.randn(*s, dtype=torch.bfloat16) * 0.05)(S)), 'w_out': _w((lambda *s: torch.randn(*s, dtype=torch.bfloat16) * 0.05)(Fh, D))}
    _B_in = _B_setup()
    return {'A_x': _A_in['x'], 'A_wqkv': _A_in['wqkv'], 'A_wo': _A_in['wo'], 'B_x': _B_in['x'], 'B_w_in': _B_in['w_in'], 'B_w_sp': _B_in['w_sp'], 'B_b_sp': _B_in['b_sp'], 'B_w_out': _B_in['w_out'], 'gate_w': _w((lambda *s: torch.randn(*s, dtype=torch.bfloat16) * 0.05)(1024) * 16.0)}

def reference(A_x, A_wqkv, A_wo, B_x, B_w_in, B_w_sp, B_b_sp, B_w_out, gate_w):

    def _A_ref(x, wqkv, wo):
        B, S, D = x.shape
        H = 32
        Dh = D // H
        qkv = (lambda x, *d: x.permute(*d))((x @ wqkv).reshape(B, S, 3, H, Dh), 2, 0, 3, 1, 4)
        q, k, v = (qkv[0], qkv[1], qkv[2])
        scores = torch.einsum('bhqd,bhkd->bhqk', q, k) * Dh ** (-0.5)
        p = _softmax(scores, axis=-1)
        o = torch.einsum('bhqk,bhkd->bhqd', p, v)
        return (lambda x, *d: x.permute(*d))(o, 0, 2, 1, 3).reshape(B, S, D) @ wo

    def _B_ref(x, w_in, w_sp, b_sp, w_out):
        B, S, D = x.shape
        F = w_in.shape[-1]
        Fh = F // 2
        z = _gelu(torch.einsum('bsd,df->bsf', x, w_in))
        z1 = z[..., :Fh].clone()
        z2 = _layernorm(z[..., Fh:], axis=-1)
        g = torch.einsum('ts,bsf->btf', w_sp, z2) + b_sp[None, :, None]
        return torch.einsum('bsf,fd->bsd', z1 * g, w_out)
    x = A_x
    g = _sigmoid((x * gate_w).sum(-1, keepdims=True))
    a = _A_ref(x, A_wqkv, A_wo)
    b = _B_ref(x, B_w_in, B_w_sp, B_b_sp, B_w_out)
    return g * a + (1 - g) * b
```

```cpp
#include <hip/hip_runtime.h>
#include <hip/hip_bf16.h>
#include <stdint.h>

#define LAS __attribute__((address_space(3)))
typedef short bf16x8 __attribute__((ext_vector_type(8)));
typedef short s16x4 __attribute__((ext_vector_type(4)));
typedef float f32x2 __attribute__((ext_vector_type(2)));
typedef float f32x4 __attribute__((ext_vector_type(4)));
typedef float f32x16 __attribute__((ext_vector_type(16)));
typedef unsigned u32x4 __attribute__((ext_vector_type(4)));
typedef unsigned u32x2 __attribute__((ext_vector_type(2)));
typedef LAS unsigned char* ldsp;
typedef unsigned short u16;

namespace gm {
constexpr int SEQ = 2048, DM = 1024, NH = 32, FH = 1792;
constexpr size_t WS_CTL = 0;
constexpr size_t CTL_BYTES = 16384;
constexpr size_t WS_GATE = 16384;
constexpr size_t WS_STAT = 32768;
constexpr size_t WS_Q = 1u << 20;
constexpr size_t WS_K = WS_Q + (4u << 20);
constexpr size_t WS_V = WS_K + (4u << 20);
constexpr size_t WS_Z = WS_V + (4u << 20);
constexpr size_t WS_ACT = WS_Z + (8u << 20);
constexpr float QSCALE = 0.17677669529663687f * 1.4426950408889634f;

enum { X_RAW = 0, X_OWN = 1 };
enum { W_P384 = 0, W_P448 = 1, W_P256 = 2, W_OWN = 3 };

__device__ __forceinline__ unsigned pk_bf16(float lo, float hi) {
    typedef __bf16 bf16x2_t __attribute__((ext_vector_type(2)));
    f32x2 v = {lo, hi}; bf16x2_t b = __builtin_convertvector(v, bf16x2_t); return __builtin_bit_cast(unsigned, b);
}
__device__ __forceinline__ float bf_lo(unsigned w) { return __uint_as_float(w << 16); }
__device__ __forceinline__ float bf_hi(unsigned w) { return __uint_as_float(w & 0xffff0000u); }
__device__ __forceinline__ s16x4 tr_read(ldsp p) { typedef short v4s __attribute__((ext_vector_type(4))); return __builtin_amdgcn_ds_read_tr16_b64_v4i16((LAS v4s*)p); }
template <int I> __device__ __forceinline__ void glds_run(const char* sbase, unsigned voff) {
    asm volatile("global_load_lds_dwordx4 %0, %1 offset:%2" ::"v"(voff), "s"(sbase), "n"((I & 3) * 1024) : "memory");
}
template <int I> __device__ __forceinline__ void glds_set(const char* sbase, unsigned voff, unsigned dst) {
    asm volatile("s_add_u32 m0, %2, %3\n\ts_nop 0\n\tglobal_load_lds_dwordx4 %0, %1 offset:%4" ::"v"(voff), "s"(sbase), "s"(dst), "n"((I & ~3) * 1024), "n"((I & 3) * 1024) : "memory", "scc");
}
#define GM_BAR() do { asm volatile("" ::: "memory"); __builtin_amdgcn_s_barrier(); asm volatile("" ::: "memory"); } while (0)
#define GM_SB() __builtin_amdgcn_sched_barrier(0)

template <int BM_, int BN_, int XI_, int WI_, int NSX_, int NSW_> struct Cfg {
    static constexpr int BM = BM_, BN = BN_, XI = XI_, WI = WI_, NSX = NSX_, NSW = NSW_;
    static constexpr int XB = BM * 128, WB = BN * 128, WOFF = NSX * XB, LDS = NSX * XB + NSW * WB;
    static constexpr int PX = BM / 32, PW = BN / 32;
    static constexpr int WPITCH = (WI == W_P384) ? 384 : (WI == W_P448) ? 448 : (WI == W_P256) ? 256 : 0;
};
template <class C> struct Src { const char* x; const char* w; const char* w2; int ksplit; unsigned xstep, wstep; unsigned xv[C::PX]; unsigned wv[C::PW]; };
template <int WI> __device__ __forceinline__ int w_swz(int r, int x) {
    if (WI == W_P384) return x ^ (((((r >> 1) & 1) | (((r >> 3) & 1) << 1))) << 5);
    if (WI == W_P448) return x ^ (((r >> 3) & 1) << 5);
    if (WI == W_P256) return x ^ (((r & 3) | (((r >> 3) & 1) << 2)) << 5);
    return x;
}
template <class C> __device__ __forceinline__ const char* w_tile(const Src<C>& s, int kt) { return kt < s.ksplit ? s.w + (size_t)kt * s.wstep : s.w2 + (size_t)(kt - s.ksplit) * s.wstep; }
template <class C> __device__ __forceinline__ void src_x_raw(Src<C>& s, const void* base, unsigned pitch, int lid, int lane) {
    s.x = (const char*)base - 4096; s.xstep = 128;
#pragma unroll
    for (int i = 0; i < C::PX; ++i) { const int row = 8 * (lid * C::PX + i) + (lane >> 3); s.xv[i] = row * pitch + (((lane & 7) ^ ((row >> 1) & 7)) * 16) + 4096 - (i & 3) * 1024; }
}
template <class C> __device__ __forceinline__ void src_x_own(Src<C>& s, const void* base, int lid, int lane) {
    s.x = (const char*)base - 4096; s.xstep = C::XB;
#pragma unroll
    for (int i = 0; i < C::PX; ++i) s.xv[i] = (lid * C::PX + i) * 1024 + lane * 16 + 4096 - (i & 3) * 1024;
}
template <class C> __device__ __forceinline__ void src_w_own(Src<C>& s, const void* base, int lid, int lane) {
    s.w = (const char*)base - 4096; s.w2 = s.w; s.ksplit = 1 << 20; s.wstep = C::WB;
#pragma unroll
    for (int i = 0; i < C::PW; ++i) s.wv[i] = (lid * C::PW + i) * 1024 + lane * 16 + 4096 - (i & 3) * 1024;
}
template <class C> __device__ __forceinline__ void src_w_raw(Src<C>& s, const void* base, unsigned pitch, unsigned segstride, int lid, int lane) {
    s.w = (const char*)base - 4096; s.w2 = s.w; s.ksplit = 1 << 20; s.wstep = 64 * pitch;
#pragma unroll
    for (int i = 0; i < C::PW; ++i) { const int o = 1024 * (lid * C::PW + i) + 16 * lane; const int r = o / C::WPITCH, x = w_swz<C::WI>(r, o % C::WPITCH);
        s.wv[i] = r * pitch + (x >> 7) * segstride + (x & 127) + 4096 - (i & 3) * 1024; }
}
template <class C, int I0, int I1, bool FIRST> __device__ __forceinline__ void stage_rng(unsigned dx, unsigned dw, const Src<C>& s, const char* xb, const char* wb) {
    if constexpr (I0 < I1) {
        if constexpr (I0 < C::PW) { if constexpr (FIRST || (I0 & 3) == 0) glds_set<I0>(wb, s.wv[I0], dw); else glds_run<I0>(wb, s.wv[I0]); }
        else { constexpr int j = I0 - C::PW; if constexpr (FIRST || (j & 3) == 0) glds_set<j>(xb, s.xv[j], dx); else glds_run<j>(xb, s.xv[j]); }
        stage_rng<C, I0 + 1, I1, false>(dx, dw, s, xb, wb);
    }
}
template <int MT_, int NT_, int N0_, unsigned SWAP_, bool LD_> struct Wv { static constexpr int MT = MT_, NT = NT_, N0 = N0_; static constexpr unsigned SWAP = SWAP_; static constexpr bool LD = LD_; };
template <class W> struct Frag { bf16x8 x[W::MT]; s16x4 wl[W::NT], wh[W::NT]; };
template <class C, class W> struct RdAddr { unsigned x[2]; unsigned w[(C::WI == W_P384) ? 4 : (C::WI == W_P256) ? W::NT : 2]; };
template <class C, class W> __device__ __forceinline__ void rd_init(RdAddr<C, W>& a, int m0, int n0, int lane) {
    const int fr = lane & 15, fq = lane >> 4, g = lane >> 4, q = (lane & 15) >> 2, p = lane & 3;
    if (C::XI == X_RAW) { for (int kk = 0; kk < 2; ++kk) a.x[kk] = (m0 + fr) * 128 + (((kk * 4 + fq) ^ (fr >> 1)) * 16); }
    else { for (int kk = 0; kk < 2; ++kk) a.x[kk] = (kk * 4 + fq) * (C::BM * 16) + (m0 + fr) * 16; }
    if (C::WI == W_P384) { const int s = ((q >> 1) & 1) | ((g & 1) << 1);
        for (int c = 0; c < 4; ++c) a.w[c] = C::WOFF + (8 * g + q) * 384 + ((c ^ s) * 32) + p * 8; }
    else if (C::WI == W_P448) { const int s = g & 1;
        for (int c = 0; c < 2; ++c) a.w[c] = C::WOFF + (8 * g + q) * 448 + (((c ^ s) - c) * 32) + p * 8; }
    else if (C::WI == W_P256) { const int s = (q & 3) | ((g & 1) << 2);
        for (int t = 0; t < W::NT; ++t) a.w[t] = C::WOFF + (8 * g + q) * 256 + (((n0 + t) ^ s) * 32) + p * 8; }
    else { for (int h = 0; h < 2; ++h) a.w[h] = C::WOFF + (p >> 1) * 1024 + (8 * g + 4 * (h ^ (p >> 1)) + q) * 16 + (p & 1) * 8 + W::N0 * 2048; }
}
template <class C, class W> __device__ __forceinline__ s16x4 rd_w(ldsp b, const RdAddr<C, W>& a, int t, int kk, int h) {
    const int c = W::N0 + t;
    if (C::WI == W_P384) return tr_read(b + a.w[c & 3] + (c >> 2) * 128 + kk * 32 * 384 + h * 4 * 384);
    else if (C::WI == W_P448) return tr_read(b + a.w[c & 1] + c * 32 + kk * 32 * 448 + h * 4 * 448);
    else if (C::WI == W_P256) return tr_read(b + a.w[t] + kk * 32 * 256 + h * 4 * 256);
    else return tr_read(b + a.w[h] + t * 2048 + kk * 512);
}
template <class C, class W> __device__ __forceinline__ void rd_one(Frag<W>& f, ldsp bx, ldsp bw, const RdAddr<C, W>& a, int kk, int r) {
    if (r < 2) { if (r == 0) f.wl[0] = rd_w<C, W>(bw, a, 0, kk, 0); else f.wh[0] = rd_w<C, W>(bw, a, 0, kk, 1); }
    else if (r < 2 + W::MT) { const int m = r - 2; f.x[m] = *(const LAS bf16x8*)(bx + a.x[kk] + m * ((C::XI == X_RAW) ? 2048 : 256)); }
    else { const int u = r - W::MT; const int t = u >> 1; if (u & 1) f.wh[t] = rd_w<C, W>(bw, a, t, kk, 1); else f.wl[t] = rd_w<C, W>(bw, a, t, kk, 0); }
}
template <class W> __device__ __forceinline__ void mma_one(f32x4 (&acc)[W::NT][W::MT], const Frag<W>& f, int i) {
    const int t = i / W::MT, m = i % W::MT;
    const bf16x8 w = __builtin_shufflevector(f.wl[t], f.wh[t], 0, 1, 2, 3, 4, 5, 6, 7);
    acc[t][m] = ((W::SWAP >> t) & 1) ? __builtin_amdgcn_mfma_f32_16x16x32_bf16(f.x[m], w, acc[t][m], 0, 0, 0)
                                     : __builtin_amdgcn_mfma_f32_16x16x32_bf16(w, f.x[m], acc[t][m], 0, 0, 0);
}
template <int RFRONT_, int VA_> struct Sch { static constexpr int RFRONT = RFRONT_, VA = VA_; };
template <class C, class W, class S, int V0, int V1, int I>
__device__ __forceinline__ void step_slot(f32x4 (&acc)[W::NT][W::MT], const Frag<W>& fc, Frag<W>& fn, ldsp bx, ldsp bw, const RdAddr<C, W>& a, int ksn,
                                          const Src<C>& s, const char* xb, const char* wb, unsigned dx, unsigned dw) {
    constexpr int NM = W::NT * W::MT, NR = 2 * W::NT + W::MT, NV = V1 - V0;
    constexpr int RS = (NM * S::RFRONT / 16) < 1 ? 1 : (NM * S::RFRONT / 16);
    if constexpr (I < NM) {
        mma_one<W>(acc, fc, I);
        if constexpr (I < RS) {
#pragma unroll
            for (int r = I * NR / RS; r < (I + 1) * NR / RS; ++r) rd_one<C, W>(fn, bx, bw, a, ksn, r);
        }
        if constexpr (W::LD) {
            constexpr int v0 = V0 + I * NV / NM, v1 = V0 + (I + 1) * NV / NM;
            stage_rng<C, v0, v1, v0 == V0>(dx, dw, s, xb, wb);
        }
        GM_SB();
        step_slot<C, W, S, V0, V1, I + 1>(acc, fc, fn, bx, bw, a, ksn, s, xb, wb, dx, dw);
    }
}
template <class C, class W, class S>
__device__ __forceinline__ void gemm_loop(ldsp lds, const Src<C>& s, int nt, f32x4 (&acc)[W::NT][W::MT], int lid, int m0, int n0, int lane) {
    constexpr int PX = C::PX, PW = C::PW, NSX = C::NSX, NSW = C::NSW, NV = PX + PW;
    static_assert(S::VA >= PW && S::VA <= NV, "all W pieces go out in the step behind the barrier");
    static_assert(NSX >= NSW && NSW >= 2, "ring depths");
    RdAddr<C, W> a; rd_init<C, W>(a, m0, n0, lane);
    const unsigned ldb = (unsigned)(uintptr_t)lds;
    const unsigned lx0 = ldb + lid * PX * 1024, lw0 = ldb + C::WOFF + lid * PW * 1024;
    Frag<W> f0, f1;
    const char* wb = w_tile<C>(s, NSW - 1); const char* xb = s.x + (size_t)(NSX - 1) * s.xstep;
    unsigned dw = lw0 + (NSW - 1) * C::WB, dx = lx0 + (NSX - 1) * C::XB;
    if constexpr (W::LD) {
#pragma unroll
        for (int i = 0; i < NSX - 1; ++i) {
            if (i < NSW - 1) stage_rng<C, 0, PW, true>(0, lw0 + i * C::WB, s, nullptr, w_tile<C>(s, i));
            stage_rng<C, PW, NV, true>(lx0 + i * C::XB, 0, s, s.x + (size_t)i * s.xstep, nullptr);
        }
        stage_rng<C, 0, S::VA, true>(dx, dw, s, xb, wb);
        constexpr int Y = (NSX - 2) * PX + (NSW - 2) * PW + S::VA;
        asm volatile("s_waitcnt vmcnt(%0)" ::"n"(Y) : "memory");
    }
    GM_BAR();
#pragma unroll
    for (int r = 0; r < 2 * W::NT + W::MT; ++r) rd_one<C, W>(f0, lds, lds, a, 0, r);
    int bx = 0, bw = 0, kx = NSX - 1, kw = NSW - 1;
    for (int t = 0; t < nt; ++t) {
        ldsp cx = lds + bx * C::XB, cw = lds + bw * C::WB;
        const int nbx = (bx + 1 == NSX) ? 0 : bx + 1, nbw = (bw + 1 == NSW) ? 0 : bw + 1;
        ldsp nx = lds + nbx * C::XB, nw = lds + nbw * C::WB;
        GM_SB();
#define GM_STEP(FC, FN, BX, BW, KSN, V0, V1) step_slot<C, W, S, V0, V1, 0>(acc, FC, FN, BX, BW, a, KSN, s, xb, wb, dx, dw)
        GM_STEP(f0, f1, cx, cw, 1, S::VA, NV);
        asm volatile("s_waitcnt lgkmcnt(0)" ::: "memory");
        if constexpr (W::LD) {
            constexpr int pw = 1 - NSW, px = 1 - NSX, last = (pw > px) ? pw : px, nper = (-1) - last, Y = nper * NV + ((pw > px) ? PX : 0);
            asm volatile("s_waitcnt vmcnt(%0)" ::"n"(Y) : "memory");
        }
        GM_BAR();
        GM_SB();
        if constexpr (W::LD) {
            kx = (kx + 1 < nt) ? kx + 1 : kx; kw = (kw + 1 < nt) ? kw + 1 : kw;
            xb = s.x + (size_t)kx * s.xstep; wb = w_tile<C>(s, kw);
            dx = lx0 + bx * C::XB; dw = lw0 + bw * C::WB;
        }
        GM_STEP(f1, f0, nx, nw, 0, 0, S::VA);
#undef GM_STEP
        bx = nbx; bw = nbw;
    }
    GM_SB();
    asm volatile("s_waitcnt vmcnt(0) lgkmcnt(0)" ::: "memory");
    GM_BAR();
}
template <class W> __device__ __forceinline__ void acc_zero(f32x4 (&acc)[W::NT][W::MT]) {
#pragma unroll
    for (int t = 0; t < W::NT; ++t)
#pragma unroll
        for (int m = 0; m < W::MT; ++m) acc[t][m] = (f32x4){0.f, 0.f, 0.f, 0.f};
}
__device__ __forceinline__ u32x4 pair16(const f32x4& A, const f32x4& B) {
    const unsigned a0 = pk_bf16(A[0], A[1]), a1 = pk_bf16(A[2], A[3]), b0 = pk_bf16(B[0], B[1]), b1 = pk_bf16(B[2], B[3]);
    const auto r0 = __builtin_amdgcn_permlane16_swap(a0, b0, false, false); const auto r1 = __builtin_amdgcn_permlane16_swap(a1, b1, false, false);
    return (u32x4){r0[0], r1[0], r0[1], r1[1]};
}

struct Params {
    const u16* ax; const u16* wqkv; const u16* wo; const u16* bx; const u16* win; const u16* wsp; const u16* bsp; const u16* wout; const u16* gw;
    u16* out; unsigned char* ws;
};

using CfgQ = Cfg<256, 192, X_RAW, W_P384, 3, 2>;
template <int WN> __device__ __forceinline__ void qkv_wave(const Params& p, ldsp lds, int pair, int panel, int wid, int lane) {
    using C = CfgQ; using W = Wv<4, 6, WN * 6, WN ? 0x3Cu : 0u, WN == 0>;
    const int lid = wid & 3, m0 = (wid & 3) * 64;
    Src<C> s;
    src_x_raw<C>(s, p.ax + (size_t)panel * 256 * DM, DM * 2, lid, lane);
    src_w_raw<C>(s, p.wqkv + pair * 64, 3 * DM * 2, DM * 2, lid, lane);
    f32x4 acc[6][4]; acc_zero<W>(acc);
    gemm_loop<C, W, Sch<12, 7>>(lds, s, DM / 64, acc, lid, m0, 0, lane);
    const int fr = lane & 15, fq = lane >> 4;
    const int row0 = panel * 256 + m0;
#pragma unroll
    for (int t = 0; t < 6; t += 2) {
        const int T = WN * 6 + t, kind = T >> 2, head = pair * 2 + ((T >> 1) & 1);
        if (kind < 2) {
            unsigned char* base = p.ws + (kind == 0 ? WS_Q : WS_K);
            const int c = 2 * (fq & 1) + (fq >> 1);
            const float sc = kind == 0 ? QSCALE : 1.0f;
#pragma unroll
            for (int m = 0; m < 4; ++m) { const int row = row0 + m * 16 + fr;
                *(u32x4*)(base + ((size_t)((head * 64 + (row >> 5)) * 4 + c) * 512 + (row & 31) * 16)) = pair16(acc[t][m] * sc, acc[t + 1][m] * sc); }
        } else {
#pragma unroll
            for (int tt = 0; tt < 2; ++tt)
#pragma unroll
                for (int m = 0; m < 4; m += 2) {
                    const f32x4 A = acc[t + tt][m], B = acc[t + tt][m + 1];
                    const unsigned a0 = pk_bf16(A[0], A[1]), a1 = pk_bf16(A[2], A[3]), b0 = pk_bf16(B[0], B[1]), b1 = pk_bf16(B[2], B[3]);
                    const auto r0 = __builtin_amdgcn_permlane32_swap(a0, b0, false, false); const auto r1 = __builtin_amdgcn_permlane32_swap(a1, b1, false, false);
                    const int kb = (row0 >> 4) + m + (fq >> 1), h = fq & 1, d = tt * 16 + fr;
                    *(u32x4*)(p.ws + WS_V + ((size_t)((head * 128 + kb) * 2 + h) * 512 + d * 16)) = (u32x4){r0[0], r1[0], r0[1], r1[1]};
                }
        }
    }
}

using CfgZ = Cfg<128, 224, X_RAW, W_P448, 3, 3>;
using CfgS = Cfg<128, 224, X_RAW, W_OWN, 3, 3>;
__device__ __forceinline__ float gelu_tanh(float x) {
    const float t = x * x * (-0.044715f * 2.0f * 0.7978845608028654f * 1.4426950408889634f) + (-2.0f * 0.7978845608028654f * 1.4426950408889634f);
    return x * __builtin_amdgcn_rcpf(1.0f + __builtin_amdgcn_exp2f(x * t));
}
__device__ __forceinline__ float red_fq(float v) {
    const auto a = __builtin_amdgcn_permlane16_swap(__float_as_uint(v), __float_as_uint(v), false, false); v = __uint_as_float(a[0]) + __uint_as_float(a[1]);
    const auto b = __builtin_amdgcn_permlane32_swap(__float_as_uint(v), __float_as_uint(v), false, false); return __uint_as_float(b[0]) + __uint_as_float(b[1]);
}
template <int WN> __device__ __forceinline__ void z2_wave(const Params& p, ldsp lds, int panel, int j, int wid, int lane) {
    using C = CfgZ; using W = Wv<2, 7, WN * 7, 0u, WN == 0>;
    const int lid = wid & 3, m0 = (wid & 3) * 32;
    Src<C> s;
    src_x_raw<C>(s, p.ax + (size_t)panel * 128 * DM, DM * 2, lid, lane);
    src_w_raw<C>(s, p.win + FH + j * 224, 2 * FH * 2, 128, lid, lane);
    f32x4 acc[7][2]; acc_zero<W>(acc);
    gemm_loop<C, W, Sch<12, 7>>(lds, s, DM / 64, acc, lid, m0, 0, lane);
    const int fr = lane & 15, fq = lane >> 4;
    LAS f32x2* P = (LAS f32x2*)lds;
    LAS f32x2* R = (LAS f32x2*)(lds + 4096);
#pragma unroll
    for (int m = 0; m < 2; ++m) {
        float sum = 0.f;
#pragma unroll
        for (int t = 0; t < 7; ++t) {
#pragma unroll
            for (int e = 0; e < 4; ++e) acc[t][m][e] = gelu_tanh(acc[t][m][e]);
            sum += (acc[t][m][0] + acc[t][m][1]) + (acc[t][m][2] + acc[t][m][3]); }
        const float mean = red_fq(sum) * (1.0f / 112.0f); float q = 0.f;
#pragma unroll
        for (int t = 0; t < 7; ++t) { const f32x4 d = acc[t][m] - mean; q += (d[0] * d[0] + d[1] * d[1]) + (d[2] * d[2] + d[3] * d[3]); }
        q = red_fq(q);
        if (fq == 0) P[(m0 + m * 16 + fr) * 2 + WN] = (f32x2){mean, q};
    }
    asm volatile("s_waitcnt lgkmcnt(0)" ::: "memory"); GM_BAR();
    unsigned* cnt = (unsigned*)(p.ws + WS_CTL) + 64 * panel;
    unsigned long long* slots = (unsigned long long*)(p.ws + WS_STAT) + (size_t)panel * 128 * 8;
    if (wid == 0) {
#pragma unroll
        for (int i = 0; i < 2; ++i) { const int row = i * 64 + lane;
            const f32x2 a = P[row * 2], b = P[row * 2 + 1];
            const float mt = 0.5f * (a.x + b.x), dl = a.x - b.x, m2 = a.y + b.y + 56.0f * dl * dl;
            __hip_atomic_store(slots + row * 8 + j, ((unsigned long long)__float_as_uint(m2) << 32) | __float_as_uint(mt), __ATOMIC_RELAXED, __HIP_MEMORY_SCOPE_AGENT); }
        asm volatile("s_waitcnt vmcnt(0)" ::: "memory");
        if (lane == 0) __hip_atomic_fetch_add(cnt, 1u, __ATOMIC_RELAXED, __HIP_MEMORY_SCOPE_AGENT);
        unsigned spins = 0;
        while ((unsigned)__builtin_amdgcn_readfirstlane(__hip_atomic_load(cnt, __ATOMIC_RELAXED, __HIP_MEMORY_SCOPE_AGENT)) < 8u) { __builtin_amdgcn_s_sleep(1); if (++spins > (1u << 24)) break; }
        asm volatile("" ::: "memory");
#pragma unroll
        for (int i = 0; i < 2; ++i) { const int row = i * 64 + lane;
            float mt8[8], m28[8], ms = 0.f;
#pragma unroll
            for (int t = 0; t < 8; ++t) { const unsigned long long w = __hip_atomic_load(slots + row * 8 + t, __ATOMIC_RELAXED, __HIP_MEMORY_SCOPE_AGENT); mt8[t] = __uint_as_float((unsigned)w); m28[t] = __uint_as_float((unsigned)(w >> 32)); ms += mt8[t]; }
            const float mean = ms * 0.125f; float q = 0.f;
#pragma unroll
            for (int t = 0; t < 8; ++t) { const float dm = mt8[t] - mean; q += m28[t] + 224.0f * dm * dm; }
            R[row] = (f32x2){mean, __builtin_amdgcn_rsqf(q * (1.0f / 1792.0f) + 1e-5f)}; }
    }
    asm volatile("s_waitcnt lgkmcnt(0)" ::: "memory"); GM_BAR();
    const f32x2 r0 = R[m0 + fr], r1 = R[m0 + 16 + fr];
    unsigned char* zb = p.ws + WS_Z + (size_t)j * 32 * 28672;
#pragma unroll
    for (int t = 0; t < 7; ++t) { acc[t][0] = (acc[t][0] - r0.x) * r0.y; acc[t][1] = (acc[t][1] - r1.x) * r1.y; }
    const int srow = panel * 128 + m0;
#pragma unroll
    for (int m = 0; m < 2; ++m)
#pragma unroll
        for (int t = 0; t < 6; t += 2) {
            const int c = 2 * (WN * 7 + t + (fq & 1)) + (fq >> 1), k = srow + m * 16 + fr;
            *(u32x4*)(zb + (size_t)((k >> 6) * 28 + c) * 1024 + (((k & 63) ^ (4 * (c & 1))) * 16)) = pair16(acc[t][m], acc[t + 1][m]);
        }
    { const int c = 2 * (WN * 7 + 6) + (fq >> 1), k = srow + (fq & 1) * 16 + fr;
      *(u32x4*)(zb + (size_t)((k >> 6) * 28 + c) * 1024 + (((k & 63) ^ (4 * (c & 1))) * 16)) = pair16(acc[6][0], acc[6][1]); }
}
template <int WN> __device__ __forceinline__ void sp_wave(const Params& p, ldsp lds, int panel, int j, int wid, int lane) {
    using W = Wv<2, 7, WN * 7, 0u, WN == 0>;
    const int lid = wid & 3, m0 = (wid & 3) * 32;
    const int fr = lane & 15, fq = lane >> 4;
    unsigned z1[7][2][2];
    {   using C = CfgZ; Src<C> s;
        src_x_raw<C>(s, p.ax + (size_t)panel * 128 * DM, DM * 2, lid, lane);
        src_w_raw<C>(s, p.win + j * 224, 2 * FH * 2, 128, lid, lane);
        f32x4 acc[7][2]; acc_zero<W>(acc);
        gemm_loop<C, W, Sch<12, 7>>(lds, s, DM / 64, acc, lid, m0, 0, lane);
#pragma unroll
        for (int t = 0; t < 7; ++t)
#pragma unroll
            for (int m = 0; m < 2; ++m) { z1[t][m][0] = pk_bf16(gelu_tanh(acc[t][m][0]), gelu_tanh(acc[t][m][1])); z1[t][m][1] = pk_bf16(gelu_tanh(acc[t][m][2]), gelu_tanh(acc[t][m][3])); }
    }
    using C = CfgS; Src<C> s;
    src_x_raw<C>(s, p.wsp + (size_t)panel * 128 * SEQ, SEQ * 2, lid, lane);
    src_w_own<C>(s, p.ws + WS_Z + (size_t)j * 32 * 28672, lid, lane);
    f32x4 acc[7][2]; acc_zero<W>(acc);
    gemm_loop<C, W, Sch<12, 7>>(lds, s, SEQ / 64, acc, lid, m0, 0, lane);
    const float* gate = (const float*)(p.ws + WS_GATE);
    unsigned char* ub = p.ws + WS_ACT + (size_t)panel * 352 * 2048;
#pragma unroll
    for (int m = 0; m < 2; ++m) {
        const int trow = panel * 128 + m0 + m * 16 + fr;
        const float b = __uint_as_float((unsigned)p.bsp[trow] << 16), g1 = 1.0f - gate[trow];
#pragma unroll
        for (int t = 0; t < 7; ++t) {
            acc[t][m][0] = (acc[t][m][0] + b) * g1 * bf_lo(z1[t][m][0]); acc[t][m][1] = (acc[t][m][1] + b) * g1 * bf_hi(z1[t][m][0]);
            acc[t][m][2] = (acc[t][m][2] + b) * g1 * bf_lo(z1[t][m][1]); acc[t][m][3] = (acc[t][m][3] + b) * g1 * bf_hi(z1[t][m][1]); }
    }
#pragma unroll
    for (int m = 0; m < 2; ++m)
#pragma unroll
        for (int t = 0; t < 6; t += 2) {
            const int c = j * 28 + 2 * (WN * 7 + t + (fq & 1)) + (fq >> 1);
            *(u32x4*)(ub + (size_t)c * 2048 + (m0 + m * 16 + fr) * 16) = pair16(acc[t][m], acc[t + 1][m]);
        }
    { const int c = j * 28 + 2 * (WN * 7 + 6) + (fq >> 1);
      *(u32x4*)(ub + (size_t)c * 2048 + (m0 + (fq & 1) * 16 + fr) * 16) = pair16(acc[6][0], acc[6][1]); }
}

using CfgO = Cfg<128, 128, X_OWN, W_P256, 4, 4>;
template <bool LD> __device__ __forceinline__ void out_wave(const Params& p, ldsp lds, int panel, int c, int wid, int lane) {
    using C = CfgO; using W = Wv<4, 2, 0, 0u, LD>;
    const int lid = wid & 3, m0 = (wid >> 2) * 64, n0 = (wid & 3) * 2;
    Src<C> s;
    src_x_own<C>(s, p.ws + WS_ACT + (size_t)panel * 352 * 2048, lid, lane);
    src_w_raw<C>(s, p.wout + c * 128, DM * 2, 128, lid, lane);
    s.w2 = (const char*)(p.wo + c * 128) - 4096; s.ksplit = FH / 64;
    f32x4 acc[2][4]; acc_zero<W>(acc);
    gemm_loop<C, W, Sch<16, 4>>(lds, s, (FH + DM) / 64, acc, lid, m0, n0, lane);
    const int fr = lane & 15, fq = lane >> 4;
#pragma unroll
    for (int m = 0; m < 4; ++m)
        *(u32x4*)(p.out + (size_t)(panel * 128 + m0 + m * 16 + fr) * DM + c * 128 + (n0 + (fq & 1)) * 16 + (fq >> 1) * 8) = pair16(acc[0][m], acc[1][m]);
}

constexpr int AT_STAGE = 16384, AT_NS = 4;
#define MFMA32(a, b, c) __builtin_amdgcn_mfma_f32_32x32x16_bf16(a, b, c, 0, 0, 0)
#define MFMA16(a, b, c) __builtin_amdgcn_mfma_f32_16x16x32_bf16(a, b, c, 0, 0, 0)
#define PIN(x) asm volatile("" : "+v"(x))
__device__ __forceinline__ void attn_wg(const Params& p, ldsp lds, int head, int qb, int wid, int lane) {
    const unsigned char* Kg = p.ws + WS_K + (size_t)head * 131072; const unsigned char* Vg = p.ws + WS_V + (size_t)head * 131072;
    const unsigned ldb = (unsigned)(uintptr_t)lds;
    const unsigned voff = wid * 1024 + lane * 16;
    const char* kb = (const char*)Kg; const char* vb = (const char*)Vg;
    auto stage = [&](int st, int slot) {
        asm volatile("s_add_u32 m0, %2, 0\n\ts_nop 0\n\tglobal_load_lds_dwordx4 %0, %1" ::"v"(voff), "s"(kb + (size_t)st * 8192), "s"(ldb + slot * AT_STAGE + wid * 1024) : "memory", "scc");
        asm volatile("s_add_u32 m0, %2, 0\n\ts_nop 0\n\tglobal_load_lds_dwordx4 %0, %1" ::"v"(voff), "s"(vb + (size_t)st * 8192), "s"(ldb + slot * AT_STAGE + 8192 + wid * 1024) : "memory", "scc");
    };
    stage(0, 0); stage(1, 1); stage(2, 2);
    const int q0 = qb * 512 + wid * 64;
    bf16x8 qf[2][2];
#pragma unroll
    for (int a = 0; a < 2; ++a)
#pragma unroll
        for (int d = 0; d < 2; ++d) qf[a][d] = *(const bf16x8*)(p.ws + WS_Q + (size_t)(head * 64 + (q0 >> 5) + a) * 2048 + d * 1024 + lane * 16);
    const float gt0 = ((const float*)(p.ws + WS_GATE))[q0 + (lane & 31)], gt1 = ((const float*)(p.ws + WS_GATE))[q0 + 32 + (lane & 31)];
    bf16x8 sel;
    { const short one = ((lane & 15) == ((lane >> 4) & 1)) ? (short)0x3f80 : (short)0;
#pragma unroll
      for (int i = 0; i < 8; ++i) sel[i] = one; }
    asm volatile("s_waitcnt vmcnt(4)" ::: "memory");
    asm volatile("s_waitcnt vmcnt(0)" ::: "memory");
    GM_BAR();
    f32x16 o[2], negm[2], S[2]; f32x4 l[2]; u32x4 pw[2][2];
    const f32x16 zero16 = {};
    bf16x8 kf[2], vf[2];
    kf[0] = *(const LAS bf16x8*)(lds + lane * 16); kf[1] = *(const LAS bf16x8*)(lds + 1024 + lane * 16);
#pragma unroll
    for (int a = 0; a < 2; ++a) {
        f32x16 s = MFMA32(kf[0], qf[a][0], zero16); s = MFMA32(kf[1], qf[a][1], s);
        float m = fmaxf(fmaxf(s[0], s[1]), fmaxf(s[2], s[3]));
#pragma unroll
        for (int r = 4; r < 16; r += 2) m = fmaxf(m, fmaxf(s[r], s[r + 1]));
        const auto rr = __builtin_amdgcn_permlane32_swap(__float_as_uint(m), __float_as_uint(m), false, false);
        m = -fmaxf(__uint_as_float(rr[0]), __uint_as_float(rr[1]));
#pragma unroll
        for (int r = 0; r < 16; ++r) negm[a][r] = m;
        PIN(negm[a]);
        o[a] = zero16; l[a] = (f32x4){0.f, 0.f, 0.f, 0.f};
    }
    for (int st = 0; st < 16; ++st) {
        const int slot = st & 3;
        if (st + 3 < 16) stage(st + 3, (st + 3) & 3);
        ldsp kbase = lds + slot * AT_STAGE + lane * 16, vbase = kbase + 8192;
#pragma unroll
        for (int kt = 0; kt < 4; ++kt) {
            kf[0] = *(const LAS bf16x8*)(kbase + kt * 2048); kf[1] = *(const LAS bf16x8*)(kbase + kt * 2048 + 1024);
            vf[0] = *(const LAS bf16x8*)(vbase + kt * 2048); vf[1] = *(const LAS bf16x8*)(vbase + kt * 2048 + 1024);
#pragma unroll
            for (int a = 0; a < 2; ++a) { S[a] = MFMA32(kf[0], qf[a][0], negm[a]); S[a] = MFMA32(kf[1], qf[a][1], S[a]); }
#pragma unroll
            for (int a = 0; a < 2; ++a) {
#pragma unroll
                for (int r = 0; r < 16; ++r) S[a][r] = __builtin_amdgcn_exp2f(S[a][r]);
#pragma unroll
                for (int h = 0; h < 2; ++h)
#pragma unroll
                    for (int i = 0; i < 4; ++i) pw[a][h][i] = pk_bf16(S[a][8 * h + 2 * i], S[a][8 * h + 2 * i + 1]);
                o[a] = MFMA32(vf[0], __builtin_bit_cast(bf16x8, pw[a][0]), o[a]); o[a] = MFMA32(vf[1], __builtin_bit_cast(bf16x8, pw[a][1]), o[a]);
                l[a] = MFMA16(sel, __builtin_bit_cast(bf16x8, pw[a][0]), l[a]); l[a] = MFMA16(sel, __builtin_bit_cast(bf16x8, pw[a][1]), l[a]);
            }
        }
        if (st + 3 < 16) asm volatile("s_waitcnt vmcnt(4) lgkmcnt(0)" ::: "memory"); else asm volatile("s_waitcnt vmcnt(0) lgkmcnt(0)" ::: "memory");
        GM_BAR();
    }
    unsigned char* ob = p.ws + WS_ACT;
#pragma unroll
    for (int a = 0; a < 2; ++a) {
        const int q = lane & 31, hh = lane >> 5;
        const float l0 = __shfl(l[a][0], q & 15), l1 = __shfl(l[a][1], q & 15);
        const float lsum = (q < 16) ? l0 : l1;
        const float sc = (a ? gt1 : gt0) * __builtin_amdgcn_rcpf(lsum);
        const int row = q0 + a * 32 + q, panel = row >> 7;
#pragma unroll
        for (int i = 0; i < 4; i += 2) {
            const unsigned a0 = pk_bf16(o[a][4 * i] * sc, o[a][4 * i + 1] * sc), a1 = pk_bf16(o[a][4 * i + 2] * sc, o[a][4 * i + 3] * sc);
            const unsigned b0 = pk_bf16(o[a][4 * i + 4] * sc, o[a][4 * i + 5] * sc), b1 = pk_bf16(o[a][4 * i + 6] * sc, o[a][4 * i + 7] * sc);
            const auto r0 = __builtin_amdgcn_permlane32_swap(a0, b0, false, false); const auto r1 = __builtin_amdgcn_permlane32_swap(a1, b1, false, false);
            const int c = 224 + head * 4 + i + hh;
            *(u32x4*)(ob + ((size_t)panel * 352 + c) * 2048 + (row & 127) * 16) = (u32x4){r0[0], r1[0], r0[1], r1[1]};
        }
    }
}

__device__ __forceinline__ void gate_rows(const Params& p, int row, int lane) {
    const u32x4* xr = (const u32x4*)(p.ax + (size_t)row * DM) + lane * 2; const u32x4* gw = (const u32x4*)p.gw + lane * 2;
    float s = 0.f;
#pragma unroll
    for (int i = 0; i < 2; ++i) { const u32x4 a = xr[i], b = gw[i];
#pragma unroll
        for (int e = 0; e < 4; ++e) s += bf_lo(a[e]) * bf_lo(b[e]) + bf_hi(a[e]) * bf_hi(b[e]); }
#pragma unroll
    for (int o = 32; o >= 1; o >>= 1) s += __shfl_xor(s, o);
    if (lane == 0) ((float*)(p.ws + WS_GATE))[row] = __builtin_amdgcn_rcpf(1.0f + __builtin_amdgcn_exp2f(-1.4426950408889634f * s));
}
__global__ __launch_bounds__(512, 2) void k1(Params p) {
    extern __shared__ __attribute__((aligned(16))) unsigned char smem[]; ldsp lds = (ldsp)smem;
    const int lane = threadIdx.x & 63, wid = __builtin_amdgcn_readfirstlane(threadIdx.x >> 6);
    const int b = blockIdx.x, x = b & 7, role = (b >> 3) & 1, idx = b >> 4;
    gate_rows(p, b * 8 + wid, lane);
    if (role == 0) { const int pair = 2 * x + (idx >> 3), panel = idx & 7;
        if (wid < 4) qkv_wave<0>(p, lds, pair, panel, wid, lane); else qkv_wave<1>(p, lds, pair, panel, wid, lane); }
    else { const int panel = 4 * (x >> 1) + (idx >> 2), j = 4 * (x & 1) + (idx & 3);
        if (wid < 4) z2_wave<0>(p, lds, panel, j, wid, lane); else z2_wave<1>(p, lds, panel, j, wid, lane); }
}
__global__ __launch_bounds__(512, 2) void k2(Params p) {
    extern __shared__ __attribute__((aligned(16))) unsigned char smem[]; ldsp lds = (ldsp)smem;
    const int lane = threadIdx.x & 63, wid = __builtin_amdgcn_readfirstlane(threadIdx.x >> 6);
    const int b = blockIdx.x, x = b & 7, role = (b >> 3) & 1, idx = b >> 4;
    if (role == 0) attn_wg(p, lds, 4 * x + (idx >> 2), idx & 3, wid, lane);
    else { const int panel = 4 * (x >> 1) + (idx >> 2), j = 4 * (x & 1) + (idx & 3);
        if (wid < 4) sp_wave<0>(p, lds, panel, j, wid, lane); else sp_wave<1>(p, lds, panel, j, wid, lane); }
}
__global__ __launch_bounds__(512, 2) void k3(Params p) {
    extern __shared__ __attribute__((aligned(16))) unsigned char smem[]; ldsp lds = (ldsp)smem;
    const int lane = threadIdx.x & 63, wid = __builtin_amdgcn_readfirstlane(threadIdx.x >> 6);
    const int b = blockIdx.x, x = b & 7, idx = b >> 3;
    const int panel = 4 * (x >> 1) + (idx >> 2), c = 4 * (x & 1) + (idx & 3);
    if (wid < 4) out_wave<true>(p, lds, panel, c, wid, lane); else out_wave<false>(p, lds, panel, c, wid, lane);
}
}

extern "C" void kernel_launch(void* const* d_in, const int* in_sizes, int n_in, void* d_out, int out_size, void* d_ws, size_t ws_size, hipStream_t stream) {
    using namespace gm;
    Params p;
    p.ax = (const u16*)d_in[0]; p.wqkv = (const u16*)d_in[1]; p.wo = (const u16*)d_in[2]; p.bx = (const u16*)d_in[3]; p.win = (const u16*)d_in[4];
    p.wsp = (const u16*)d_in[5]; p.bsp = (const u16*)d_in[6]; p.wout = (const u16*)d_in[7]; p.gw = (const u16*)d_in[8];
    p.out = (u16*)d_out; p.ws = (unsigned char*)d_ws;
    constexpr int L1 = CfgQ::LDS, L2 = CfgZ::LDS, L3 = CfgO::LDS;
    hipFuncSetAttribute((const void*)k1, hipFuncAttributeMaxDynamicSharedMemorySize, L1);
    hipFuncSetAttribute((const void*)k2, hipFuncAttributeMaxDynamicSharedMemorySize, L2);
    hipFuncSetAttribute((const void*)k3, hipFuncAttributeMaxDynamicSharedMemorySize, L3);
    hipMemsetAsync(p.ws + WS_CTL, 0, CTL_BYTES, stream);
    k1<<<256, 512, L1, stream>>>(p);
    k2<<<256, 512, L2, stream>>>(p);
    k3<<<128, 512, L3, stream>>>(p);
}
```

```cpp
#include <hip/hip_runtime.h>
#include <hip/hip_bf16.h>
#include <stdint.h>

#define LAS __attribute__((address_space(3)))
typedef short bf16x8 __attribute__((ext_vector_type(8)));
typedef short s16x4 __attribute__((ext_vector_type(4)));
typedef float f32x2 __attribute__((ext_vector_type(2)));
typedef float f32x4 __attribute__((ext_vector_type(4)));
typedef float f32x16 __attribute__((ext_vector_type(16)));
typedef unsigned u32x4 __attribute__((ext_vector_type(4)));
typedef unsigned u32x2 __attribute__((ext_vector_type(2)));
typedef LAS unsigned char* ldsp;
typedef unsigned short u16;

namespace gm {
constexpr int SEQ = 2048, DM = 1024, NH = 32, FH = 1792;
constexpr size_t WS_CTL = 0;
constexpr size_t CTL_BYTES = 16384;
constexpr size_t WS_GATE = 16384;
constexpr size_t WS_STAT = 32768;
constexpr size_t WS_Q = 1u << 20;
constexpr size_t WS_K = WS_Q + (4u << 20);
constexpr size_t WS_V = WS_K + (4u << 20);
constexpr size_t WS_Z = WS_V + (4u << 20);
constexpr size_t WS_ACT = WS_Z + (8u << 20);
constexpr int LDS_PFS = 147456 - 0;
constexpr float QSCALE = 0.17677669529663687f * 1.4426950408889634f;

#define TRACE(t) do {} while (0)
#define DBG_PARAMS
#define DBG_ARGS(id)
#define STAMP(k, i) do {} while (0)
constexpr int C_TMO = 0, C_GATE = 64, C_Q = 128  , C_S = C_Q + 64 * 16  , C_Z = C_S + 64 * 16  ,
              C_U = C_Z + 64 * 8  , C_O = C_U + 64 * 16  , C_END = C_O + 64 * 4;
static_assert(C_END * 4 <= (int)CTL_BYTES, "control block");
#define RLX_AGENT __ATOMIC_RELAXED, __HIP_MEMORY_SCOPE_AGENT
typedef __attribute__((address_space(1))) unsigned gu32;
typedef __attribute__((address_space(1))) unsigned long long gu64;
typedef __amdgpu_buffer_rsrc_t rsrc_t;
__device__ __forceinline__ rsrc_t ws_rsrc(unsigned char* ws) { return __builtin_amdgcn_make_buffer_rsrc(ws, 0, 64u << 20, 0x00020000); }
__device__ __forceinline__ void st16_wt(rsrc_t r, unsigned off, u32x4 v) { __builtin_amdgcn_raw_buffer_store_b128(v, r, off, 0, 16); }
__device__ __forceinline__ void signal(unsigned char* ws, int word) { __hip_atomic_fetch_add((gu32*)(ws + WS_CTL) + word, 1u, RLX_AGENT); }
__device__ __forceinline__ void wait2(unsigned char* ws, int w0, unsigned n0, int w1, unsigned n1) {
    gu32* ctl = (gu32*)(ws + WS_CTL);
    for (unsigned spins = 0;;) {
        const unsigned a = __builtin_amdgcn_readfirstlane(__hip_atomic_load(ctl + w0, RLX_AGENT)), b = __builtin_amdgcn_readfirstlane(__hip_atomic_load(ctl + w1, RLX_AGENT));
        if (a >= n0 && b >= n1) break;
        __builtin_amdgcn_s_sleep(1);
        if (++spins > (1u << 20)) { __hip_atomic_store(ctl + C_TMO, 1u, RLX_AGENT); break; }
        if ((spins & 255u) == 0u && __builtin_amdgcn_readfirstlane(__hip_atomic_load(ctl + C_TMO, RLX_AGENT))) break;
    }
    __builtin_amdgcn_fence(__ATOMIC_ACQUIRE, "agent");
    asm volatile("s_waitcnt vmcnt(0)" ::: "memory");
}
enum { X_RAW = 0, X_OWN = 1 };
enum { W_P384 = 0, W_P448 = 1, W_P256 = 2, W_OWN = 3 };

__device__ __forceinline__ unsigned pk_bf16(float lo, float hi) {
    typedef __bf16 bf16x2_t __attribute__((ext_vector_type(2)));
    f32x2 v = {lo, hi}; bf16x2_t b = __builtin_convertvector(v, bf16x2_t); return __builtin_bit_cast(unsigned, b);
}
__device__ __forceinline__ float bf_lo(unsigned w) { return __uint_as_float(w << 16); }
__device__ __forceinline__ float bf_hi(unsigned w) { return __uint_as_float(w & 0xffff0000u); }
__device__ __forceinline__ s16x4 tr_read(ldsp p) { typedef short v4s __attribute__((ext_vector_type(4))); return __builtin_amdgcn_ds_read_tr16_b64_v4i16((LAS v4s*)p); }
template <int I> __device__ __forceinline__ void glds_run(const char* sbase, unsigned voff) {
    asm volatile("global_load_lds_dwordx4 %0, %1 offset:%2" ::"v"(voff), "s"(sbase), "n"((I & 3) * 1024) : "memory");
}
template <int I> __device__ __forceinline__ void glds_set(const char* sbase, unsigned voff, unsigned dst) {
    asm volatile("s_add_u32 m0, %2, %3\n\ts_nop 0\n\tglobal_load_lds_dwordx4 %0, %1 offset:%4" ::"v"(voff), "s"(sbase), "s"(dst), "n"((I & ~3) * 1024), "n"((I & 3) * 1024) : "memory", "scc");
}
__device__ __forceinline__ void pf_lines(const void* base, unsigned voff, unsigned ldsdst) {
    asm volatile("s_mov_b32 m0, %2\n\ts_nop 0\n\tglobal_load_lds_dword %0, %1" ::"v"(voff), "s"(base), "s"(ldsdst) : "memory");
}
#define GM_BAR() do { asm volatile("" ::: "memory"); __builtin_amdgcn_s_barrier(); asm volatile("" ::: "memory"); } while (0)
#define GM_SB() __builtin_amdgcn_sched_barrier(0)

template <int BM_, int BN_, int XI_, int WI_, int NSX_, int NSW_> struct Cfg {
    static constexpr int BM = BM_, BN = BN_, XI = XI_, WI = WI_, NSX = NSX_, NSW = NSW_;
    static constexpr int XB = BM * 128, WB = BN * 128, WOFF = NSX * XB, LDS = NSX * XB + NSW * WB;
    static constexpr int PX = BM / 32, PW = BN / 32;
    static constexpr int WPITCH = (WI == W_P384) ? 384 : (WI == W_P448) ? 448 : (WI == W_P256) ? 256 : 0;
};
template <class C> struct Src { const char* x; const char* w; const char* w2; int ksplit; unsigned xstep, wstep; unsigned xv[C::PX]; unsigned wv[C::PW]; };
template <int WI> __device__ __forceinline__ int w_swz(int r, int x) {
    if (WI == W_P384) return x ^ (((((r >> 1) & 1) | (((r >> 3) & 1) << 1))) << 5);
    if (WI == W_P448) return x ^ (((r >> 3) & 1) << 5);
    if (WI == W_P256) return x ^ (((r & 3) | (((r >> 3) & 1) << 2)) << 5);
    return x;
}
template <class C> __device__ __forceinline__ const char* w_tile(const Src<C>& s, int kt) { return kt < s.ksplit ? s.w + (size_t)kt * s.wstep : s.w2 + (size_t)(kt - s.ksplit) * s.wstep; }
template <class C> __device__ __forceinline__ void src_x_raw(Src<C>& s, const void* base, unsigned pitch, int lid, int lane) {
    s.x = (const char*)base - 4096; s.xstep = 128;
#pragma unroll
    for (int i = 0; i < C::PX; ++i) { const int row = 8 * (lid * C::PX + i) + (lane >> 3); s.xv[i] = row * pitch + (((lane & 7) ^ ((row >> 1) & 7)) * 16) + 4096 - (i & 3) * 1024; }
}
template <class C> __device__ __forceinline__ void src_x_own(Src<C>& s, const void* base, int lid, int lane) {
    s.x = (const char*)base - 4096; s.xstep = C::XB;
#pragma unroll
    for (int i = 0; i < C::PX; ++i) s.xv[i] = (lid * C::PX + i) * 1024 + lane * 16 + 4096 - (i & 3) * 1024;
}
template <class C> __device__ __forceinline__ void src_w_own(Src<C>& s, const void* base, int lid, int lane) {
    s.w = (const char*)base - 4096; s.w2 = s.w; s.ksplit = 1 << 20; s.wstep = C::WB;
#pragma unroll
    for (int i = 0; i < C::PW; ++i) s.wv[i] = (lid * C::PW + i) * 1024 + lane * 16 + 4096 - (i & 3) * 1024;
}
template <class C> __device__ __forceinline__ void src_w_raw(Src<C>& s, const void* base, unsigned pitch, unsigned segstride, int lid, int lane) {
    s.w = (const char*)base - 4096; s.w2 = s.w; s.ksplit = 1 << 20; s.wstep = 64 * pitch;
#pragma unroll
    for (int i = 0; i < C::PW; ++i) { const int o = 1024 * (lid * C::PW + i) + 16 * lane; const int r = o / C::WPITCH, x = w_swz<C::WI>(r, o % C::WPITCH);
        s.wv[i] = r * pitch + (x >> 7) * segstride + (x & 127) + 4096 - (i & 3) * 1024; }
}
template <class C, int I0, int I1, bool FIRST> __device__ __forceinline__ void stage_rng(unsigned dx, unsigned dw, const Src<C>& s, const char* xb, const char* wb) {
    if constexpr (I0 < I1) {
        if constexpr (I0 < C::PW) { if constexpr (FIRST || (I0 & 3) == 0) glds_set<I0>(wb, s.wv[I0], dw); else glds_run<I0>(wb, s.wv[I0]); }
        else { constexpr int j = I0 - C::PW; if constexpr (FIRST || (j & 3) == 0) glds_set<j>(xb, s.xv[j], dx); else glds_run<j>(xb, s.xv[j]); }
        stage_rng<C, I0 + 1, I1, false>(dx, dw, s, xb, wb);
    }
}
template <int MT_, int NT_, int N0_, unsigned SWAP_, bool LD_> struct Wv { static constexpr int MT = MT_, NT = NT_, N0 = N0_; static constexpr unsigned SWAP = SWAP_; static constexpr bool LD = LD_; };
template <class W> struct Frag { bf16x8 x[W::MT]; s16x4 wl[W::NT], wh[W::NT]; };
template <class C, class W> struct RdAddr { unsigned x[2]; unsigned w[(C::WI == W_P384) ? 4 : (C::WI == W_P256) ? W::NT : 2]; };
template <class C, class W> __device__ __forceinline__ void rd_init(RdAddr<C, W>& a, int m0, int n0, int lane) {
    const int fr = lane & 15, fq = lane >> 4, g = lane >> 4, q = (lane & 15) >> 2, p = lane & 3;
    if (C::XI == X_RAW) { for (int kk = 0; kk < 2; ++kk) a.x[kk] = (m0 + fr) * 128 + (((kk * 4 + fq) ^ (fr >> 1)) * 16); }
    else { for (int kk = 0; kk < 2; ++kk) a.x[kk] = (kk * 4 + fq) * (C::BM * 16) + (m0 + fr) * 16; }
    if (C::WI == W_P384) { const int s = ((q >> 1) & 1) | ((g & 1) << 1);
        for (int c = 0; c < 4; ++c) a.w[c] = C::WOFF + (8 * g + q) * 384 + ((c ^ s) * 32) + p * 8; }
    else if (C::WI == W_P448) { const int s = g & 1;
        for (int c = 0; c < 2; ++c) a.w[c] = C::WOFF + (8 * g + q) * 448 + (((c ^ s) - c) * 32) + p * 8; }
    else if (C::WI == W_P256) { const int s = (q & 3) | ((g & 1) << 2);
        for (int t = 0; t < W::NT; ++t) a.w[t] = C::WOFF + (8 * g + q) * 256 + (((n0 + t) ^ s) * 32) + p * 8; }
    else { for (int h = 0; h < 2; ++h) a.w[h] = C::WOFF + (p >> 1) * 1024 + (8 * g + 4 * (h ^ (p >> 1)) + q) * 16 + (p & 1) * 8 + W::N0 * 2048; }
}
template <class C, class W> __device__ __forceinline__ s16x4 rd_w(ldsp b, const RdAddr<C, W>& a, int t, int kk, int h) {
    const int c = W::N0 + t;
    if (C::WI == W_P384) return tr_read(b + a.w[c & 3] + (c >> 2) * 128 + kk * 32 * 384 + h * 4 * 384);
    else if (C::WI == W_P448) return tr_read(b + a.w[c & 1] + c * 32 + kk * 32 * 448 + h * 4 * 448);
    else if (C::WI == W_P256) return tr_read(b + a.w[t] + kk * 32 * 256 + h * 4 * 256);
    else return tr_read(b + a.w[h] + t * 2048 + kk * 512);
}
template <class C, class W> __device__ __forceinline__ void rd_one(Frag<W>& f, ldsp bx, ldsp bw, const RdAddr<C, W>& a, int kk, int r) {
    if (r < 2) { if (r == 0) f.wl[0] = rd_w<C, W>(bw, a, 0, kk, 0); else f.wh[0] = rd_w<C, W>(bw, a, 0, kk, 1); }
    else if (r < 2 + W::MT) { const int m = r - 2; f.x[m] = *(const LAS bf16x8*)(bx + a.x[kk] + m * ((C::XI == X_RAW) ? 2048 : 256)); }
    else { const int u = r - W::MT; const int t = u >> 1; if (u & 1) f.wh[t] = rd_w<C, W>(bw, a, t, kk, 1); else f.wl[t] = rd_w<C, W>(bw, a, t, kk, 0); }
}
template <class W> __device__ __forceinline__ void mma_one(f32x4 (&acc)[W::NT][W::MT], const Frag<W>& f, int i) {
    const int t = i / W::MT, m = i % W::MT;
    const bf16x8 w = __builtin_shufflevector(f.wl[t], f.wh[t], 0, 1, 2, 3, 4, 5, 6, 7);
    acc[t][m] = ((W::SWAP >> t) & 1) ? __builtin_amdgcn_mfma_f32_16x16x32_bf16(f.x[m], w, acc[t][m], 0, 0, 0)
                                     : __builtin_amdgcn_mfma_f32_16x16x32_bf16(w, f.x[m], acc[t][m], 0, 0, 0);
}
template <int RFRONT_, int VA_, int RF1_ = RFRONT_> struct Sch { static constexpr int RFRONT = RFRONT_, VA = VA_, RF1 = RF1_; };
template <class C, class W, class S, int RF, int V0, int V1, int I>
__device__ __forceinline__ void step_slot(f32x4 (&acc)[W::NT][W::MT], const Frag<W>& fc, Frag<W>& fn, ldsp bx, ldsp bw, const RdAddr<C, W>& a, int ksn,
                                          const Src<C>& s, const char* xb, const char* wb, unsigned dx, unsigned dw) {
    constexpr int NM = W::NT * W::MT, NR = 2 * W::NT + W::MT, NV = V1 - V0;
    constexpr int RS = (NM * RF / 16) < 1 ? 1 : (NM * RF / 16);
    if constexpr (I < NM) {
        mma_one<W>(acc, fc, I);
        if constexpr (I < RS) {
#pragma unroll
            for (int r = I * NR / RS; r < (I + 1) * NR / RS; ++r) rd_one<C, W>(fn, bx, bw, a, ksn, r);
        }
        if constexpr (W::LD) {
            constexpr int v0 = V0 + I * NV / NM, v1 = V0 + (I + 1) * NV / NM;
            stage_rng<C, v0, v1, v0 == V0>(dx, dw, s, xb, wb);
        }
        GM_SB();
        step_slot<C, W, S, RF, V0, V1, I + 1>(acc, fc, fn, bx, bw, a, ksn, s, xb, wb, dx, dw);
    }
}
struct NoHook { static constexpr bool PRE = false; __device__ __forceinline__ void pre_x() const {} __device__ __forceinline__ void at(int) const {} };
template <class C, class W, class S, class H>
__device__ __forceinline__ void gemm_loop(ldsp lds, const Src<C>& s, int nt, f32x4 (&acc)[W::NT][W::MT], int lid, int m0, int n0, int lane, H& hook DBG_PARAMS) {
    constexpr int PX = C::PX, PW = C::PW, NSX = C::NSX, NSW = C::NSW, NV = PX + PW;
    static_assert(S::VA >= PW && S::VA <= NV, "all W pieces go out in the step behind the barrier");
    static_assert(NSX >= NSW && NSW >= 2, "ring depths");
    RdAddr<C, W> a; rd_init<C, W>(a, m0, n0, lane);
    const unsigned ldb = (unsigned)(uintptr_t)lds;
    const unsigned lx0 = ldb + lid * PX * 1024, lw0 = ldb + C::WOFF + lid * PW * 1024;
    Frag<W> f0, f1;
    const char* wb = w_tile<C>(s, NSW - 1); const char* xb = s.x + (size_t)(NSX - 1) * s.xstep;
    unsigned dw = lw0 + (NSW - 1) * C::WB, dx = lx0 + (NSX - 1) * C::XB;
    if constexpr (H::PRE) {
        if constexpr (W::LD) {
#pragma unroll
            for (int i = 0; i < NSW; ++i) stage_rng<C, 0, PW, true>(0, lw0 + i * C::WB, s, nullptr, w_tile<C>(s, i));
        }
        hook.pre_x();
        if constexpr (W::LD) {
#pragma unroll
            for (int i = 0; i < NSX - 1; ++i) stage_rng<C, PW, NV, true>(lx0 + i * C::XB, 0, s, s.x + (size_t)i * s.xstep, nullptr);
            if constexpr (S::VA > PW) stage_rng<C, PW, S::VA, true>(dx, dw, s, xb, wb);
            asm volatile("s_waitcnt vmcnt(%0)" ::"n"(S::VA - PW) : "memory");
        }
    } else if constexpr (W::LD) {
#pragma unroll
        for (int i = 0; i < NSX - 1; ++i) {
            if (i < NSW - 1) stage_rng<C, 0, PW, true>(0, lw0 + i * C::WB, s, nullptr, w_tile<C>(s, i));
            stage_rng<C, PW, NV, true>(lx0 + i * C::XB, 0, s, s.x + (size_t)i * s.xstep, nullptr);
        }
        stage_rng<C, 0, S::VA, true>(dx, dw, s, xb, wb);
        constexpr int Y = (NSX - 2) * PX + (NSW - 2) * PW + S::VA;
        asm volatile("s_waitcnt vmcnt(%0)" ::"n"(Y) : "memory");
    }
    GM_BAR();
#pragma unroll
    for (int r = 0; r < 2 * W::NT + W::MT; ++r) rd_one<C, W>(f0, lds, lds, a, 0, r);
    int bx = 0, bw = 0, kx = NSX - 1, kw = NSW - 1;
    __builtin_amdgcn_s_waitcnt(0xC07F);
    for (int t = 0; t < nt; ++t) {
        ldsp cx = lds + bx * C::XB, cw = lds + bw * C::WB;
        const int nbx = (bx + 1 == NSX) ? 0 : bx + 1, nbw = (bw + 1 == NSW) ? 0 : bw + 1;
        ldsp nx = lds + nbx * C::XB, nw = lds + nbw * C::WB;
        TRACE(t);
        GM_SB();
#define GM_STEP(FC, FN, BX, BW, KSN, RF, V0, V1) step_slot<C, W, S, RF, V0, V1, 0>(acc, FC, FN, BX, BW, a, KSN, s, xb, wb, dx, dw)
        GM_STEP(f0, f1, cx, cw, 1, S::RFRONT, S::VA, NV);
        hook.at(t);
        asm volatile("s_waitcnt lgkmcnt(0)" ::: "memory");
        if constexpr (W::LD) {
            constexpr int pw = 1 - NSW, px = 1 - NSX, last = (pw > px) ? pw : px, nper = (-1) - last, Y = nper * NV + ((pw > px) ? PX : 0);
            asm volatile("s_waitcnt vmcnt(%0)" ::"n"(Y) : "memory");
        }
        GM_BAR();
        GM_SB();
        if constexpr (W::LD) {
            kx = (kx + 1 < nt) ? kx + 1 : kx; kw = (kw + 1 < nt) ? kw + 1 : kw;
            xb = s.x + (size_t)kx * s.xstep; wb = w_tile<C>(s, kw);
            dx = lx0 + bx * C::XB; dw = lw0 + bw * C::WB;
        }
        GM_STEP(f1, f0, nx, nw, 0, S::RF1, 0, S::VA);
#undef GM_STEP
        bx = nbx; bw = nbw;
    }
    GM_SB();
    asm volatile("s_waitcnt vmcnt(0) lgkmcnt(0)" ::: "memory");
    GM_BAR();
}
template <class W> __device__ __forceinline__ void acc_zero(f32x4 (&acc)[W::NT][W::MT]) {
#pragma unroll
    for (int t = 0; t < W::NT; ++t)
#pragma unroll
        for (int m = 0; m < W::MT; ++m) acc[t][m] = (f32x4){0.f, 0.f, 0.f, 0.f};
}
__device__ __forceinline__ u32x4 pair16(const f32x4& A, const f32x4& B) {
    const unsigned a0 = pk_bf16(A[0], A[1]), a1 = pk_bf16(A[2], A[3]), b0 = pk_bf16(B[0], B[1]), b1 = pk_bf16(B[2], B[3]);
    const auto r0 = __builtin_amdgcn_permlane16_swap(a0, b0, false, false); const auto r1 = __builtin_amdgcn_permlane16_swap(a1, b1, false, false);
    return (u32x4){r0[0], r1[0], r0[1], r1[1]};
}

namespace g32 {
enum { W_P448 = 1, W_P256 = 2, W_OWNT = 3 };
template <int BM_, int BN_, int XI_, int WI_, int NS_> struct Cfg {
    static constexpr int BM = BM_, BN = BN_, XI = XI_, WI = WI_, NS = NS_;
    static constexpr int XB = BM * 128, WB = BN * 128, WOFF = NS * XB, LDS = NS * (XB + WB);
    static constexpr int PX = BM / 32, PW = BN / 32;
    static constexpr int WPITCH = (WI == W_P448) ? 448 : (WI == W_P256) ? 256 : 16;
};
template <class C> __device__ __forceinline__ void src_w_raw(Src<C>& s, const void* base, unsigned pitch, int lid, int lane) {
    s.w = (const char*)base - 4096; s.w2 = s.w; s.ksplit = 1 << 20; s.wstep = 64 * pitch;
#pragma unroll
    for (int i = 0; i < C::PW; ++i) { const int o = 1024 * (lid * C::PW + i) + 16 * lane; const int r = o / C::WPITCH; int x = o % C::WPITCH;
        if (C::WI == W_P256) x ^= (r & 3) << 6;
        s.wv[i] = r * pitch + x + 4096 - (i & 3) * 1024; }
}
template <int MT_, int NT_, bool SWAP_, bool LD_> struct Wv { static constexpr int MT = MT_, NT = NT_; static constexpr bool SWAP = SWAP_, LD = LD_; };
template <class C, class W> struct Frag { bf16x8 x[W::MT]; s16x4 wl[W::NT], wh[W::NT]; };
template <class C, class W> struct RdAddr { unsigned x[(C::XI == X_RAW) ? 4 : 1]; unsigned w[(C::WI == W_P256) ? W::NT : 1]; };
template <class C, class W> __device__ __forceinline__ void rd_init(RdAddr<C, W>& a, int m0, int n0  , int lane) {
    const int r = lane & 31, h = lane >> 5, g = lane >> 4, q = (lane & 15) >> 2, p = lane & 3;
    if (C::XI == X_RAW) { for (int ks = 0; ks < 4; ++ks) a.x[ks] = (m0 + r) * 128 + (((2 * ks + h) ^ ((r >> 1) & 7)) * 16); }
    else a.x[0] = h * (C::BM * 16) + (m0 + r) * 16;
    const int rowp = 8 * (g >> 1) + q, colp = (g & 1) * 32 + p * 8;
    if (C::WI == W_P448) a.w[0] = C::WOFF + rowp * 448 + colp + n0 * 64;
    else if (C::WI == W_P256) { for (int t = 0; t < W::NT; ++t) a.w[t] = C::WOFF + rowp * 256 + (((n0 + t) ^ q) * 64) + colp; }
    else a.w[0] = C::WOFF + h * (C::BN * 16) + (n0 * 32 + r) * 16;
}
template <class C, class W> constexpr int n_reads() { return W::MT + (C::WI == W_OWNT ? W::NT : 2 * W::NT); }
template <class C, class W> __device__ __forceinline__ void rd_one(Frag<C, W>& f, ldsp bx, ldsp bw, const RdAddr<C, W>& a, int ks, int r) {
    constexpr int PER = (C::WI == W_OWNT) ? 1 : 2;
    if (r >= PER && r < PER + W::MT) { const int m = r - PER;
        if (C::XI == X_RAW) f.x[m] = *(const LAS bf16x8*)(bx + a.x[ks] + m * 4096);
        else f.x[m] = *(const LAS bf16x8*)(bx + a.x[0] + ks * (2 * C::BM * 16) + m * 512);
        return; }
    const int u = r < PER ? r : r - W::MT, t = u / PER, hh = u % PER;
    if (C::WI == W_OWNT) { const bf16x8 v = *(const LAS bf16x8*)(bw + a.w[0] + ks * (2 * C::BN * 16) + t * 512); f.wl[t] = __builtin_shufflevector(v, v, 0, 1, 2, 3); f.wh[t] = __builtin_shufflevector(v, v, 4, 5, 6, 7); }
    else { s16x4 v;
        if (C::WI == W_P448) v = tr_read(bw + a.w[0] + t * 64 + (ks * 16 + hh * 4) * 448);
        else v = tr_read(bw + a.w[t] + (ks * 16 + hh * 4) * 256);
        if (hh) f.wh[t] = v; else f.wl[t] = v; }
}
template <class C, class W> __device__ __forceinline__ void rd_frags(Frag<C, W>& f, ldsp bx, ldsp bw, const RdAddr<C, W>& a, int ks) {
#pragma unroll
    for (int r = 0; r < n_reads<C, W>(); ++r) rd_one<C, W>(f, bx, bw, a, ks, r);
}
template <class C, class W> __device__ __forceinline__ void mma_one(f32x16 (&acc)[W::NT][W::MT], const Frag<C, W>& f, int i) {
    const int t = i / W::MT, m = i % W::MT;
    const bf16x8 w = __builtin_shufflevector(f.wl[t], f.wh[t], 0, 1, 2, 3, 4, 5, 6, 7);
    acc[t][m] = W::SWAP ? __builtin_amdgcn_mfma_f32_32x32x16_bf16(w, f.x[m], acc[t][m], 0, 0, 0) : __builtin_amdgcn_mfma_f32_32x32x16_bf16(f.x[m], w, acc[t][m], 0, 0, 0);
}
template <class C, class W, int V0, int V1, int I>
__device__ __forceinline__ void step_slot(f32x16 (&acc)[W::NT][W::MT], const Frag<C, W>& fc, Frag<C, W>& fn, ldsp bx, ldsp bw, const RdAddr<C, W>& a, int ksn,
                                          const Src<C>& s, const char* xb, const char* wb, unsigned dx, unsigned dw) {
    constexpr int NM = W::NT * W::MT, NR = n_reads<C, W>(), NV = V1 - V0;
    if constexpr (I < NM) {
        mma_one<C, W>(acc, fc, I);
#pragma unroll
        for (int r = I * NR / NM; r < (I + 1) * NR / NM; ++r) rd_one<C, W>(fn, bx, bw, a, ksn, r);
        if constexpr (W::LD) { constexpr int v0 = V0 + I * NV / NM, v1 = V0 + (I + 1) * NV / NM; stage_rng<C, v0, v1, v0 == V0>(dx, dw, s, xb, wb); }
        GM_SB();
        step_slot<C, W, V0, V1, I + 1>(acc, fc, fn, bx, bw, a, ksn, s, xb, wb, dx, dw);
    }
}
template <class C, class W, int D, class H>
__device__ __forceinline__ void gemm_loop(ldsp lds, const Src<C>& s, int nt, f32x16 (&acc)[W::NT][W::MT], int lid, int m0, int n0, int lane, H& hook DBG_PARAMS) {
    constexpr int PX = C::PX, PW = C::PW, NS = C::NS, NV = PX + PW;
    constexpr int Q1 = (NV + 3) / 4, Q2 = (NV + 1) / 2, Q3 = (3 * NV + 3) / 4;
    RdAddr<C, W> a; rd_init<C, W>(a, m0, n0, lane);
    const unsigned ldb = (unsigned)(uintptr_t)lds;
    const unsigned lx0 = ldb + lid * PX * 1024, lw0 = ldb + C::WOFF + lid * PW * 1024;
    Frag<C, W> f0, f1, f2, f3;
    const char* wb = w_tile<C>(s, NS - 1); const char* xb = s.x + (size_t)(NS - 1) * s.xstep;
    unsigned dw = lw0 + (NS - 1) * C::WB, dx = lx0 + (NS - 1) * C::XB;
    constexpr int PRE = (D == 1) ? Q1 : Q2;
    if constexpr (H::PRE) {
        static_assert(PRE <= PW, "prologue split");
        if constexpr (W::LD) {
#pragma unroll
            for (int i = 0; i < NS - 1; ++i) stage_rng<C, 0, PW, true>(0, lw0 + i * C::WB, s, nullptr, w_tile<C>(s, i));
            stage_rng<C, 0, PRE, true>(dx, dw, s, xb, wb);
        }
        hook.pre_x();
        if constexpr (W::LD) {
#pragma unroll
            for (int i = 0; i < NS - 1; ++i) stage_rng<C, PW, NV, true>(lx0 + i * C::XB, 0, s, s.x + (size_t)i * s.xstep, nullptr);
            asm volatile("s_waitcnt vmcnt(0)" ::: "memory");
        }
    } else if constexpr (W::LD) {
#pragma unroll
        for (int i = 0; i < NS - 1; ++i) stage_rng<C, 0, NV, true>(lx0 + i * C::XB, lw0 + i * C::WB, s, s.x + (size_t)i * s.xstep, w_tile<C>(s, i));
        stage_rng<C, 0, PRE, true>(dx, dw, s, xb, wb);
        asm volatile("s_waitcnt vmcnt(%0)" ::"n"((NS - 2) * NV + PRE) : "memory");
    }
    GM_BAR();
    rd_frags<C, W>(f0, lds, lds, a, 0);
    if (D == 2) rd_frags<C, W>(f1, lds, lds, a, 1);
    int slot = 0, kn = NS - 1;
    __builtin_amdgcn_s_waitcnt(0xC07F);
    for (int t = 0; t < nt; ++t) {
        ldsp cx = lds + slot * C::XB, cw = lds + slot * C::WB;
        const int ns = (slot + 1 == NS) ? 0 : slot + 1;
        ldsp nx = lds + ns * C::XB, nw = lds + ns * C::WB;
        TRACE(t);
        GM_SB();
#define G32_STEP(FC, FN, BX, BW, KSN, V0, V1) step_slot<C, W, V0, V1, 0>(acc, FC, FN, BX, BW, a, KSN, s, xb, wb, dx, dw)
        if constexpr (D == 1) { G32_STEP(f0, f1, cx, cw, 1, Q1, Q2); G32_STEP(f1, f2, cx, cw, 2, Q2, Q3); G32_STEP(f2, f3, cx, cw, 3, Q3, NV); }
        else { G32_STEP(f0, f2, cx, cw, 2, Q2, Q3); G32_STEP(f1, f3, cx, cw, 3, Q3, NV); }
        hook.at(t);
        asm volatile("s_waitcnt lgkmcnt(0)" ::: "memory");
        if constexpr (W::LD) asm volatile("s_waitcnt vmcnt(%0)" ::"n"((NS - 2) * NV) : "memory");
        GM_BAR();
        GM_SB();
        if constexpr (W::LD) { kn = (kn + 1 < nt) ? kn + 1 : kn; xb = s.x + (size_t)kn * s.xstep; wb = w_tile<C>(s, kn); dx = lx0 + slot * C::XB; dw = lw0 + slot * C::WB; }
        if constexpr (D == 1) { G32_STEP(f3, f0, nx, nw, 0, 0, Q1); }
        else { G32_STEP(f2, f0, nx, nw, 0, 0, Q1); G32_STEP(f3, f1, nx, nw, 1, Q1, Q2); }
#undef G32_STEP
        slot = ns;
    }
    GM_SB();
    asm volatile("s_waitcnt vmcnt(0) lgkmcnt(0)" ::: "memory");
    GM_BAR();
}
template <class W> __device__ __forceinline__ void acc_zero(f32x16 (&acc)[W::NT][W::MT]) {
#pragma unroll
    for (int t = 0; t < W::NT; ++t)
#pragma unroll
        for (int m = 0; m < W::MT; ++m) acc[t][m] = (f32x16){};
}
__device__ __forceinline__ u32x4 pair32(unsigned a0, unsigned a1, unsigned b0, unsigned b1) {
    const auto r0 = __builtin_amdgcn_permlane32_swap(a0, b0, false, false); const auto r1 = __builtin_amdgcn_permlane32_swap(a1, b1, false, false);
    return (u32x4){r0[0], r1[0], r0[1], r1[1]};
}
}

struct Params {
    const u16* ax; const u16* wqkv; const u16* wo; const u16* bx; const u16* win; const u16* wsp; const u16* bsp; const u16* wout; const u16* gw;
    u16* out; unsigned char* ws;
};

using CfgQ = Cfg<256, 192, X_RAW, W_P384, 3, 2>;
struct Warm {
    const char* base; unsigned pitch; int lpr_log2; int cnt; unsigned scratch;
    __device__ __forceinline__ void issue(int n, int wid, int lane) const {
        if (n >= 0 && n < cnt && wid == 4 + (n & 3)) { const int idx = n * 64 + lane; pf_lines(base, (unsigned)(idx >> lpr_log2) * pitch + (unsigned)(idx & ((1 << lpr_log2) - 1)) * 128u, scratch); }
    }
};
struct WarmHook {
    static constexpr bool PRE = false; int wid, lane, t0; Warm w0, w1;
    __device__ __forceinline__ void pre_x() const {}
    __device__ __forceinline__ void at(int t) const { w0.issue(t - t0, wid, lane); w1.issue(t - t0 - w0.cnt, wid, lane); }
};
struct SigHook {
    static constexpr bool PRE = false; unsigned char* ws; int wid, lane; Warm w0;
    __device__ __forceinline__ void pre_x() const {}
    __device__ __forceinline__ void at(int t) const {
        if (t == 1 && wid >= 4) asm volatile("s_waitcnt vmcnt(0)" ::: "memory");
        if (t == 2 && wid == 4 && lane == 0) signal(ws, C_GATE);
        w0.issue(t - 2, wid, lane);
    }
};
template <int WN> __device__ __forceinline__ void qkv_wave(const Params& p, ldsp lds, int pair, int panel, int wid, int lane) {
    using C = CfgQ; using W = Wv<4, 6, WN * 6, WN ? 0x3Cu : 0u, WN == 0>;
    const int lid = wid & 3, m0 = (wid & 3) * 64;
    Src<C> s;
    src_x_raw<C>(s, p.ax + (size_t)panel * 256 * DM, DM * 2, lid, lane);
    src_w_raw<C>(s, p.wqkv + pair * 64, 3 * DM * 2, DM * 2, lid, lane);
    f32x4 acc[6][4]; acc_zero<W>(acc);
    STAMP(0, 1);
    Warm none{nullptr, 0, 0, 0, 0};
    SigHook hk{p.ws, wid, lane, none};
    gemm_loop<C, W, Sch<12, 7>>(lds, s, DM / 64, acc, lid, m0, 0, lane, hk DBG_ARGS(0));
    STAMP(0, 2);
    const int fr = lane & 15, fq = lane >> 4;
    const int row0 = panel * 256 + m0;
    const rsrc_t rs = ws_rsrc(p.ws);
#pragma unroll
    for (int t = 0; t < 6; t += 2) {
        const int T = WN * 6 + t, kind = T >> 2, head = pair * 2 + ((T >> 1) & 1);
        if (kind < 2) {
            const unsigned base = (unsigned)(kind == 0 ? WS_Q : WS_K);
            const int c = 2 * (fq & 1) + (fq >> 1);
            const float sc = kind == 0 ? QSCALE : 1.0f;
#pragma unroll
            for (int m = 0; m < 4; ++m) { const int row = row0 + m * 16 + fr;
                st16_wt(rs, base + ((head * 64 + (row >> 5)) * 4 + c) * 512 + (row & 31) * 16, pair16(acc[t][m] * sc, acc[t + 1][m] * sc)); }
        } else {
#pragma unroll
            for (int tt = 0; tt < 2; ++tt)
#pragma unroll
                for (int m = 0; m < 4; m += 2) {
                    const f32x4 A = acc[t + tt][m], B = acc[t + tt][m + 1];
                    const unsigned a0 = pk_bf16(A[0], A[1]), a1 = pk_bf16(A[2], A[3]), b0 = pk_bf16(B[0], B[1]), b1 = pk_bf16(B[2], B[3]);
                    const auto r0 = __builtin_amdgcn_permlane32_swap(a0, b0, false, false); const auto r1 = __builtin_amdgcn_permlane32_swap(a1, b1, false, false);
                    const int kb = (row0 >> 4) + m + (fq >> 1), h = fq & 1, d = tt * 16 + fr;
                    st16_wt(rs, (unsigned)WS_V + ((head * 128 + kb) * 2 + h) * 512 + d * 16, (u32x4){r0[0], r1[0], r0[1], r1[1]});
                }
        }
    }
    asm volatile("s_waitcnt vmcnt(0)" ::: "memory");
    GM_BAR();
    if (threadIdx.x == 0) signal(p.ws, C_Q + 64 * pair);
}

using CfgZ = g32::Cfg<128, 224, X_RAW, g32::W_P448, 3>;
using CfgS = g32::Cfg<128, 224, X_RAW, g32::W_OWNT, 3>;
using CfgO = g32::Cfg<128, 128, X_OWN, g32::W_P256, 4>;
__device__ __forceinline__ float gelu_tanh(float x) {
    const float t = x * x * (-0.044715f * 2.0f * 0.7978845608028654f * 1.4426950408889634f) + (-2.0f * 0.7978845608028654f * 1.4426950408889634f);
    return x * __builtin_amdgcn_rcpf(1.0f + __builtin_amdgcn_exp2f(x * t));
}
template <int CTRL> __device__ __forceinline__ float dpp_add(float x) { return x + __int_as_float(__builtin_amdgcn_update_dpp(0, __float_as_int(x), CTRL, 0xf, 0xf, false)); }
__device__ __forceinline__ float red32_pair(float a, float b) {
    const auto r = __builtin_amdgcn_permlane16_swap(__float_as_uint(a), __float_as_uint(b), false, false);
    float x = __uint_as_float(r[0]) + __uint_as_float(r[1]);
    x = dpp_add<0x128>(x); x = dpp_add<0x124>(x); x = dpp_add<0x122>(x); x = dpp_add<0x121>(x);
    return x;
}
constexpr int LDS_TAB = CfgZ::LDS;
template <int NT> struct LnHook {
    static constexpr bool PRE = false;
    const Params& p; ldsp lds; int panel, j, wid, lane, n0; unsigned (&z2)[NT][8]; Warm w0;
    __device__ __forceinline__ void pre_x() const {}
    __device__ __forceinline__ void at(int t) {
        LAS f32x2* R = (LAS f32x2*)(lds + LDS_TAB + 2048);
        if (t >= 6) w0.issue(2 * (t - 6), wid, lane), w0.issue(2 * (t - 6) + 1, wid, lane);
        if (t == 2) {
            if (wid == 4) {
                gu32* ctl = (gu32*)(p.ws + WS_CTL);
                for (unsigned spins = 0;;) {
                    if ((unsigned)__builtin_amdgcn_readfirstlane(__hip_atomic_load(ctl + C_S + 64 * panel, RLX_AGENT)) >= 8u) break;
                    __builtin_amdgcn_s_sleep(1);
                    if (++spins > (1u << 20)) { __hip_atomic_store(ctl + C_TMO, 1u, RLX_AGENT); break; }
                }
                asm volatile("" ::: "memory");
                gu64* slots = (gu64*)(p.ws + WS_STAT) + (size_t)panel * 128 * 8;
#pragma unroll
                for (int i = 0; i < 2; ++i) { const int row = i * 64 + lane;
                    float mt8[8], m28[8], ms = 0.f;
#pragma unroll
                    for (int u = 0; u < 8; ++u) { const unsigned long long w = __hip_atomic_load(slots + row * 8 + u, RLX_AGENT); mt8[u] = __uint_as_float((unsigned)w); m28[u] = __uint_as_float((unsigned)(w >> 32)); ms += mt8[u]; }
                    const float mean = ms * 0.125f; float q = 0.f;
#pragma unroll
                    for (int u = 0; u < 8; ++u) { const float dm = mt8[u] - mean; q += m28[u] + 224.0f * dm * dm; }
                    R[row] = (f32x2){mean, __builtin_amdgcn_rsqf(q * (1.0f / 1792.0f) + 1e-5f)}; }
            }
        } else if (t == 3) {
            const int r = lane & 31, h = lane >> 5, m0 = (wid & 3) * 32;
            const rsrc_t rs = ws_rsrc(p.ws);
            const int srow = panel * 128 + m0;
#pragma unroll
            for (int i = 0; i < 4; i += 2) {
                const f32x4 ra = *(const LAS f32x4*)(R + m0 + 8 * i + 4 * h), rb = *(const LAS f32x4*)(R + m0 + 8 * i + 4 * h + 2);
                const f32x4 rc = *(const LAS f32x4*)(R + m0 + 8 * i + 8 + 4 * h), rd = *(const LAS f32x4*)(R + m0 + 8 * i + 8 + 4 * h + 2);
                const int k = srow + 8 * (i + h);
                const unsigned base = (unsigned)WS_Z + ((j * 32 + (k >> 6)) * 8 + ((k & 63) >> 3)) * 3584 + (n0 * 32 + r) * 16;
#pragma unroll
                for (int tt = 0; tt < NT; ++tt) {
                    const unsigned a0 = z2[tt][2 * i], a1 = z2[tt][2 * i + 1], b0 = z2[tt][2 * i + 2], b1 = z2[tt][2 * i + 3];
                    st16_wt(rs, base + tt * 512, g32::pair32(pk_bf16((bf_lo(a0) - ra[0]) * ra[1], (bf_hi(a0) - ra[2]) * ra[3]), pk_bf16((bf_lo(a1) - rb[0]) * rb[1], (bf_hi(a1) - rb[2]) * rb[3]),
                                                              pk_bf16((bf_lo(b0) - rc[0]) * rc[1], (bf_hi(b0) - rc[2]) * rc[3]), pk_bf16((bf_lo(b1) - rd[0]) * rd[1], (bf_hi(b1) - rd[2]) * rd[3])));
                }
            }
        } else if (t == 5) {
            asm volatile("s_waitcnt vmcnt(0)" ::: "memory");
        } else if (t == 6) {
            if (threadIdx.x == 256) signal(p.ws, C_Z + 64 * j);
        }
    }
};
struct WaitHook {
    static constexpr bool PRE = true;
    unsigned char* ws; int wid, w0; unsigned n0; int w1; unsigned n1; int tlate, wl; unsigned nl;
    __device__ __forceinline__ void pre_x() const { if (wid == 4) wait2(ws, w0, n0, w1, n1); GM_BAR(); }
    __device__ __forceinline__ void at(int t) const { if (t == tlate && wid == 4) wait2(ws, wl, nl, wl, nl); }
};
template <bool LD> __device__ __forceinline__ void b_wave(const Params& p, ldsp lds, int panel, int j, int wid, int lane) {
    constexpr int NT = LD ? 3 : 4;
    const int lid = wid & 3, m0 = lid * 32, n0 = LD ? 0 : 3;
    const int r = lane & 31, h = lane >> 5;
    unsigned z2[NT][8], z1[NT][8];
    const unsigned pfs = (unsigned)(uintptr_t)lds + LDS_PFS;
    {
        using C = CfgZ; using W = g32::Wv<1, NT, false, LD>; Src<C> s;
        src_x_raw<C>(s, p.ax + (size_t)panel * 128 * DM, DM * 2, lid, lane);
        g32::src_w_raw<C>(s, p.win + FH + j * 224, 2 * FH * 2, lid, lane);
        f32x16 acc[NT][1]; g32::acc_zero<W>(acc);
        STAMP(0, 1);
        Warm wz1{(const char*)p.win + ((j * 224 * 2) & ~127) + (size_t)(panel & 3) * 256 * (2 * FH * 2), 2 * FH * 2, 2, 13, pfs};
        SigHook hk{p.ws, wid, lane, wz1};
        g32::gemm_loop<C, W, 1>(lds, s, DM / 64, acc, lid, m0, n0, lane, hk DBG_ARGS(1));
        STAMP(0, 2);
        LAS f32x2* P = (LAS f32x2*)(lds + LDS_TAB);
        float sm[16], sq[16];
#pragma unroll
        for (int q = 0; q < 16; ++q) { sm[q] = 0.f; sq[q] = 0.f; }
#pragma unroll
        for (int t = 0; t < NT; ++t) {
#pragma unroll
            for (int q = 0; q < 16; q += 2) {
                z2[t][q >> 1] = pk_bf16(gelu_tanh(acc[t][0][q]), gelu_tanh(acc[t][0][q + 1]));
                const float v0 = bf_lo(z2[t][q >> 1]), v1 = bf_hi(z2[t][q >> 1]);
                sm[q] += v0; sq[q] += v0 * v0; sm[q + 1] += v1; sq[q + 1] += v1 * v1; }
        }
#pragma unroll
        for (int k = 0; k < 8; ++k) {
            const float a = red32_pair(sm[2 * k], sm[2 * k + 1]), b = red32_pair(sq[2 * k], sq[2 * k + 1]);
            const int q = 2 * k + ((lane >> 4) & 1), row = m0 + 8 * (q >> 2) + 4 * h + (q & 3);
            if ((lane & 15) == 0) P[row * 2 + (LD ? 0 : 1)] = (f32x2){a, b};
        }
        asm volatile("s_waitcnt lgkmcnt(0)" ::: "memory"); GM_BAR();
        if (wid == 4) {
            gu64* slots = (gu64*)(p.ws + WS_STAT) + (size_t)panel * 128 * 8;
#pragma unroll
            for (int i = 0; i < 2; ++i) { const int row = i * 64 + lane;
                const f32x2 a = P[row * 2], b = P[row * 2 + 1];
                const float su = a.x + b.x, mt = su * (1.0f / 224.0f), m2 = fmaxf(a.y + b.y - su * mt, 0.f);
                __hip_atomic_store(slots + row * 8 + j, ((unsigned long long)__float_as_uint(m2) << 32) | __float_as_uint(mt), RLX_AGENT); }
            asm volatile("s_waitcnt vmcnt(0)" ::: "memory");
            if (lane == 0) signal(p.ws, C_S + 64 * panel);
        }
    }
    STAMP(0, 3);
    {
        using C = CfgZ; using W = g32::Wv<1, NT, true, LD>; Src<C> s;
        src_x_raw<C>(s, p.ax + (size_t)panel * 128 * DM, DM * 2, lid, lane);
        g32::src_w_raw<C>(s, p.win + j * 224, 2 * FH * 2, lid, lane);
        f32x16 acc[NT][1]; g32::acc_zero<W>(acc);
        Warm wsp{(const char*)(p.wsp + (size_t)(panel * 128 + (j & 3) * 32) * SEQ), SEQ * 2, 5, 16, pfs};
        LnHook<NT> hk{p, lds, panel, j, wid, lane, n0, z2, wsp};
        g32::gemm_loop<C, W, 1>(lds, s, DM / 64, acc, lid, m0, n0, lane, hk DBG_ARGS(2));
        STAMP(0, 4);
#pragma unroll
        for (int t = 0; t < NT; ++t)
#pragma unroll
            for (int q = 0; q < 16; q += 2) z1[t][q >> 1] = pk_bf16(gelu_tanh(acc[t][0][q]), gelu_tanh(acc[t][0][q + 1]));
    }
    STAMP(0, 5);
    {
        using C = CfgS; using W = g32::Wv<1, NT, true, LD>; Src<C> s;
        src_x_raw<C>(s, p.wsp + (size_t)panel * 128 * SEQ, SEQ * 2, lid, lane);
        src_w_own<C>(s, p.ws + WS_Z + (size_t)j * 32 * 28672, lid, lane);
        if (wid == 4) wait2(p.ws, C_Z + 64 * j, 16u, C_GATE, 256u);
        GM_BAR();
        STAMP(0, 6);
        f32x16 acc[NT][1]; g32::acc_zero<W>(acc);
        Warm wa{(const char*)(p.wout + j * 128) + (size_t)(panel & 3) * 448 * (DM * 2), DM * 2, 1, 14, pfs};
        Warm wb{(const char*)(p.wo + j * 128) + (size_t)(panel & 3) * 256 * (DM * 2), DM * 2, 1, 8, pfs};
        WarmHook hk{wid, lane, 2, wa, wb};
        g32::gemm_loop<C, W, 1>(lds, s, SEQ / 64, acc, lid, m0, n0, lane, hk DBG_ARGS(3));
        STAMP(0, 7);
        const rsrc_t rs = ws_rsrc(p.ws);
        const int trow = panel * 128 + m0 + r;
        const float b = __uint_as_float((unsigned)p.bsp[trow] << 16), g1 = 1.0f - ((const float*)(p.ws + WS_GATE))[trow];
        const unsigned ub = (unsigned)WS_ACT + (panel * 352 + j * 28 + n0 * 4 + h) * 2048 + (m0 + r) * 16;
#pragma unroll
        for (int t = 0; t < NT; ++t)
#pragma unroll
            for (int i = 0; i < 4; i += 2) {
                unsigned w4[4];
#pragma unroll
                for (int e = 0; e < 4; ++e) { const int q = 4 * i + 2 * e; const unsigned z = z1[t][q >> 1];
                    w4[e] = pk_bf16((acc[t][0][q] + b) * g1 * bf_lo(z), (acc[t][0][q + 1] + b) * g1 * bf_hi(z)); }
                st16_wt(rs, ub + (t * 4 + i) * 2048, g32::pair32(w4[0], w4[1], w4[2], w4[3]));
            }
        asm volatile("s_waitcnt vmcnt(0)" ::: "memory");
        GM_BAR();
        if (threadIdx.x == 256) signal(p.ws, C_U + 64 * panel);
    }
    STAMP(0, 8);
    {
        using C = CfgO; using W = g32::Wv<2, 1, true, LD>;
        const int mo = (wid >> 2) * 64;
        Src<C> s;
        src_x_own<C>(s, p.ws + WS_ACT + (size_t)panel * 352 * 2048, lid, lane);
        g32::src_w_raw<C>(s, p.wout + j * 128, DM * 2, lid, lane);
        s.w2 = (const char*)(p.wo + j * 128) - 4096; s.ksplit = FH / 64;
        f32x16 acc[1][2]; g32::acc_zero<W>(acc);
        WaitHook hk{p.ws, wid, C_U + 64 * panel, 8u, C_U + 64 * panel, 8u, FH / 64 - C::NS, C_O + 64 * (panel >> 2), 32u};
        g32::gemm_loop<C, W, 2>(lds, s, (FH + DM) / 64, acc, lid, mo, lid, lane, hk DBG_ARGS(4));
        STAMP(0, 9);
#pragma unroll
        for (int m = 0; m < 2; ++m)
#pragma unroll
            for (int i = 0; i < 4; i += 2) { const f32x16& A = acc[0][m];
                *(LAS u32x4*)(lds + (mo + m * 32 + r) * 272 + lid * 64 + (i + h) * 16) =
                    g32::pair32(pk_bf16(A[4 * i], A[4 * i + 1]), pk_bf16(A[4 * i + 2], A[4 * i + 3]), pk_bf16(A[4 * i + 4], A[4 * i + 5]), pk_bf16(A[4 * i + 6], A[4 * i + 7])); }
        asm volatile("s_waitcnt lgkmcnt(0)" ::: "memory"); GM_BAR();
#pragma unroll
        for (int i = 0; i < 4; ++i) { const int row = wid * 16 + i * 4 + (lane >> 4);
            *(u32x4*)(p.out + (size_t)(panel * 128 + row) * DM + j * 128 + (lane & 15) * 8) = *(const LAS u32x4*)(lds + row * 272 + (lane & 15) * 16); }
    }
}

constexpr int AT_STAGE = 16384, AT_NS = 4;
#define MFMA32(a, b, c) __builtin_amdgcn_mfma_f32_32x32x16_bf16(a, b, c, 0, 0, 0)
#define MFMA16(a, b, c) __builtin_amdgcn_mfma_f32_16x16x32_bf16(a, b, c, 0, 0, 0)
#define PIN(x) asm volatile("" : "+v"(x))
__device__ __forceinline__ void attn_wg(const Params& p, ldsp lds, int head, int qb, int wid, int lane) {
    if (wid == 4) wait2(p.ws, C_Q + 64 * (head >> 1), 8u, C_GATE, 256u);
    GM_BAR();
    STAMP(0, 3);
    const unsigned char* Kg = p.ws + WS_K + (size_t)head * 131072; const unsigned char* Vg = p.ws + WS_V + (size_t)head * 131072;
    const unsigned ldb = (unsigned)(uintptr_t)lds;
    const unsigned voff = wid * 1024 + lane * 16;
    const char* kb = (const char*)Kg; const char* vb = (const char*)Vg;
    auto stage = [&](int st, int slot) {
        asm volatile("s_add_u32 m0, %2, 0\n\ts_nop 0\n\tglobal_load_lds_dwordx4 %0, %1" ::"v"(voff), "s"(kb + (size_t)st * 8192), "s"(ldb + slot * AT_STAGE + wid * 1024) : "memory", "scc");
        asm volatile("s_add_u32 m0, %2, 0\n\ts_nop 0\n\tglobal_load_lds_dwordx4 %0, %1" ::"v"(voff), "s"(vb + (size_t)st * 8192), "s"(ldb + slot * AT_STAGE + 8192 + wid * 1024) : "memory", "scc");
    };
    stage(0, 0); stage(1, 1); stage(2, 2);
    const int q0 = qb * 512 + wid * 64;
    bf16x8 qf[2][2];
#pragma unroll
    for (int a = 0; a < 2; ++a)
#pragma unroll
        for (int d = 0; d < 2; ++d) qf[a][d] = *(const bf16x8*)(p.ws + WS_Q + (size_t)(head * 64 + (q0 >> 5) + a) * 2048 + d * 1024 + lane * 16);
    const float gt0 = ((const float*)(p.ws + WS_GATE))[q0 + (lane & 31)], gt1 = ((const float*)(p.ws + WS_GATE))[q0 + 32 + (lane & 31)];
    bf16x8 sel;
    { const short one = ((lane & 15) == ((lane >> 4) & 1)) ? (short)0x3f80 : (short)0;
#pragma unroll
      for (int i = 0; i < 8; ++i) sel[i] = one; }
    asm volatile("s_waitcnt vmcnt(0)" ::: "memory");
    GM_BAR();
    f32x16 o[2], negm[2]; f32x4 l[2];
    const f32x16 zero16 = {};
    bf16x8 kA[2], kB[2], vA[2], vB[2];
    ldsp lb = lds + lane * 16;
    kA[0] = *(const LAS bf16x8*)(lb); kA[1] = *(const LAS bf16x8*)(lb + 1024);
#pragma unroll
    for (int a = 0; a < 2; ++a) {
        f32x16 s = MFMA32(kA[0], qf[a][0], zero16); s = MFMA32(kA[1], qf[a][1], s);
        float m = fmaxf(fmaxf(s[0], s[1]), fmaxf(s[2], s[3]));
#pragma unroll
        for (int r = 4; r < 16; r += 2) m = fmaxf(m, fmaxf(s[r], s[r + 1]));
        const auto rr = __builtin_amdgcn_permlane32_swap(__float_as_uint(m), __float_as_uint(m), false, false);
        m = -fmaxf(__uint_as_float(rr[0]), __uint_as_float(rr[1]));
#pragma unroll
        for (int r = 0; r < 16; ++r) negm[a][r] = m;
        PIN(negm[a]);
        o[a] = zero16; l[a] = (f32x4){0.f, 0.f, 0.f, 0.f};
    }
    f32x16 S0, S1; u32x4 p0l, p0h, p1l, p1h;
    S0 = MFMA32(kA[0], qf[0][0], negm[0]); S0 = MFMA32(kA[1], qf[0][1], S0);
    p1l = (u32x4){0u, 0u, 0u, 0u}; p1h = p1l;
    vB[0] = (bf16x8){0, 0, 0, 0, 0, 0, 0, 0}; vB[1] = vB[0];
    STAMP(0, 4);
#define EX4(S, i) do { S[i] = __builtin_amdgcn_exp2f(S[i]); S[i + 1] = __builtin_amdgcn_exp2f(S[i + 1]); S[i + 2] = __builtin_amdgcn_exp2f(S[i + 2]); S[i + 3] = __builtin_amdgcn_exp2f(S[i + 3]); } while (0)
#define PK4(P, S, i) do { P[0] = pk_bf16(S[i], S[i + 1]); P[1] = pk_bf16(S[i + 2], S[i + 3]); P[2] = pk_bf16(S[i + 4], S[i + 5]); P[3] = pk_bf16(S[i + 6], S[i + 7]); } while (0)
#define BC(x) __builtin_bit_cast(bf16x8, x)
#define ASTEP(SC, SN, PCl, PCh, PPl, PPh, KF, QN, VF, AP, LOADS) do { \
        SN = MFMA32(KF[0], qf[QN][0], negm[QN]); EX4(SC, 0); PIN(SC); GM_SB(); \
        SN = MFMA32(KF[1], qf[QN][1], SN); EX4(SC, 4); LOADS; PIN(SC); GM_SB(); \
        o[AP] = MFMA32(VF[0], BC(PPl), o[AP]); EX4(SC, 8); PIN(SC); GM_SB(); \
        o[AP] = MFMA32(VF[1], BC(PPh), o[AP]); EX4(SC, 12); PIN(SC); GM_SB(); \
        l[AP] = MFMA16(sel, BC(PPl), l[AP]); PK4(PCl, SC, 0); PIN(PCl); GM_SB(); \
        l[AP] = MFMA16(sel, BC(PPh), l[AP]); PK4(PCh, SC, 8); PIN(PCh); GM_SB(); } while (0)
#define LDK(KF, base, kt) do { KF[0] = *(const LAS bf16x8*)((base) + (kt) * 2048); KF[1] = *(const LAS bf16x8*)((base) + (kt) * 2048 + 1024); } while (0)
#define LDV(VF, base, kt) do { VF[0] = *(const LAS bf16x8*)((base) + 8192 + (kt) * 2048); VF[1] = *(const LAS bf16x8*)((base) + 8192 + (kt) * 2048 + 1024); } while (0)
    for (int st = 0; st < 16; ++st) {
        ldsp cb = lb + (st & 3) * AT_STAGE, nb = lb + ((st + 1) & 3) * AT_STAGE;
        ASTEP(S0, S1, p0l, p0h, p1l, p1h, kA, 1, vB, 1, { LDK(kB, cb, 1); LDV(vA, cb, 0); });
        ASTEP(S1, S0, p1l, p1h, p0l, p0h, kB, 0, vA, 0, {});
        ASTEP(S0, S1, p0l, p0h, p1l, p1h, kB, 1, vA, 1, { LDK(kA, cb, 2); LDV(vB, cb, 1); });
        ASTEP(S1, S0, p1l, p1h, p0l, p0h, kA, 0, vB, 0, {});
        ASTEP(S0, S1, p0l, p0h, p1l, p1h, kA, 1, vB, 1, { LDK(kB, cb, 3); LDV(vA, cb, 2); });
        ASTEP(S1, S0, p1l, p1h, p0l, p0h, kB, 0, vA, 0, {});
        if (st + 1 < 16) {
            if (st + 2 < 16) asm volatile("s_waitcnt vmcnt(2)" ::: "memory"); else asm volatile("s_waitcnt vmcnt(0)" ::: "memory");
            GM_BAR();
            if (st + 3 < 16) stage(st + 3, (st + 3) & 3);
        }
        ASTEP(S0, S1, p0l, p0h, p1l, p1h, kB, 1, vA, 1, { LDK(kA, nb, 0); LDV(vB, cb, 3); });
        ASTEP(S1, S0, p1l, p1h, p0l, p0h, kA, 0, vB, 0, {});
    }
    o[1] = MFMA32(vB[0], BC(p1l), o[1]); o[1] = MFMA32(vB[1], BC(p1h), o[1]);
    l[1] = MFMA16(sel, BC(p1l), l[1]); l[1] = MFMA16(sel, BC(p1h), l[1]);
#undef EX4
#undef PK4
#undef ASTEP
#undef LDK
#undef LDV
    STAMP(0, 5);
    const rsrc_t rs = ws_rsrc(p.ws);
#pragma unroll
    for (int a = 0; a < 2; ++a) {
        const int q = lane & 31, hh = lane >> 5;
        const float l0 = __shfl(l[a][0], q & 15), l1 = __shfl(l[a][1], q & 15);
        const float lsum = (q < 16) ? l0 : l1;
        const float sc = (a ? gt1 : gt0) * __builtin_amdgcn_rcpf(lsum);
        const int row = q0 + a * 32 + q, panel = row >> 7;
#pragma unroll
        for (int i = 0; i < 4; i += 2) {
            const unsigned a0 = pk_bf16(o[a][4 * i] * sc, o[a][4 * i + 1] * sc), a1 = pk_bf16(o[a][4 * i + 2] * sc, o[a][4 * i + 3] * sc);
            const unsigned b0 = pk_bf16(o[a][4 * i + 4] * sc, o[a][4 * i + 5] * sc), b1 = pk_bf16(o[a][4 * i + 6] * sc, o[a][4 * i + 7] * sc);
            const auto r0 = __builtin_amdgcn_permlane32_swap(a0, b0, false, false); const auto r1 = __builtin_amdgcn_permlane32_swap(a1, b1, false, false);
            const int c = 224 + head * 4 + i + hh;
            st16_wt(rs, (unsigned)WS_ACT + (panel * 352 + c) * 2048 + (row & 127) * 16, (u32x4){r0[0], r1[0], r0[1], r1[1]});
        }
    }
    asm volatile("s_waitcnt vmcnt(0)" ::: "memory");
    GM_BAR();
    if (threadIdx.x == 0) signal(p.ws, C_O + 64 * qb);
}

__device__ __forceinline__ void gate_row(const Params& p, int row, int lane) {
    const u32x4* xr = (const u32x4*)(p.ax + (size_t)row * DM) + lane * 2; const u32x4* gw = (const u32x4*)p.gw + lane * 2;
    float s = 0.f;
#pragma unroll
    for (int i = 0; i < 2; ++i) { const u32x4 a = xr[i], b = gw[i];
#pragma unroll
        for (int e = 0; e < 4; ++e) s += bf_lo(a[e]) * bf_lo(b[e]) + bf_hi(a[e]) * bf_hi(b[e]); }
#pragma unroll
    for (int o = 32; o >= 1; o >>= 1) s += __shfl_xor(s, o);
    if (lane == 0) __hip_atomic_store((gu32*)(p.ws + WS_GATE) + row, __float_as_uint(__builtin_amdgcn_rcpf(1.0f + __builtin_amdgcn_exp2f(-1.4426950408889634f * s))), RLX_AGENT);
}
constexpr int LDS_BYTES = CfgQ::LDS + 256;
static_assert(LDS_TAB + 3072 <= LDS_BYTES && CfgO::LDS <= LDS_BYTES && AT_NS * AT_STAGE <= LDS_BYTES, "LDS map");
__global__ __launch_bounds__(512, 2) void fused(Params p) {
    extern __shared__ __attribute__((aligned(16))) unsigned char smem[]; ldsp lds = (ldsp)smem;
    const int lane = threadIdx.x & 63, wid = __builtin_amdgcn_readfirstlane(threadIdx.x >> 6);
    const int b = blockIdx.x, x = b & 7, role = (b >> 3) & 1, idx = b >> 4;
    STAMP(0, 0);
    if (wid >= 4) { gate_row(p, b * 8 + (wid - 4) * 2, lane); gate_row(p, b * 8 + (wid - 4) * 2 + 1, lane); }
    if (role == 0) {
        const int pair = 4 * (x >> 1) + (idx >> 2), panel = 4 * (x & 1) + (idx & 3);
        if (wid < 4) qkv_wave<0>(p, lds, pair, panel, wid, lane); else qkv_wave<1>(p, lds, pair, panel, wid, lane);
        STAMP(0, 6);
        attn_wg(p, lds, 4 * x + (idx >> 2), idx & 3, wid, lane);
    } else {
        const int panel = 8 * (x & 1) + 4 * ((x >> 1) & 1) + (idx >> 2), j = 4 * (x >> 2) + (idx & 3);
        if (wid < 4) b_wave<true>(p, lds, panel, j, wid, lane); else b_wave<false>(p, lds, panel, j, wid, lane);
    }
    STAMP(0, 15);
}
}

extern "C" void kernel_launch(void* const* d_in, const int* in_sizes, int n_in, void* d_out, int out_size, void* d_ws, size_t ws_size, hipStream_t stream) {
    using namespace gm;
    Params p;
    p.ax = (const u16*)d_in[0]; p.wqkv = (const u16*)d_in[1]; p.wo = (const u16*)d_in[2]; p.bx = (const u16*)d_in[3]; p.win = (const u16*)d_in[4];
    p.wsp = (const u16*)d_in[5]; p.bsp = (const u16*)d_in[6]; p.wout = (const u16*)d_in[7]; p.gw = (const u16*)d_in[8];
    p.out = (u16*)d_out; p.ws = (unsigned char*)d_ws;
    (void)hipFuncSetAttribute((const void*)fused, hipFuncAttributeMaxDynamicSharedMemorySize, LDS_BYTES);
    (void)hipMemsetAsync(p.ws + WS_CTL, 0, CTL_BYTES, stream);
    fused<<<256, 512, LDS_BYTES, stream>>>(p);
}
```

```cpp
#include <hip/hip_runtime.h>
#include <hip/hip_bf16.h>
#include <stdint.h>

#define LAS __attribute__((address_space(3)))
typedef short bf16x8 __attribute__((ext_vector_type(8)));
typedef short s16x4 __attribute__((ext_vector_type(4)));
typedef float f32x2 __attribute__((ext_vector_type(2)));
typedef float f32x4 __attribute__((ext_vector_type(4)));
typedef float f32x16 __attribute__((ext_vector_type(16)));
typedef unsigned u32x4 __attribute__((ext_vector_type(4)));
typedef unsigned u32x2 __attribute__((ext_vector_type(2)));
typedef LAS unsigned char* ldsp;
typedef unsigned short u16;

#define SPD 1
namespace gm {
constexpr int SEQ = 2048, DM = 1024, NH = 32, FH = 1792;
constexpr size_t WS_CTL = 0;
constexpr size_t CTL_BYTES = 16384;
constexpr size_t WS_GATE = 16384;
constexpr size_t WS_STAT = 32768;
constexpr size_t WS_Q = 1u << 20;
constexpr size_t WS_K = WS_Q + (4u << 20);
constexpr size_t WS_V = WS_K + (4u << 20);
constexpr size_t WS_Z = WS_V + (4u << 20);
constexpr size_t WS_ACT = WS_Z + (8u << 20);
constexpr int LDS_PFS = 147456 - 0;
constexpr float QSCALE = 0.17677669529663687f * 1.4426950408889634f;

#define TRACE(t) do {} while (0)
#define DBG_PARAMS
#define DBG_ARGS(id)
#define DBG_FWD
#define STAMP(k, i) do {} while (0)
constexpr int C_EPOCH = 0, C_TMO = 64, F_Q = 128  , F_S = F_Q + 512  , F_Z = F_S + 512  ,
              F_U = F_Z + 256  , F_O = F_U + 512  , C_END = F_O + 128;
static_assert(C_END * 4 <= (int)CTL_BYTES, "control block");
#define GM_VM0() __builtin_amdgcn_s_waitcnt(0x0F70)
#define RLX_AGENT __ATOMIC_RELAXED, __HIP_MEMORY_SCOPE_AGENT
typedef __attribute__((address_space(1))) unsigned gu32;
typedef __attribute__((address_space(1))) unsigned long long gu64;
typedef __amdgpu_buffer_rsrc_t rsrc_t;
__device__ __forceinline__ rsrc_t ws_rsrc(unsigned char* ws) { return __builtin_amdgcn_make_buffer_rsrc(ws, 0, 64u << 20, 0x00020000); }
__device__ __forceinline__ void st16_wt(rsrc_t r, unsigned off, u32x4 v) { __builtin_amdgcn_raw_buffer_store_b128(v, r, off, 0, 16); }
__device__ __forceinline__ void signal(unsigned char* ws, int word, unsigned tag) { __hip_atomic_store((gu32*)(ws + WS_CTL) + word, tag, RLX_AGENT); }
__device__ __forceinline__ void poll_flags(unsigned char* ws, unsigned tag, int w0, int n, bool all128, int wq, int lane) {
    gu32* ctl = (gu32*)(ws + WS_CTL);
    for (unsigned spins = 0;;) {
        bool ok = __hip_atomic_load(ctl + w0 + (lane < n ? lane : 0), RLX_AGENT) == tag;
        if (all128) { const unsigned q0 = __hip_atomic_load(ctl + wq + (lane >> 3) * 32 + (lane & 7), RLX_AGENT), q1 = __hip_atomic_load(ctl + wq + 256 + (lane >> 3) * 32 + (lane & 7), RLX_AGENT); ok = ok && q0 == tag && q1 == tag; }
        if (__all(ok)) break;
        __builtin_amdgcn_s_sleep(2);
        if (++spins > (1u << 19)) { __hip_atomic_store(ctl + C_TMO, tag, RLX_AGENT); break; }
        if ((spins & 255u) == 0u && __builtin_amdgcn_readfirstlane(__hip_atomic_load(ctl + C_TMO, RLX_AGENT)) == tag) break;
    }
}
__device__ __forceinline__ void wait_flags(unsigned char* ws, unsigned tag, int w0, int n, bool all128, int wq, int lane) {
    poll_flags(ws, tag, w0, n, all128, wq, lane);
    GM_VM0();
}
__device__ __forceinline__ u32x4 ld16_dev(rsrc_t r, unsigned off) { return __builtin_amdgcn_raw_buffer_load_b128(r, off, 0, 16); }
enum { X_RAW = 0, X_OWN = 1 };
enum { W_P384 = 0, W_P448 = 1, W_P256 = 2, W_OWN = 3 };

__device__ __forceinline__ unsigned pk_bf16(float lo, float hi) {
    typedef __bf16 bf16x2_t __attribute__((ext_vector_type(2)));
    f32x2 v = {lo, hi}; bf16x2_t b = __builtin_convertvector(v, bf16x2_t); return __builtin_bit_cast(unsigned, b);
}
__device__ __forceinline__ float bf_lo(unsigned w) { return __uint_as_float(w << 16); }
__device__ __forceinline__ float bf_hi(unsigned w) { return __uint_as_float(w & 0xffff0000u); }
__device__ __forceinline__ s16x4 tr_read(ldsp p) { typedef short v4s __attribute__((ext_vector_type(4))); return __builtin_amdgcn_ds_read_tr16_b64_v4i16((LAS v4s*)p); }
template <int I, bool SC = false> __device__ __forceinline__ void glds_run(const char* sbase, unsigned voff) {
    if constexpr (SC) asm volatile("global_load_lds_dwordx4 %0, %1 offset:%2 sc1" ::"v"(voff), "s"(sbase), "n"((I & 3) * 1024) : "memory");
    else asm volatile("global_load_lds_dwordx4 %0, %1 offset:%2" ::"v"(voff), "s"(sbase), "n"((I & 3) * 1024) : "memory");
}
template <int I, bool SC = false> __device__ __forceinline__ void glds_set(const char* sbase, unsigned voff, unsigned dst) {
    if constexpr (SC) asm volatile("s_add_u32 m0, %2, %3\n\ts_nop 0\n\tglobal_load_lds_dwordx4 %0, %1 offset:%4 sc1" ::"v"(voff), "s"(sbase), "s"(dst), "n"((I & ~3) * 1024), "n"((I & 3) * 1024) : "memory", "scc");
    else asm volatile("s_add_u32 m0, %2, %3\n\ts_nop 0\n\tglobal_load_lds_dwordx4 %0, %1 offset:%4" ::"v"(voff), "s"(sbase), "s"(dst), "n"((I & ~3) * 1024), "n"((I & 3) * 1024) : "memory", "scc");
}
__device__ __forceinline__ void pf_lines(const void* base, unsigned voff, unsigned ldsdst) {
    asm volatile("s_mov_b32 m0, %2\n\ts_nop 0\n\tglobal_load_lds_dword %0, %1" ::"v"(voff), "s"(base), "s"(ldsdst) : "memory");
}
#define GM_BAR() do { asm volatile("" ::: "memory"); __builtin_amdgcn_s_barrier(); asm volatile("" ::: "memory"); } while (0)
#define GM_SB() __builtin_amdgcn_sched_barrier(0)
__device__ __forceinline__ bool at_tile(int t, int k) { asm volatile("" : "+s"(t)); return t == k; }

template <int BM_, int BN_, int XI_, int WI_, int NSX_, int NSW_> struct Cfg {
    static constexpr int BM = BM_, BN = BN_, XI = XI_, WI = WI_, NSX = NSX_, NSW = NSW_;
    static constexpr int XB = BM * 128, WB = BN * 128, WOFF = NSX * XB, LDS = NSX * XB + NSW * WB;
    static constexpr int PX = BM / 32, PW = BN / 32;
    static constexpr int WPITCH = (WI == W_P384) ? 384 : (WI == W_P448) ? 448 : (WI == W_P256) ? 256 : 0;
    static constexpr bool XSC = false, WSC = false;
};
template <class C> struct Src { const char* x; const char* w; const char* w2; int ksplit; unsigned xstep, wstep; unsigned xv[C::PX]; unsigned wv[C::PW]; };
template <int WI> __device__ __forceinline__ int w_swz(int r, int x) {
    if (WI == W_P384) return x ^ (((((r >> 1) & 1) | (((r >> 3) & 1) << 1))) << 5);
    if (WI == W_P448) return x ^ (((r >> 3) & 1) << 5);
    if (WI == W_P256) return x ^ (((r & 3) | (((r >> 3) & 1) << 2)) << 5);
    return x;
}
template <class C> __device__ __forceinline__ const char* w_tile(const Src<C>& s, int kt) { return (kt < s.ksplit ? s.w : s.w2) + (unsigned)kt * s.wstep; }
template <class C> __device__ __forceinline__ void set_w2(Src<C>& s, const void* base, int ksplit) { s.ksplit = ksplit; s.w2 = (const char*)base - 4096 - (size_t)ksplit * s.wstep; }
template <class C> __device__ __forceinline__ void src_x_raw(Src<C>& s, const void* base, unsigned pitch, int lid, int lane) {
    s.x = (const char*)base - 4096; s.xstep = 128;
#pragma unroll
    for (int i = 0; i < C::PX; ++i) { const int row = 8 * (lid * C::PX + i) + (lane >> 3); s.xv[i] = row * pitch + (((lane & 7) ^ ((row >> 1) & 7)) * 16) + 4096 - (i & 3) * 1024; }
}
template <class C> __device__ __forceinline__ void src_x_own(Src<C>& s, const void* base, int lid, int lane) {
    s.x = (const char*)base - 4096; s.xstep = C::XB;
#pragma unroll
    for (int i = 0; i < C::PX; ++i) s.xv[i] = (lid * C::PX + i) * 1024 + lane * 16 + 4096 - (i & 3) * 1024;
}
template <class C> __device__ __forceinline__ void src_w_own(Src<C>& s, const void* base, int lid, int lane) {
    s.w = (const char*)base - 4096; s.w2 = s.w; s.ksplit = 1 << 20; s.wstep = C::WB;
#pragma unroll
    for (int i = 0; i < C::PW; ++i) s.wv[i] = (lid * C::PW + i) * 1024 + lane * 16 + 4096 - (i & 3) * 1024;
}
template <class C> __device__ __forceinline__ void src_w_raw(Src<C>& s, const void* base, unsigned pitch, unsigned segstride, int lid, int lane) {
    s.w = (const char*)base - 4096; s.w2 = s.w; s.ksplit = 1 << 20; s.wstep = 64 * pitch;
#pragma unroll
    for (int i = 0; i < C::PW; ++i) { const int o = 1024 * (lid * C::PW + i) + 16 * lane; const int r = o / C::WPITCH, x = w_swz<C::WI>(r, o % C::WPITCH);
        s.wv[i] = r * pitch + (x >> 7) * segstride + (x & 127) + 4096 - (i & 3) * 1024; }
}
template <class C, int I0, int I1, bool FIRST> __device__ __forceinline__ void stage_rng(unsigned dx, unsigned dw, const Src<C>& s, const char* xb, const char* wb) {
    if constexpr (I0 < I1) {
        if constexpr (I0 < C::PW) { if constexpr (FIRST || (I0 & 3) == 0) glds_set<I0, C::WSC>(wb, s.wv[I0], dw); else glds_run<I0, C::WSC>(wb, s.wv[I0]); }
        else { constexpr int j = I0 - C::PW; if constexpr (FIRST || (j & 3) == 0) glds_set<j, C::XSC>(xb, s.xv[j], dx); else glds_run<j, C::XSC>(xb, s.xv[j]); }
        stage_rng<C, I0 + 1, I1, false>(dx, dw, s, xb, wb);
    }
}
template <int MT_, int NT_, int N0_, unsigned SWAP_, bool LD_> struct Wv { static constexpr int MT = MT_, NT = NT_, N0 = N0_; static constexpr unsigned SWAP = SWAP_; static constexpr bool LD = LD_; };
template <class W> struct Frag { bf16x8 x[W::MT]; s16x4 wl[W::NT], wh[W::NT]; };
template <class C, class W> struct RdAddr { unsigned x[2]; unsigned w[(C::WI == W_P384) ? 4 : (C::WI == W_P256) ? W::NT : 2]; };
template <class C, class W> __device__ __forceinline__ void rd_init(RdAddr<C, W>& a, int m0, int n0, int lane) {
    const int fr = lane & 15, fq = lane >> 4, g = lane >> 4, q = (lane & 15) >> 2, p = lane & 3;
    if (C::XI == X_RAW) { for (int kk = 0; kk < 2; ++kk) a.x[kk] = (m0 + fr) * 128 + (((kk * 4 + fq) ^ (fr >> 1)) * 16); }
    else { for (int kk = 0; kk < 2; ++kk) a.x[kk] = (kk * 4 + fq) * (C::BM * 16) + (m0 + fr) * 16; }
    if (C::WI == W_P384) { const int s = ((q >> 1) & 1) | ((g & 1) << 1);
        for (int c = 0; c < 4; ++c) a.w[c] = C::WOFF + (8 * g + q) * 384 + ((c ^ s) * 32) + p * 8; }
    else if (C::WI == W_P448) { const int s = g & 1;
        for (int c = 0; c < 2; ++c) a.w[c] = C::WOFF + (8 * g + q) * 448 + (((c ^ s) - c) * 32) + p * 8; }
    else if (C::WI == W_P256) { const int s = (q & 3) | ((g & 1) << 2);
        for (int t = 0; t < W::NT; ++t) a.w[t] = C::WOFF + (8 * g + q) * 256 + (((n0 + t) ^ s) * 32) + p * 8; }
    else { for (int h = 0; h < 2; ++h) a.w[h] = C::WOFF + (p >> 1) * 1024 + (8 * g + 4 * (h ^ (p >> 1)) + q) * 16 + (p & 1) * 8 + W::N0 * 2048; }
}
template <class C, class W> __device__ __forceinline__ s16x4 rd_w(ldsp b, const RdAddr<C, W>& a, int t, int kk, int h) {
    const int c = W::N0 + t;
    if (C::WI == W_P384) return tr_read(b + a.w[c & 3] + (c >> 2) * 128 + kk * 32 * 384 + h * 4 * 384);
    else if (C::WI == W_P448) return tr_read(b + a.w[c & 1] + c * 32 + kk * 32 * 448 + h * 4 * 448);
    else if (C::WI == W_P256) return tr_read(b + a.w[t] + kk * 32 * 256 + h * 4 * 256);
    else return tr_read(b + a.w[h] + t * 2048 + kk * 512);
}
template <class C, class W> __device__ __forceinline__ void rd_one(Frag<W>& f, ldsp bx, ldsp bw, const RdAddr<C, W>& a, int kk, int r) {
    if (r < 2) { if (r == 0) f.wl[0] = rd_w<C, W>(bw, a, 0, kk, 0); else f.wh[0] = rd_w<C, W>(bw, a, 0, kk, 1); }
    else if (r < 2 + W::MT) { const int m = r - 2; f.x[m] = *(const LAS bf16x8*)(bx + a.x[kk] + m * ((C::XI == X_RAW) ? 2048 : 256)); }
    else { const int u = r - W::MT; const int t = u >> 1; if (u & 1) f.wh[t] = rd_w<C, W>(bw, a, t, kk, 1); else f.wl[t] = rd_w<C, W>(bw, a, t, kk, 0); }
}
template <class W> __device__ __forceinline__ void mma_one(f32x4 (&acc)[W::NT][W::MT], const Frag<W>& f, int i) {
    const int t = i / W::MT, m = i % W::MT;
    const bf16x8 w = __builtin_shufflevector(f.wl[t], f.wh[t], 0, 1, 2, 3, 4, 5, 6, 7);
    acc[t][m] = ((W::SWAP >> t) & 1) ? __builtin_amdgcn_mfma_f32_16x16x32_bf16(f.x[m], w, acc[t][m], 0, 0, 0)
                                     : __builtin_amdgcn_mfma_f32_16x16x32_bf16(w, f.x[m], acc[t][m], 0, 0, 0);
}
template <int RFRONT_, int VA_, int RF1_ = RFRONT_> struct Sch { static constexpr int RFRONT = RFRONT_, VA = VA_, RF1 = RF1_; };
template <class C, class W, class S, int RF, int V0, int V1, int I>
__device__ __forceinline__ void step_slot(f32x4 (&acc)[W::NT][W::MT], const Frag<W>& fc, Frag<W>& fn, ldsp bx, ldsp bw, const RdAddr<C, W>& a, int ksn,
                                          const Src<C>& s, const char* xb, const char* wb, unsigned dx, unsigned dw) {
    constexpr int NM = W::NT * W::MT, NR = 2 * W::NT + W::MT, NV = V1 - V0;
    constexpr int RS = (NM * RF / 16) < 1 ? 1 : (NM * RF / 16);
    if constexpr (I < NM) {
        mma_one<W>(acc, fc, I);
        if constexpr (I < RS) {
#pragma unroll
            for (int r = I * NR / RS; r < (I + 1) * NR / RS; ++r) rd_one<C, W>(fn, bx, bw, a, ksn, r);
        }
        if constexpr (W::LD) {
            constexpr int v0 = V0 + I * NV / NM, v1 = V0 + (I + 1) * NV / NM;
            stage_rng<C, v0, v1, v0 == V0>(dx, dw, s, xb, wb);
        }
        GM_SB();
        step_slot<C, W, S, RF, V0, V1, I + 1>(acc, fc, fn, bx, bw, a, ksn, s, xb, wb, dx, dw);
    }
}
struct NoHook { static constexpr bool PRE = false; __device__ __forceinline__ void pre_x() const {} __device__ __forceinline__ void at(int) const {} };
template <class C, class W, class S, class H>
__device__ __forceinline__ void gemm_loop(ldsp lds, const Src<C>& s, int nt, f32x4 (&acc)[W::NT][W::MT], int lid, int m0, int n0, int lane, H& hook DBG_PARAMS) {
    constexpr int PX = C::PX, PW = C::PW, NSX = C::NSX, NSW = C::NSW, NV = PX + PW;
    static_assert(S::VA >= PW && S::VA <= NV, "all W pieces go out in the step behind the barrier");
    static_assert(NSX >= NSW && NSW >= 2, "ring depths");
    RdAddr<C, W> a; rd_init<C, W>(a, m0, n0, lane);
    const unsigned ldb = (unsigned)(uintptr_t)lds;
    const unsigned lx0 = ldb + lid * PX * 1024, lw0 = ldb + C::WOFF + lid * PW * 1024;
    Frag<W> f0, f1;
    const char* wb = w_tile<C>(s, NSW - 1); const char* xb = s.x + (size_t)(NSX - 1) * s.xstep;
    unsigned dw = lw0 + (NSW - 1) * C::WB, dx = lx0 + (NSX - 1) * C::XB;
    if constexpr (H::PRE) {
        if constexpr (W::LD) {
#pragma unroll
            for (int i = 0; i < NSW; ++i) stage_rng<C, 0, PW, true>(0, lw0 + i * C::WB, s, nullptr, w_tile<C>(s, i));
        }
        hook.pre_x();
        if constexpr (W::LD) {
#pragma unroll
            for (int i = 0; i < NSX - 1; ++i) stage_rng<C, PW, NV, true>(lx0 + i * C::XB, 0, s, s.x + (size_t)i * s.xstep, nullptr);
            if constexpr (S::VA > PW) stage_rng<C, PW, S::VA, true>(dx, dw, s, xb, wb);
            asm volatile("s_waitcnt vmcnt(%0)" ::"n"(S::VA - PW) : "memory");
        }
    } else if constexpr (W::LD) {
#pragma unroll
        for (int i = 0; i < NSX - 1; ++i) {
            if (i < NSW - 1) stage_rng<C, 0, PW, true>(0, lw0 + i * C::WB, s, nullptr, w_tile<C>(s, i));
            stage_rng<C, PW, NV, true>(lx0 + i * C::XB, 0, s, s.x + (size_t)i * s.xstep, nullptr);
        }
        stage_rng<C, 0, S::VA, true>(dx, dw, s, xb, wb);
        constexpr int Y = (NSX - 2) * PX + (NSW - 2) * PW + S::VA;
        asm volatile("s_waitcnt vmcnt(%0)" ::"n"(Y) : "memory");
    }
    GM_BAR();
#pragma unroll
    for (int r = 0; r < 2 * W::NT + W::MT; ++r) rd_one<C, W>(f0, lds, lds, a, 0, r);
    int bx = 0, bw = 0, kx = NSX - 1, kw = NSW - 1;
    __builtin_amdgcn_s_waitcnt(0xC07F);
    for (int t = 0; t < nt; ++t) {
        ldsp cx = lds + bx * C::XB, cw = lds + bw * C::WB;
        const int nbx = (bx + 1 == NSX) ? 0 : bx + 1, nbw = (bw + 1 == NSW) ? 0 : bw + 1;
        ldsp nx = lds + nbx * C::XB, nw = lds + nbw * C::WB;
        TRACE(t);
        GM_SB();
#define GM_STEP(FC, FN, BX, BW, KSN, RF, V0, V1) step_slot<C, W, S, RF, V0, V1, 0>(acc, FC, FN, BX, BW, a, KSN, s, xb, wb, dx, dw)
        GM_STEP(f0, f1, cx, cw, 1, S::RFRONT, S::VA, NV);
        hook.at(t);
        asm volatile("s_waitcnt lgkmcnt(0)" ::: "memory");
        if constexpr (W::LD) {
            constexpr int pw = 1 - NSW, px = 1 - NSX, last = (pw > px) ? pw : px, nper = (-1) - last, Y = nper * NV + ((pw > px) ? PX : 0);
            asm volatile("s_waitcnt vmcnt(%0)" ::"n"(Y) : "memory");
        }
        GM_BAR();
        GM_SB();
        if constexpr (W::LD) {
            kx = (kx + 1 < nt) ? kx + 1 : kx; kw = (kw + 1 < nt) ? kw + 1 : kw;
            xb = s.x + (size_t)kx * s.xstep; wb = w_tile<C>(s, kw);
            dx = lx0 + bx * C::XB; dw = lw0 + bw * C::WB;
        }
        GM_STEP(f1, f0, nx, nw, 0, S::RF1, 0, S::VA);
#undef GM_STEP
        bx = nbx; bw = nbw;
    }
    GM_SB();
    asm volatile("s_waitcnt vmcnt(0) lgkmcnt(0)" ::: "memory");
    GM_BAR();
}
template <class W> __device__ __forceinline__ void acc_zero(f32x4 (&acc)[W::NT][W::MT]) {
#pragma unroll
    for (int t = 0; t < W::NT; ++t)
#pragma unroll
        for (int m = 0; m < W::MT; ++m) acc[t][m] = (f32x4){0.f, 0.f, 0.f, 0.f};
}
__device__ __forceinline__ u32x4 pair16(const f32x4& A, const f32x4& B) {
    const unsigned a0 = pk_bf16(A[0], A[1]), a1 = pk_bf16(A[2], A[3]), b0 = pk_bf16(B[0], B[1]), b1 = pk_bf16(B[2], B[3]);
    const auto r0 = __builtin_amdgcn_permlane16_swap(a0, b0, false, false); const auto r1 = __builtin_amdgcn_permlane16_swap(a1, b1, false, false);
    return (u32x4){r0[0], r1[0], r0[1], r1[1]};
}

namespace g32 {
enum { W_P448 = 1, W_P256 = 2, W_OWNT = 3 };
template <int BM_, int BN_, int XI_, int WI_, int NS_> struct Cfg {
    static constexpr int BM = BM_, BN = BN_, XI = XI_, WI = WI_, NS = NS_;
    static constexpr int XB = BM * 128, WB = BN * 128, WOFF = NS * XB, LDS = NS * (XB + WB);
    static constexpr int PX = BM / 32, PW = BN / 32;
    static constexpr int WPITCH = (WI == W_P448) ? 448 : (WI == W_P256) ? 256 : 16;
    static constexpr bool XSC = (XI == X_OWN), WSC = (WI == W_OWNT);
};
template <class C> __device__ __forceinline__ void src_w_raw(Src<C>& s, const void* base, unsigned pitch, int lid, int lane) {
    s.w = (const char*)base - 4096; s.w2 = s.w; s.ksplit = 1 << 20; s.wstep = 64 * pitch;
#pragma unroll
    for (int i = 0; i < C::PW; ++i) { const int o = 1024 * (lid * C::PW + i) + 16 * lane; const int r = o / C::WPITCH; int x = o % C::WPITCH;
        if (C::WI == W_P256) x ^= (r & 3) << 6;
        s.wv[i] = r * pitch + x + 4096 - (i & 3) * 1024; }
}
template <int MT_, int NT_, bool LD_> struct Wv { static constexpr int MT = MT_, NT = NT_; static constexpr bool LD = LD_; };
template <class C, class W> struct Frag { bf16x8 x[W::MT]; s16x4 wl[W::NT], wh[W::NT]; };
template <class C, class W> struct RdAddr { unsigned x[(C::XI == X_RAW) ? 4 : 1]; unsigned w[(C::WI == W_P256) ? W::NT : 1]; };
template <class C, class W> __device__ __forceinline__ void rd_init(RdAddr<C, W>& a, int m0, int n0  , int lane) {
    const int r = lane & 31, h = lane >> 5, g = lane >> 4, q = (lane & 15) >> 2, p = lane & 3;
    if (C::XI == X_RAW) { for (int ks = 0; ks < 4; ++ks) a.x[ks] = (m0 + r) * 128 + (((2 * ks + h) ^ ((r >> 1) & 7)) * 16); }
    else a.x[0] = h * (C::BM * 16) + (m0 + r) * 16;
    const int rowp = 8 * (g >> 1) + q, colp = (g & 1) * 32 + p * 8;
    if (C::WI == W_P448) a.w[0] = C::WOFF + rowp * 448 + colp + n0 * 64;
    else if (C::WI == W_P256) { for (int t = 0; t < W::NT; ++t) a.w[t] = C::WOFF + rowp * 256 + (((n0 + t) ^ q) * 64) + colp; }
    else a.w[0] = C::WOFF + h * (C::BN * 16) + (n0 * 32 + r) * 16;
}
template <class C, class W> constexpr int n_reads() { return W::MT + (C::WI == W_OWNT ? W::NT : 2 * W::NT); }
template <class C, class W> __device__ __forceinline__ void rd_one(Frag<C, W>& f, ldsp bx, ldsp bw, const RdAddr<C, W>& a, int ks, int r) {
    constexpr int PER = (C::WI == W_OWNT) ? 1 : 2;
    if (r >= PER && r < PER + W::MT) { const int m = r - PER;
        if (C::XI == X_RAW) f.x[m] = *(const LAS bf16x8*)(bx + a.x[ks] + m * 4096);
        else f.x[m] = *(const LAS bf16x8*)(bx + a.x[0] + ks * (2 * C::BM * 16) + m * 512);
        return; }
    const int u = r < PER ? r : r - W::MT, t = u / PER, hh = u % PER;
    if (C::WI == W_OWNT) { const bf16x8 v = *(const LAS bf16x8*)(bw + a.w[0] + ks * (2 * C::BN * 16) + t * 512); f.wl[t] = __builtin_shufflevector(v, v, 0, 1, 2, 3); f.wh[t] = __builtin_shufflevector(v, v, 4, 5, 6, 7); }
    else { s16x4 v;
        if (C::WI == W_P448) v = tr_read(bw + a.w[0] + t * 64 + (ks * 16 + hh * 4) * 448);
        else v = tr_read(bw + a.w[t] + (ks * 16 + hh * 4) * 256);
        if (hh) f.wh[t] = v; else f.wl[t] = v; }
}
template <class C, class W> __device__ __forceinline__ void rd_frags(Frag<C, W>& f, ldsp bx, ldsp bw, const RdAddr<C, W>& a, int ks) {
#pragma unroll
    for (int r = 0; r < n_reads<C, W>(); ++r) rd_one<C, W>(f, bx, bw, a, ks, r);
}
template <class C, class W, bool SWAP  > __device__ __forceinline__ void mma_one(f32x16 (&acc)[W::NT][W::MT], const Frag<C, W>& f, int i) {
    const int t = i / W::MT, m = i % W::MT;
    const bf16x8 w = __builtin_shufflevector(f.wl[t], f.wh[t], 0, 1, 2, 3, 4, 5, 6, 7);
    acc[t][m] = SWAP ? __builtin_amdgcn_mfma_f32_32x32x16_bf16(w, f.x[m], acc[t][m], 0, 0, 0) : __builtin_amdgcn_mfma_f32_32x32x16_bf16(f.x[m], w, acc[t][m], 0, 0, 0);
}
struct NoFill { template <int S> __device__ __forceinline__ void chunk() {} };
template <class C, class W, bool SWAP, int V0, int V1, int BASE, int I, class F>
__device__ __forceinline__ void step_slot(f32x16 (&acc)[W::NT][W::MT], const Frag<C, W>& fc, Frag<C, W>& fn, ldsp bx, ldsp bw, const RdAddr<C, W>& a, int ksn,
                                          const Src<C>& s, const char* xb, const char* wb, unsigned dx, unsigned dw, F& fill) {
    constexpr int NM = W::NT * W::MT, NR = n_reads<C, W>(), NV = V1 - V0;
    if constexpr (I < NM) {
        mma_one<C, W, SWAP>(acc, fc, I);
#pragma unroll
        for (int r = I * NR / NM; r < (I + 1) * NR / NM; ++r) rd_one<C, W>(fn, bx, bw, a, ksn, r);
        if constexpr (W::LD) { constexpr int v0 = V0 + I * NV / NM, v1 = V0 + (I + 1) * NV / NM; stage_rng<C, v0, v1, v0 == V0>(dx, dw, s, xb, wb); }
        fill.template chunk<BASE + I>();
        GM_SB();
        step_slot<C, W, SWAP, V0, V1, BASE, I + 1, F>(acc, fc, fn, bx, bw, a, ksn, s, xb, wb, dx, dw, fill);
    }
}
template <class C, class WF  , int D> struct Pipe {
    static constexpr int PX = C::PX, PW = C::PW, NS = C::NS, NV = PX + PW;
    static constexpr int Q1 = (NV + 3) / 4, Q2 = (NV + 1) / 2, Q3 = (3 * NV + 3) / 4;
    static constexpr int PRE = (D == 1) ? Q1 : Q2;
    ldsp lds; int nt, xmod;
    RdAddr<C, WF> a; unsigned lx0, lw0;
    Frag<C, WF> f0, f1, f2, f3;
    __device__ __forceinline__ Pipe(ldsp lds_, const Src<C>& s, int nt_, int xmod_, int lid, int m0, int n0, int lane) : lds(lds_), nt(nt_), xmod(xmod_) {
        rd_init<C, WF>(a, m0, n0, lane);
        const unsigned ldb = (unsigned)(uintptr_t)lds;
        lx0 = ldb + lid * PX * 1024; lw0 = ldb + C::WOFF + lid * PW * 1024;
    }
    __device__ __forceinline__ void issue_x(const Src<C>& s) {
        if constexpr (WF::LD) {
#pragma unroll
            for (int i = 0; i < NS - 1; ++i) stage_rng<C, PW, NV, true>(lx0 + i * C::XB, 0, s, s.x + (size_t)i * s.xstep, nullptr);
        }
    }
    __device__ __forceinline__ void issue_w(const Src<C>& s) {
        static_assert(PRE <= PW, "prologue split");
        if constexpr (WF::LD) {
#pragma unroll
            for (int i = 0; i < NS - 1; ++i) stage_rng<C, 0, PW, true>(0, lw0 + i * C::WB, s, nullptr, w_tile<C>(s, i));
            stage_rng<C, 0, PRE, true>(0, lw0 + (NS - 1) * C::WB, s, nullptr, w_tile<C>(s, NS - 1));
        }
    }
    __device__ __forceinline__ void landed() {
        if constexpr (WF::LD) asm volatile("s_waitcnt vmcnt(0)" ::: "memory");
        GM_BAR();
        rd_frags<C, WF>(f0, lds, lds, a, 0);
        if (D == 2) rd_frags<C, WF>(f1, lds, lds, a, 1);
    }
    __device__ __forceinline__ void begin(const Src<C>& s) {
        if constexpr (WF::LD) {
#pragma unroll
            for (int i = 0; i < NS - 1; ++i) stage_rng<C, 0, NV, true>(lx0 + i * C::XB, lw0 + i * C::WB, s, s.x + (size_t)i * s.xstep, w_tile<C>(s, i));
            stage_rng<C, 0, PRE, true>(0, lw0 + (NS - 1) * C::WB, s, nullptr, w_tile<C>(s, NS - 1));
            asm volatile("s_waitcnt vmcnt(%0)" ::"n"((NS - 2) * NV + PRE) : "memory");
        }
        GM_BAR();
        rd_frags<C, WF>(f0, lds, lds, a, 0);
        if (D == 2) rd_frags<C, WF>(f1, lds, lds, a, 1);
    }
    template <bool SWAP, class H> __device__ __forceinline__ void run(const Src<C>& s, int t0, int t1, f32x16 (&acc)[WF::NT][WF::MT], H& hook DBG_PARAMS) {
        NoFill nf; run_fill<SWAP>(s, t0, t1, t0, acc, hook, nf DBG_FWD);
    }
    template <bool SWAP, class H, class F> __device__ __forceinline__ void run_fill(const Src<C>& s, int t0, int t1, int tb, f32x16 (&acc)[WF::NT][WF::MT], H& hook, F& fill DBG_PARAMS) {
        using W = WF; constexpr int NM = WF::MT * WF::NT;
        int sl = t0 % NS, kn = t0 - 1 + NS; kn = kn < nt ? kn : nt - 1;
        const char* xb = s.x + (unsigned)(kn & (xmod - 1)) * s.xstep; const char* wb = w_tile<C>(s, kn);
        unsigned dx = lx0 + ((sl == 0) ? NS - 1 : sl - 1) * C::XB, dw = lw0 + ((sl == 0) ? NS - 1 : sl - 1) * C::WB;
        __builtin_amdgcn_s_waitcnt(0xC07F);
        for (int t = t0; t < t1; ++t) {
            const int ns = (sl + 1 == NS) ? 0 : sl + 1;
            ldsp cx = lds + sl * C::XB, cw = lds + sl * C::WB, nx = lds + ns * C::XB, nw = lds + ns * C::WB;
            TRACE(t - tb);
            hook.top(t - tb);
            GM_SB();
#define G32_STEP(FC, FN, BX, BW, KSN, V0, V1, ST) step_slot<C, W, SWAP, V0, V1, ST * NM, 0, F>(acc, FC, FN, BX, BW, a, KSN, s, xb, wb, dx, dw, fill)
            if constexpr (D == 1) { G32_STEP(f0, f1, cx, cw, 1, Q1, Q2, 0); G32_STEP(f1, f2, cx, cw, 2, Q2, Q3, 1); G32_STEP(f2, f3, cx, cw, 3, Q3, NV, 2); }
            else { G32_STEP(f0, f2, cx, cw, 2, Q2, Q3, 0); G32_STEP(f1, f3, cx, cw, 3, Q3, NV, 1); }
            hook.at(t - tb);
            asm volatile("s_waitcnt lgkmcnt(0)" ::: "memory");
            if constexpr (W::LD) asm volatile("s_waitcnt vmcnt(%0)" ::"n"((NS - 2) * NV) : "memory");
            GM_BAR();
            GM_SB();
            if constexpr (W::LD) { kn = (kn + 1 < nt) ? kn + 1 : kn; xb = s.x + (unsigned)(kn & (xmod - 1)) * s.xstep; wb = w_tile<C>(s, kn); dx = lx0 + sl * C::XB; dw = lw0 + sl * C::WB; }
            if constexpr (D == 1) { G32_STEP(f3, f0, nx, nw, 0, 0, Q1, 3); }
            else { G32_STEP(f2, f0, nx, nw, 0, 0, Q1, 2); G32_STEP(f3, f1, nx, nw, 1, Q1, Q2, 3); }
#undef G32_STEP
            sl = ns;
        }
        GM_SB();
    }
    __device__ __forceinline__ void end() {
        asm volatile("s_waitcnt vmcnt(0) lgkmcnt(0)" ::: "memory");
        GM_BAR();
    }
};
template <class W> __device__ __forceinline__ void acc_zero(f32x16 (&acc)[W::NT][W::MT]) {
#pragma unroll
    for (int t = 0; t < W::NT; ++t)
#pragma unroll
        for (int m = 0; m < W::MT; ++m) acc[t][m] = (f32x16){};
}
__device__ __forceinline__ u32x4 pair32(unsigned a0, unsigned a1, unsigned b0, unsigned b1) {
    const auto r0 = __builtin_amdgcn_permlane32_swap(a0, b0, false, false); const auto r1 = __builtin_amdgcn_permlane32_swap(a1, b1, false, false);
    return (u32x4){r0[0], r1[0], r0[1], r1[1]};
}
}

struct Params {
    const u16* ax; const u16* wqkv; const u16* wo; const u16* bx; const u16* win; const u16* wsp; const u16* bsp; const u16* wout; const u16* gw;
    u16* out; unsigned char* ws;
};
struct Ctx : Params { unsigned tag; };

using CfgQ = Cfg<256, 192, X_RAW, W_P384, 3, 2>;
struct Warm {
    const char* base; unsigned pitch; int lpr_log2; int cnt; unsigned scratch;
    __device__ __forceinline__ unsigned mask(int t0, int wid) const {
        if (wid < 4 || cnt <= 0) return 0u;
        return ((0x11111111u << (wid - 4)) & (cnt >= 32 ? ~0u : ((1u << cnt) - 1u))) << t0;
    }
    __device__ __forceinline__ void issue(int n, int wid, int lane) const {
        if (n >= 0 && n < cnt && wid == 4 + (n & 3)) { const int idx = n * 64 + lane; pf_lines(base, (unsigned)(idx >> lpr_log2) * pitch + (unsigned)(idx & ((1 << lpr_log2) - 1)) * 128u, scratch); }
    }
};

struct SigHook {
    static constexpr bool PRE = false; int wid, lane; Warm w0, w1; unsigned act;
    __device__ __forceinline__ void pre_x() const {}
    __device__ __forceinline__ void top(int) const {}
    __device__ __forceinline__ void at(int t) const { if ((act >> t) & 1u) { w0.issue(t - 1, wid, lane); w1.issue(t - 1, wid, lane); } }
};
template <int WN> __device__ __forceinline__ void qkv_wave(const Ctx& p, ldsp lds, int pair, int panel, int wid, int lane) {
    using C = CfgQ; using W = Wv<4, 6, WN * 6, WN ? 0x3Cu : 0u, WN == 0>;
    const int lid = wid & 3, m0 = (wid & 3) * 64;
    Src<C> s;
    src_x_raw<C>(s, p.ax + (size_t)panel * 256 * DM, DM * 2, lid, lane);
    src_w_raw<C>(s, p.wqkv + pair * 64, 3 * DM * 2, DM * 2, lid, lane);
    f32x4 acc[6][4]; acc_zero<W>(acc);
    STAMP(0, 1);
    Warm none{nullptr, 0, 0, 0, 0};
    SigHook hk{wid, lane, none, none, 0u};
    gemm_loop<C, W, Sch<12, 7>>(lds, s, DM / 64, acc, lid, m0, 0, lane, hk DBG_ARGS(0));
    STAMP(0, 2);
    const int fr = lane & 15, fq = lane >> 4;
    const int row0 = panel * 256 + m0;
    const rsrc_t rs = ws_rsrc(p.ws);
#pragma unroll
    for (int t = 0; t < 6; t += 2) {
        const int T = WN * 6 + t, kind = T >> 2, head = pair * 2 + ((T >> 1) & 1);
        if (kind < 2) {
            const unsigned base = (unsigned)(kind == 0 ? WS_Q : WS_K);
            const int c = 2 * (fq & 1) + (fq >> 1);
            const float sc = kind == 0 ? QSCALE : 1.0f;
#pragma unroll
            for (int m = 0; m < 4; ++m) { const int row = row0 + m * 16 + fr;
                st16_wt(rs, base + ((head * 64 + (row >> 5)) * 4 + c) * 512 + (row & 31) * 16, pair16(acc[t][m] * sc, acc[t + 1][m] * sc)); }
        } else {
#pragma unroll
            for (int tt = 0; tt < 2; ++tt)
#pragma unroll
                for (int m = 0; m < 4; m += 2) {
                    const f32x4 A = acc[t + tt][m], B = acc[t + tt][m + 1];
                    const unsigned a0 = pk_bf16(A[0], A[1]), a1 = pk_bf16(A[2], A[3]), b0 = pk_bf16(B[0], B[1]), b1 = pk_bf16(B[2], B[3]);
                    const auto r0 = __builtin_amdgcn_permlane32_swap(a0, b0, false, false); const auto r1 = __builtin_amdgcn_permlane32_swap(a1, b1, false, false);
                    const int kb = (row0 >> 4) + m + (fq >> 1), h = fq & 1, d = tt * 16 + fr;
                    st16_wt(rs, (unsigned)WS_V + ((head * 128 + kb) * 2 + h) * 512 + d * 16, (u32x4){r0[0], r1[0], r0[1], r1[1]});
                }
        }
    }
    asm volatile("s_waitcnt vmcnt(0)" ::: "memory");
    GM_BAR();
    if (threadIdx.x == 0) signal(p.ws, F_Q + pair * 32 + panel, p.tag);
}

using CfgZ = g32::Cfg<128, 224, X_RAW, g32::W_P448, 3>;
using CfgS = g32::Cfg<128, 224, X_RAW, g32::W_OWNT, 3>;
using CfgO = g32::Cfg<128, 128, X_OWN, g32::W_P256, 4>;
__device__ __forceinline__ float gelu_tanh(float x) {
    const float t = x * x * (-0.044715f * 2.0f * 0.7978845608028654f * 1.4426950408889634f) + (-2.0f * 0.7978845608028654f * 1.4426950408889634f);
    return x * __builtin_amdgcn_rcpf(1.0f + __builtin_amdgcn_exp2f(x * t));
}
template <int N> __device__ __forceinline__ void gelu_pairs(unsigned* z) {
    constexpr float C1 = -0.044715f * 2.0f * 0.7978845608028654f * 1.4426950408889634f, C2 = -2.0f * 0.7978845608028654f * 1.4426950408889634f;
    f32x2 x[N], e[N];
#pragma unroll
    for (int i = 0; i < N; ++i) x[i] = (f32x2){bf_lo(z[i]), bf_hi(z[i])};
#pragma unroll
    for (int i = 0; i < N; ++i) e[i] = x[i] * (x[i] * x[i] * C1 + C2);
    GM_SB();
#pragma unroll
    for (int i = 0; i < N; ++i) { e[i].x = __builtin_amdgcn_exp2f(e[i].x); e[i].y = __builtin_amdgcn_exp2f(e[i].y); }
    GM_SB();
#pragma unroll
    for (int i = 0; i < N; ++i) e[i] = e[i] + 1.0f;
#pragma unroll
    for (int i = 0; i < N; ++i) { e[i].x = __builtin_amdgcn_rcpf(e[i].x); e[i].y = __builtin_amdgcn_rcpf(e[i].y); }
    GM_SB();
#pragma unroll
    for (int i = 0; i < N; ++i) { const f32x2 y = x[i] * e[i]; z[i] = pk_bf16(y.x, y.y); }
    GM_SB();
}
__device__ __forceinline__ void gelu16(f32x16& a) {
    constexpr float C1 = -0.044715f * 2.0f * 0.7978845608028654f * 1.4426950408889634f, C2 = -2.0f * 0.7978845608028654f * 1.4426950408889634f;
    f32x16 e = a * (a * a * C1 + C2);
    GM_SB();
#pragma unroll
    for (int i = 0; i < 16; ++i) e[i] = __builtin_amdgcn_exp2f(e[i]);
    GM_SB();
    e = e + 1.0f;
#pragma unroll
    for (int i = 0; i < 16; ++i) e[i] = __builtin_amdgcn_rcpf(e[i]);
    GM_SB();
    a = a * e;
}
template <int CTRL> __device__ __forceinline__ float dpp_add(float x) { return x + __int_as_float(__builtin_amdgcn_update_dpp(0, __float_as_int(x), CTRL, 0xf, 0xf, false)); }
__device__ __forceinline__ float red32_pair(float a, float b) {
    const auto r = __builtin_amdgcn_permlane16_swap(__float_as_uint(a), __float_as_uint(b), false, false);
    float x = __uint_as_float(r[0]) + __uint_as_float(r[1]);
    x = dpp_add<0x128>(x); x = dpp_add<0x124>(x); x = dpp_add<0x122>(x); x = dpp_add<0x121>(x);
    return x;
}
constexpr int LDS_TAB = CfgZ::LDS;
template <int NT> struct GeluFill {
    static constexpr int NSLOT = 4 * NT, STRIDE = NSLOT / 4;
    unsigned (&z)[NT][8];
    f32x2 x, e;
    template <int S> __device__ __forceinline__ void chunk() {
        constexpr float C1 = -0.044715f * 2.0f * 0.7978845608028654f * 1.4426950408889634f, C2 = -2.0f * 0.7978845608028654f * 1.4426950408889634f;
        if constexpr (S % STRIDE == 0) { constexpr int pa = S / STRIDE;
            x = (f32x2){bf_lo(z[0][pa]), bf_hi(z[0][pa])};
            const f32x2 w = x * (x * x * C1 + C2);
            e.x = __builtin_amdgcn_exp2f(w.x); e.y = __builtin_amdgcn_exp2f(w.y);
#pragma unroll
            for (int g = 0; g + 1 < 2 * NT; ++g) z[g >> 1][(g & 1) * 4 + pa] = z[(g + 1) >> 1][((g + 1) & 1) * 4 + pa];
            asm volatile("" : "+v"(x), "+v"(e));
        }
        if constexpr (S % STRIDE == 2) { constexpr int pb = S / STRIDE;
            const f32x2 d = e + 1.0f;
            const f32x2 y = x * (f32x2){__builtin_amdgcn_rcpf(d.x), __builtin_amdgcn_rcpf(d.y)};
            z[NT - 1][4 + pb] = pk_bf16(y.x, y.y);
            asm volatile("" : "+v"(z[NT - 1][4 + pb]));
        }
    }
};
template <int NT, bool LD> struct LnHook {
    const Ctx& p; ldsp lds; int panel, j, wid, lane, n0; unsigned (&z2)[NT][8];
    u32x4 w[4];
    __device__ __forceinline__ void normalise(int tt) {
        LAS f32x2* R = (LAS f32x2*)(lds + LDS_TAB + 2048);
        const int r = lane & 31, h = lane >> 5, m0 = (wid & 3) * 32;
        const rsrc_t rs = ws_rsrc(p.ws);
        const int srow = panel * 128 + m0;
#pragma unroll
        for (int i = 0; i < 4; i += 2) {
            const f32x4 ra = *(const LAS f32x4*)(R + m0 + 8 * i + 4 * h), rb = *(const LAS f32x4*)(R + m0 + 8 * i + 4 * h + 2);
            const f32x4 rc = *(const LAS f32x4*)(R + m0 + 8 * i + 8 + 4 * h), rd = *(const LAS f32x4*)(R + m0 + 8 * i + 8 + 4 * h + 2);
            const int k = srow + 8 * (i + h);
            const unsigned base = (unsigned)WS_Z + ((j * 32 + (k >> 6)) * 8 + ((k & 63) >> 3)) * 3584 + (n0 * 32 + r) * 16;
            const unsigned a0 = z2[tt][2 * i], a1 = z2[tt][2 * i + 1], b0 = z2[tt][2 * i + 2], b1 = z2[tt][2 * i + 3];
            st16_wt(rs, base + tt * 512, g32::pair32(pk_bf16((bf_lo(a0) - ra[0]) * ra[1], (bf_hi(a0) - ra[2]) * ra[3]), pk_bf16((bf_lo(a1) - rb[0]) * rb[1], (bf_hi(a1) - rb[2]) * rb[3]),
                                                      pk_bf16((bf_lo(b0) - rc[0]) * rc[1], (bf_hi(b0) - rc[2]) * rc[3]), pk_bf16((bf_lo(b1) - rd[0]) * rd[1], (bf_hi(b1) - rd[2]) * rd[3])));
        }
    }
    __device__ __forceinline__ void normalise_at(int t) {
        if (t >= 5 && t < 9) { if (at_tile(t, 5)) normalise(0); if (at_tile(t, 6)) normalise(1); if (at_tile(t, 7)) normalise(2); if constexpr (NT == 4) { if (at_tile(t, 8)) normalise(3); } }
    }
    __device__ __forceinline__ void fetch() {
        const unsigned off = (unsigned)WS_STAT + ((panel * 8 + (lane >> 5) * 4) * 128 + (wid - 4) * 32 + (lane & 31)) * 16;
#pragma unroll
        for (int v = 0; v < 4; ++v) w[v] = ld16_dev(ws_rsrc(p.ws), off + v * 2048);
    }
    __device__ __forceinline__ bool fetched() const { bool ok = true;
#pragma unroll
        for (int v = 0; v < 4; ++v) ok = ok && w[v][1] == p.tag && w[v][3] == p.tag;
        return __all(ok); }
    __device__ __forceinline__ void top(int t) {
        if constexpr (LD) normalise_at(t);
        else { if (t == 4) fetch(); }
    }
    __device__ __forceinline__ void at(int t) {
        if (t < 4 || t > 11) return;
        if constexpr (!LD) {
            if (at_tile(t, 4)) {
                for (unsigned spins = 0; !fetched();) {
                    __builtin_amdgcn_s_sleep(2); fetch();
                    if (++spins > (1u << 19)) { __hip_atomic_store((gu32*)(p.ws + WS_CTL) + C_TMO, p.tag, RLX_AGENT); break; }
                }
                LAS f32x2* R = (LAS f32x2*)(lds + LDS_TAB + 2048);
                float ms = 0.f;
#pragma unroll
                for (int v = 0; v < 4; ++v) ms += __uint_as_float(w[v][0]);
                { const auto r = __builtin_amdgcn_permlane32_swap(__float_as_uint(ms), __float_as_uint(ms), false, false); ms = __uint_as_float(r[0]) + __uint_as_float(r[1]); }
                const float mean = ms * 0.125f; float q = 0.f;
#pragma unroll
                for (int v = 0; v < 4; ++v) { const float dm = __uint_as_float(w[v][0]) - mean; q += __uint_as_float(w[v][2]) + 224.0f * dm * dm; }
                { const auto r = __builtin_amdgcn_permlane32_swap(__float_as_uint(q), __float_as_uint(q), false, false); q = __uint_as_float(r[0]) + __uint_as_float(r[1]); }
                if (lane < 32) R[(wid - 4) * 32 + lane] = (f32x2){mean, __builtin_amdgcn_rsqf(q * (1.0f / 1792.0f) + 1e-5f)};
                GM_VM0();
            }
            normalise_at(t);
        }
        if (at_tile(t, 10)) asm volatile("s_waitcnt vmcnt(0)" ::: "memory");
        if constexpr (!LD) { if (at_tile(t, 11)) { if (threadIdx.x == 256) signal(p.ws, F_Z + j * 32 + panel, p.tag); } }
    }
};
struct SpHook {
    static constexpr bool PRE = false; const Ctx& p; ldsp lds; int panel, wid, lane;
    __device__ __forceinline__ void pre_x() const {}
    __device__ __forceinline__ void top(int) const {}
    __device__ __forceinline__ void at(int t) {
        if (t == 10 && (wid == 4 || wid == 5)) {
            poll_flags(p.ws, p.tag, F_Q, 1, true, F_Q, lane);
            const int row = (wid - 4) * 64 + lane, trow = panel * 128 + row;
            ((LAS f32x2*)(lds + LDS_TAB + 2048))[row] = (f32x2){__uint_as_float((unsigned)p.bsp[trow] << 16), 1.0f - __uint_as_float(__hip_atomic_load((gu32*)(p.ws + WS_GATE) + trow, RLX_AGENT))};
            GM_VM0();
        }
    }
};
struct WaitHook {
    unsigned char* ws; unsigned tag; int wid, lane, tlate, wl, nl;
    __device__ __forceinline__ void top(int) const {}
    __device__ __forceinline__ void at(int t) const { if (t == tlate && wid == 4) wait_flags(ws, tag, wl, nl, false, 0, lane); }
};
template <bool LD> __device__ __forceinline__ void b_wave(const Ctx& p, ldsp lds, int panel, int j, int wid, int lane) {
    constexpr int NT = LD ? 3 : 4;
    const int lid = wid & 3, m0 = lid * 32, n0 = LD ? 0 : 3;
    const int r = lane & 31, h = lane >> 5;
    unsigned z2[NT][8], z1[NT][8];
    const unsigned pfs = (unsigned)(uintptr_t)lds + LDS_PFS;
    {
        using C = CfgZ; using W = g32::Wv<1, NT, LD>; Src<C> s;
        src_x_raw<C>(s, p.ax + (size_t)panel * 128 * DM, DM * 2, lid, lane);
        g32::src_w_raw<C>(s, p.win + FH + j * 224, 2 * FH * 2, lid, lane);
        set_w2<C>(s, p.win + j * 224, DM / 64);
        g32::Pipe<C, W, 1> pipe(lds, s, 2 * DM / 64, DM / 64, lid, m0, n0, lane);
        f32x16 acc[NT][1]; g32::acc_zero<W>(acc);
        STAMP(0, 1);
        Warm wz1{(const char*)p.win + ((j * 224 * 2) & ~127) + (size_t)(panel & 3) * 256 * (2 * FH * 2), 2 * FH * 2, 2, 13, pfs};
        Warm none{nullptr, 0, 0, 0, 0};
        SigHook hk{wid, lane, wz1, none, wz1.mask(1, wid)};
        pipe.begin(s);
        pipe.template run<false>(s, 0, DM / 64, acc, hk DBG_ARGS(1));
        STAMP(0, 2);
        {
            float sm[16], sq[16];
#pragma unroll
            for (int t = 0; t < NT; ++t) {
                gelu16(acc[t][0]);
#pragma unroll
                for (int q = 0; q < 16; ++q) { const float v = acc[t][0][q]; sm[q] = t ? sm[q] + v : v; sq[q] = t ? sq[q] + v * v : v * v; }
#pragma unroll
                for (int q = 0; q < 16; q += 2) z2[t][q >> 1] = pk_bf16(acc[t][0][q], acc[t][0][q + 1]);
                acc[t][0] = (f32x16){};
            }
            float ra[16];
#pragma unroll
            for (int k = 0; k < 8; ++k) {
                const auto a = __builtin_amdgcn_permlane16_swap(__float_as_uint(sm[2 * k]), __float_as_uint(sm[2 * k + 1]), false, false); ra[k] = __uint_as_float(a[0]) + __uint_as_float(a[1]);
                const auto b2 = __builtin_amdgcn_permlane16_swap(__float_as_uint(sq[2 * k]), __float_as_uint(sq[2 * k + 1]), false, false); ra[8 + k] = __uint_as_float(b2[0]) + __uint_as_float(b2[1]); }
#pragma unroll
            for (int k = 0; k < 16; ++k) ra[k] = dpp_add<0x128>(ra[k]);
#pragma unroll
            for (int k = 0; k < 16; ++k) ra[k] = dpp_add<0x124>(ra[k]);
#pragma unroll
            for (int k = 0; k < 16; ++k) ra[k] = dpp_add<0x122>(ra[k]);
#pragma unroll
            for (int k = 0; k < 16; ++k) ra[k] = dpp_add<0x121>(ra[k]);
            float va = ra[0], vb = ra[8];
#pragma unroll
            for (int k = 1; k < 8; ++k) { const bool sel = (lane & 7) == k; va = sel ? ra[k] : va; vb = sel ? ra[8 + k] : vb; }
            LAS f32x2* P = (LAS f32x2*)(lds + LDS_TAB);
            { const int q = 2 * (lane & 7) + ((lane >> 4) & 1), row = m0 + 8 * (q >> 2) + 4 * h + (q & 3);
              if ((lane & 8) == 0) P[row * 2 + (LD ? 0 : 1)] = (f32x2){va, vb}; }
            asm volatile("s_waitcnt lgkmcnt(0)" ::: "memory"); GM_BAR();
            if constexpr (!LD) {
                if (lane < 32) {
                    const int row = lid * 32 + lane;
                    const f32x2 a = P[row * 2], b = P[row * 2 + 1];
                    const float su = a.x + b.x, mt = su * (1.0f / 224.0f), m2 = fmaxf(a.y + b.y - su * mt, 0.f);
                    st16_wt(ws_rsrc(p.ws), (unsigned)WS_STAT + ((panel * 8 + j) * 128 + row) * 16, (u32x4){__float_as_uint(mt), p.tag, __float_as_uint(m2), p.tag});
                }
            }
        }
        STAMP(0, 3);
        LnHook<NT, LD> hl{p, lds, panel, j, wid, lane, n0, z2};
        pipe.template run<true>(s, DM / 64, 2 * DM / 64, acc, hl DBG_ARGS(2));
        pipe.end();
        STAMP(0, 4);
#pragma unroll
        for (int t = 0; t < NT; ++t)
#pragma unroll
            for (int q = 0; q < 16; q += 2) z1[t][q >> 1] = pk_bf16(acc[t][0][q], acc[t][0][q + 1]);
    }
    {
        using C = CfgS; using W = g32::Wv<1, NT, LD>; Src<C> s;
        src_x_raw<C>(s, p.wsp + (size_t)panel * 128 * SEQ, SEQ * 2, lid, lane);
        src_w_own<C>(s, p.ws + WS_Z + (size_t)j * 32 * 28672, lid, lane);
        g32::Pipe<C, W, SPD> pipe(lds, s, SEQ / 64, SEQ / 64, lid, m0, n0, lane);
        pipe.issue_x(s);
        if (wid == 4) wait_flags(p.ws, p.tag, F_Z + j * 32, 16, false, 0, lane);
        GM_BAR();
        pipe.issue_w(s);
        STAMP(0, 5);
        STAMP(0, 6);
        pipe.landed();
        f32x16 acc[NT][1]; g32::acc_zero<W>(acc);
        SpHook hk{p, lds, panel, wid, lane};
        { GeluFill<NT> gf{z1};
          pipe.template run_fill<true>(s, 0, 2 * NT, 0, acc, hk, gf DBG_ARGS(3)); }
        g32::NoFill nf;
        pipe.template run_fill<true>(s, 2 * NT, SEQ / 64, 0, acc, hk, nf DBG_ARGS(3));
        pipe.end();
        STAMP(0, 7);
        const rsrc_t rs = ws_rsrc(p.ws);
        const f32x2 bg = ((const LAS f32x2*)(lds + LDS_TAB + 2048))[m0 + r]; const float b = bg.x, g1 = bg.y;
        const unsigned ub = (unsigned)WS_ACT + (panel * 352 + j * 28 + n0 * 4 + h) * 2048 + (m0 + r) * 16;
#pragma unroll
        for (int t = 0; t < NT; ++t)
#pragma unroll
            for (int i = 0; i < 4; i += 2) {
                unsigned w4[4];
#pragma unroll
                for (int e = 0; e < 4; ++e) { const int q = 4 * i + 2 * e; const unsigned z = z1[t][q >> 1];
                    w4[e] = pk_bf16((acc[t][0][q] + b) * g1 * bf_lo(z), (acc[t][0][q + 1] + b) * g1 * bf_hi(z)); }
                st16_wt(rs, ub + (t * 4 + i) * 2048, g32::pair32(w4[0], w4[1], w4[2], w4[3]));
            }
        asm volatile("s_waitcnt vmcnt(0)" ::: "memory");
        GM_BAR();
        if (threadIdx.x == 256) signal(p.ws, F_U + panel * 32 + j, p.tag);
    }
    STAMP(0, 8);
    {
        using C = CfgO; using W = g32::Wv<2, 1, LD>;
        const int mo = (wid >> 2) * 64;
        Src<C> s;
        src_x_own<C>(s, p.ws + WS_ACT + (size_t)panel * 352 * 2048, lid, lane);
        g32::src_w_raw<C>(s, p.wout + j * 128, DM * 2, lid, lane);
        set_w2<C>(s, p.wo + j * 128, FH / 64);
        f32x16 acc[1][2]; g32::acc_zero<W>(acc);
        WaitHook hk{p.ws, p.tag, wid, lane, FH / 64 - C::NS, F_O + (panel >> 2) * 32, 32};
        g32::Pipe<C, W, 2> pipe(lds, s, (FH + DM) / 64, 64, lid, mo, lid, lane);
        pipe.issue_w(s);
        if (wid == 4) wait_flags(p.ws, p.tag, F_U + panel * 32, 8, false, 0, lane);
        GM_BAR();
        pipe.issue_x(s);
        pipe.landed();
        pipe.template run<true>(s, 0, (FH + DM) / 64, acc, hk DBG_ARGS(4));
        pipe.end();
        STAMP(0, 9);
#pragma unroll
        for (int m = 0; m < 2; ++m)
#pragma unroll
            for (int i = 0; i < 4; i += 2) { const f32x16& A = acc[0][m];
                *(LAS u32x4*)(lds + (mo + m * 32 + r) * 272 + lid * 64 + (i + h) * 16) =
                    g32::pair32(pk_bf16(A[4 * i], A[4 * i + 1]), pk_bf16(A[4 * i + 2], A[4 * i + 3]), pk_bf16(A[4 * i + 4], A[4 * i + 5]), pk_bf16(A[4 * i + 6], A[4 * i + 7])); }
        asm volatile("s_waitcnt lgkmcnt(0)" ::: "memory"); GM_BAR();
#pragma unroll
        for (int i = 0; i < 4; ++i) { const int row = wid * 16 + i * 4 + (lane >> 4);
            *(u32x4*)(p.out + (size_t)(panel * 128 + row) * DM + j * 128 + (lane & 15) * 8) = *(const LAS u32x4*)(lds + row * 272 + (lane & 15) * 16); }
    }
}

constexpr int AT_STAGE = 16384, AT_NS = 4;
#define MFMA32(a, b, c) __builtin_amdgcn_mfma_f32_32x32x16_bf16(a, b, c, 0, 0, 0)
#define MFMA16(a, b, c) __builtin_amdgcn_mfma_f32_16x16x32_bf16(a, b, c, 0, 0, 0)
#define PIN(x) asm volatile("" : "+v"(x))
__device__ __forceinline__ void attn_wg(const Ctx& p, ldsp lds, int head, int qb, int x, int a, int wid, int lane) {
    if (wid == 4) wait_flags(p.ws, p.tag, F_Q + (head >> 1) * 32, 8, false, 0, lane);
    GM_BAR();
    STAMP(0, 3);
    const unsigned char* Kg = p.ws + WS_K + (size_t)head * 131072; const unsigned char* Vg = p.ws + WS_V + (size_t)head * 131072;
    const unsigned ldb = (unsigned)(uintptr_t)lds;
    const unsigned voff = wid * 1024 + lane * 16;
    const char* kb = (const char*)Kg; const char* vb = (const char*)Vg;
    auto stage = [&](int st, int slot) {
        asm volatile("s_add_u32 m0, %2, 0\n\ts_nop 0\n\tglobal_load_lds_dwordx4 %0, %1 sc1" ::"v"(voff), "s"(kb + (size_t)st * 8192), "s"(ldb + slot * AT_STAGE + wid * 1024) : "memory", "scc");
        asm volatile("s_add_u32 m0, %2, 0\n\ts_nop 0\n\tglobal_load_lds_dwordx4 %0, %1 sc1" ::"v"(voff), "s"(vb + (size_t)st * 8192), "s"(ldb + slot * AT_STAGE + 8192 + wid * 1024) : "memory", "scc");
    };
    stage(0, 0); stage(1, 1); stage(2, 2);
    const int q0 = qb * 512 + wid * 64;
    bf16x8 qf[2][2];
#pragma unroll
    for (int a = 0; a < 2; ++a)
#pragma unroll
        for (int d = 0; d < 2; ++d) qf[a][d] = __builtin_bit_cast(bf16x8, ld16_dev(ws_rsrc(p.ws), (unsigned)WS_Q + (head * 64 + (q0 >> 5) + a) * 2048 + d * 1024 + lane * 16));
    bf16x8 sel;
    { const short one = ((lane & 15) == ((lane >> 4) & 1)) ? (short)0x3f80 : (short)0;
#pragma unroll
      for (int i = 0; i < 8; ++i) sel[i] = one; }
    asm volatile("s_waitcnt vmcnt(0)" ::: "memory");
    GM_BAR();
    f32x16 o[2], negm[2]; f32x4 l[2];
    const f32x16 zero16 = {};
    bf16x8 kA[2], kB[2], vA[2], vB[2];
    ldsp lb = lds + lane * 16;
    kA[0] = *(const LAS bf16x8*)(lb); kA[1] = *(const LAS bf16x8*)(lb + 1024);
#pragma unroll
    for (int a = 0; a < 2; ++a) {
        f32x16 s = MFMA32(kA[0], qf[a][0], zero16); s = MFMA32(kA[1], qf[a][1], s);
        float m = fmaxf(fmaxf(s[0], s[1]), fmaxf(s[2], s[3]));
#pragma unroll
        for (int r = 4; r < 16; r += 2) m = fmaxf(m, fmaxf(s[r], s[r + 1]));
        const auto rr = __builtin_amdgcn_permlane32_swap(__float_as_uint(m), __float_as_uint(m), false, false);
        m = -fmaxf(__uint_as_float(rr[0]), __uint_as_float(rr[1]));
#pragma unroll
        for (int r = 0; r < 16; ++r) negm[a][r] = m;
        PIN(negm[a]);
        o[a] = zero16; l[a] = (f32x4){0.f, 0.f, 0.f, 0.f};
    }
    f32x16 S0, S1; u32x4 p0l, p0h, p1l, p1h;
    S0 = MFMA32(kA[0], qf[0][0], negm[0]); S0 = MFMA32(kA[1], qf[0][1], S0);
    p1l = (u32x4){0u, 0u, 0u, 0u}; p1h = p1l;
    vB[0] = (bf16x8){0, 0, 0, 0, 0, 0, 0, 0}; vB[1] = vB[0];
    STAMP(0, 4);
#define EX4(S, i) do { S[i] = __builtin_amdgcn_exp2f(S[i]); S[i + 1] = __builtin_amdgcn_exp2f(S[i + 1]); S[i + 2] = __builtin_amdgcn_exp2f(S[i + 2]); S[i + 3] = __builtin_amdgcn_exp2f(S[i + 3]); } while (0)
#define PK4(P, S, i) do { P[0] = pk_bf16(S[i], S[i + 1]); P[1] = pk_bf16(S[i + 2], S[i + 3]); P[2] = pk_bf16(S[i + 4], S[i + 5]); P[3] = pk_bf16(S[i + 6], S[i + 7]); } while (0)
#define BC(x) __builtin_bit_cast(bf16x8, x)
#define ASTEP(SC, SN, PCl, PCh, PPl, PPh, KF, QN, VF, AP, LOADS) do { \
        SN = MFMA32(KF[0], qf[QN][0], negm[QN]); EX4(SC, 0); PIN(SC); GM_SB(); \
        SN = MFMA32(KF[1], qf[QN][1], SN); EX4(SC, 4); LOADS; PIN(SC); GM_SB(); \
        o[AP] = MFMA32(VF[0], BC(PPl), o[AP]); EX4(SC, 8); PIN(SC); GM_SB(); \
        o[AP] = MFMA32(VF[1], BC(PPh), o[AP]); EX4(SC, 12); PIN(SC); GM_SB(); \
        l[AP] = MFMA16(sel, BC(PPl), l[AP]); PK4(PCl, SC, 0); PIN(PCl); GM_SB(); \
        l[AP] = MFMA16(sel, BC(PPh), l[AP]); PK4(PCh, SC, 8); PIN(PCh); GM_SB(); } while (0)
#define LDK(KF, base, kt) do { KF[0] = *(const LAS bf16x8*)((base) + (kt) * 2048); KF[1] = *(const LAS bf16x8*)((base) + (kt) * 2048 + 1024); } while (0)
#define LDV(VF, base, kt) do { VF[0] = *(const LAS bf16x8*)((base) + 8192 + (kt) * 2048); VF[1] = *(const LAS bf16x8*)((base) + 8192 + (kt) * 2048 + 1024); } while (0)
    float gval = 0.f;
    for (int st = 0; st < 16; ++st) {
        ldsp cb = lb + (st & 3) * AT_STAGE, nb = lb + ((st + 1) & 3) * AT_STAGE;
        if (st < 5) {
            const int n = st * 8 + wid; const unsigned pfs = (unsigned)(uintptr_t)lds + LDS_PFS;
            const int p0 = 8 * (x & 1) + 4 * ((x >> 1) & 1), j0 = 4 * (x >> 2);
            if (n < 16) pf_lines(p.wsp + (size_t)(p0 * 128 + a * 32) * SEQ, (unsigned)(lane & 31) * (SEQ * 2) + (unsigned)(2 * n + (lane >> 5)) * 128u, pfs);
            else if (n < 30) pf_lines(p.wout + (size_t)(a * 112) * DM + j0 * 128, (unsigned)((n - 16) * 8 + (lane >> 3)) * (DM * 2) + (unsigned)(lane & 7) * 128u, pfs);
            else if (n < 38) pf_lines(p.wo + (size_t)(a * 64) * DM + j0 * 128, (unsigned)((n - 30) * 8 + (lane >> 3)) * (DM * 2) + (unsigned)(lane & 7) * 128u, pfs);
        }
        if (st == 8) {
            poll_flags(p.ws, p.tag, F_Q, 1, true, F_Q, lane);
            gval = __uint_as_float(__hip_atomic_load((gu32*)(p.ws + WS_GATE) + q0 + lane, RLX_AGENT));
        }
        ASTEP(S0, S1, p0l, p0h, p1l, p1h, kA, 1, vB, 1, { LDK(kB, cb, 1); LDV(vA, cb, 0); });
        ASTEP(S1, S0, p1l, p1h, p0l, p0h, kB, 0, vA, 0, {});
        ASTEP(S0, S1, p0l, p0h, p1l, p1h, kB, 1, vA, 1, { LDK(kA, cb, 2); LDV(vB, cb, 1); });
        ASTEP(S1, S0, p1l, p1h, p0l, p0h, kA, 0, vB, 0, {});
        ASTEP(S0, S1, p0l, p0h, p1l, p1h, kA, 1, vB, 1, { LDK(kB, cb, 3); LDV(vA, cb, 2); });
        ASTEP(S1, S0, p1l, p1h, p0l, p0h, kB, 0, vA, 0, {});
        if (st + 1 < 16) {
            if (st + 2 < 16) asm volatile("s_waitcnt vmcnt(2)" ::: "memory"); else asm volatile("s_waitcnt vmcnt(0)" ::: "memory");
            GM_BAR();
            if (st + 3 < 16) stage(st + 3, (st + 3) & 3);
        }
        ASTEP(S0, S1, p0l, p0h, p1l, p1h, kB, 1, vA, 1, { LDK(kA, nb, 0); LDV(vB, cb, 3); });
        ASTEP(S1, S0, p1l, p1h, p0l, p0h, kA, 0, vB, 0, {});
    }
    o[1] = MFMA32(vB[0], BC(p1l), o[1]); o[1] = MFMA32(vB[1], BC(p1h), o[1]);
    l[1] = MFMA16(sel, BC(p1l), l[1]); l[1] = MFMA16(sel, BC(p1h), l[1]);
#undef EX4
#undef PK4
#undef ASTEP
#undef LDK
#undef LDV
    STAMP(0, 5);
    const rsrc_t rs = ws_rsrc(p.ws);
    const float gt0 = __shfl(gval, lane & 31), gt1 = __shfl(gval, 32 + (lane & 31));
#pragma unroll
    for (int a = 0; a < 2; ++a) {
        const int q = lane & 31, hh = lane >> 5;
        const float l0 = __shfl(l[a][0], q & 15), l1 = __shfl(l[a][1], q & 15);
        const float lsum = (q < 16) ? l0 : l1;
        const float sc = (a ? gt1 : gt0) * __builtin_amdgcn_rcpf(lsum);
        const int row = q0 + a * 32 + q, panel = row >> 7;
#pragma unroll
        for (int i = 0; i < 4; i += 2) {
            const unsigned a0 = pk_bf16(o[a][4 * i] * sc, o[a][4 * i + 1] * sc), a1 = pk_bf16(o[a][4 * i + 2] * sc, o[a][4 * i + 3] * sc);
            const unsigned b0 = pk_bf16(o[a][4 * i + 4] * sc, o[a][4 * i + 5] * sc), b1 = pk_bf16(o[a][4 * i + 6] * sc, o[a][4 * i + 7] * sc);
            const auto r0 = __builtin_amdgcn_permlane32_swap(a0, b0, false, false); const auto r1 = __builtin_amdgcn_permlane32_swap(a1, b1, false, false);
            const int c = 224 + head * 4 + i + hh;
            st16_wt(rs, (unsigned)WS_ACT + (panel * 352 + c) * 2048 + (row & 127) * 16, (u32x4){r0[0], r1[0], r0[1], r1[1]});
        }
    }
    asm volatile("s_waitcnt vmcnt(0)" ::: "memory");
    GM_BAR();
    if (threadIdx.x == 0) signal(p.ws, F_O + qb * 32 + head, p.tag);
}

__device__ __forceinline__ void gate_row(const Ctx& p, int row, int lane) {
    const u32x4* xr = (const u32x4*)(p.ax + (size_t)row * DM) + lane * 2; const u32x4* gw = (const u32x4*)p.gw + lane * 2;
    float s = 0.f;
#pragma unroll
    for (int i = 0; i < 2; ++i) { const u32x4 a = xr[i], b = gw[i];
#pragma unroll
        for (int e = 0; e < 4; ++e) s += bf_lo(a[e]) * bf_lo(b[e]) + bf_hi(a[e]) * bf_hi(b[e]); }
#pragma unroll
    for (int o = 32; o >= 1; o >>= 1) s += __shfl_xor(s, o);
    if (lane == 0) __hip_atomic_store((gu32*)(p.ws + WS_GATE) + row, __float_as_uint(__builtin_amdgcn_rcpf(1.0f + __builtin_amdgcn_exp2f(-1.4426950408889634f * s))), RLX_AGENT);
}
constexpr int LDS_BYTES = CfgQ::LDS + 256;
static_assert(LDS_TAB + 3072 <= LDS_BYTES && CfgO::LDS <= LDS_BYTES && AT_NS * AT_STAGE <= LDS_BYTES, "LDS map");
__global__ __launch_bounds__(512, 2) void fused(Params pp) {
    extern __shared__ __attribute__((aligned(16))) unsigned char smem[]; ldsp lds = (ldsp)smem;
    const int lane = threadIdx.x & 63, wid = __builtin_amdgcn_readfirstlane(threadIdx.x >> 6);
    const int b = blockIdx.x, x = b & 7, role = (b >> 3) & 1, idx = b >> 4;
    Ctx p; static_cast<Params&>(p) = pp;
    p.tag = (unsigned)__builtin_amdgcn_readfirstlane(__hip_atomic_load((gu32*)(pp.ws + WS_CTL) + C_EPOCH, RLX_AGENT)) + 1u;
    STAMP(0, 0);
    if (role == 0) {
        const int pair = 4 * (x >> 1) + (idx >> 2), panel = 4 * (x & 1) + (idx & 3);
        if (wid >= 4) {
#pragma unroll
            for (int i = 0; i < 4; ++i) gate_row(p, panel * 256 + pair * 16 + (wid - 4) * 4 + i, lane);
        }
        if (wid < 4) qkv_wave<0>(p, lds, pair, panel, wid, lane); else qkv_wave<1>(p, lds, pair, panel, wid, lane);
        STAMP(0, 6);
        attn_wg(p, lds, 4 * x + (idx >> 2), idx & 3, x, idx, wid, lane);
    } else {
        const int panel = 8 * (x & 1) + 4 * ((x >> 1) & 1) + (idx >> 2), j = 4 * (x >> 2) + (idx & 3);
        if (wid < 4) b_wave<true>(p, lds, panel, j, wid, lane); else b_wave<false>(p, lds, panel, j, wid, lane);
        if (panel == 0 && j == 0 && threadIdx.x == 0) __hip_atomic_store((gu32*)(pp.ws + WS_CTL) + C_EPOCH, p.tag, RLX_AGENT);
    }
    STAMP(0, 15);
}
}

extern "C" void kernel_launch(void* const* d_in, const int* in_sizes, int n_in, void* d_out, int out_size, void* d_ws, size_t ws_size, hipStream_t stream) {
    using namespace gm;
    Params p;
    p.ax = (const u16*)d_in[0]; p.wqkv = (const u16*)d_in[1]; p.wo = (const u16*)d_in[2]; p.bx = (const u16*)d_in[3]; p.win = (const u16*)d_in[4];
    p.wsp = (const u16*)d_in[5]; p.bsp = (const u16*)d_in[6]; p.wout = (const u16*)d_in[7]; p.gw = (const u16*)d_in[8];
    p.out = (u16*)d_out; p.ws = (unsigned char*)d_ws;
    (void)hipFuncSetAttribute((const void*)fused, hipFuncAttributeMaxDynamicSharedMemorySize, LDS_BYTES);
    fused<<<256, 512, LDS_BYTES, stream>>>(p);
}
```

```cpp
#include <hip/hip_runtime.h>
#include <hip/hip_bf16.h>
#include <stdint.h>

#define LAS __attribute__((address_space(3)))
typedef short bf16x8 __attribute__((ext_vector_type(8)));
typedef short s16x4 __attribute__((ext_vector_type(4)));
typedef float f32x2 __attribute__((ext_vector_type(2)));
typedef float f32x4 __attribute__((ext_vector_type(4)));
typedef float f32x16 __attribute__((ext_vector_type(16)));
typedef unsigned u32x4 __attribute__((ext_vector_type(4)));
typedef unsigned u32x2 __attribute__((ext_vector_type(2)));
typedef LAS unsigned char* ldsp;
typedef unsigned short u16;

#define PF_POLICY "sc1"
#define PFD 5
#define SPD 1
namespace gm {
constexpr int SEQ = 2048, DM = 1024, NH = 32, FH = 1792;
constexpr size_t WS_CTL = 0;
constexpr size_t CTL_BYTES = 16384;
constexpr size_t WS_GATE = 16384;
constexpr size_t WS_STAT = 32768;
constexpr size_t WS_Q = 1u << 20;
constexpr size_t WS_K = WS_Q + (4u << 20);
constexpr size_t WS_V = WS_K + (4u << 20);
constexpr size_t WS_Z = WS_V + (4u << 20);
constexpr size_t WS_ACT = WS_Z + (8u << 20);
constexpr int LDS_PFS = 147456 - 0;
constexpr float QSCALE = 0.17677669529663687f * 1.4426950408889634f;

#define TRACE(t) do {} while (0)
#define DBG_PARAMS
#define DBG_ARGS(id)
#define DBG_FWD
#define STAMP(k, i) do {} while (0)
constexpr int C_EPOCH = 0, C_TMO = 64, F_Q = 128  , F_S = F_Q + 512  , F_Z = F_S + 512  ,
              F_U = F_Z + 256  , F_O = F_U + 512  , C_END = F_O + 128;
static_assert(C_END * 4 <= (int)CTL_BYTES, "control block");
#define GM_VM0() __builtin_amdgcn_s_waitcnt(0x0F70)
#define RLX_AGENT __ATOMIC_RELAXED, __HIP_MEMORY_SCOPE_AGENT
typedef __attribute__((address_space(1))) unsigned gu32;
typedef __attribute__((address_space(1))) unsigned long long gu64;
typedef __amdgpu_buffer_rsrc_t rsrc_t;
__device__ __forceinline__ rsrc_t ws_rsrc(unsigned char* ws) { return __builtin_amdgcn_make_buffer_rsrc(ws, 0, 64u << 20, 0x00020000); }
__device__ __forceinline__ void st16_wt(rsrc_t r, unsigned off, u32x4 v) { __builtin_amdgcn_raw_buffer_store_b128(v, r, off, 0, 16); }
__device__ __forceinline__ void signal(unsigned char* ws, int word, unsigned tag) { __hip_atomic_store((gu32*)(ws + WS_CTL) + word, tag, RLX_AGENT); }
__device__ __forceinline__ void poll_flags(unsigned char* ws, unsigned tag, int w0, int n, bool all128, int wq, int lane) {
    gu32* ctl = (gu32*)(ws + WS_CTL);
    for (unsigned spins = 0;;) {
        bool ok = __hip_atomic_load(ctl + w0 + (lane < n ? lane : 0), RLX_AGENT) == tag;
        if (all128) { const unsigned q0 = __hip_atomic_load(ctl + wq + (lane >> 3) * 32 + (lane & 7), RLX_AGENT), q1 = __hip_atomic_load(ctl + wq + 256 + (lane >> 3) * 32 + (lane & 7), RLX_AGENT); ok = ok && q0 == tag && q1 == tag; }
        if (__all(ok)) break;
        __builtin_amdgcn_s_sleep(2);
        if (++spins > (1u << 19)) { __hip_atomic_store(ctl + C_TMO, tag, RLX_AGENT); break; }
        if ((spins & 255u) == 0u && __builtin_amdgcn_readfirstlane(__hip_atomic_load(ctl + C_TMO, RLX_AGENT)) == tag) break;
    }
}
__device__ __forceinline__ void wait_flags(unsigned char* ws, unsigned tag, int w0, int n, bool all128, int wq, int lane) {
    poll_flags(ws, tag, w0, n, all128, wq, lane);
    GM_VM0();
}
__device__ __forceinline__ u32x4 ld16_dev(rsrc_t r, unsigned off) { return __builtin_amdgcn_raw_buffer_load_b128(r, off, 0, 16); }
enum { X_RAW = 0, X_OWN = 1 };
enum { W_P384 = 0, W_P448 = 1, W_P256 = 2, W_OWN = 3 };

__device__ __forceinline__ unsigned pk_bf16(float lo, float hi) {
    typedef __bf16 bf16x2_t __attribute__((ext_vector_type(2)));
    f32x2 v = {lo, hi}; bf16x2_t b = __builtin_convertvector(v, bf16x2_t); return __builtin_bit_cast(unsigned, b);
}
__device__ __forceinline__ float bf_lo(unsigned w) { return __uint_as_float(w << 16); }
__device__ __forceinline__ float bf_hi(unsigned w) { return __uint_as_float(w & 0xffff0000u); }
__device__ __forceinline__ s16x4 tr_read(ldsp p) { typedef short v4s __attribute__((ext_vector_type(4))); return __builtin_amdgcn_ds_read_tr16_b64_v4i16((LAS v4s*)p); }
template <int I, bool SC = false> __device__ __forceinline__ void glds_run(const char* sbase, unsigned voff) {
    if constexpr (SC) asm volatile("global_load_lds_dwordx4 %0, %1 offset:%2 sc1" ::"v"(voff), "s"(sbase), "n"((I & 3) * 1024) : "memory");
    else asm volatile("global_load_lds_dwordx4 %0, %1 offset:%2" ::"v"(voff), "s"(sbase), "n"((I & 3) * 1024) : "memory");
}
template <int I, bool SC = false> __device__ __forceinline__ void glds_set(const char* sbase, unsigned voff, unsigned dst) {
    if constexpr (SC) asm volatile("s_add_u32 m0, %2, %3\n\ts_nop 0\n\tglobal_load_lds_dwordx4 %0, %1 offset:%4 sc1" ::"v"(voff), "s"(sbase), "s"(dst), "n"((I & ~3) * 1024), "n"((I & 3) * 1024) : "memory", "scc");
    else asm volatile("s_add_u32 m0, %2, %3\n\ts_nop 0\n\tglobal_load_lds_dwordx4 %0, %1 offset:%4" ::"v"(voff), "s"(sbase), "s"(dst), "n"((I & ~3) * 1024), "n"((I & 3) * 1024) : "memory", "scc");
}
__device__ __forceinline__ void pf_lines(const void* base, unsigned voff, unsigned ldsdst) {
    asm volatile("s_mov_b32 m0, %2\n\ts_nop 0\n\tglobal_load_lds_dword %0, %1 " PF_POLICY ::"v"(voff), "s"(base), "s"(ldsdst) : "memory");
}
#define GM_BAR() do { asm volatile("" ::: "memory"); __builtin_amdgcn_s_barrier(); asm volatile("" ::: "memory"); } while (0)
#define GM_SB() __builtin_amdgcn_sched_barrier(0)
__device__ __forceinline__ bool at_tile(int t, int k) { asm volatile("" : "+s"(t)); return t == k; }

template <int BM_, int BN_, int XI_, int WI_, int NSX_, int NSW_> struct Cfg {
    static constexpr int BM = BM_, BN = BN_, XI = XI_, WI = WI_, NSX = NSX_, NSW = NSW_;
    static constexpr int XB = BM * 128, WB = BN * 128, WOFF = NSX * XB, LDS = NSX * XB + NSW * WB;
    static constexpr int PX = BM / 32, PW = BN / 32;
    static constexpr int WPITCH = (WI == W_P384) ? 384 : (WI == W_P448) ? 448 : (WI == W_P256) ? 256 : 0;
    static constexpr bool XSC = false, WSC = false;
};
template <class C> struct Src { const char* x; const char* w; const char* w2; int ksplit; unsigned xstep, wstep; unsigned xv[C::PX]; unsigned wv[C::PW]; };
template <int WI> __device__ __forceinline__ int w_swz(int r, int x) {
    if (WI == W_P384) return x ^ (((((r >> 1) & 1) | (((r >> 3) & 1) << 1))) << 5);
    if (WI == W_P448) return x ^ (((r >> 3) & 1) << 5);
    if (WI == W_P256) return x ^ (((r & 3) | (((r >> 3) & 1) << 2)) << 5);
    return x;
}
template <class C> __device__ __forceinline__ const char* w_tile(const Src<C>& s, int kt) { return (kt < s.ksplit ? s.w : s.w2) + (unsigned)kt * s.wstep; }
template <class C> __device__ __forceinline__ void set_w2(Src<C>& s, const void* base, int ksplit) { s.ksplit = ksplit; s.w2 = (const char*)base - 4096 - (size_t)ksplit * s.wstep; }
template <class C> __device__ __forceinline__ void src_x_raw(Src<C>& s, const void* base, unsigned pitch, int lid, int lane) {
    s.x = (const char*)base - 4096; s.xstep = 128;
#pragma unroll
    for (int i = 0; i < C::PX; ++i) { const int row = 8 * (lid * C::PX + i) + (lane >> 3); s.xv[i] = row * pitch + (((lane & 7) ^ ((row >> 1) & 7)) * 16) + 4096 - (i & 3) * 1024; }
}
template <class C> __device__ __forceinline__ void src_x_own(Src<C>& s, const void* base, int lid, int lane) {
    s.x = (const char*)base - 4096; s.xstep = C::XB;
#pragma unroll
    for (int i = 0; i < C::PX; ++i) s.xv[i] = (lid * C::PX + i) * 1024 + lane * 16 + 4096 - (i & 3) * 1024;
}
template <class C> __device__ __forceinline__ void src_w_own(Src<C>& s, const void* base, int lid, int lane) {
    s.w = (const char*)base - 4096; s.w2 = s.w; s.ksplit = 1 << 20; s.wstep = C::WB;
#pragma unroll
    for (int i = 0; i < C::PW; ++i) s.wv[i] = (lid * C::PW + i) * 1024 + lane * 16 + 4096 - (i & 3) * 1024;
}
template <class C> __device__ __forceinline__ void src_w_raw(Src<C>& s, const void* base, unsigned pitch, unsigned segstride, int lid, int lane) {
    s.w = (const char*)base - 4096; s.w2 = s.w; s.ksplit = 1 << 20; s.wstep = 64 * pitch;
#pragma unroll
    for (int i = 0; i < C::PW; ++i) { const int o = 1024 * (lid * C::PW + i) + 16 * lane; const int r = o / C::WPITCH, x = w_swz<C::WI>(r, o % C::WPITCH);
        s.wv[i] = r * pitch + (x >> 7) * segstride + (x & 127) + 4096 - (i & 3) * 1024; }
}
template <class C, int I0, int I1, bool FIRST> __device__ __forceinline__ void stage_rng(unsigned dx, unsigned dw, const Src<C>& s, const char* xb, const char* wb) {
    if constexpr (I0 < I1) {
        if constexpr (I0 < C::PW) { if constexpr (FIRST || (I0 & 3) == 0) glds_set<I0, C::WSC>(wb, s.wv[I0], dw); else glds_run<I0, C::WSC>(wb, s.wv[I0]); }
        else { constexpr int j = I0 - C::PW; if constexpr (FIRST || (j & 3) == 0) glds_set<j, C::XSC>(xb, s.xv[j], dx); else glds_run<j, C::XSC>(xb, s.xv[j]); }
        stage_rng<C, I0 + 1, I1, false>(dx, dw, s, xb, wb);
    }
}
template <int MT_, int NT_, int N0_, unsigned SWAP_, bool LD_> struct Wv { static constexpr int MT = MT_, NT = NT_, N0 = N0_; static constexpr unsigned SWAP = SWAP_; static constexpr bool LD = LD_; };
template <class W> struct Frag { bf16x8 x[W::MT]; s16x4 wl[W::NT], wh[W::NT]; };
template <class C, class W> struct RdAddr { unsigned x[2]; unsigned w[(C::WI == W_P384) ? 4 : (C::WI == W_P256) ? W::NT : 2]; };
template <class C, class W> __device__ __forceinline__ void rd_init(RdAddr<C, W>& a, int m0, int n0, int lane) {
    const int fr = lane & 15, fq = lane >> 4, g = lane >> 4, q = (lane & 15) >> 2, p = lane & 3;
    if (C::XI == X_RAW) { for (int kk = 0; kk < 2; ++kk) a.x[kk] = (m0 + fr) * 128 + (((kk * 4 + fq) ^ (fr >> 1)) * 16); }
    else { for (int kk = 0; kk < 2; ++kk) a.x[kk] = (kk * 4 + fq) * (C::BM * 16) + (m0 + fr) * 16; }
    if (C::WI == W_P384) { const int s = ((q >> 1) & 1) | ((g & 1) << 1);
        for (int c = 0; c < 4; ++c) a.w[c] = C::WOFF + (8 * g + q) * 384 + ((c ^ s) * 32) + p * 8; }
    else if (C::WI == W_P448) { const int s = g & 1;
        for (int c = 0; c < 2; ++c) a.w[c] = C::WOFF + (8 * g + q) * 448 + (((c ^ s) - c) * 32) + p * 8; }
    else if (C::WI == W_P256) { const int s = (q & 3) | ((g & 1) << 2);
        for (int t = 0; t < W::NT; ++t) a.w[t] = C::WOFF + (8 * g + q) * 256 + (((n0 + t) ^ s) * 32) + p * 8; }
    else { for (int h = 0; h < 2; ++h) a.w[h] = C::WOFF + (p >> 1) * 1024 + (8 * g + 4 * (h ^ (p >> 1)) + q) * 16 + (p & 1) * 8 + W::N0 * 2048; }
}
template <class C, class W> __device__ __forceinline__ s16x4 rd_w(ldsp b, const RdAddr<C, W>& a, int t, int kk, int h) {
    const int c = W::N0 + t;
    if (C::WI == W_P384) return tr_read(b + a.w[c & 3] + (c >> 2) * 128 + kk * 32 * 384 + h * 4 * 384);
    else if (C::WI == W_P448) return tr_read(b + a.w[c & 1] + c * 32 + kk * 32 * 448 + h * 4 * 448);
    else if (C::WI == W_P256) return tr_read(b + a.w[t] + kk * 32 * 256 + h * 4 * 256);
    else return tr_read(b + a.w[h] + t * 2048 + kk * 512);
}
template <class C, class W> __device__ __forceinline__ void rd_one(Frag<W>& f, ldsp bx, ldsp bw, const RdAddr<C, W>& a, int kk, int r) {
    if (r < 2) { if (r == 0) f.wl[0] = rd_w<C, W>(bw, a, 0, kk, 0); else f.wh[0] = rd_w<C, W>(bw, a, 0, kk, 1); }
    else if (r < 2 + W::MT) { const int m = r - 2; f.x[m] = *(const LAS bf16x8*)(bx + a.x[kk] + m * ((C::XI == X_RAW) ? 2048 : 256)); }
    else { const int u = r - W::MT; const int t = u >> 1; if (u & 1) f.wh[t] = rd_w<C, W>(bw, a, t, kk, 1); else f.wl[t] = rd_w<C, W>(bw, a, t, kk, 0); }
}
template <class W> __device__ __forceinline__ void mma_one(f32x4 (&acc)[W::NT][W::MT], const Frag<W>& f, int i) {
    const int t = i / W::MT, m = i % W::MT;
    const bf16x8 w = __builtin_shufflevector(f.wl[t], f.wh[t], 0, 1, 2, 3, 4, 5, 6, 7);
    acc[t][m] = ((W::SWAP >> t) & 1) ? __builtin_amdgcn_mfma_f32_16x16x32_bf16(f.x[m], w, acc[t][m], 0, 0, 0)
                                     : __builtin_amdgcn_mfma_f32_16x16x32_bf16(w, f.x[m], acc[t][m], 0, 0, 0);
}
template <int RFRONT_, int VA_, int RF1_ = RFRONT_> struct Sch { static constexpr int RFRONT = RFRONT_, VA = VA_, RF1 = RF1_; };
template <class C, class W, class S, int RF, int V0, int V1, int I>
__device__ __forceinline__ void step_slot(f32x4 (&acc)[W::NT][W::MT], const Frag<W>& fc, Frag<W>& fn, ldsp bx, ldsp bw, const RdAddr<C, W>& a, int ksn,
                                          const Src<C>& s, const char* xb, const char* wb, unsigned dx, unsigned dw) {
    constexpr int NM = W::NT * W::MT, NR = 2 * W::NT + W::MT, NV = V1 - V0;
    constexpr int RS = (NM * RF / 16) < 1 ? 1 : (NM * RF / 16);
    if constexpr (I < NM) {
        mma_one<W>(acc, fc, I);
        if constexpr (I < RS) {
#pragma unroll
            for (int r = I * NR / RS; r < (I + 1) * NR / RS; ++r) rd_one<C, W>(fn, bx, bw, a, ksn, r);
        }
        if constexpr (W::LD) {
            constexpr int v0 = V0 + I * NV / NM, v1 = V0 + (I + 1) * NV / NM;
            stage_rng<C, v0, v1, v0 == V0>(dx, dw, s, xb, wb);
        }
        GM_SB();
        step_slot<C, W, S, RF, V0, V1, I + 1>(acc, fc, fn, bx, bw, a, ksn, s, xb, wb, dx, dw);
    }
}
struct NoHook { static constexpr bool PRE = false; __device__ __forceinline__ void pre_x() const {} __device__ __forceinline__ void at(int) const {} };
template <class C, class W, class S, class H>
__device__ __forceinline__ void gemm_loop(ldsp lds, const Src<C>& s, int nt, f32x4 (&acc)[W::NT][W::MT], int lid, int m0, int n0, int lane, H& hook DBG_PARAMS) {
    constexpr int PX = C::PX, PW = C::PW, NSX = C::NSX, NSW = C::NSW, NV = PX + PW;
    static_assert(S::VA >= PW && S::VA <= NV, "all W pieces go out in the step behind the barrier");
    static_assert(NSX >= NSW && NSW >= 2, "ring depths");
    RdAddr<C, W> a; rd_init<C, W>(a, m0, n0, lane);
    const unsigned ldb = (unsigned)(uintptr_t)lds;
    const unsigned lx0 = ldb + lid * PX * 1024, lw0 = ldb + C::WOFF + lid * PW * 1024;
    Frag<W> f0, f1;
    const char* wb = w_tile<C>(s, NSW - 1); const char* xb = s.x + (size_t)(NSX - 1) * s.xstep;
    unsigned dw = lw0 + (NSW - 1) * C::WB, dx = lx0 + (NSX - 1) * C::XB;
    if constexpr (H::PRE) {
        if constexpr (W::LD) {
#pragma unroll
            for (int i = 0; i < NSW; ++i) stage_rng<C, 0, PW, true>(0, lw0 + i * C::WB, s, nullptr, w_tile<C>(s, i));
        }
        hook.pre_x();
        if constexpr (W::LD) {
#pragma unroll
            for (int i = 0; i < NSX - 1; ++i) stage_rng<C, PW, NV, true>(lx0 + i * C::XB, 0, s, s.x + (size_t)i * s.xstep, nullptr);
            if constexpr (S::VA > PW) stage_rng<C, PW, S::VA, true>(dx, dw, s, xb, wb);
            asm volatile("s_waitcnt vmcnt(%0)" ::"n"(S::VA - PW) : "memory");
        }
    } else if constexpr (W::LD) {
#pragma unroll
        for (int i = 0; i < NSX - 1; ++i) {
            if (i < NSW - 1) stage_rng<C, 0, PW, true>(0, lw0 + i * C::WB, s, nullptr, w_tile<C>(s, i));
            stage_rng<C, PW, NV, true>(lx0 + i * C::XB, 0, s, s.x + (size_t)i * s.xstep, nullptr);
        }
        stage_rng<C, 0, S::VA, true>(dx, dw, s, xb, wb);
        constexpr int Y = (NSX - 2) * PX + (NSW - 2) * PW + S::VA;
        asm volatile("s_waitcnt vmcnt(%0)" ::"n"(Y) : "memory");
    }
    GM_BAR();
#pragma unroll
    for (int r = 0; r < 2 * W::NT + W::MT; ++r) rd_one<C, W>(f0, lds, lds, a, 0, r);
    int bx = 0, bw = 0, kx = NSX - 1, kw = NSW - 1;
    __builtin_amdgcn_s_waitcnt(0xC07F);
    for (int t = 0; t < nt; ++t) {
        ldsp cx = lds + bx * C::XB, cw = lds + bw * C::WB;
        const int nbx = (bx + 1 == NSX) ? 0 : bx + 1, nbw = (bw + 1 == NSW) ? 0 : bw + 1;
        ldsp nx = lds + nbx * C::XB, nw = lds + nbw * C::WB;
        TRACE(t);
        GM_SB();
#define GM_STEP(FC, FN, BX, BW, KSN, RF, V0, V1) step_slot<C, W, S, RF, V0, V1, 0>(acc, FC, FN, BX, BW, a, KSN, s, xb, wb, dx, dw)
        GM_STEP(f0, f1, cx, cw, 1, S::RFRONT, S::VA, NV);
        hook.at(t);
        asm volatile("s_waitcnt lgkmcnt(0)" ::: "memory");
        if constexpr (W::LD) {
            constexpr int pw = 1 - NSW, px = 1 - NSX, last = (pw > px) ? pw : px, nper = (-1) - last, Y = nper * NV + ((pw > px) ? PX : 0);
            asm volatile("s_waitcnt vmcnt(%0)" ::"n"(Y) : "memory");
        }
        GM_BAR();
        GM_SB();
        if constexpr (W::LD) {
            kx = (kx + 1 < nt) ? kx + 1 : kx; kw = (kw + 1 < nt) ? kw + 1 : kw;
            xb = s.x + (size_t)kx * s.xstep; wb = w_tile<C>(s, kw);
            dx = lx0 + bx * C::XB; dw = lw0 + bw * C::WB;
        }
        GM_STEP(f1, f0, nx, nw, 0, S::RF1, 0, S::VA);
#undef GM_STEP
        bx = nbx; bw = nbw;
    }
    GM_SB();
    asm volatile("s_waitcnt vmcnt(0) lgkmcnt(0)" ::: "memory");
    GM_BAR();
}
template <class W> __device__ __forceinline__ void acc_zero(f32x4 (&acc)[W::NT][W::MT]) {
#pragma unroll
    for (int t = 0; t < W::NT; ++t)
#pragma unroll
        for (int m = 0; m < W::MT; ++m) acc[t][m] = (f32x4){0.f, 0.f, 0.f, 0.f};
}
__device__ __forceinline__ u32x4 pair16(const f32x4& A, const f32x4& B) {
    const unsigned a0 = pk_bf16(A[0], A[1]), a1 = pk_bf16(A[2], A[3]), b0 = pk_bf16(B[0], B[1]), b1 = pk_bf16(B[2], B[3]);
    const auto r0 = __builtin_amdgcn_permlane16_swap(a0, b0, false, false); const auto r1 = __builtin_amdgcn_permlane16_swap(a1, b1, false, false);
    return (u32x4){r0[0], r1[0], r0[1], r1[1]};
}

namespace g32 {
enum { W_P448 = 1, W_P256 = 2, W_OWNT = 3 };
template <int BM_, int BN_, int XI_, int WI_, int NS_> struct Cfg {
    static constexpr int BM = BM_, BN = BN_, XI = XI_, WI = WI_, NS = NS_;
    static constexpr int XB = BM * 128, WB = BN * 128, WOFF = NS * XB, LDS = NS * (XB + WB);
    static constexpr int PX = BM / 32, PW = BN / 32;
    static constexpr int WPITCH = (WI == W_P448) ? 448 : (WI == W_P256) ? 256 : 16;
    static constexpr bool XSC = (XI == X_OWN), WSC = (WI == W_OWNT);
};
template <class C> __device__ __forceinline__ void src_w_raw(Src<C>& s, const void* base, unsigned pitch, int lid, int lane) {
    s.w = (const char*)base - 4096; s.w2 = s.w; s.ksplit = 1 << 20; s.wstep = 64 * pitch;
#pragma unroll
    for (int i = 0; i < C::PW; ++i) { const int o = 1024 * (lid * C::PW + i) + 16 * lane; const int r = o / C::WPITCH; int x = o % C::WPITCH;
        if (C::WI == W_P256) x ^= (r & 3) << 6;
        s.wv[i] = r * pitch + x + 4096 - (i & 3) * 1024; }
}
template <int MT_, int NT_, bool LD_> struct Wv { static constexpr int MT = MT_, NT = NT_; static constexpr bool LD = LD_; };
template <class C, class W> struct Frag { bf16x8 x[W::MT]; s16x4 wl[W::NT], wh[W::NT]; };
template <class C, class W> struct RdAddr { unsigned x[(C::XI == X_RAW) ? 4 : 1]; unsigned w[(C::WI == W_P256) ? W::NT : 1]; };
template <class C, class W> __device__ __forceinline__ void rd_init(RdAddr<C, W>& a, int m0, int n0  , int lane) {
    const int r = lane & 31, h = lane >> 5, g = lane >> 4, q = (lane & 15) >> 2, p = lane & 3;
    if (C::XI == X_RAW) { for (int ks = 0; ks < 4; ++ks) a.x[ks] = (m0 + r) * 128 + (((2 * ks + h) ^ ((r >> 1) & 7)) * 16); }
    else a.x[0] = h * (C::BM * 16) + (m0 + r) * 16;
    const int rowp = 8 * (g >> 1) + q, colp = (g & 1) * 32 + p * 8;
    if (C::WI == W_P448) a.w[0] = C::WOFF + rowp * 448 + colp + n0 * 64;
    else if (C::WI == W_P256) { for (int t = 0; t < W::NT; ++t) a.w[t] = C::WOFF + rowp * 256 + (((n0 + t) ^ q) * 64) + colp; }
    else a.w[0] = C::WOFF + h * (C::BN * 16) + (n0 * 32 + r) * 16;
}
template <class C, class W> constexpr int n_reads() { return W::MT + (C::WI == W_OWNT ? W::NT : 2 * W::NT); }
template <class C, class W> __device__ __forceinline__ void rd_one(Frag<C, W>& f, ldsp bx, ldsp bw, const RdAddr<C, W>& a, int ks, int r) {
    constexpr int PER = (C::WI == W_OWNT) ? 1 : 2;
    if (r >= PER && r < PER + W::MT) { const int m = r - PER;
        if (C::XI == X_RAW) f.x[m] = *(const LAS bf16x8*)(bx + a.x[ks] + m * 4096);
        else f.x[m] = *(const LAS bf16x8*)(bx + a.x[0] + ks * (2 * C::BM * 16) + m * 512);
        return; }
    const int u = r < PER ? r : r - W::MT, t = u / PER, hh = u % PER;
    if (C::WI == W_OWNT) { const bf16x8 v = *(const LAS bf16x8*)(bw + a.w[0] + ks * (2 * C::BN * 16) + t * 512); f.wl[t] = __builtin_shufflevector(v, v, 0, 1, 2, 3); f.wh[t] = __builtin_shufflevector(v, v, 4, 5, 6, 7); }
    else { s16x4 v;
        if (C::WI == W_P448) v = tr_read(bw + a.w[0] + t * 64 + (ks * 16 + hh * 4) * 448);
        else v = tr_read(bw + a.w[t] + (ks * 16 + hh * 4) * 256);
        if (hh) f.wh[t] = v; else f.wl[t] = v; }
}
template <class C, class W> __device__ __forceinline__ void rd_frags(Frag<C, W>& f, ldsp bx, ldsp bw, const RdAddr<C, W>& a, int ks) {
#pragma unroll
    for (int r = 0; r < n_reads<C, W>(); ++r) rd_one<C, W>(f, bx, bw, a, ks, r);
}
template <class C, class W, bool SWAP  > __device__ __forceinline__ void mma_one(f32x16 (&acc)[W::NT][W::MT], const Frag<C, W>& f, int i) {
    const int t = i / W::MT, m = i % W::MT;
    const bf16x8 w = __builtin_shufflevector(f.wl[t], f.wh[t], 0, 1, 2, 3, 4, 5, 6, 7);
    acc[t][m] = SWAP ? __builtin_amdgcn_mfma_f32_32x32x16_bf16(w, f.x[m], acc[t][m], 0, 0, 0) : __builtin_amdgcn_mfma_f32_32x32x16_bf16(f.x[m], w, acc[t][m], 0, 0, 0);
}
struct NoFill { template <int S> __device__ __forceinline__ void chunk() {} };
template <class C, class W, bool SWAP, int V0, int V1, int BASE, int I, class F>
__device__ __forceinline__ void step_slot(f32x16 (&acc)[W::NT][W::MT], const Frag<C, W>& fc, Frag<C, W>& fn, ldsp bx, ldsp bw, const RdAddr<C, W>& a, int ksn,
                                          const Src<C>& s, const char* xb, const char* wb, unsigned dx, unsigned dw, F& fill) {
    constexpr int NM = W::NT * W::MT, NR = n_reads<C, W>(), NV = V1 - V0;
    if constexpr (I < NM) {
        mma_one<C, W, SWAP>(acc, fc, I);
#pragma unroll
        for (int r = I * NR / NM; r < (I + 1) * NR / NM; ++r) rd_one<C, W>(fn, bx, bw, a, ksn, r);
        if constexpr (W::LD) { constexpr int v0 = V0 + I * NV / NM, v1 = V0 + (I + 1) * NV / NM; stage_rng<C, v0, v1, v0 == V0>(dx, dw, s, xb, wb); }
        fill.template chunk<BASE + I>();
        GM_SB();
        step_slot<C, W, SWAP, V0, V1, BASE, I + 1, F>(acc, fc, fn, bx, bw, a, ksn, s, xb, wb, dx, dw, fill);
    }
}
template <class C, class WF  , int D> struct Pipe {
    static constexpr int PX = C::PX, PW = C::PW, NS = C::NS, NV = PX + PW;
    static constexpr int Q1 = (NV + 3) / 4, Q2 = (NV + 1) / 2, Q3 = (3 * NV + 3) / 4;
    static constexpr int PRE = (D == 1) ? Q1 : Q2;
    ldsp lds; int nt, xmod;
    RdAddr<C, WF> a; unsigned lx0, lw0;
    Frag<C, WF> f0, f1, f2, f3;
    __device__ __forceinline__ Pipe(ldsp lds_, const Src<C>& s, int nt_, int xmod_, int lid, int m0, int n0, int lane) : lds(lds_), nt(nt_), xmod(xmod_) {
        rd_init<C, WF>(a, m0, n0, lane);
        const unsigned ldb = (unsigned)(uintptr_t)lds;
        lx0 = ldb + lid * PX * 1024; lw0 = ldb + C::WOFF + lid * PW * 1024;
    }
    __device__ __forceinline__ void issue_x(const Src<C>& s) {
        if constexpr (WF::LD) {
#pragma unroll
            for (int i = 0; i < NS - 1; ++i) stage_rng<C, PW, NV, true>(lx0 + i * C::XB, 0, s, s.x + (size_t)i * s.xstep, nullptr);
        }
    }
    __device__ __forceinline__ void issue_w(const Src<C>& s) {
        static_assert(PRE <= PW, "prologue split");
        if constexpr (WF::LD) {
#pragma unroll
            for (int i = 0; i < NS - 1; ++i) stage_rng<C, 0, PW, true>(0, lw0 + i * C::WB, s, nullptr, w_tile<C>(s, i));
            stage_rng<C, 0, PRE, true>(0, lw0 + (NS - 1) * C::WB, s, nullptr, w_tile<C>(s, NS - 1));
        }
    }
    __device__ __forceinline__ void landed() {
        if constexpr (WF::LD) asm volatile("s_waitcnt vmcnt(0)" ::: "memory");
        GM_BAR();
        rd_frags<C, WF>(f0, lds, lds, a, 0);
        if (D == 2) rd_frags<C, WF>(f1, lds, lds, a, 1);
    }
    __device__ __forceinline__ void begin(const Src<C>& s) {
        if constexpr (WF::LD) {
#pragma unroll
            for (int i = 0; i < NS - 1; ++i) stage_rng<C, 0, NV, true>(lx0 + i * C::XB, lw0 + i * C::WB, s, s.x + (size_t)i * s.xstep, w_tile<C>(s, i));
            stage_rng<C, 0, PRE, true>(0, lw0 + (NS - 1) * C::WB, s, nullptr, w_tile<C>(s, NS - 1));
            asm volatile("s_waitcnt vmcnt(%0)" ::"n"((NS - 2) * NV + PRE) : "memory");
        }
        GM_BAR();
        rd_frags<C, WF>(f0, lds, lds, a, 0);
        if (D == 2) rd_frags<C, WF>(f1, lds, lds, a, 1);
    }
    template <bool SWAP, class H> __device__ __forceinline__ void run(const Src<C>& s, int t0, int t1, f32x16 (&acc)[WF::NT][WF::MT], H& hook DBG_PARAMS) {
        NoFill nf; run_fill<SWAP>(s, t0, t1, t0, acc, hook, nf DBG_FWD);
    }
    template <bool SWAP, class H, class F> __device__ __forceinline__ void run_fill(const Src<C>& s, int t0, int t1, int tb, f32x16 (&acc)[WF::NT][WF::MT], H& hook, F& fill DBG_PARAMS) {
        using W = WF; constexpr int NM = WF::MT * WF::NT;
        int sl = t0 % NS, kn = t0 - 1 + NS; kn = kn < nt ? kn : nt - 1;
        const char* xb = s.x + (unsigned)(kn & (xmod - 1)) * s.xstep; const char* wb = w_tile<C>(s, kn);
        unsigned dx = lx0 + ((sl == 0) ? NS - 1 : sl - 1) * C::XB, dw = lw0 + ((sl == 0) ? NS - 1 : sl - 1) * C::WB;
        __builtin_amdgcn_s_waitcnt(0xC07F);
        for (int t = t0; t < t1; ++t) {
            const int ns = (sl + 1 == NS) ? 0 : sl + 1;
            ldsp cx = lds + sl * C::XB, cw = lds + sl * C::WB, nx = lds + ns * C::XB, nw = lds + ns * C::WB;
            TRACE(t - tb);
            hook.top(t - tb);
            GM_SB();
#define G32_STEP(FC, FN, BX, BW, KSN, V0, V1, ST) step_slot<C, W, SWAP, V0, V1, ST * NM, 0, F>(acc, FC, FN, BX, BW, a, KSN, s, xb, wb, dx, dw, fill)
            if constexpr (D == 1) { G32_STEP(f0, f1, cx, cw, 1, Q1, Q2, 0); G32_STEP(f1, f2, cx, cw, 2, Q2, Q3, 1); G32_STEP(f2, f3, cx, cw, 3, Q3, NV, 2); }
            else { G32_STEP(f0, f2, cx, cw, 2, Q2, Q3, 0); G32_STEP(f1, f3, cx, cw, 3, Q3, NV, 1); }
            hook.at(t - tb);
            asm volatile("s_waitcnt lgkmcnt(0)" ::: "memory");
            if constexpr (W::LD) asm volatile("s_waitcnt vmcnt(%0)" ::"n"((NS - 2) * NV) : "memory");
            GM_BAR();
            GM_SB();
            if constexpr (W::LD) { kn = (kn + 1 < nt) ? kn + 1 : kn; xb = s.x + (unsigned)(kn & (xmod - 1)) * s.xstep; wb = w_tile<C>(s, kn); dx = lx0 + sl * C::XB; dw = lw0 + sl * C::WB; }
            if constexpr (D == 1) { G32_STEP(f3, f0, nx, nw, 0, 0, Q1, 3); }
            else { G32_STEP(f2, f0, nx, nw, 0, 0, Q1, 2); G32_STEP(f3, f1, nx, nw, 1, Q1, Q2, 3); }
#undef G32_STEP
            sl = ns;
        }
        GM_SB();
    }
    __device__ __forceinline__ void end() {
        asm volatile("s_waitcnt vmcnt(0) lgkmcnt(0)" ::: "memory");
        GM_BAR();
    }
};
template <class W> __device__ __forceinline__ void acc_zero(f32x16 (&acc)[W::NT][W::MT]) {
#pragma unroll
    for (int t = 0; t < W::NT; ++t)
#pragma unroll
        for (int m = 0; m < W::MT; ++m) acc[t][m] = (f32x16){};
}
__device__ __forceinline__ u32x4 pair32(unsigned a0, unsigned a1, unsigned b0, unsigned b1) {
    const auto r0 = __builtin_amdgcn_permlane32_swap(a0, b0, false, false); const auto r1 = __builtin_amdgcn_permlane32_swap(a1, b1, false, false);
    return (u32x4){r0[0], r1[0], r0[1], r1[1]};
}
}

struct Params {
    const u16* ax; const u16* wqkv; const u16* wo; const u16* bx; const u16* win; const u16* wsp; const u16* bsp; const u16* wout; const u16* gw;
    u16* out; unsigned char* ws;
};
struct Ctx : Params { unsigned tag; };

using CfgQ = Cfg<256, 192, X_RAW, W_P384, 3, 2>;
struct Warm {
    const char* base; unsigned pitch; int lpr_log2; int cnt; unsigned scratch;
    __device__ __forceinline__ unsigned mask(int t0, int wid) const {
        if (wid < 4 || cnt <= 0) return 0u;
        return ((0x11111111u << (wid - 4)) & (cnt >= 32 ? ~0u : ((1u << cnt) - 1u))) << t0;
    }
    __device__ __forceinline__ void issue(int n, int wid, int lane) const {
        if (n >= 0 && n < cnt && wid == 4 + (n & 3)) { const int idx = n * 64 + lane; pf_lines(base, (unsigned)(idx >> lpr_log2) * pitch + (unsigned)(idx & ((1 << lpr_log2) - 1)) * 128u, scratch); }
    }
};

struct AheadHook {
    static constexpr bool PRE = false;
    int wid, lane, nt; unsigned scratch;
    const char* b0; unsigned v0, step0;
    const char* b1; unsigned v1, step1;
    __device__ __forceinline__ void pre_x() const {}
    __device__ __forceinline__ void top(int) const {}
    __device__ __forceinline__ void first() const {
        if (wid >= 4) for (int k = 3 + ((wid - 3) & 3); k < PFD; k += 4) { pf_lines(b0, v0 + k * step0, scratch); if (b1) pf_lines(b1, v1 + k * step1, scratch); }
    }
    __device__ __forceinline__ void at(int t) const {
        if (wid == 4 + (t & 3) && t + PFD < nt) { pf_lines(b0, v0 + (t + PFD) * step0, scratch); if (b1) pf_lines(b1, v1 + (t + PFD) * step1, scratch); }
    }
};
template <int WN> __device__ __forceinline__ void qkv_wave(const Ctx& p, ldsp lds, int pair, int panel, int wid, int lane) {
    using C = CfgQ; using W = Wv<4, 6, WN * 6, WN ? 0x3Cu : 0u, WN == 0>;
    const int lid = wid & 3, m0 = (wid & 3) * 64;
    Src<C> s;
    src_x_raw<C>(s, p.ax + (size_t)panel * 256 * DM, DM * 2, lid, lane);
    src_w_raw<C>(s, p.wqkv + pair * 64, 3 * DM * 2, DM * 2, lid, lane);
    f32x4 acc[6][4]; acc_zero<W>(acc);
    STAMP(0, 1);
    const int wl = lane < 48 ? lane : 47;
    AheadHook hk{wid, lane, DM / 64, (unsigned)(uintptr_t)lds + LDS_PFS,
                 (const char*)(p.wqkv + pair * 64), (unsigned)(((panel & 3) * 16 + wl / 3) * (3 * DM * 2) + (wl % 3) * (DM * 2)), 64u * 3 * DM * 2,
                 (const char*)(p.ax + (size_t)(panel * 256 + (pair & 3) * 64) * DM), (unsigned)(lane * DM * 2), 128u};
    hk.first();
    gemm_loop<C, W, Sch<12, 7>>(lds, s, DM / 64, acc, lid, m0, 0, lane, hk DBG_ARGS(0));
    STAMP(0, 2);
    const int fr = lane & 15, fq = lane >> 4;
    const int row0 = panel * 256 + m0;
    const rsrc_t rs = ws_rsrc(p.ws);
#pragma unroll
    for (int t = 0; t < 6; t += 2) {
        const int T = WN * 6 + t, kind = T >> 2, head = pair * 2 + ((T >> 1) & 1);
        if (kind < 2) {
            const unsigned base = (unsigned)(kind == 0 ? WS_Q : WS_K);
            const int c = 2 * (fq & 1) + (fq >> 1);
            const float sc = kind == 0 ? QSCALE : 1.0f;
#pragma unroll
            for (int m = 0; m < 4; ++m) { const int row = row0 + m * 16 + fr;
                st16_wt(rs, base + ((head * 64 + (row >> 5)) * 4 + c) * 512 + (row & 31) * 16, pair16(acc[t][m] * sc, acc[t + 1][m] * sc)); }
        } else {
#pragma unroll
            for (int tt = 0; tt < 2; ++tt)
#pragma unroll
                for (int m = 0; m < 4; m += 2) {
                    const f32x4 A = acc[t + tt][m], B = acc[t + tt][m + 1];
                    const unsigned a0 = pk_bf16(A[0], A[1]), a1 = pk_bf16(A[2], A[3]), b0 = pk_bf16(B[0], B[1]), b1 = pk_bf16(B[2], B[3]);
                    const auto r0 = __builtin_amdgcn_permlane32_swap(a0, b0, false, false); const auto r1 = __builtin_amdgcn_permlane32_swap(a1, b1, false, false);
                    const int kb = (row0 >> 4) + m + (fq >> 1), h = fq & 1, d = tt * 16 + fr;
                    st16_wt(rs, (unsigned)WS_V + ((head * 128 + kb) * 2 + h) * 512 + d * 16, (u32x4){r0[0], r1[0], r0[1], r1[1]});
                }
        }
    }
    asm volatile("s_waitcnt vmcnt(0)" ::: "memory");
    GM_BAR();
    if (threadIdx.x == 0) signal(p.ws, F_Q + pair * 32 + panel, p.tag);
}

using CfgZ = g32::Cfg<128, 224, X_RAW, g32::W_P448, 3>;
using CfgS = g32::Cfg<128, 224, X_RAW, g32::W_OWNT, 3>;
using CfgO = g32::Cfg<128, 128, X_OWN, g32::W_P256, 4>;
__device__ __forceinline__ float gelu_tanh(float x) {
    const float t = x * x * (-0.044715f * 2.0f * 0.7978845608028654f * 1.4426950408889634f) + (-2.0f * 0.7978845608028654f * 1.4426950408889634f);
    return x * __builtin_amdgcn_rcpf(1.0f + __builtin_amdgcn_exp2f(x * t));
}
template <int N> __device__ __forceinline__ void gelu_pairs(unsigned* z) {
    constexpr float C1 = -0.044715f * 2.0f * 0.7978845608028654f * 1.4426950408889634f, C2 = -2.0f * 0.7978845608028654f * 1.4426950408889634f;
    f32x2 x[N], e[N];
#pragma unroll
    for (int i = 0; i < N; ++i) x[i] = (f32x2){bf_lo(z[i]), bf_hi(z[i])};
#pragma unroll
    for (int i = 0; i < N; ++i) e[i] = x[i] * (x[i] * x[i] * C1 + C2);
    GM_SB();
#pragma unroll
    for (int i = 0; i < N; ++i) { e[i].x = __builtin_amdgcn_exp2f(e[i].x); e[i].y = __builtin_amdgcn_exp2f(e[i].y); }
    GM_SB();
#pragma unroll
    for (int i = 0; i < N; ++i) e[i] = e[i] + 1.0f;
#pragma unroll
    for (int i = 0; i < N; ++i) { e[i].x = __builtin_amdgcn_rcpf(e[i].x); e[i].y = __builtin_amdgcn_rcpf(e[i].y); }
    GM_SB();
#pragma unroll
    for (int i = 0; i < N; ++i) { const f32x2 y = x[i] * e[i]; z[i] = pk_bf16(y.x, y.y); }
    GM_SB();
}
__device__ __forceinline__ void gelu16(f32x16& a) {
    constexpr float C1 = -0.044715f * 2.0f * 0.7978845608028654f * 1.4426950408889634f, C2 = -2.0f * 0.7978845608028654f * 1.4426950408889634f;
    f32x16 e = a * (a * a * C1 + C2);
    GM_SB();
#pragma unroll
    for (int i = 0; i < 16; ++i) e[i] = __builtin_amdgcn_exp2f(e[i]);
    GM_SB();
    e = e + 1.0f;
#pragma unroll
    for (int i = 0; i < 16; ++i) e[i] = __builtin_amdgcn_rcpf(e[i]);
    GM_SB();
    a = a * e;
}
template <int CTRL> __device__ __forceinline__ float dpp_add(float x) { return x + __int_as_float(__builtin_amdgcn_update_dpp(0, __float_as_int(x), CTRL, 0xf, 0xf, false)); }
__device__ __forceinline__ float red32_pair(float a, float b) {
    const auto r = __builtin_amdgcn_permlane16_swap(__float_as_uint(a), __float_as_uint(b), false, false);
    float x = __uint_as_float(r[0]) + __uint_as_float(r[1]);
    x = dpp_add<0x128>(x); x = dpp_add<0x124>(x); x = dpp_add<0x122>(x); x = dpp_add<0x121>(x);
    return x;
}
constexpr int LDS_TAB = CfgZ::LDS;
template <int NT> struct GeluFill {
    static constexpr int NSLOT = 4 * NT, STRIDE = NSLOT / 4;
    unsigned (&z)[NT][8];
    f32x2 x, e;
    template <int S> __device__ __forceinline__ void chunk() {
        constexpr float C1 = -0.044715f * 2.0f * 0.7978845608028654f * 1.4426950408889634f, C2 = -2.0f * 0.7978845608028654f * 1.4426950408889634f;
        if constexpr (S % STRIDE == 0) { constexpr int pa = S / STRIDE;
            x = (f32x2){bf_lo(z[0][pa]), bf_hi(z[0][pa])};
            const f32x2 w = x * (x * x * C1 + C2);
            e.x = __builtin_amdgcn_exp2f(w.x); e.y = __builtin_amdgcn_exp2f(w.y);
#pragma unroll
            for (int g = 0; g + 1 < 2 * NT; ++g) z[g >> 1][(g & 1) * 4 + pa] = z[(g + 1) >> 1][((g + 1) & 1) * 4 + pa];
            asm volatile("" : "+v"(x), "+v"(e));
        }
        if constexpr (S % STRIDE == 2) { constexpr int pb = S / STRIDE;
            const f32x2 d = e + 1.0f;
            const f32x2 y = x * (f32x2){__builtin_amdgcn_rcpf(d.x), __builtin_amdgcn_rcpf(d.y)};
            z[NT - 1][4 + pb] = pk_bf16(y.x, y.y);
            asm volatile("" : "+v"(z[NT - 1][4 + pb]));
        }
    }
};
struct ZHook {
    int wid, lane; unsigned scratch; const char* b2; const char* b1; unsigned v;
    __device__ __forceinline__ void touch(int k) const { pf_lines(k < 16 ? b2 : b1, v + (unsigned)(k & 15) * (64u * 2 * FH * 2), scratch); }
    __device__ __forceinline__ void top(int) const {}
    __device__ __forceinline__ void first() const { if (wid >= 4) for (int k = 3 + ((wid - 3) & 3); k < PFD; k += 4) touch(k); }
    __device__ __forceinline__ void at(int t) const { if (wid == 4 + (t & 3)) { touch(t + PFD); if (t >= PFD) touch(t + 16); } }
};
template <int NT, bool LD> struct LnHook {
    const Ctx& p; ldsp lds; int panel, j, wid, lane, n0; unsigned (&z2)[NT][8];
    u32x4 w[4];
    __device__ __forceinline__ void normalise(int tt) {
        LAS f32x2* R = (LAS f32x2*)(lds + LDS_TAB + 2048);
        const int r = lane & 31, h = lane >> 5, m0 = (wid & 3) * 32;
        const rsrc_t rs = ws_rsrc(p.ws);
        const int srow = panel * 128 + m0;
#pragma unroll
        for (int i = 0; i < 4; i += 2) {
            const f32x4 ra = *(const LAS f32x4*)(R + m0 + 8 * i + 4 * h), rb = *(const LAS f32x4*)(R + m0 + 8 * i + 4 * h + 2);
            const f32x4 rc = *(const LAS f32x4*)(R + m0 + 8 * i + 8 + 4 * h), rd = *(const LAS f32x4*)(R + m0 + 8 * i + 8 + 4 * h + 2);
            const int k = srow + 8 * (i + h);
            const unsigned base = (unsigned)WS_Z + ((j * 32 + (k >> 6)) * 8 + ((k & 63) >> 3)) * 3584 + (n0 * 32 + r) * 16;
            const unsigned a0 = z2[tt][2 * i], a1 = z2[tt][2 * i + 1], b0 = z2[tt][2 * i + 2], b1 = z2[tt][2 * i + 3];
            st16_wt(rs, base + tt * 512, g32::pair32(pk_bf16((bf_lo(a0) - ra[0]) * ra[1], (bf_hi(a0) - ra[2]) * ra[3]), pk_bf16((bf_lo(a1) - rb[0]) * rb[1], (bf_hi(a1) - rb[2]) * rb[3]),
                                                      pk_bf16((bf_lo(b0) - rc[0]) * rc[1], (bf_hi(b0) - rc[2]) * rc[3]), pk_bf16((bf_lo(b1) - rd[0]) * rd[1], (bf_hi(b1) - rd[2]) * rd[3])));
        }
    }
    __device__ __forceinline__ void normalise_at(int t) {
        if (t >= 5 && t < 9) { if (at_tile(t, 5)) normalise(0); if (at_tile(t, 6)) normalise(1); if (at_tile(t, 7)) normalise(2); if constexpr (NT == 4) { if (at_tile(t, 8)) normalise(3); } }
    }
    __device__ __forceinline__ void fetch() {
        const unsigned off = (unsigned)WS_STAT + ((panel * 8 + (lane >> 5) * 4) * 128 + (wid - 4) * 32 + (lane & 31)) * 16;
#pragma unroll
        for (int v = 0; v < 4; ++v) w[v] = ld16_dev(ws_rsrc(p.ws), off + v * 2048);
    }
    __device__ __forceinline__ bool fetched() const { bool ok = true;
#pragma unroll
        for (int v = 0; v < 4; ++v) ok = ok && w[v][1] == p.tag && w[v][3] == p.tag;
        return __all(ok); }
    __device__ __forceinline__ void top(int t) {
        if constexpr (LD) normalise_at(t);
        else { if (t == 4) fetch(); }
    }
    __device__ __forceinline__ void at(int t) {
        if (t < 4 || t > 11) return;
        if constexpr (!LD) {
            if (at_tile(t, 4)) {
                for (unsigned spins = 0; !fetched();) {
                    __builtin_amdgcn_s_sleep(2); fetch();
                    if (++spins > (1u << 19)) { __hip_atomic_store((gu32*)(p.ws + WS_CTL) + C_TMO, p.tag, RLX_AGENT); break; }
                }
                LAS f32x2* R = (LAS f32x2*)(lds + LDS_TAB + 2048);
                float ms = 0.f;
#pragma unroll
                for (int v = 0; v < 4; ++v) ms += __uint_as_float(w[v][0]);
                { const auto r = __builtin_amdgcn_permlane32_swap(__float_as_uint(ms), __float_as_uint(ms), false, false); ms = __uint_as_float(r[0]) + __uint_as_float(r[1]); }
                const float mean = ms * 0.125f; float q = 0.f;
#pragma unroll
                for (int v = 0; v < 4; ++v) { const float dm = __uint_as_float(w[v][0]) - mean; q += __uint_as_float(w[v][2]) + 224.0f * dm * dm; }
                { const auto r = __builtin_amdgcn_permlane32_swap(__float_as_uint(q), __float_as_uint(q), false, false); q = __uint_as_float(r[0]) + __uint_as_float(r[1]); }
                if (lane < 32) R[(wid - 4) * 32 + lane] = (f32x2){mean, __builtin_amdgcn_rsqf(q * (1.0f / 1792.0f) + 1e-5f)};
                GM_VM0();
            }
            normalise_at(t);
        }
        if (at_tile(t, 10)) asm volatile("s_waitcnt vmcnt(0)" ::: "memory");
        if constexpr (!LD) { if (at_tile(t, 11)) { if (threadIdx.x == 256) signal(p.ws, F_Z + j * 32 + panel, p.tag); } }
    }
};
struct SpHook {
    static constexpr bool PRE = false; const Ctx& p; ldsp lds; int panel, wid, lane;
    __device__ __forceinline__ void pre_x() const {}
    __device__ __forceinline__ void top(int) const {}
    __device__ __forceinline__ void at(int t) {
        if (t == 10 && (wid == 4 || wid == 5)) {
            poll_flags(p.ws, p.tag, F_Q, 1, true, F_Q, lane);
            const int row = (wid - 4) * 64 + lane, trow = panel * 128 + row;
            ((LAS f32x2*)(lds + LDS_TAB + 2048))[row] = (f32x2){__uint_as_float((unsigned)p.bsp[trow] << 16), 1.0f - __uint_as_float(__hip_atomic_load((gu32*)(p.ws + WS_GATE) + trow, RLX_AGENT))};
            GM_VM0();
        }
    }
};
struct WaitHook {
    unsigned char* ws; unsigned tag; int wid, lane, tlate, wl, nl;
    __device__ __forceinline__ void top(int) const {}
    __device__ __forceinline__ void at(int t) const { if (t == tlate && wid == 4) wait_flags(ws, tag, wl, nl, false, 0, lane); }
};
template <bool LD> __device__ __forceinline__ void b_wave(const Ctx& p, ldsp lds, int panel, int j, int wid, int lane) {
    constexpr int NT = LD ? 3 : 4;
    const int lid = wid & 3, m0 = lid * 32, n0 = LD ? 0 : 3;
    const int r = lane & 31, h = lane >> 5;
    unsigned z2[NT][8], z1[NT][8];
    const unsigned pfs = (unsigned)(uintptr_t)lds + LDS_PFS;
    {
        using C = CfgZ; using W = g32::Wv<1, NT, LD>; Src<C> s;
        src_x_raw<C>(s, p.ax + (size_t)panel * 128 * DM, DM * 2, lid, lane);
        g32::src_w_raw<C>(s, p.win + FH + j * 224, 2 * FH * 2, lid, lane);
        set_w2<C>(s, p.win + j * 224, DM / 64);
        g32::Pipe<C, W, 1> pipe(lds, s, 2 * DM / 64, DM / 64, lid, m0, n0, lane);
        f32x16 acc[NT][1]; g32::acc_zero<W>(acc);
        STAMP(0, 1);
        const unsigned wv = (unsigned)(((panel & 3) * 16 + (lane >> 2)) * (2 * FH * 2) + (lane & 3) * 128);
        ZHook hk{wid, lane, pfs, (const char*)p.win + (((FH + j * 224) * 2) & ~127), (const char*)p.win + ((j * 224 * 2) & ~127), wv};
        hk.first();
        pipe.begin(s);
        pipe.template run<false>(s, 0, DM / 64, acc, hk DBG_ARGS(1));
        STAMP(0, 2);
        {
            float sm[16], sq[16];
#pragma unroll
            for (int t = 0; t < NT; ++t) {
                gelu16(acc[t][0]);
#pragma unroll
                for (int q = 0; q < 16; ++q) { const float v = acc[t][0][q]; sm[q] = t ? sm[q] + v : v; sq[q] = t ? sq[q] + v * v : v * v; }
#pragma unroll
                for (int q = 0; q < 16; q += 2) z2[t][q >> 1] = pk_bf16(acc[t][0][q], acc[t][0][q + 1]);
                acc[t][0] = (f32x16){};
            }
            float ra[16];
#pragma unroll
            for (int k = 0; k < 8; ++k) {
                const auto a = __builtin_amdgcn_permlane16_swap(__float_as_uint(sm[2 * k]), __float_as_uint(sm[2 * k + 1]), false, false); ra[k] = __uint_as_float(a[0]) + __uint_as_float(a[1]);
                const auto b2 = __builtin_amdgcn_permlane16_swap(__float_as_uint(sq[2 * k]), __float_as_uint(sq[2 * k + 1]), false, false); ra[8 + k] = __uint_as_float(b2[0]) + __uint_as_float(b2[1]); }
#pragma unroll
            for (int k = 0; k < 16; ++k) ra[k] = dpp_add<0x128>(ra[k]);
#pragma unroll
            for (int k = 0; k < 16; ++k) ra[k] = dpp_add<0x124>(ra[k]);
#pragma unroll
            for (int k = 0; k < 16; ++k) ra[k] = dpp_add<0x122>(ra[k]);
#pragma unroll
            for (int k = 0; k < 16; ++k) ra[k] = dpp_add<0x121>(ra[k]);
            float va = ra[0], vb = ra[8];
#pragma unroll
            for (int k = 1; k < 8; ++k) { const bool sel = (lane & 7) == k; va = sel ? ra[k] : va; vb = sel ? ra[8 + k] : vb; }
            LAS f32x2* P = (LAS f32x2*)(lds + LDS_TAB);
            { const int q = 2 * (lane & 7) + ((lane >> 4) & 1), row = m0 + 8 * (q >> 2) + 4 * h + (q & 3);
              if ((lane & 8) == 0) P[row * 2 + (LD ? 0 : 1)] = (f32x2){va, vb}; }
            asm volatile("s_waitcnt lgkmcnt(0)" ::: "memory"); GM_BAR();
            if constexpr (!LD) {
                if (lane < 32) {
                    const int row = lid * 32 + lane;
                    const f32x2 a = P[row * 2], b = P[row * 2 + 1];
                    const float su = a.x + b.x, mt = su * (1.0f / 224.0f), m2 = fmaxf(a.y + b.y - su * mt, 0.f);
                    st16_wt(ws_rsrc(p.ws), (unsigned)WS_STAT + ((panel * 8 + j) * 128 + row) * 16, (u32x4){__float_as_uint(mt), p.tag, __float_as_uint(m2), p.tag});
                }
            }
        }
        STAMP(0, 3);
        LnHook<NT, LD> hl{p, lds, panel, j, wid, lane, n0, z2};
        pipe.template run<true>(s, DM / 64, 2 * DM / 64, acc, hl DBG_ARGS(2));
        pipe.end();
        STAMP(0, 4);
#pragma unroll
        for (int t = 0; t < NT; ++t)
#pragma unroll
            for (int q = 0; q < 16; q += 2) z1[t][q >> 1] = pk_bf16(acc[t][0][q], acc[t][0][q + 1]);
    }
    {
        using C = CfgS; using W = g32::Wv<1, NT, LD>; Src<C> s;
        src_x_raw<C>(s, p.wsp + (size_t)panel * 128 * SEQ, SEQ * 2, lid, lane);
        src_w_own<C>(s, p.ws + WS_Z + (size_t)j * 32 * 28672, lid, lane);
        g32::Pipe<C, W, SPD> pipe(lds, s, SEQ / 64, SEQ / 64, lid, m0, n0, lane);
        pipe.issue_x(s);
        if (wid == 4) wait_flags(p.ws, p.tag, F_Z + j * 32, 16, false, 0, lane);
        GM_BAR();
        pipe.issue_w(s);
        STAMP(0, 5);
        STAMP(0, 6);
        pipe.landed();
        f32x16 acc[NT][1]; g32::acc_zero<W>(acc);
        SpHook hk{p, lds, panel, wid, lane};
        { GeluFill<NT> gf{z1};
          pipe.template run_fill<true>(s, 0, 2 * NT, 0, acc, hk, gf DBG_ARGS(3)); }
        g32::NoFill nf;
        pipe.template run_fill<true>(s, 2 * NT, SEQ / 64, 0, acc, hk, nf DBG_ARGS(3));
        pipe.end();
        STAMP(0, 7);
        const rsrc_t rs = ws_rsrc(p.ws);
        const f32x2 bg = ((const LAS f32x2*)(lds + LDS_TAB + 2048))[m0 + r]; const float b = bg.x, g1 = bg.y;
        const unsigned ub = (unsigned)WS_ACT + (panel * 352 + j * 28 + n0 * 4 + h) * 2048 + (m0 + r) * 16;
#pragma unroll
        for (int t = 0; t < NT; ++t)
#pragma unroll
            for (int i = 0; i < 4; i += 2) {
                unsigned w4[4];
#pragma unroll
                for (int e = 0; e < 4; ++e) { const int q = 4 * i + 2 * e; const unsigned z = z1[t][q >> 1];
                    w4[e] = pk_bf16((acc[t][0][q] + b) * g1 * bf_lo(z), (acc[t][0][q + 1] + b) * g1 * bf_hi(z)); }
                st16_wt(rs, ub + (t * 4 + i) * 2048, g32::pair32(w4[0], w4[1], w4[2], w4[3]));
            }
        asm volatile("s_waitcnt vmcnt(0)" ::: "memory");
        GM_BAR();
        if (threadIdx.x == 256) signal(p.ws, F_U + panel * 32 + j, p.tag);
    }
    STAMP(0, 8);
    {
        using C = CfgO; using W = g32::Wv<2, 1, LD>;
        const int mo = (wid >> 2) * 64;
        Src<C> s;
        src_x_own<C>(s, p.ws + WS_ACT + (size_t)panel * 352 * 2048, lid, lane);
        g32::src_w_raw<C>(s, p.wout + j * 128, DM * 2, lid, lane);
        set_w2<C>(s, p.wo + j * 128, FH / 64);
        f32x16 acc[1][2]; g32::acc_zero<W>(acc);
        WaitHook hk{p.ws, p.tag, wid, lane, FH / 64 - C::NS, F_O + (panel >> 2) * 32, 32};
        g32::Pipe<C, W, 2> pipe(lds, s, (FH + DM) / 64, 64, lid, mo, lid, lane);
        pipe.issue_w(s);
        if (wid == 4) wait_flags(p.ws, p.tag, F_U + panel * 32, 8, false, 0, lane);
        GM_BAR();
        pipe.issue_x(s);
        pipe.landed();
        pipe.template run<true>(s, 0, (FH + DM) / 64, acc, hk DBG_ARGS(4));
        pipe.end();
        STAMP(0, 9);
#pragma unroll
        for (int m = 0; m < 2; ++m)
#pragma unroll
            for (int i = 0; i < 4; i += 2) { const f32x16& A = acc[0][m];
                *(LAS u32x4*)(lds + (mo + m * 32 + r) * 272 + lid * 64 + (i + h) * 16) =
                    g32::pair32(pk_bf16(A[4 * i], A[4 * i + 1]), pk_bf16(A[4 * i + 2], A[4 * i + 3]), pk_bf16(A[4 * i + 4], A[4 * i + 5]), pk_bf16(A[4 * i + 6], A[4 * i + 7])); }
        asm volatile("s_waitcnt lgkmcnt(0)" ::: "memory"); GM_BAR();
#pragma unroll
        for (int i = 0; i < 4; ++i) { const int row = wid * 16 + i * 4 + (lane >> 4);
            *(u32x4*)(p.out + (size_t)(panel * 128 + row) * DM + j * 128 + (lane & 15) * 8) = *(const LAS u32x4*)(lds + row * 272 + (lane & 15) * 16); }
    }
}

constexpr int AT_STAGE = 16384, AT_NS = 4;
#define MFMA32(a, b, c) __builtin_amdgcn_mfma_f32_32x32x16_bf16(a, b, c, 0, 0, 0)
#define MFMA16(a, b, c) __builtin_amdgcn_mfma_f32_16x16x32_bf16(a, b, c, 0, 0, 0)
#define PIN(x) asm volatile("" : "+v"(x))
__device__ __forceinline__ void attn_wg(const Ctx& p, ldsp lds, int head, int qb, int x, int a, int wid, int lane) {
    if (wid == 4) wait_flags(p.ws, p.tag, F_Q + (head >> 1) * 32, 8, false, 0, lane);
    GM_BAR();
    STAMP(0, 3);
    const unsigned char* Kg = p.ws + WS_K + (size_t)head * 131072; const unsigned char* Vg = p.ws + WS_V + (size_t)head * 131072;
    const unsigned ldb = (unsigned)(uintptr_t)lds;
    const unsigned voff = wid * 1024 + lane * 16;
    const char* kb = (const char*)Kg; const char* vb = (const char*)Vg;
    auto stage = [&](int st, int slot) {
        asm volatile("s_add_u32 m0, %2, 0\n\ts_nop 0\n\tglobal_load_lds_dwordx4 %0, %1 sc1" ::"v"(voff), "s"(kb + (size_t)st * 8192), "s"(ldb + slot * AT_STAGE + wid * 1024) : "memory", "scc");
        asm volatile("s_add_u32 m0, %2, 0\n\ts_nop 0\n\tglobal_load_lds_dwordx4 %0, %1 sc1" ::"v"(voff), "s"(vb + (size_t)st * 8192), "s"(ldb + slot * AT_STAGE + 8192 + wid * 1024) : "memory", "scc");
    };
    stage(0, 0); stage(1, 1); stage(2, 2);
    const int q0 = qb * 512 + wid * 64;
    bf16x8 qf[2][2];
#pragma unroll
    for (int a = 0; a < 2; ++a)
#pragma unroll
        for (int d = 0; d < 2; ++d) qf[a][d] = __builtin_bit_cast(bf16x8, ld16_dev(ws_rsrc(p.ws), (unsigned)WS_Q + (head * 64 + (q0 >> 5) + a) * 2048 + d * 1024 + lane * 16));
    bf16x8 sel;
    { const short one = ((lane & 15) == ((lane >> 4) & 1)) ? (short)0x3f80 : (short)0;
#pragma unroll
      for (int i = 0; i < 8; ++i) sel[i] = one; }
    asm volatile("s_waitcnt vmcnt(0)" ::: "memory");
    GM_BAR();
    f32x16 o[2], negm[2]; f32x4 l[2];
    const f32x16 zero16 = {};
    bf16x8 kA[2], kB[2], vA[2], vB[2];
    ldsp lb = lds + lane * 16;
    kA[0] = *(const LAS bf16x8*)(lb); kA[1] = *(const LAS bf16x8*)(lb + 1024);
#pragma unroll
    for (int a = 0; a < 2; ++a) {
        f32x16 s = MFMA32(kA[0], qf[a][0], zero16); s = MFMA32(kA[1], qf[a][1], s);
        float m = fmaxf(fmaxf(s[0], s[1]), fmaxf(s[2], s[3]));
#pragma unroll
        for (int r = 4; r < 16; r += 2) m = fmaxf(m, fmaxf(s[r], s[r + 1]));
        const auto rr = __builtin_amdgcn_permlane32_swap(__float_as_uint(m), __float_as_uint(m), false, false);
        m = -fmaxf(__uint_as_float(rr[0]), __uint_as_float(rr[1]));
#pragma unroll
        for (int r = 0; r < 16; ++r) negm[a][r] = m;
        PIN(negm[a]);
        o[a] = zero16; l[a] = (f32x4){0.f, 0.f, 0.f, 0.f};
    }
    f32x16 S0, S1; u32x4 p0l, p0h, p1l, p1h;
    S0 = MFMA32(kA[0], qf[0][0], negm[0]); S0 = MFMA32(kA[1], qf[0][1], S0);
    p1l = (u32x4){0u, 0u, 0u, 0u}; p1h = p1l;
    vB[0] = (bf16x8){0, 0, 0, 0, 0, 0, 0, 0}; vB[1] = vB[0];
    STAMP(0, 4);
#define EX4(S, i) do { S[i] = __builtin_amdgcn_exp2f(S[i]); S[i + 1] = __builtin_amdgcn_exp2f(S[i + 1]); S[i + 2] = __builtin_amdgcn_exp2f(S[i + 2]); S[i + 3] = __builtin_amdgcn_exp2f(S[i + 3]); } while (0)
#define PK4(P, S, i) do { P[0] = pk_bf16(S[i], S[i + 1]); P[1] = pk_bf16(S[i + 2], S[i + 3]); P[2] = pk_bf16(S[i + 4], S[i + 5]); P[3] = pk_bf16(S[i + 6], S[i + 7]); } while (0)
#define BC(x) __builtin_bit_cast(bf16x8, x)
#define ASTEP(SC, SN, PCl, PCh, PPl, PPh, KF, QN, VF, AP, LOADS) do { \
        SN = MFMA32(KF[0], qf[QN][0], negm[QN]); EX4(SC, 0); PIN(SC); GM_SB(); \
        SN = MFMA32(KF[1], qf[QN][1], SN); EX4(SC, 4); LOADS; PIN(SC); GM_SB(); \
        o[AP] = MFMA32(VF[0], BC(PPl), o[AP]); EX4(SC, 8); PIN(SC); GM_SB(); \
        o[AP] = MFMA32(VF[1], BC(PPh), o[AP]); EX4(SC, 12); PIN(SC); GM_SB(); \
        l[AP] = MFMA16(sel, BC(PPl), l[AP]); PK4(PCl, SC, 0); PIN(PCl); GM_SB(); \
        l[AP] = MFMA16(sel, BC(PPh), l[AP]); PK4(PCh, SC, 8); PIN(PCh); GM_SB(); } while (0)
#define LDK(KF, base, kt) do { KF[0] = *(const LAS bf16x8*)((base) + (kt) * 2048); KF[1] = *(const LAS bf16x8*)((base) + (kt) * 2048 + 1024); } while (0)
#define LDV(VF, base, kt) do { VF[0] = *(const LAS bf16x8*)((base) + 8192 + (kt) * 2048); VF[1] = *(const LAS bf16x8*)((base) + 8192 + (kt) * 2048 + 1024); } while (0)
    float gval = 0.f;
    for (int st = 0; st < 16; ++st) {
        ldsp cb = lb + (st & 3) * AT_STAGE, nb = lb + ((st + 1) & 3) * AT_STAGE;
        if (st < 5) {
            const int n = st * 8 + wid; const unsigned pfs = (unsigned)(uintptr_t)lds + LDS_PFS;
            const int p0 = 8 * (x & 1) + 4 * ((x >> 1) & 1), j0 = 4 * (x >> 2);
            if (n < 16) pf_lines(p.wsp + (size_t)(p0 * 128 + a * 32) * SEQ, (unsigned)(lane & 31) * (SEQ * 2) + (unsigned)(2 * n + (lane >> 5)) * 128u, pfs);
            else if (n < 30) pf_lines(p.wout + (size_t)(a * 112) * DM + j0 * 128, (unsigned)((n - 16) * 8 + (lane >> 3)) * (DM * 2) + (unsigned)(lane & 7) * 128u, pfs);
            else if (n < 38) pf_lines(p.wo + (size_t)(a * 64) * DM + j0 * 128, (unsigned)((n - 30) * 8 + (lane >> 3)) * (DM * 2) + (unsigned)(lane & 7) * 128u, pfs);
        }
        if (st == 8) {
            poll_flags(p.ws, p.tag, F_Q, 1, true, F_Q, lane);
            gval = __uint_as_float(__hip_atomic_load((gu32*)(p.ws + WS_GATE) + q0 + lane, RLX_AGENT));
        }
        ASTEP(S0, S1, p0l, p0h, p1l, p1h, kA, 1, vB, 1, { LDK(kB, cb, 1); LDV(vA, cb, 0); });
        ASTEP(S1, S0, p1l, p1h, p0l, p0h, kB, 0, vA, 0, {});
        ASTEP(S0, S1, p0l, p0h, p1l, p1h, kB, 1, vA, 1, { LDK(kA, cb, 2); LDV(vB, cb, 1); });
        ASTEP(S1, S0, p1l, p1h, p0l, p0h, kA, 0, vB, 0, {});
        ASTEP(S0, S1, p0l, p0h, p1l, p1h, kA, 1, vB, 1, { LDK(kB, cb, 3); LDV(vA, cb, 2); });
        ASTEP(S1, S0, p1l, p1h, p0l, p0h, kB, 0, vA, 0, {});
        if (st + 1 < 16) {
            if (st + 2 < 16) asm volatile("s_waitcnt vmcnt(2)" ::: "memory"); else asm volatile("s_waitcnt vmcnt(0)" ::: "memory");
            GM_BAR();
            if (st + 3 < 16) stage(st + 3, (st + 3) & 3);
        }
        ASTEP(S0, S1, p0l, p0h, p1l, p1h, kB, 1, vA, 1, { LDK(kA, nb, 0); LDV(vB, cb, 3); });
        ASTEP(S1, S0, p1l, p1h, p0l, p0h, kA, 0, vB, 0, {});
    }
    o[1] = MFMA32(vB[0], BC(p1l), o[1]); o[1] = MFMA32(vB[1], BC(p1h), o[1]);
    l[1] = MFMA16(sel, BC(p1l), l[1]); l[1] = MFMA16(sel, BC(p1h), l[1]);
#undef EX4
#undef PK4
#undef ASTEP
#undef LDK
#undef LDV
    STAMP(0, 5);
    const rsrc_t rs = ws_rsrc(p.ws);
    const float gt0 = __shfl(gval, lane & 31), gt1 = __shfl(gval, 32 + (lane & 31));
#pragma unroll
    for (int a = 0; a < 2; ++a) {
        const int q = lane & 31, hh = lane >> 5;
        const float l0 = __shfl(l[a][0], q & 15), l1 = __shfl(l[a][1], q & 15);
        const float lsum = (q < 16) ? l0 : l1;
        const float sc = (a ? gt1 : gt0) * __builtin_amdgcn_rcpf(lsum);
        const int row = q0 + a * 32 + q, panel = row >> 7;
#pragma unroll
        for (int i = 0; i < 4; i += 2) {
            const unsigned a0 = pk_bf16(o[a][4 * i] * sc, o[a][4 * i + 1] * sc), a1 = pk_bf16(o[a][4 * i + 2] * sc, o[a][4 * i + 3] * sc);
            const unsigned b0 = pk_bf16(o[a][4 * i + 4] * sc, o[a][4 * i + 5] * sc), b1 = pk_bf16(o[a][4 * i + 6] * sc, o[a][4 * i + 7] * sc);
            const auto r0 = __builtin_amdgcn_permlane32_swap(a0, b0, false, false); const auto r1 = __builtin_amdgcn_permlane32_swap(a1, b1, false, false);
            const int c = 224 + head * 4 + i + hh;
            st16_wt(rs, (unsigned)WS_ACT + (panel * 352 + c) * 2048 + (row & 127) * 16, (u32x4){r0[0], r1[0], r0[1], r1[1]});
        }
    }
    asm volatile("s_waitcnt vmcnt(0)" ::: "memory");
    GM_BAR();
    if (threadIdx.x == 0) signal(p.ws, F_O + qb * 32 + head, p.tag);
}

__device__ __forceinline__ void gate_row(const Ctx& p, int row, int lane) {
    const u32x4* xr = (const u32x4*)(p.ax + (size_t)row * DM) + lane * 2; const u32x4* gw = (const u32x4*)p.gw + lane * 2;
    float s = 0.f;
#pragma unroll
    for (int i = 0; i < 2; ++i) { const u32x4 a = xr[i], b = gw[i];
#pragma unroll
        for (int e = 0; e < 4; ++e) s += bf_lo(a[e]) * bf_lo(b[e]) + bf_hi(a[e]) * bf_hi(b[e]); }
#pragma unroll
    for (int o = 32; o >= 1; o >>= 1) s += __shfl_xor(s, o);
    if (lane == 0) __hip_atomic_store((gu32*)(p.ws + WS_GATE) + row, __float_as_uint(__builtin_amdgcn_rcpf(1.0f + __builtin_amdgcn_exp2f(-1.4426950408889634f * s))), RLX_AGENT);
}
constexpr int LDS_BYTES = CfgQ::LDS + 256;
static_assert(LDS_TAB + 3072 <= LDS_BYTES && CfgO::LDS <= LDS_BYTES && AT_NS * AT_STAGE <= LDS_BYTES, "LDS map");
__global__ __launch_bounds__(512, 2) void fused(Params pp) {
    extern __shared__ __attribute__((aligned(16))) unsigned char smem[]; ldsp lds = (ldsp)smem;
    const int lane = threadIdx.x & 63, wid = __builtin_amdgcn_readfirstlane(threadIdx.x >> 6);
    const int b = blockIdx.x, x = b & 7, role = (b >> 3) & 1, idx = b >> 4;
    Ctx p; static_cast<Params&>(p) = pp;
    p.tag = (unsigned)__builtin_amdgcn_readfirstlane(__hip_atomic_load((gu32*)(pp.ws + WS_CTL) + C_EPOCH, RLX_AGENT)) + 1u;
    STAMP(0, 0);
    if (role == 0) {
        const int pair = 4 * (x >> 1) + (idx >> 2), panel = 4 * (x & 1) + (idx & 3);
        if (wid >= 4) {
#pragma unroll
            for (int i = 0; i < 4; ++i) gate_row(p, panel * 256 + pair * 16 + (wid - 4) * 4 + i, lane);
        }
        if (wid < 4) qkv_wave<0>(p, lds, pair, panel, wid, lane); else qkv_wave<1>(p, lds, pair, panel, wid, lane);
        STAMP(0, 6);
        attn_wg(p, lds, 4 * x + (idx >> 2), idx & 3, x, idx, wid, lane);
    } else {
        const int panel = 8 * (x & 1) + 4 * ((x >> 1) & 1) + (idx >> 2), j = 4 * (x >> 2) + (idx & 3);
        if (wid < 4) b_wave<true>(p, lds, panel, j, wid, lane); else b_wave<false>(p, lds, panel, j, wid, lane);
        if (panel == 0 && j == 0 && threadIdx.x == 0) __hip_atomic_store((gu32*)(pp.ws + WS_CTL) + C_EPOCH, p.tag, RLX_AGENT);
    }
    STAMP(0, 15);
}
}

extern "C" void kernel_launch(void* const* d_in, const int* in_sizes, int n_in, void* d_out, int out_size, void* d_ws, size_t ws_size, hipStream_t stream) {
    using namespace gm;
    Params p;
    p.ax = (const u16*)d_in[0]; p.wqkv = (const u16*)d_in[1]; p.wo = (const u16*)d_in[2]; p.bx = (const u16*)d_in[3]; p.win = (const u16*)d_in[4];
    p.wsp = (const u16*)d_in[5]; p.bsp = (const u16*)d_in[6]; p.wout = (const u16*)d_in[7]; p.gw = (const u16*)d_in[8];
    p.out = (u16*)d_out; p.ws = (unsigned char*)d_ws;
    (void)hipFuncSetAttribute((const void*)fused, hipFuncAttributeMaxDynamicSharedMemorySize, LDS_BYTES);
    fused<<<256, 512, LDS_BYTES, stream>>>(p);
}
```

```cpp
#include <hip/hip_runtime.h>
#include <hip/hip_bf16.h>
#include <stdint.h>

#define LAS __attribute__((address_space(3)))
typedef short bf16x8 __attribute__((ext_vector_type(8)));
typedef short s16x4 __attribute__((ext_vector_type(4)));
typedef float f32x2 __attribute__((ext_vector_type(2)));
typedef float f32x4 __attribute__((ext_vector_type(4)));
typedef float f32x16 __attribute__((ext_vector_type(16)));
typedef unsigned u32x4 __attribute__((ext_vector_type(4)));
typedef unsigned u32x2 __attribute__((ext_vector_type(2)));
typedef LAS unsigned char* ldsp;
typedef unsigned short u16;

#define NA 16
#define SPD 1
namespace gm {
constexpr int SEQ = 2048, DM = 1024, NH = 32, FH = 1792;
constexpr size_t WS_CTL = 0;
constexpr size_t CTL_BYTES = 16384;
constexpr size_t WS_GATE = 16384;
constexpr size_t WS_STAT = 32768;
constexpr size_t WS_Q = 1u << 20;
constexpr size_t WS_K = WS_Q + (4u << 20);
constexpr size_t WS_V = WS_K + (4u << 20);
constexpr size_t WS_Z = WS_V + (4u << 20);
constexpr size_t WS_ACT = WS_Z + (8u << 20);
constexpr int LDS_PFS = 147456 - 0;
constexpr size_t WS_P = WS_ACT + (12u << 20);
constexpr float QSCALE = 0.17677669529663687f * 1.4426950408889634f;

#define TRACE(t) do {} while (0)
#define DBG_PARAMS
#define DBG_ARGS(id)
#define DBG_FWD
#define STAMP(k, i) do {} while (0)
constexpr int C_EPOCH = 0, C_TMO = 64, F_Q = 128  , F_S = F_Q + 512  , F_Z = F_S + 512  ,
              F_U = F_Z + 256  , F_O = F_U + 512  , F_P = F_O + 128  , C_END = F_P + 512;
static_assert(C_END * 4 <= (int)CTL_BYTES, "control block");
#define GM_VM0() __builtin_amdgcn_s_waitcnt(0x0F70)
#define RLX_AGENT __ATOMIC_RELAXED, __HIP_MEMORY_SCOPE_AGENT
typedef __attribute__((address_space(1))) unsigned gu32;
typedef __attribute__((address_space(1))) unsigned long long gu64;
typedef __amdgpu_buffer_rsrc_t rsrc_t;
__device__ __forceinline__ rsrc_t ws_rsrc(unsigned char* ws) { return __builtin_amdgcn_make_buffer_rsrc(ws, 0, 64u << 20, 0x00020000); }
__device__ __forceinline__ void st16_wt(rsrc_t r, unsigned off, u32x4 v) { __builtin_amdgcn_raw_buffer_store_b128(v, r, off, 0, 16); }
__device__ __forceinline__ void signal(unsigned char* ws, int word, unsigned tag) { __hip_atomic_store((gu32*)(ws + WS_CTL) + word, tag, RLX_AGENT); }
__device__ __forceinline__ void poll_flags(unsigned char* ws, unsigned tag, int w0, int n, bool all128, int wq, int lane) {
    gu32* ctl = (gu32*)(ws + WS_CTL);
    for (unsigned spins = 0;;) {
        bool ok = __hip_atomic_load(ctl + w0 + (lane < n ? lane : 0), RLX_AGENT) == tag;
        if (all128) { const unsigned q0 = __hip_atomic_load(ctl + wq + (lane >> 3) * 32 + (lane & 7), RLX_AGENT), q1 = __hip_atomic_load(ctl + wq + 256 + (lane >> 3) * 32 + (lane & 7), RLX_AGENT); ok = ok && q0 == tag && q1 == tag; }
        if (__all(ok)) break;
        __builtin_amdgcn_s_sleep(2);
        if (++spins > (1u << 19)) { __hip_atomic_store(ctl + C_TMO, tag, RLX_AGENT); break; }
        if ((spins & 255u) == 0u && __builtin_amdgcn_readfirstlane(__hip_atomic_load(ctl + C_TMO, RLX_AGENT)) == tag) break;
    }
}
__device__ __forceinline__ void wait_flags(unsigned char* ws, unsigned tag, int w0, int n, bool all128, int wq, int lane) {
    poll_flags(ws, tag, w0, n, all128, wq, lane);
    GM_VM0();
}
__device__ __forceinline__ u32x4 ld16_dev(rsrc_t r, unsigned off) { return __builtin_amdgcn_raw_buffer_load_b128(r, off, 0, 16); }
enum { X_RAW = 0, X_OWN = 1 };
enum { W_P384 = 0, W_P448 = 1, W_P256 = 2, W_OWN = 3 };

__device__ __forceinline__ unsigned pk_bf16(float lo, float hi) {
    typedef __bf16 bf16x2_t __attribute__((ext_vector_type(2)));
    f32x2 v = {lo, hi}; bf16x2_t b = __builtin_convertvector(v, bf16x2_t); return __builtin_bit_cast(unsigned, b);
}
__device__ __forceinline__ float bf_lo(unsigned w) { return __uint_as_float(w << 16); }
__device__ __forceinline__ float bf_hi(unsigned w) { return __uint_as_float(w & 0xffff0000u); }
__device__ __forceinline__ s16x4 tr_read(ldsp p) { typedef short v4s __attribute__((ext_vector_type(4))); return __builtin_amdgcn_ds_read_tr16_b64_v4i16((LAS v4s*)p); }
template <int I, bool SC = false> __device__ __forceinline__ void glds_run(const char* sbase, unsigned voff) {
    if constexpr (SC) asm volatile("global_load_lds_dwordx4 %0, %1 offset:%2 sc1" ::"v"(voff), "s"(sbase), "n"((I & 3) * 1024) : "memory");
    else asm volatile("global_load_lds_dwordx4 %0, %1 offset:%2" ::"v"(voff), "s"(sbase), "n"((I & 3) * 1024) : "memory");
}
template <int I, bool SC = false> __device__ __forceinline__ void glds_set(const char* sbase, unsigned voff, unsigned dst) {
    if constexpr (SC) asm volatile("s_add_u32 m0, %2, %3\n\ts_nop 0\n\tglobal_load_lds_dwordx4 %0, %1 offset:%4 sc1" ::"v"(voff), "s"(sbase), "s"(dst), "n"((I & ~3) * 1024), "n"((I & 3) * 1024) : "memory", "scc");
    else asm volatile("s_add_u32 m0, %2, %3\n\ts_nop 0\n\tglobal_load_lds_dwordx4 %0, %1 offset:%4" ::"v"(voff), "s"(sbase), "s"(dst), "n"((I & ~3) * 1024), "n"((I & 3) * 1024) : "memory", "scc");
}
__device__ __forceinline__ void pf_lines(const void* base, unsigned voff, unsigned ldsdst) {
    asm volatile("s_mov_b32 m0, %2\n\ts_nop 0\n\tglobal_load_lds_dword %0, %1" ::"v"(voff), "s"(base), "s"(ldsdst) : "memory");
}
#define GM_BAR() do { asm volatile("" ::: "memory"); __builtin_amdgcn_s_barrier(); asm volatile("" ::: "memory"); } while (0)
#define GM_SB() __builtin_amdgcn_sched_barrier(0)
__device__ __forceinline__ bool at_tile(int t, int k) { asm volatile("" : "+s"(t)); return t == k; }

template <int BM_, int BN_, int XI_, int WI_, int NSX_, int NSW_> struct Cfg {
    static constexpr int BM = BM_, BN = BN_, XI = XI_, WI = WI_, NSX = NSX_, NSW = NSW_;
    static constexpr int XB = BM * 128, WB = BN * 128, WOFF = NSX * XB, LDS = NSX * XB + NSW * WB;
    static constexpr int PX = BM / 32, PW = BN / 32;
    static constexpr int WPITCH = (WI == W_P384) ? 384 : (WI == W_P448) ? 448 : (WI == W_P256) ? 256 : 0;
    static constexpr bool XSC = false, WSC = false;
};
template <class C> struct Src { const char* x; const char* w; const char* w2; int ksplit; unsigned xstep, wstep; unsigned xv[C::PX]; unsigned wv[C::PW]; };
template <int WI> __device__ __forceinline__ int w_swz(int r, int x) {
    if (WI == W_P384) return x ^ (((((r >> 1) & 1) | (((r >> 3) & 1) << 1))) << 5);
    if (WI == W_P448) return x ^ (((r >> 3) & 1) << 5);
    if (WI == W_P256) return x ^ (((r & 3) | (((r >> 3) & 1) << 2)) << 5);
    return x;
}
template <class C> __device__ __forceinline__ const char* w_tile(const Src<C>& s, int kt) { return (kt < s.ksplit ? s.w : s.w2) + (unsigned)kt * s.wstep; }
template <class C> __device__ __forceinline__ void set_w2(Src<C>& s, const void* base, int ksplit) { s.ksplit = ksplit; s.w2 = (const char*)base - 4096 - (size_t)ksplit * s.wstep; }
template <class C> __device__ __forceinline__ void src_x_raw(Src<C>& s, const void* base, unsigned pitch, int lid, int lane) {
    s.x = (const char*)base - 4096; s.xstep = 128;
#pragma unroll
    for (int i = 0; i < C::PX; ++i) { const int row = 8 * (lid * C::PX + i) + (lane >> 3); s.xv[i] = row * pitch + (((lane & 7) ^ ((row >> 1) & 7)) * 16) + 4096 - (i & 3) * 1024; }
}
template <class C> __device__ __forceinline__ void src_x_own(Src<C>& s, const void* base, int lid, int lane) {
    s.x = (const char*)base - 4096; s.xstep = C::XB;
#pragma unroll
    for (int i = 0; i < C::PX; ++i) s.xv[i] = (lid * C::PX + i) * 1024 + lane * 16 + 4096 - (i & 3) * 1024;
}
template <class C> __device__ __forceinline__ void src_w_own(Src<C>& s, const void* base, int lid, int lane) {
    s.w = (const char*)base - 4096; s.w2 = s.w; s.ksplit = 1 << 20; s.wstep = C::WB;
#pragma unroll
    for (int i = 0; i < C::PW; ++i) s.wv[i] = (lid * C::PW + i) * 1024 + lane * 16 + 4096 - (i & 3) * 1024;
}
template <class C> __device__ __forceinline__ void src_w_raw(Src<C>& s, const void* base, unsigned pitch, unsigned segstride, int lid, int lane) {
    s.w = (const char*)base - 4096; s.w2 = s.w; s.ksplit = 1 << 20; s.wstep = 64 * pitch;
#pragma unroll
    for (int i = 0; i < C::PW; ++i) { const int o = 1024 * (lid * C::PW + i) + 16 * lane; const int r = o / C::WPITCH, x = w_swz<C::WI>(r, o % C::WPITCH);
        s.wv[i] = r * pitch + (x >> 7) * segstride + (x & 127) + 4096 - (i & 3) * 1024; }
}
template <class C, int I0, int I1, bool FIRST> __device__ __forceinline__ void stage_rng(unsigned dx, unsigned dw, const Src<C>& s, const char* xb, const char* wb) {
    if constexpr (I0 < I1) {
        if constexpr (I0 < C::PW) { if constexpr (FIRST || (I0 & 3) == 0) glds_set<I0, C::WSC>(wb, s.wv[I0], dw); else glds_run<I0, C::WSC>(wb, s.wv[I0]); }
        else { constexpr int j = I0 - C::PW; if constexpr (FIRST || (j & 3) == 0) glds_set<j, C::XSC>(xb, s.xv[j], dx); else glds_run<j, C::XSC>(xb, s.xv[j]); }
        stage_rng<C, I0 + 1, I1, false>(dx, dw, s, xb, wb);
    }
}
template <int MT_, int NT_, int N0_, unsigned SWAP_, bool LD_> struct Wv { static constexpr int MT = MT_, NT = NT_, N0 = N0_; static constexpr unsigned SWAP = SWAP_; static constexpr bool LD = LD_; };
template <class W> struct Frag { bf16x8 x[W::MT]; s16x4 wl[W::NT], wh[W::NT]; };
template <class C, class W> struct RdAddr { unsigned x[2]; unsigned w[(C::WI == W_P384) ? 4 : (C::WI == W_P256) ? W::NT : 2]; };
template <class C, class W> __device__ __forceinline__ void rd_init(RdAddr<C, W>& a, int m0, int n0, int lane) {
    const int fr = lane & 15, fq = lane >> 4, g = lane >> 4, q = (lane & 15) >> 2, p = lane & 3;
    if (C::XI == X_RAW) { for (int kk = 0; kk < 2; ++kk) a.x[kk] = (m0 + fr) * 128 + (((kk * 4 + fq) ^ (fr >> 1)) * 16); }
    else { for (int kk = 0; kk < 2; ++kk) a.x[kk] = (kk * 4 + fq) * (C::BM * 16) + (m0 + fr) * 16; }
    if (C::WI == W_P384) { const int s = ((q >> 1) & 1) | ((g & 1) << 1);
        for (int c = 0; c < 4; ++c) a.w[c] = C::WOFF + (8 * g + q) * 384 + ((c ^ s) * 32) + p * 8; }
    else if (C::WI == W_P448) { const int s = g & 1;
        for (int c = 0; c < 2; ++c) a.w[c] = C::WOFF + (8 * g + q) * 448 + (((c ^ s) - c) * 32) + p * 8; }
    else if (C::WI == W_P256) { const int s = (q & 3) | ((g & 1) << 2);
        for (int t = 0; t < W::NT; ++t) a.w[t] = C::WOFF + (8 * g + q) * 256 + (((n0 + t) ^ s) * 32) + p * 8; }
    else { for (int h = 0; h < 2; ++h) a.w[h] = C::WOFF + (p >> 1) * 1024 + (8 * g + 4 * (h ^ (p >> 1)) + q) * 16 + (p & 1) * 8 + W::N0 * 2048; }
}
template <class C, class W> __device__ __forceinline__ s16x4 rd_w(ldsp b, const RdAddr<C, W>& a, int t, int kk, int h) {
    const int c = W::N0 + t;
    if (C::WI == W_P384) return tr_read(b + a.w[c & 3] + (c >> 2) * 128 + kk * 32 * 384 + h * 4 * 384);
    else if (C::WI == W_P448) return tr_read(b + a.w[c & 1] + c * 32 + kk * 32 * 448 + h * 4 * 448);
    else if (C::WI == W_P256) return tr_read(b + a.w[t] + kk * 32 * 256 + h * 4 * 256);
    else return tr_read(b + a.w[h] + t * 2048 + kk * 512);
}
template <class C, class W> __device__ __forceinline__ void rd_one(Frag<W>& f, ldsp bx, ldsp bw, const RdAddr<C, W>& a, int kk, int r) {
    if (r < 2) { if (r == 0) f.wl[0] = rd_w<C, W>(bw, a, 0, kk, 0); else f.wh[0] = rd_w<C, W>(bw, a, 0, kk, 1); }
    else if (r < 2 + W::MT) { const int m = r - 2; f.x[m] = *(const LAS bf16x8*)(bx + a.x[kk] + m * ((C::XI == X_RAW) ? 2048 : 256)); }
    else { const int u = r - W::MT; const int t = u >> 1; if (u & 1) f.wh[t] = rd_w<C, W>(bw, a, t, kk, 1); else f.wl[t] = rd_w<C, W>(bw, a, t, kk, 0); }
}
template <class W> __device__ __forceinline__ void mma_one(f32x4 (&acc)[W::NT][W::MT], const Frag<W>& f, int i) {
    const int t = i / W::MT, m = i % W::MT;
    const bf16x8 w = __builtin_shufflevector(f.wl[t], f.wh[t], 0, 1, 2, 3, 4, 5, 6, 7);
    acc[t][m] = ((W::SWAP >> t) & 1) ? __builtin_amdgcn_mfma_f32_16x16x32_bf16(f.x[m], w, acc[t][m], 0, 0, 0)
                                     : __builtin_amdgcn_mfma_f32_16x16x32_bf16(w, f.x[m], acc[t][m], 0, 0, 0);
}
template <int RFRONT_, int VA_, int RF1_ = RFRONT_> struct Sch { static constexpr int RFRONT = RFRONT_, VA = VA_, RF1 = RF1_; };
template <class C, class W, class S, int RF, int V0, int V1, int I>
__device__ __forceinline__ void step_slot(f32x4 (&acc)[W::NT][W::MT], const Frag<W>& fc, Frag<W>& fn, ldsp bx, ldsp bw, const RdAddr<C, W>& a, int ksn,
                                          const Src<C>& s, const char* xb, const char* wb, unsigned dx, unsigned dw) {
    constexpr int NM = W::NT * W::MT, NR = 2 * W::NT + W::MT, NV = V1 - V0;
    constexpr int RS = (NM * RF / 16) < 1 ? 1 : (NM * RF / 16);
    if constexpr (I < NM) {
        mma_one<W>(acc, fc, I);
        if constexpr (I < RS) {
#pragma unroll
            for (int r = I * NR / RS; r < (I + 1) * NR / RS; ++r) rd_one<C, W>(fn, bx, bw, a, ksn, r);
        }
        if constexpr (W::LD) {
            constexpr int v0 = V0 + I * NV / NM, v1 = V0 + (I + 1) * NV / NM;
            stage_rng<C, v0, v1, v0 == V0>(dx, dw, s, xb, wb);
        }
        GM_SB();
        step_slot<C, W, S, RF, V0, V1, I + 1>(acc, fc, fn, bx, bw, a, ksn, s, xb, wb, dx, dw);
    }
}
struct NoHook { static constexpr bool PRE = false; __device__ __forceinline__ void pre_x() const {} __device__ __forceinline__ void at(int) const {} };
template <class C, class W, class S, class H>
__device__ __forceinline__ void gemm_loop(ldsp lds, const Src<C>& s, int nt, f32x4 (&acc)[W::NT][W::MT], int lid, int m0, int n0, int lane, H& hook DBG_PARAMS) {
    constexpr int PX = C::PX, PW = C::PW, NSX = C::NSX, NSW = C::NSW, NV = PX + PW;
    static_assert(S::VA >= PW && S::VA <= NV, "all W pieces go out in the step behind the barrier");
    static_assert(NSX >= NSW && NSW >= 2, "ring depths");
    RdAddr<C, W> a; rd_init<C, W>(a, m0, n0, lane);
    const unsigned ldb = (unsigned)(uintptr_t)lds;
    const unsigned lx0 = ldb + lid * PX * 1024, lw0 = ldb + C::WOFF + lid * PW * 1024;
    Frag<W> f0, f1;
    const char* wb = w_tile<C>(s, NSW - 1); const char* xb = s.x + (size_t)(NSX - 1) * s.xstep;
    unsigned dw = lw0 + (NSW - 1) * C::WB, dx = lx0 + (NSX - 1) * C::XB;
    if constexpr (H::PRE) {
        if constexpr (W::LD) {
#pragma unroll
            for (int i = 0; i < NSW; ++i) stage_rng<C, 0, PW, true>(0, lw0 + i * C::WB, s, nullptr, w_tile<C>(s, i));
        }
        hook.pre_x();
        if constexpr (W::LD) {
#pragma unroll
            for (int i = 0; i < NSX - 1; ++i) stage_rng<C, PW, NV, true>(lx0 + i * C::XB, 0, s, s.x + (size_t)i * s.xstep, nullptr);
            if constexpr (S::VA > PW) stage_rng<C, PW, S::VA, true>(dx, dw, s, xb, wb);
            asm volatile("s_waitcnt vmcnt(%0)" ::"n"(S::VA - PW) : "memory");
        }
    } else if constexpr (W::LD) {
#pragma unroll
        for (int i = 0; i < NSX - 1; ++i) {
            if (i < NSW - 1) stage_rng<C, 0, PW, true>(0, lw0 + i * C::WB, s, nullptr, w_tile<C>(s, i));
            stage_rng<C, PW, NV, true>(lx0 + i * C::XB, 0, s, s.x + (size_t)i * s.xstep, nullptr);
        }
        stage_rng<C, 0, S::VA, true>(dx, dw, s, xb, wb);
        constexpr int Y = (NSX - 2) * PX + (NSW - 2) * PW + S::VA;
        asm volatile("s_waitcnt vmcnt(%0)" ::"n"(Y) : "memory");
    }
    GM_BAR();
#pragma unroll
    for (int r = 0; r < 2 * W::NT + W::MT; ++r) rd_one<C, W>(f0, lds, lds, a, 0, r);
    int bx = 0, bw = 0, kx = NSX - 1, kw = NSW - 1;
    __builtin_amdgcn_s_waitcnt(0xC07F);
    for (int t = 0; t < nt; ++t) {
        ldsp cx = lds + bx * C::XB, cw = lds + bw * C::WB;
        const int nbx = (bx + 1 == NSX) ? 0 : bx + 1, nbw = (bw + 1 == NSW) ? 0 : bw + 1;
        ldsp nx = lds + nbx * C::XB, nw = lds + nbw * C::WB;
        TRACE(t);
        GM_SB();
#define GM_STEP(FC, FN, BX, BW, KSN, RF, V0, V1) step_slot<C, W, S, RF, V0, V1, 0>(acc, FC, FN, BX, BW, a, KSN, s, xb, wb, dx, dw)
        GM_STEP(f0, f1, cx, cw, 1, S::RFRONT, S::VA, NV);
        hook.at(t);
        asm volatile("s_waitcnt lgkmcnt(0)" ::: "memory");
        if constexpr (W::LD) {
            constexpr int pw = 1 - NSW, px = 1 - NSX, last = (pw > px) ? pw : px, nper = (-1) - last, Y = nper * NV + ((pw > px) ? PX : 0);
            asm volatile("s_waitcnt vmcnt(%0)" ::"n"(Y) : "memory");
        }
        GM_BAR();
        GM_SB();
        if constexpr (W::LD) {
            kx = (kx + 1 < nt) ? kx + 1 : kx; kw = (kw + 1 < nt) ? kw + 1 : kw;
            xb = s.x + (size_t)kx * s.xstep; wb = w_tile<C>(s, kw);
            dx = lx0 + bx * C::XB; dw = lw0 + bw * C::WB;
        }
        GM_STEP(f1, f0, nx, nw, 0, S::RF1, 0, S::VA);
#undef GM_STEP
        bx = nbx; bw = nbw;
    }
    GM_SB();
    asm volatile("s_waitcnt vmcnt(0) lgkmcnt(0)" ::: "memory");
    GM_BAR();
}
template <class W> __device__ __forceinline__ void acc_zero(f32x4 (&acc)[W::NT][W::MT]) {
#pragma unroll
    for (int t = 0; t < W::NT; ++t)
#pragma unroll
        for (int m = 0; m < W::MT; ++m) acc[t][m] = (f32x4){0.f, 0.f, 0.f, 0.f};
}
__device__ __forceinline__ u32x4 pair16(const f32x4& A, const f32x4& B) {
    const unsigned a0 = pk_bf16(A[0], A[1]), a1 = pk_bf16(A[2], A[3]), b0 = pk_bf16(B[0], B[1]), b1 = pk_bf16(B[2], B[3]);
    const auto r0 = __builtin_amdgcn_permlane16_swap(a0, b0, false, false); const auto r1 = __builtin_amdgcn_permlane16_swap(a1, b1, false, false);
    return (u32x4){r0[0], r1[0], r0[1], r1[1]};
}

namespace g32 {
enum { W_P448 = 1, W_P256 = 2, W_OWNT = 3 };
template <int BM_, int BN_, int XI_, int WI_, int NS_, int NL_ = 4> struct Cfg {
    static constexpr int BM = BM_, BN = BN_, XI = XI_, WI = WI_, NS = NS_, NL = NL_;
    static constexpr int XB = BM * 128, WB = BN * 128, WOFF = NS * XB, LDS = NS * (XB + WB);
    static constexpr int PX = BM / 8 / NL, PW = BN / 8 / NL;
    static constexpr int WPITCH = (WI == W_P448) ? 448 : (WI == W_P256) ? 256 : 16;
    static constexpr bool XSC = (XI == X_OWN), WSC = (WI == W_OWNT);
};
template <class C> __device__ __forceinline__ void src_w_raw(Src<C>& s, const void* base, unsigned pitch, int lid, int lane) {
    s.w = (const char*)base - 4096; s.w2 = s.w; s.ksplit = 1 << 20; s.wstep = 64 * pitch;
#pragma unroll
    for (int i = 0; i < C::PW; ++i) { const int o = 1024 * (lid * C::PW + i) + 16 * lane; const int r = o / C::WPITCH; int x = o % C::WPITCH;
        if (C::WI == W_P256) x ^= (r & 3) << 6;
        s.wv[i] = r * pitch + x + 4096 - (i & 3) * 1024; }
}
template <int MT_, int NT_, bool LD_> struct Wv { static constexpr int MT = MT_, NT = NT_; static constexpr bool LD = LD_; };
template <class C, class W> struct Frag { bf16x8 x[W::MT]; s16x4 wl[W::NT], wh[W::NT]; };
template <class C, class W> struct RdAddr { unsigned x[(C::XI == X_RAW) ? 4 : 1]; unsigned w[(C::WI == W_P256) ? W::NT : 1]; };
template <class C, class W> __device__ __forceinline__ void rd_init(RdAddr<C, W>& a, int m0, int n0  , int lane) {
    const int r = lane & 31, h = lane >> 5, g = lane >> 4, q = (lane & 15) >> 2, p = lane & 3;
    if (C::XI == X_RAW) { for (int ks = 0; ks < 4; ++ks) a.x[ks] = (m0 + r) * 128 + (((2 * ks + h) ^ ((r >> 1) & 7)) * 16); }
    else a.x[0] = h * (C::BM * 16) + (m0 + r) * 16;
    const int rowp = 8 * (g >> 1) + q, colp = (g & 1) * 32 + p * 8;
    if (C::WI == W_P448) a.w[0] = C::WOFF + rowp * 448 + colp + n0 * 64;
    else if (C::WI == W_P256) { for (int t = 0; t < W::NT; ++t) a.w[t] = C::WOFF + rowp * 256 + (((n0 + t) ^ q) * 64) + colp; }
    else a.w[0] = C::WOFF + h * (C::BN * 16) + (n0 * 32 + r) * 16;
}
template <class C, class W> constexpr int n_reads() { return W::MT + (C::WI == W_OWNT ? W::NT : 2 * W::NT); }
template <class C, class W> __device__ __forceinline__ void rd_one(Frag<C, W>& f, ldsp bx, ldsp bw, const RdAddr<C, W>& a, int ks, int r) {
    constexpr int PER = (C::WI == W_OWNT) ? 1 : 2;
    if (r >= PER && r < PER + W::MT) { const int m = r - PER;
        if (C::XI == X_RAW) f.x[m] = *(const LAS bf16x8*)(bx + a.x[ks] + m * 4096);
        else f.x[m] = *(const LAS bf16x8*)(bx + a.x[0] + ks * (2 * C::BM * 16) + m * 512);
        return; }
    const int u = r < PER ? r : r - W::MT, t = u / PER, hh = u % PER;
    if (C::WI == W_OWNT) { const bf16x8 v = *(const LAS bf16x8*)(bw + a.w[0] + ks * (2 * C::BN * 16) + t * 512); f.wl[t] = __builtin_shufflevector(v, v, 0, 1, 2, 3); f.wh[t] = __builtin_shufflevector(v, v, 4, 5, 6, 7); }
    else { s16x4 v;
        if (C::WI == W_P448) v = tr_read(bw + a.w[0] + t * 64 + (ks * 16 + hh * 4) * 448);
        else v = tr_read(bw + a.w[t] + (ks * 16 + hh * 4) * 256);
        if (hh) f.wh[t] = v; else f.wl[t] = v; }
}
template <class C, class W> __device__ __forceinline__ void rd_frags(Frag<C, W>& f, ldsp bx, ldsp bw, const RdAddr<C, W>& a, int ks) {
#pragma unroll
    for (int r = 0; r < n_reads<C, W>(); ++r) rd_one<C, W>(f, bx, bw, a, ks, r);
}
template <class C, class W, bool SWAP  > __device__ __forceinline__ void mma_one(f32x16 (&acc)[W::NT][W::MT], const Frag<C, W>& f, int i) {
    const int t = i / W::MT, m = i % W::MT;
    const bf16x8 w = __builtin_shufflevector(f.wl[t], f.wh[t], 0, 1, 2, 3, 4, 5, 6, 7);
    acc[t][m] = SWAP ? __builtin_amdgcn_mfma_f32_32x32x16_bf16(w, f.x[m], acc[t][m], 0, 0, 0) : __builtin_amdgcn_mfma_f32_32x32x16_bf16(f.x[m], w, acc[t][m], 0, 0, 0);
}
struct NoFill { template <int S> __device__ __forceinline__ void chunk() {} };
template <class C, class W, bool SWAP, int V0, int V1, int BASE, int I, class F>
__device__ __forceinline__ void step_slot(f32x16 (&acc)[W::NT][W::MT], const Frag<C, W>& fc, Frag<C, W>& fn, ldsp bx, ldsp bw, const RdAddr<C, W>& a, int ksn,
                                          const Src<C>& s, const char* xb, const char* wb, unsigned dx, unsigned dw, F& fill) {
    constexpr int NM = W::NT * W::MT, NR = n_reads<C, W>(), NV = V1 - V0;
    if constexpr (I < NM) {
        mma_one<C, W, SWAP>(acc, fc, I);
#pragma unroll
        for (int r = I * NR / NM; r < (I + 1) * NR / NM; ++r) rd_one<C, W>(fn, bx, bw, a, ksn, r);
        if constexpr (W::LD) { constexpr int v0 = V0 + I * NV / NM, v1 = V0 + (I + 1) * NV / NM; stage_rng<C, v0, v1, v0 == V0>(dx, dw, s, xb, wb); }
        fill.template chunk<BASE + I>();
        GM_SB();
        step_slot<C, W, SWAP, V0, V1, BASE, I + 1, F>(acc, fc, fn, bx, bw, a, ksn, s, xb, wb, dx, dw, fill);
    }
}
template <class C, class WF  , int D> struct Pipe {
    static constexpr int PX = C::PX, PW = C::PW, NS = C::NS, NV = PX + PW;
#define QSPLIT 0
    static constexpr int Q1 = QSPLIT == 0 ? (NV + 3) / 4 : QSPLIT == 1 ? (NV + 1) / 2 : PW, Q2 = QSPLIT == 0 ? (NV + 1) / 2 : QSPLIT == 1 ? NV : NV, Q3 = QSPLIT == 0 ? (3 * NV + 3) / 4 : NV;
    static constexpr int PRE = (D == 1) ? Q1 : Q2;
    ldsp lds; int nt, xmod;
    RdAddr<C, WF> a; unsigned lx0, lw0;
    Frag<C, WF> f0, f1, f2, f3;
    __device__ __forceinline__ Pipe(ldsp lds_, const Src<C>& s, int nt_, int xmod_, int lid, int m0, int n0, int lane) : lds(lds_), nt(nt_), xmod(xmod_) {
        rd_init<C, WF>(a, m0, n0, lane);
        const unsigned ldb = (unsigned)(uintptr_t)lds;
        lx0 = ldb + lid * PX * 1024; lw0 = ldb + C::WOFF + lid * PW * 1024;
    }
    __device__ __forceinline__ void issue_x(const Src<C>& s) {
        if constexpr (WF::LD) {
#pragma unroll
            for (int i = 0; i < NS - 1; ++i) stage_rng<C, PW, NV, true>(lx0 + i * C::XB, 0, s, s.x + (size_t)i * s.xstep, nullptr);
        }
    }
    __device__ __forceinline__ void issue_w(const Src<C>& s) {
        static_assert(PRE <= PW, "prologue split");
        if constexpr (WF::LD) {
#pragma unroll
            for (int i = 0; i < NS - 1; ++i) stage_rng<C, 0, PW, true>(0, lw0 + i * C::WB, s, nullptr, w_tile<C>(s, i));
            stage_rng<C, 0, PRE, true>(0, lw0 + (NS - 1) * C::WB, s, nullptr, w_tile<C>(s, NS - 1));
        }
    }
    __device__ __forceinline__ void landed() {
        if constexpr (WF::LD) asm volatile("s_waitcnt vmcnt(0)" ::: "memory");
        GM_BAR();
        rd_frags<C, WF>(f0, lds, lds, a, 0);
        if (D == 2) rd_frags<C, WF>(f1, lds, lds, a, 1);
    }
    __device__ __forceinline__ void begin(const Src<C>& s) {
        if constexpr (WF::LD) {
#pragma unroll
            for (int i = 0; i < NS - 1; ++i) stage_rng<C, 0, NV, true>(lx0 + i * C::XB, lw0 + i * C::WB, s, s.x + (size_t)i * s.xstep, w_tile<C>(s, i));
            stage_rng<C, 0, PRE, true>(0, lw0 + (NS - 1) * C::WB, s, nullptr, w_tile<C>(s, NS - 1));
            asm volatile("s_waitcnt vmcnt(%0)" ::"n"((NS - 2) * NV + PRE) : "memory");
        }
        GM_BAR();
        rd_frags<C, WF>(f0, lds, lds, a, 0);
        if (D == 2) rd_frags<C, WF>(f1, lds, lds, a, 1);
    }
    template <bool SWAP, class H> __device__ __forceinline__ void run(const Src<C>& s, int t0, int t1, f32x16 (&acc)[WF::NT][WF::MT], H& hook DBG_PARAMS) {
        NoFill nf; run_fill<SWAP>(s, t0, t1, t0, acc, hook, nf DBG_FWD);
    }
    template <bool SWAP, class H, class F> __device__ __forceinline__ void run_fill(const Src<C>& s, int t0, int t1, int tb, f32x16 (&acc)[WF::NT][WF::MT], H& hook, F& fill DBG_PARAMS) {
        using W = WF; constexpr int NM = WF::MT * WF::NT;
        int sl = t0 % NS, kn = t0 - 1 + NS; kn = kn < nt ? kn : nt - 1;
        const char* xb = s.x + (unsigned)(kn & (xmod - 1)) * s.xstep; const char* wb = w_tile<C>(s, kn);
        unsigned dx = lx0 + ((sl == 0) ? NS - 1 : sl - 1) * C::XB, dw = lw0 + ((sl == 0) ? NS - 1 : sl - 1) * C::WB;
        __builtin_amdgcn_s_waitcnt(0xC07F);
        for (int t = t0; t < t1; ++t) {
            const int ns = (sl + 1 == NS) ? 0 : sl + 1;
            ldsp cx = lds + sl * C::XB, cw = lds + sl * C::WB, nx = lds + ns * C::XB, nw = lds + ns * C::WB;
            TRACE(t - tb);
            hook.top(t - tb);
            GM_SB();
#define G32_STEP(FC, FN, BX, BW, KSN, V0, V1, ST) step_slot<C, W, SWAP, V0, V1, ST * NM, 0, F>(acc, FC, FN, BX, BW, a, KSN, s, xb, wb, dx, dw, fill)
            if constexpr (D == 1) { G32_STEP(f0, f1, cx, cw, 1, Q1, Q2, 0); G32_STEP(f1, f2, cx, cw, 2, Q2, Q3, 1); G32_STEP(f2, f3, cx, cw, 3, Q3, NV, 2); }
            else { G32_STEP(f0, f2, cx, cw, 2, Q2, Q3, 0); G32_STEP(f1, f3, cx, cw, 3, Q3, NV, 1); }
            hook.at(t - tb);
            asm volatile("s_waitcnt lgkmcnt(0)" ::: "memory");
            if constexpr (W::LD) asm volatile("s_waitcnt vmcnt(%0)" ::"n"((NS - 2) * NV) : "memory");
            GM_BAR();
            GM_SB();
            if constexpr (W::LD) { kn = (kn + 1 < nt) ? kn + 1 : kn; xb = s.x + (unsigned)(kn & (xmod - 1)) * s.xstep; wb = w_tile<C>(s, kn); dx = lx0 + sl * C::XB; dw = lw0 + sl * C::WB; }
            if constexpr (D == 1) { G32_STEP(f3, f0, nx, nw, 0, 0, Q1, 3); }
            else { G32_STEP(f2, f0, nx, nw, 0, 0, Q1, 2); G32_STEP(f3, f1, nx, nw, 1, Q1, Q2, 3); }
#undef G32_STEP
            sl = ns;
        }
        GM_SB();
    }
    __device__ __forceinline__ void end() {
        asm volatile("s_waitcnt vmcnt(0) lgkmcnt(0)" ::: "memory");
        GM_BAR();
    }
};
template <class C, class WF> struct PipeKS {
    static constexpr int PX = C::PX, PW = C::PW, NS = C::NS, NV = PX + PW, NM = WF::MT * WF::NT;
    ldsp lds; int nt, kh;
    RdAddr<C, WF> a; unsigned lx0, lw0;
    Frag<C, WF> a0, a1, b0, b1;
    __device__ __forceinline__ PipeKS(ldsp lds_, int nt_, int kh_, int lid, int m0, int n0, int lane) : lds(lds_), nt(nt_), kh(kh_) {
        rd_init<C, WF>(a, m0, n0, lane);
        static_assert(C::XI == X_OWN && C::WI == W_P256, "layouts of the output GEMM");
        a.x[0] += kh * 2 * (2 * C::BM * 16);
#pragma unroll
        for (int t = 0; t < WF::NT; ++t) a.w[t] += kh * 32 * 256;
        const unsigned ldb = (unsigned)(uintptr_t)lds;
        lx0 = ldb + lid * PX * 1024; lw0 = ldb + C::WOFF + lid * PW * 1024;
    }
    __device__ __forceinline__ void issue_w(const Src<C>& s) {
        if constexpr (WF::LD) {
#pragma unroll
            for (int i = 0; i < NS; ++i) stage_rng<C, 0, PW, true>(0, lw0 + i * C::WB, s, nullptr, w_tile<C>(s, i)); }
    }
    __device__ __forceinline__ void issue_x(const Src<C>& s) {
        if constexpr (WF::LD) {
#pragma unroll
            for (int i = 0; i < NS; ++i) stage_rng<C, PW, NV, true>(lx0 + i * C::XB, 0, s, s.x + (size_t)i * s.xstep, nullptr); }
    }
    __device__ __forceinline__ void landed() {
        if constexpr (WF::LD) asm volatile("s_waitcnt vmcnt(0)" ::: "memory");
        GM_BAR();
        rd_frags<C, WF>(a0, lds, lds, a, 0); rd_frags<C, WF>(a1, lds, lds, a, 1);
    }
    template <class H> __device__ __forceinline__ void run(const Src<C>& s, f32x16 (&acc)[WF::NT][WF::MT], H& hook DBG_PARAMS) {
        using W = WF; NoFill fill; using F = NoFill;
        int sl = 0, kn = NS;
        __builtin_amdgcn_s_waitcnt(0xC07F);
#define KS_PERIOD(T, C0, C1, N0, N1) do { \
            TRACE(T); hook.at(T); \
            asm volatile("s_waitcnt lgkmcnt(0)" ::: "memory"); \
            if constexpr (W::LD) asm volatile("s_waitcnt vmcnt(%0)" ::"n"((NS - 2) * NV) : "memory"); \
            GM_BAR(); GM_SB(); \
            const int ns = (sl + 1 == NS) ? 0 : sl + 1, kc = kn < nt ? kn : nt - 1; \
            const char* xb = s.x + (unsigned)kc * s.xstep; const char* wb = w_tile<C>(s, kc); \
            const unsigned dx = lx0 + sl * C::XB, dw = lw0 + sl * C::WB; \
            ldsp nx = lds + ns * C::XB, nw = lds + ns * C::WB; \
            step_slot<C, W, true, 0, NV / 2, 0, 0, F>(acc, C0, N0, nx, nw, a, 0, s, xb, wb, dx, dw, fill); \
            step_slot<C, W, true, NV / 2, NV, NM, 0, F>(acc, C1, N1, nx, nw, a, 1, s, xb, wb, dx, dw, fill); \
            sl = ns; ++kn; } while (0)
        for (int t = 0; t < nt; t += 2) { KS_PERIOD(t, a0, a1, b0, b1); if (t + 1 >= nt) break; KS_PERIOD(t + 1, b0, b1, a0, a1); }
#undef KS_PERIOD
        GM_SB();
        asm volatile("s_waitcnt vmcnt(0) lgkmcnt(0)" ::: "memory");
        GM_BAR();
    }
};
template <class W> __device__ __forceinline__ void acc_zero(f32x16 (&acc)[W::NT][W::MT]) {
#pragma unroll
    for (int t = 0; t < W::NT; ++t)
#pragma unroll
        for (int m = 0; m < W::MT; ++m) acc[t][m] = (f32x16){};
}
__device__ __forceinline__ u32x4 pair32(unsigned a0, unsigned a1, unsigned b0, unsigned b1) {
    const auto r0 = __builtin_amdgcn_permlane32_swap(a0, b0, false, false); const auto r1 = __builtin_amdgcn_permlane32_swap(a1, b1, false, false);
    return (u32x4){r0[0], r1[0], r0[1], r1[1]};
}
}

struct Params {
    const u16* ax; const u16* wqkv; const u16* wo; const u16* bx; const u16* win; const u16* wsp; const u16* bsp; const u16* wout; const u16* gw;
    u16* out; unsigned char* ws;
};
struct Ctx : Params { unsigned tag; };

using CfgQ = Cfg<256, 192, X_RAW, W_P384, 3, 2>;
struct Warm {
    const char* base; unsigned pitch; int lpr_log2; int cnt; unsigned scratch;
    __device__ __forceinline__ unsigned mask(int t0, int wid) const {
        if (wid < 4 || cnt <= 0) return 0u;
        return ((0x11111111u << (wid - 4)) & (cnt >= 32 ? ~0u : ((1u << cnt) - 1u))) << t0;
    }
    __device__ __forceinline__ void issue(int n, int wid, int lane) const {
        if (n >= 0 && n < cnt && wid == 4 + (n & 3)) { const int idx = n * 64 + lane; pf_lines(base, (unsigned)(idx >> lpr_log2) * pitch + (unsigned)(idx & ((1 << lpr_log2) - 1)) * 128u, scratch); }
    }
};

struct SigHook {
    static constexpr bool PRE = false; int wid, lane; Warm w0, w1; unsigned act;
    __device__ __forceinline__ void pre_x() const {}
    __device__ __forceinline__ void top(int) const {}
    __device__ __forceinline__ void at(int t) const { if ((act >> t) & 1u) { w0.issue(t - 1, wid, lane); w1.issue(t - 1, wid, lane); } }
};
template <int WN> __device__ __forceinline__ void qkv_wave(const Ctx& p, ldsp lds, int pair, int panel, int wid, int lane) {
    using C = CfgQ; using W = Wv<4, 6, WN * 6, WN ? 0x3Cu : 0u, WN == 0>;
    const int lid = wid & 3, m0 = (wid & 3) * 64;
    Src<C> s;
    src_x_raw<C>(s, p.ax + (size_t)panel * 256 * DM, DM * 2, lid, lane);
    src_w_raw<C>(s, p.wqkv + pair * 64, 3 * DM * 2, DM * 2, lid, lane);
    f32x4 acc[6][4]; acc_zero<W>(acc);
    STAMP(0, 1);
    Warm none{nullptr, 0, 0, 0, 0};
    SigHook hk{wid, lane, none, none, 0u};
    gemm_loop<C, W, Sch<12, 7>>(lds, s, DM / 64, acc, lid, m0, 0, lane, hk DBG_ARGS(0));
    STAMP(0, 2);
    const int fr = lane & 15, fq = lane >> 4;
    const int row0 = panel * 256 + m0;
    const rsrc_t rs = ws_rsrc(p.ws);
#pragma unroll
    for (int t = 0; t < 6; t += 2) {
        const int T = WN * 6 + t, kind = T >> 2, head = pair * 2 + ((T >> 1) & 1);
        if (kind < 2) {
            const unsigned base = (unsigned)(kind == 0 ? WS_Q : WS_K);
            const int c = 2 * (fq & 1) + (fq >> 1);
            const float sc = kind == 0 ? QSCALE : 1.0f;
#pragma unroll
            for (int m = 0; m < 4; ++m) { const int row = row0 + m * 16 + fr;
                st16_wt(rs, base + ((head * 64 + (row >> 5)) * 4 + c) * 512 + (row & 31) * 16, pair16(acc[t][m] * sc, acc[t + 1][m] * sc)); }
        } else {
#pragma unroll
            for (int tt = 0; tt < 2; ++tt)
#pragma unroll
                for (int m = 0; m < 4; m += 2) {
                    const f32x4 A = acc[t + tt][m], B = acc[t + tt][m + 1];
                    const unsigned a0 = pk_bf16(A[0], A[1]), a1 = pk_bf16(A[2], A[3]), b0 = pk_bf16(B[0], B[1]), b1 = pk_bf16(B[2], B[3]);
                    const auto r0 = __builtin_amdgcn_permlane32_swap(a0, b0, false, false); const auto r1 = __builtin_amdgcn_permlane32_swap(a1, b1, false, false);
                    const int kb = (row0 >> 4) + m + (fq >> 1), h = fq & 1, d = tt * 16 + fr;
                    st16_wt(rs, (unsigned)WS_V + ((head * 128 + kb) * 2 + h) * 512 + d * 16, (u32x4){r0[0], r1[0], r0[1], r1[1]});
                }
        }
    }
    asm volatile("s_waitcnt vmcnt(0)" ::: "memory");
    GM_BAR();
    if (threadIdx.x == 0) signal(p.ws, F_Q + pair * 32 + panel, p.tag);
}

using CfgZ = g32::Cfg<128, 224, X_RAW, g32::W_P448, 3>;
using CfgS = g32::Cfg<128, 224, X_RAW, g32::W_OWNT, 3>;
#define ONS 5
#define ONL 4
using CfgO = g32::Cfg<128, 128, X_OWN, g32::W_P256, ONS, ONL>;
__device__ __forceinline__ float gelu_tanh(float x) {
    const float t = x * x * (-0.044715f * 2.0f * 0.7978845608028654f * 1.4426950408889634f) + (-2.0f * 0.7978845608028654f * 1.4426950408889634f);
    return x * __builtin_amdgcn_rcpf(1.0f + __builtin_amdgcn_exp2f(x * t));
}
template <int N> __device__ __forceinline__ void gelu_pairs(unsigned* z) {
    constexpr float C1 = -0.044715f * 2.0f * 0.7978845608028654f * 1.4426950408889634f, C2 = -2.0f * 0.7978845608028654f * 1.4426950408889634f;
    f32x2 x[N], e[N];
#pragma unroll
    for (int i = 0; i < N; ++i) x[i] = (f32x2){bf_lo(z[i]), bf_hi(z[i])};
#pragma unroll
    for (int i = 0; i < N; ++i) e[i] = x[i] * (x[i] * x[i] * C1 + C2);
    GM_SB();
#pragma unroll
    for (int i = 0; i < N; ++i) { e[i].x = __builtin_amdgcn_exp2f(e[i].x); e[i].y = __builtin_amdgcn_exp2f(e[i].y); }
    GM_SB();
#pragma unroll
    for (int i = 0; i < N; ++i) e[i] = e[i] + 1.0f;
#pragma unroll
    for (int i = 0; i < N; ++i) { e[i].x = __builtin_amdgcn_rcpf(e[i].x); e[i].y = __builtin_amdgcn_rcpf(e[i].y); }
    GM_SB();
#pragma unroll
    for (int i = 0; i < N; ++i) { const f32x2 y = x[i] * e[i]; z[i] = pk_bf16(y.x, y.y); }
    GM_SB();
}
__device__ __forceinline__ void gelu16(f32x16& a) {
    constexpr float C1 = -0.044715f * 2.0f * 0.7978845608028654f * 1.4426950408889634f, C2 = -2.0f * 0.7978845608028654f * 1.4426950408889634f;
    f32x16 e = a * (a * a * C1 + C2);
    GM_SB();
#pragma unroll
    for (int i = 0; i < 16; ++i) e[i] = __builtin_amdgcn_exp2f(e[i]);
    GM_SB();
    e = e + 1.0f;
#pragma unroll
    for (int i = 0; i < 16; ++i) e[i] = __builtin_amdgcn_rcpf(e[i]);
    GM_SB();
    a = a * e;
}
template <int CTRL> __device__ __forceinline__ float dpp_add(float x) { return x + __int_as_float(__builtin_amdgcn_update_dpp(0, __float_as_int(x), CTRL, 0xf, 0xf, false)); }
__device__ __forceinline__ float red32_pair(float a, float b) {
    const auto r = __builtin_amdgcn_permlane16_swap(__float_as_uint(a), __float_as_uint(b), false, false);
    float x = __uint_as_float(r[0]) + __uint_as_float(r[1]);
    x = dpp_add<0x128>(x); x = dpp_add<0x124>(x); x = dpp_add<0x122>(x); x = dpp_add<0x121>(x);
    return x;
}
constexpr int LDS_TAB = CfgZ::LDS;
template <int NT> struct GeluFill {
    static constexpr int NSLOT = 4 * NT, STRIDE = NSLOT / 4;
    unsigned (&z)[NT][8];
    f32x2 x, e;
    template <int S> __device__ __forceinline__ void chunk() {
        constexpr float C1 = -0.044715f * 2.0f * 0.7978845608028654f * 1.4426950408889634f, C2 = -2.0f * 0.7978845608028654f * 1.4426950408889634f;
        if constexpr (S % STRIDE == 0) { constexpr int pa = S / STRIDE;
            x = (f32x2){bf_lo(z[0][pa]), bf_hi(z[0][pa])};
            const f32x2 w = x * (x * x * C1 + C2);
            e.x = __builtin_amdgcn_exp2f(w.x); e.y = __builtin_amdgcn_exp2f(w.y);
#pragma unroll
            for (int g = 0; g + 1 < 2 * NT; ++g) z[g >> 1][(g & 1) * 4 + pa] = z[(g + 1) >> 1][((g + 1) & 1) * 4 + pa];
            asm volatile("" : "+v"(x), "+v"(e));
        }
        if constexpr (S % STRIDE == 2) { constexpr int pb = S / STRIDE;
            const f32x2 d = e + 1.0f;
            const f32x2 y = x * (f32x2){__builtin_amdgcn_rcpf(d.x), __builtin_amdgcn_rcpf(d.y)};
            z[NT - 1][4 + pb] = pk_bf16(y.x, y.y);
            asm volatile("" : "+v"(z[NT - 1][4 + pb]));
        }
    }
};
template <int NT, bool LD> struct LnHook {
    const Ctx& p; ldsp lds; int panel, j, wid, lane, n0; unsigned (&z2)[NT][8];
    u32x4 w[4];
    __device__ __forceinline__ void normalise(int tt) {
        LAS f32x2* R = (LAS f32x2*)(lds + LDS_TAB + 2048);
        const int r = lane & 31, h = lane >> 5, m0 = (wid & 3) * 32;
        const rsrc_t rs = ws_rsrc(p.ws);
        const int srow = panel * 128 + m0;
#pragma unroll
        for (int i = 0; i < 4; i += 2) {
            const f32x4 ra = *(const LAS f32x4*)(R + m0 + 8 * i + 4 * h), rb = *(const LAS f32x4*)(R + m0 + 8 * i + 4 * h + 2);
            const f32x4 rc = *(const LAS f32x4*)(R + m0 + 8 * i + 8 + 4 * h), rd = *(const LAS f32x4*)(R + m0 + 8 * i + 8 + 4 * h + 2);
            const int k = srow + 8 * (i + h);
            const unsigned base = (unsigned)WS_Z + ((j * 32 + (k >> 6)) * 8 + ((k & 63) >> 3)) * 3584 + (n0 * 32 + r) * 16;
            const unsigned a0 = z2[tt][2 * i], a1 = z2[tt][2 * i + 1], b0 = z2[tt][2 * i + 2], b1 = z2[tt][2 * i + 3];
            st16_wt(rs, base + tt * 512, g32::pair32(pk_bf16(fmaf(bf_lo(a0), ra[1], ra[0]), fmaf(bf_hi(a0), ra[3], ra[2])), pk_bf16(fmaf(bf_lo(a1), rb[1], rb[0]), fmaf(bf_hi(a1), rb[3], rb[2])),
                                                      pk_bf16(fmaf(bf_lo(b0), rc[1], rc[0]), fmaf(bf_hi(b0), rc[3], rc[2])), pk_bf16(fmaf(bf_lo(b1), rd[1], rd[0]), fmaf(bf_hi(b1), rd[3], rd[2]))));
        }
    }
    __device__ __forceinline__ void normalise_at(int t) {
        if (t >= 5 && t < 9) { if (at_tile(t, 5)) normalise(0); if (at_tile(t, 6)) normalise(1); if (at_tile(t, 7)) normalise(2); if constexpr (NT == 4) { if (at_tile(t, 8)) normalise(3); } }
    }
    __device__ __forceinline__ void fetch() {
        const unsigned off = (unsigned)WS_STAT + ((panel * 8 + (lane >> 5) * 4) * 128 + (wid - 4) * 32 + (lane & 31)) * 16;
#pragma unroll
        for (int v = 0; v < 4; ++v) w[v] = ld16_dev(ws_rsrc(p.ws), off + v * 2048);
    }
    __device__ __forceinline__ bool fetched() const { bool ok = true;
#pragma unroll
        for (int v = 0; v < 4; ++v) ok = ok && w[v][1] == p.tag && w[v][3] == p.tag;
        return __all(ok); }
    __device__ __forceinline__ void top(int t) {
        if constexpr (LD) normalise_at(t);
        else { if (t == 4) fetch(); }
    }
    __device__ __forceinline__ void at(int t) {
        if (t < 4 || t > 11) return;
        if constexpr (!LD) {
            if (at_tile(t, 4)) {
                for (unsigned spins = 0; !fetched();) {
                    __builtin_amdgcn_s_sleep(2); fetch();
                    if (++spins > (1u << 19)) { __hip_atomic_store((gu32*)(p.ws + WS_CTL) + C_TMO, p.tag, RLX_AGENT); break; }
                }
                LAS f32x2* R = (LAS f32x2*)(lds + LDS_TAB + 2048);
                float ms = 0.f;
#pragma unroll
                for (int v = 0; v < 4; ++v) ms += __uint_as_float(w[v][0]);
                { const auto r = __builtin_amdgcn_permlane32_swap(__float_as_uint(ms), __float_as_uint(ms), false, false); ms = __uint_as_float(r[0]) + __uint_as_float(r[1]); }
                const float mean = ms * 0.125f; float q = 0.f;
#pragma unroll
                for (int v = 0; v < 4; ++v) { const float dm = __uint_as_float(w[v][0]) - mean; q += __uint_as_float(w[v][2]) + 224.0f * dm * dm; }
                { const auto r = __builtin_amdgcn_permlane32_swap(__float_as_uint(q), __float_as_uint(q), false, false); q = __uint_as_float(r[0]) + __uint_as_float(r[1]); }
                { const float rs_ = __builtin_amdgcn_rsqf(q * (1.0f / 1792.0f) + 1e-5f); if (lane < 32) R[(wid - 4) * 32 + lane] = (f32x2){-mean * rs_, rs_}; }
                GM_VM0();
            }
            normalise_at(t);
        }
        if (at_tile(t, 10)) asm volatile("s_waitcnt vmcnt(0)" ::: "memory");
        if constexpr (!LD) { if (at_tile(t, 11)) { if (threadIdx.x == 256) signal(p.ws, F_Z + j * 32 + panel, p.tag); } }
    }
};
struct SpHook {
    static constexpr bool PRE = false; const Ctx& p; ldsp lds; int panel, wid, lane;
    __device__ __forceinline__ void pre_x() const {}
    __device__ __forceinline__ void top(int) const {}
    __device__ __forceinline__ void at(int t) {
        if (t == 2 && (wid == 4 || wid == 5)) {
            poll_flags(p.ws, p.tag, F_Q, 1, true, F_Q, lane);
            const int row = (wid - 4) * 64 + lane, trow = panel * 128 + row;
            ((LAS f32x2*)(lds + LDS_TAB + 2048))[row] = (f32x2){__uint_as_float((unsigned)p.bsp[trow] << 16), 1.0f - __uint_as_float(__hip_atomic_load((gu32*)(p.ws + WS_GATE) + trow, RLX_AGENT))};
            GM_VM0();
        }
    }
};
struct WaitHook {
    unsigned char* ws; unsigned tag; int wid, lane, tlate, wl, nl;
    __device__ __forceinline__ void top(int) const {}
    __device__ __forceinline__ void at(int t) const { if (t == tlate && wid == 4) wait_flags(ws, tag, wl, nl, false, 0, lane); }
};
template <bool LD, bool FINAL> __device__ __forceinline__ void out_part(const Ctx& p, ldsp lds, int panel, int j, int wid, int lane) {
    using C = CfgO; using W = g32::Wv<2, 2, LD>;
    const int lid = wid & 3, r = lane & 31, h = lane >> 5;
    const int kh = wid >> 2, mo = ((wid >> 1) & 1) * 64, no = (wid & 1) * 2;
    Src<C> s;
    src_x_own<C>(s, p.ws + WS_ACT + (size_t)panel * 352 * 2048 + (FINAL ? 0 : (FH / 64 + 16 - NA) * C::XB), lid, lane);
    g32::src_w_raw<C>(s, FINAL ? p.wout + j * 128 : p.wo + (size_t)(16 - NA) * 64 * DM + j * 128, DM * 2, lid, lane);
    if (FINAL) set_w2<C>(s, p.wo + j * 128, FH / 64);
    f32x16 acc[2][2]; g32::acc_zero<W>(acc);
    WaitHook hk{p.ws, p.tag, wid, lane, FINAL && NA < 16 ? FH / 64 - C::NS : -1, F_O + (panel >> 2) * 32, 32};
    constexpr int NKT = FINAL ? FH / 64 + 16 - NA : NA;
    static_assert(NKT % 2 == 0 || true, "");
    g32::PipeKS<C, W> pipe(lds, NKT, kh, lid, mo, no, lane);
    pipe.issue_w(s);
    if (wid == 4) { if (FINAL) wait_flags(p.ws, p.tag, F_U + panel * 32, 8, false, 0, lane); else wait_flags(p.ws, p.tag, F_O + (panel >> 2) * 32, 32, false, 0, lane); }
    GM_BAR();
    pipe.issue_x(s);
    pipe.landed();
    STAMP(0, FINAL ? 10 : 7);
    pipe.run(s, acc, hk DBG_ARGS(4));
    STAMP(0, FINAL ? 9 : 8);
    ldsp mine = lds + wid * 8192 + lane * 16, theirs = lds + (wid ^ 4) * 8192 + lane * 16;
#pragma unroll
    for (int t = 0; t < 2; ++t)
#pragma unroll
        for (int i = 0; i < 4; ++i) { const f32x16& A = LD ? acc[t][1] : acc[t][0]; *(LAS f32x4*)(mine + (t * 4 + i) * 1024) = (f32x4){A[4 * i], A[4 * i + 1], A[4 * i + 2], A[4 * i + 3]}; }
    asm volatile("s_waitcnt lgkmcnt(0)" ::: "memory");
    if (FINAL && wid == 4) wait_flags(p.ws, p.tag, F_P + panel * 32 + j, 1, false, 0, lane);
    GM_BAR();
    const rsrc_t rs = ws_rsrc(p.ws);
    const unsigned pb = (unsigned)WS_P + ((panel * 8 + j) * 8 + wid) * 8192 + lane * 16;
    f32x16 o[2];
    u32x4 pv[8];
    if (FINAL) {
#pragma unroll
        for (int q = 0; q < 8; ++q) pv[q] = ld16_dev(rs, pb + q * 1024);
    }
#pragma unroll
    for (int t = 0; t < 2; ++t) { o[t] = LD ? acc[t][0] : acc[t][1];
#pragma unroll
        for (int i = 0; i < 4; ++i) { const f32x4 v = *(const LAS f32x4*)(theirs + (t * 4 + i) * 1024); o[t][4 * i] += v[0]; o[t][4 * i + 1] += v[1]; o[t][4 * i + 2] += v[2]; o[t][4 * i + 3] += v[3]; } }
    if (!FINAL) {
#pragma unroll
        for (int t = 0; t < 2; ++t)
#pragma unroll
            for (int i = 0; i < 4; ++i) st16_wt(rs, pb + (t * 4 + i) * 1024, (u32x4){__float_as_uint(o[t][4 * i]), __float_as_uint(o[t][4 * i + 1]), __float_as_uint(o[t][4 * i + 2]), __float_as_uint(o[t][4 * i + 3])});
        asm volatile("s_waitcnt vmcnt(0)" ::: "memory");
        GM_BAR();
        if (threadIdx.x == 0) signal(p.ws, F_P + panel * 32 + j, p.tag);
    } else {
#pragma unroll
        for (int t = 0; t < 2; ++t)
#pragma unroll
            for (int i = 0; i < 4; ++i)
#pragma unroll
                for (int e = 0; e < 4; ++e) o[t][4 * i + e] += __uint_as_float(pv[t * 4 + i][e]);
        ldsp st = lds + 65536;
#pragma unroll
        for (int t = 0; t < 2; ++t)
#pragma unroll
            for (int i = 0; i < 4; i += 2) { const f32x16& A = o[t];
                *(LAS u32x4*)(st + (mo + kh * 32 + r) * 272 + (no + t) * 64 + (i + h) * 16) =
                    g32::pair32(pk_bf16(A[4 * i], A[4 * i + 1]), pk_bf16(A[4 * i + 2], A[4 * i + 3]), pk_bf16(A[4 * i + 4], A[4 * i + 5]), pk_bf16(A[4 * i + 6], A[4 * i + 7])); }
        asm volatile("s_waitcnt lgkmcnt(0)" ::: "memory"); GM_BAR();
#pragma unroll
        for (int i = 0; i < 4; ++i) { const int row = wid * 16 + i * 4 + (lane >> 4);
            *(u32x4*)(p.out + (size_t)(panel * 128 + row) * DM + j * 128 + (lane & 15) * 8) = *(const LAS u32x4*)(st + row * 272 + (lane & 15) * 16); }
    }
}
template <bool LD> __device__ __forceinline__ void b_wave(const Ctx& p, ldsp lds, int panel, int j, int wid, int lane) {
    constexpr int NT = LD ? 3 : 4;
    const int lid = wid & 3, m0 = lid * 32, n0 = LD ? 0 : 3;
    const int r = lane & 31, h = lane >> 5;
    unsigned z2[NT][8], z1[NT][8];
    const unsigned pfs = (unsigned)(uintptr_t)lds + LDS_PFS;
    {
        using C = CfgZ; using W = g32::Wv<1, NT, LD>; Src<C> s;
        src_x_raw<C>(s, p.ax + (size_t)panel * 128 * DM, DM * 2, lid, lane);
        g32::src_w_raw<C>(s, p.win + FH + j * 224, 2 * FH * 2, lid, lane);
        set_w2<C>(s, p.win + j * 224, DM / 64);
        g32::Pipe<C, W, 1> pipe(lds, s, 2 * DM / 64, DM / 64, lid, m0, n0, lane);
        f32x16 acc[NT][1]; g32::acc_zero<W>(acc);
        STAMP(0, 1);
        Warm wz1{(const char*)p.win + ((j * 224 * 2) & ~127) + (size_t)(panel & 3) * 256 * (2 * FH * 2), 2 * FH * 2, 2, 13, pfs};
        Warm none{nullptr, 0, 0, 0, 0};
        SigHook hk{wid, lane, wz1, none, wz1.mask(1, wid)};
        pipe.begin(s);
        pipe.template run<false>(s, 0, DM / 64, acc, hk DBG_ARGS(1));
        STAMP(0, 2);
        {
            float sm[16], sq[16];
#pragma unroll
            for (int t = 0; t < NT; ++t) {
                gelu16(acc[t][0]);
#pragma unroll
                for (int q = 0; q < 16; ++q) { const float v = acc[t][0][q]; sm[q] = t ? sm[q] + v : v; sq[q] = t ? sq[q] + v * v : v * v; }
#pragma unroll
                for (int q = 0; q < 16; q += 2) z2[t][q >> 1] = pk_bf16(acc[t][0][q], acc[t][0][q + 1]);
                acc[t][0] = (f32x16){};
            }
            float ra[16];
#pragma unroll
            for (int k = 0; k < 8; ++k) {
                const auto a = __builtin_amdgcn_permlane16_swap(__float_as_uint(sm[2 * k]), __float_as_uint(sm[2 * k + 1]), false, false); ra[k] = __uint_as_float(a[0]) + __uint_as_float(a[1]);
                const auto b2 = __builtin_amdgcn_permlane16_swap(__float_as_uint(sq[2 * k]), __float_as_uint(sq[2 * k + 1]), false, false); ra[8 + k] = __uint_as_float(b2[0]) + __uint_as_float(b2[1]); }
#pragma unroll
            for (int k = 0; k < 16; ++k) ra[k] = dpp_add<0x128>(ra[k]);
#pragma unroll
            for (int k = 0; k < 16; ++k) ra[k] = dpp_add<0x124>(ra[k]);
#pragma unroll
            for (int k = 0; k < 16; ++k) ra[k] = dpp_add<0x122>(ra[k]);
#pragma unroll
            for (int k = 0; k < 16; ++k) ra[k] = dpp_add<0x121>(ra[k]);
            float va = ra[0], vb = ra[8];
#pragma unroll
            for (int k = 1; k < 8; ++k) { const bool sel = (lane & 7) == k; va = sel ? ra[k] : va; vb = sel ? ra[8 + k] : vb; }
            LAS f32x2* P = (LAS f32x2*)(lds + LDS_TAB);
            { const int q = 2 * (lane & 7) + ((lane >> 4) & 1), row = m0 + 8 * (q >> 2) + 4 * h + (q & 3);
              if ((lane & 8) == 0) P[row * 2 + (LD ? 0 : 1)] = (f32x2){va, vb}; }
            asm volatile("s_waitcnt lgkmcnt(0)" ::: "memory"); GM_BAR();
            if constexpr (!LD) {
                if (lane < 32) {
                    const int row = lid * 32 + lane;
                    const f32x2 a = P[row * 2], b = P[row * 2 + 1];
                    const float su = a.x + b.x, mt = su * (1.0f / 224.0f), m2 = fmaxf(a.y + b.y - su * mt, 0.f);
                    st16_wt(ws_rsrc(p.ws), (unsigned)WS_STAT + ((panel * 8 + j) * 128 + row) * 16, (u32x4){__float_as_uint(mt), p.tag, __float_as_uint(m2), p.tag});
                }
            }
        }
        STAMP(0, 3);
        LnHook<NT, LD> hl{p, lds, panel, j, wid, lane, n0, z2};
        pipe.template run<true>(s, DM / 64, 2 * DM / 64, acc, hl DBG_ARGS(2));
        pipe.end();
        STAMP(0, 4);
#pragma unroll
        for (int t = 0; t < NT; ++t)
#pragma unroll
            for (int q = 0; q < 16; q += 2) z1[t][q >> 1] = pk_bf16(acc[t][0][q], acc[t][0][q + 1]);
    }
    {
        using C = CfgS; using W = g32::Wv<1, NT, LD>; Src<C> s;
        src_x_raw<C>(s, p.wsp + (size_t)panel * 128 * SEQ, SEQ * 2, lid, lane);
        src_w_own<C>(s, p.ws + WS_Z + (size_t)j * 32 * 28672, lid, lane);
        g32::Pipe<C, W, SPD> pipe(lds, s, SEQ / 64, SEQ / 64, lid, m0, n0, lane);
        pipe.issue_x(s);
        if (wid == 4) wait_flags(p.ws, p.tag, F_Z + j * 32, 16, false, 0, lane);
        GM_BAR();
        pipe.issue_w(s);
        STAMP(0, 5);
#pragma unroll
        for (int t = 0; t < NT; ++t) { gelu_pairs<8>(z1[t]);
#pragma unroll
            for (int q = 0; q < 8; ++q) asm volatile("" : "+v"(z1[t][q])); }
        STAMP(0, 6);
        pipe.landed();
        f32x16 acc[NT][1]; g32::acc_zero<W>(acc);
        SpHook hk{p, lds, panel, wid, lane};
        pipe.template run<true>(s, 0, SEQ / 64, acc, hk DBG_ARGS(3));
        pipe.end();
        STAMP(0, 7);
        const rsrc_t rs = ws_rsrc(p.ws);
        const f32x2 bg = ((const LAS f32x2*)(lds + LDS_TAB + 2048))[m0 + r]; const float b = bg.x, g1 = bg.y;
        const unsigned ub = (unsigned)WS_ACT + (panel * 352 + j * 28 + n0 * 4 + h) * 2048 + (m0 + r) * 16;
#pragma unroll
        for (int t = 0; t < NT; ++t)
#pragma unroll
            for (int i = 0; i < 4; i += 2) {
                unsigned w4[4];
#pragma unroll
                for (int e = 0; e < 4; ++e) { const int q = 4 * i + 2 * e; const unsigned z = z1[t][q >> 1];
                    w4[e] = pk_bf16((acc[t][0][q] + b) * g1 * bf_lo(z), (acc[t][0][q + 1] + b) * g1 * bf_hi(z)); }
                st16_wt(rs, ub + (t * 4 + i) * 2048, g32::pair32(w4[0], w4[1], w4[2], w4[3]));
            }
        asm volatile("s_waitcnt vmcnt(0)" ::: "memory");
        GM_BAR();
        if (threadIdx.x == 256) signal(p.ws, F_U + panel * 32 + j, p.tag);
    }
    STAMP(0, 8);
    out_part<LD, true>(p, lds, panel, j, wid, lane);
}

constexpr int AT_STAGE = 16384, AT_NS = 4;
#define MFMA32(a, b, c) __builtin_amdgcn_mfma_f32_32x32x16_bf16(a, b, c, 0, 0, 0)
#define MFMA16(a, b, c) __builtin_amdgcn_mfma_f32_16x16x32_bf16(a, b, c, 0, 0, 0)
#define PIN(x) asm volatile("" : "+v"(x))
__device__ __forceinline__ void attn_wg(const Ctx& p, ldsp lds, int head, int qb, int x, int a, int wid, int lane) {
    if (wid == 4) wait_flags(p.ws, p.tag, F_Q + (head >> 1) * 32, 8, false, 0, lane);
    GM_BAR();
    STAMP(0, 3);
    const unsigned char* Kg = p.ws + WS_K + (size_t)head * 131072; const unsigned char* Vg = p.ws + WS_V + (size_t)head * 131072;
    const unsigned ldb = (unsigned)(uintptr_t)lds;
    const unsigned voff = wid * 1024 + lane * 16;
    const char* kb = (const char*)Kg; const char* vb = (const char*)Vg;
    auto stage = [&](int st, int slot) {
        asm volatile("s_add_u32 m0, %2, 0\n\ts_nop 0\n\tglobal_load_lds_dwordx4 %0, %1 sc1" ::"v"(voff), "s"(kb + (size_t)st * 8192), "s"(ldb + slot * AT_STAGE + wid * 1024) : "memory", "scc");
        asm volatile("s_add_u32 m0, %2, 0\n\ts_nop 0\n\tglobal_load_lds_dwordx4 %0, %1 sc1" ::"v"(voff), "s"(vb + (size_t)st * 8192), "s"(ldb + slot * AT_STAGE + 8192 + wid * 1024) : "memory", "scc");
    };
    stage(0, 0); stage(1, 1); stage(2, 2);
    const int q0 = qb * 512 + wid * 64;
    bf16x8 qf[2][2];
#pragma unroll
    for (int a = 0; a < 2; ++a)
#pragma unroll
        for (int d = 0; d < 2; ++d) qf[a][d] = __builtin_bit_cast(bf16x8, ld16_dev(ws_rsrc(p.ws), (unsigned)WS_Q + (head * 64 + (q0 >> 5) + a) * 2048 + d * 1024 + lane * 16));
    bf16x8 sel;
    { const short one = ((lane & 15) == ((lane >> 4) & 1)) ? (short)0x3f80 : (short)0;
#pragma unroll
      for (int i = 0; i < 8; ++i) sel[i] = one; }
    asm volatile("s_waitcnt vmcnt(0)" ::: "memory");
    GM_BAR();
    f32x16 o[2], negm[2]; f32x4 l[2];
    const f32x16 zero16 = {};
    bf16x8 kA[2], kB[2], vA[2], vB[2];
    ldsp lb = lds + lane * 16;
    kA[0] = *(const LAS bf16x8*)(lb); kA[1] = *(const LAS bf16x8*)(lb + 1024);
#pragma unroll
    for (int a = 0; a < 2; ++a) {
        f32x16 s = MFMA32(kA[0], qf[a][0], zero16); s = MFMA32(kA[1], qf[a][1], s);
        float m = fmaxf(fmaxf(s[0], s[1]), fmaxf(s[2], s[3]));
#pragma unroll
        for (int r = 4; r < 16; r += 2) m = fmaxf(m, fmaxf(s[r], s[r + 1]));
        const auto rr = __builtin_amdgcn_permlane32_swap(__float_as_uint(m), __float_as_uint(m), false, false);
        m = -fmaxf(__uint_as_float(rr[0]), __uint_as_float(rr[1]));
#pragma unroll
        for (int r = 0; r < 16; ++r) negm[a][r] = m;
        PIN(negm[a]);
        o[a] = zero16; l[a] = (f32x4){0.f, 0.f, 0.f, 0.f};
    }
    f32x16 S0, S1; u32x4 p0l, p0h, p1l, p1h;
    S0 = MFMA32(kA[0], qf[0][0], negm[0]); S0 = MFMA32(kA[1], qf[0][1], S0);
    p1l = (u32x4){0u, 0u, 0u, 0u}; p1h = p1l;
    vB[0] = (bf16x8){0, 0, 0, 0, 0, 0, 0, 0}; vB[1] = vB[0];
    STAMP(0, 4);
#define EX4(S, i) do { S[i] = __builtin_amdgcn_exp2f(S[i]); S[i + 1] = __builtin_amdgcn_exp2f(S[i + 1]); S[i + 2] = __builtin_amdgcn_exp2f(S[i + 2]); S[i + 3] = __builtin_amdgcn_exp2f(S[i + 3]); } while (0)
#define PK4(P, S, i) do { P[0] = pk_bf16(S[i], S[i + 1]); P[1] = pk_bf16(S[i + 2], S[i + 3]); P[2] = pk_bf16(S[i + 4], S[i + 5]); P[3] = pk_bf16(S[i + 6], S[i + 7]); } while (0)
#define BC(x) __builtin_bit_cast(bf16x8, x)
#define ASTEP(SC, SN, PCl, PCh, PPl, PPh, KF, QN, VF, AP, LOADS) do { \
        SN = MFMA32(KF[0], qf[QN][0], negm[QN]); EX4(SC, 0); PIN(SC); GM_SB(); \
        SN = MFMA32(KF[1], qf[QN][1], SN); EX4(SC, 4); LOADS; PIN(SC); GM_SB(); \
        o[AP] = MFMA32(VF[0], BC(PPl), o[AP]); EX4(SC, 8); PIN(SC); GM_SB(); \
        o[AP] = MFMA32(VF[1], BC(PPh), o[AP]); EX4(SC, 12); PIN(SC); GM_SB(); \
        l[AP] = MFMA16(sel, BC(PPl), l[AP]); PK4(PCl, SC, 0); PIN(PCl); GM_SB(); \
        l[AP] = MFMA16(sel, BC(PPh), l[AP]); PK4(PCh, SC, 8); PIN(PCh); GM_SB(); } while (0)
#define LDK(KF, base, kt) do { KF[0] = *(const LAS bf16x8*)((base) + (kt) * 2048); KF[1] = *(const LAS bf16x8*)((base) + (kt) * 2048 + 1024); } while (0)
#define LDV(VF, base, kt) do { VF[0] = *(const LAS bf16x8*)((base) + 8192 + (kt) * 2048); VF[1] = *(const LAS bf16x8*)((base) + 8192 + (kt) * 2048 + 1024); } while (0)
    float gval = 0.f;
    for (int st = 0; st < 16; ++st) {
        ldsp cb = lb + (st & 3) * AT_STAGE, nb = lb + ((st + 1) & 3) * AT_STAGE;
        if (st < 5) {
            const int n = st * 8 + wid; const unsigned pfs = (unsigned)(uintptr_t)lds + LDS_PFS;
            const int p0 = 8 * (x & 1) + 4 * ((x >> 1) & 1), j0 = 4 * (x >> 2);
            if (n < 16) pf_lines(p.wsp + (size_t)(p0 * 128 + a * 32) * SEQ, (unsigned)(lane & 31) * (SEQ * 2) + (unsigned)(2 * n + (lane >> 5)) * 128u, pfs);
            else if (n < 30) pf_lines(p.wout + (size_t)(a * 112) * DM + j0 * 128, (unsigned)((n - 16) * 8 + (lane >> 3)) * (DM * 2) + (unsigned)(lane & 7) * 128u, pfs);
            else if (n < 38) pf_lines(p.wo + (size_t)(a * 64) * DM + j0 * 128, (unsigned)((n - 30) * 8 + (lane >> 3)) * (DM * 2) + (unsigned)(lane & 7) * 128u, pfs);
        }
        if (st == 8) {
            poll_flags(p.ws, p.tag, F_Q, 1, true, F_Q, lane);
            gval = __uint_as_float(__hip_atomic_load((gu32*)(p.ws + WS_GATE) + q0 + lane, RLX_AGENT));
        }
        ASTEP(S0, S1, p0l, p0h, p1l, p1h, kA, 1, vB, 1, { LDK(kB, cb, 1); LDV(vA, cb, 0); });
        ASTEP(S1, S0, p1l, p1h, p0l, p0h, kB, 0, vA, 0, {});
        ASTEP(S0, S1, p0l, p0h, p1l, p1h, kB, 1, vA, 1, { LDK(kA, cb, 2); LDV(vB, cb, 1); });
        ASTEP(S1, S0, p1l, p1h, p0l, p0h, kA, 0, vB, 0, {});
        ASTEP(S0, S1, p0l, p0h, p1l, p1h, kA, 1, vB, 1, { LDK(kB, cb, 3); LDV(vA, cb, 2); });
        ASTEP(S1, S0, p1l, p1h, p0l, p0h, kB, 0, vA, 0, {});
        if (st + 1 < 16) {
            if (st + 2 < 16) asm volatile("s_waitcnt vmcnt(2)" ::: "memory"); else asm volatile("s_waitcnt vmcnt(0)" ::: "memory");
            GM_BAR();
            if (st + 3 < 16) stage(st + 3, (st + 3) & 3);
        }
        ASTEP(S0, S1, p0l, p0h, p1l, p1h, kB, 1, vA, 1, { LDK(kA, nb, 0); LDV(vB, cb, 3); });
        ASTEP(S1, S0, p1l, p1h, p0l, p0h, kA, 0, vB, 0, {});
    }
    o[1] = MFMA32(vB[0], BC(p1l), o[1]); o[1] = MFMA32(vB[1], BC(p1h), o[1]);
    l[1] = MFMA16(sel, BC(p1l), l[1]); l[1] = MFMA16(sel, BC(p1h), l[1]);
#undef EX4
#undef PK4
#undef ASTEP
#undef LDK
#undef LDV
    STAMP(0, 5);
    const rsrc_t rs = ws_rsrc(p.ws);
    const float gt0 = __shfl(gval, lane & 31), gt1 = __shfl(gval, 32 + (lane & 31));
#pragma unroll
    for (int a = 0; a < 2; ++a) {
        const int q = lane & 31, hh = lane >> 5;
        const float l0 = __shfl(l[a][0], q & 15), l1 = __shfl(l[a][1], q & 15);
        const float lsum = (q < 16) ? l0 : l1;
        const float sc = (a ? gt1 : gt0) * __builtin_amdgcn_rcpf(lsum);
        const int row = q0 + a * 32 + q, panel = row >> 7;
#pragma unroll
        for (int i = 0; i < 4; i += 2) {
            const unsigned a0 = pk_bf16(o[a][4 * i] * sc, o[a][4 * i + 1] * sc), a1 = pk_bf16(o[a][4 * i + 2] * sc, o[a][4 * i + 3] * sc);
            const unsigned b0 = pk_bf16(o[a][4 * i + 4] * sc, o[a][4 * i + 5] * sc), b1 = pk_bf16(o[a][4 * i + 6] * sc, o[a][4 * i + 7] * sc);
            const auto r0 = __builtin_amdgcn_permlane32_swap(a0, b0, false, false); const auto r1 = __builtin_amdgcn_permlane32_swap(a1, b1, false, false);
            const int c = 224 + head * 4 + i + hh;
            st16_wt(rs, (unsigned)WS_ACT + (panel * 352 + c) * 2048 + (row & 127) * 16, (u32x4){r0[0], r1[0], r0[1], r1[1]});
        }
    }
    asm volatile("s_waitcnt vmcnt(0)" ::: "memory");
    GM_BAR();
    if (threadIdx.x == 0) signal(p.ws, F_O + qb * 32 + head, p.tag);
}

__device__ __forceinline__ void gate_rows4(const Ctx& p, int row0, int lane) {
    u32x4 g[2], xr[4][2];
#pragma unroll
    for (int i = 0; i < 2; ++i) g[i] = ((const u32x4*)p.gw)[lane * 2 + i];
#pragma unroll
    for (int r = 0; r < 4; ++r)
#pragma unroll
        for (int i = 0; i < 2; ++i) xr[r][i] = ((const u32x4*)(p.ax + (size_t)(row0 + r) * DM))[lane * 2 + i];
    float s[4];
#pragma unroll
    for (int r = 0; r < 4; ++r) { float a = 0.f, b = 0.f;
#pragma unroll
        for (int i = 0; i < 2; ++i)
#pragma unroll
            for (int e = 0; e < 4; ++e) { a += bf_lo(xr[r][i][e]) * bf_lo(g[i][e]); b += bf_hi(xr[r][i][e]) * bf_hi(g[i][e]); }
        s[r] = a + b; }
#pragma unroll
    for (int r = 0; r < 4; ++r) s[r] = dpp_add<0x128>(s[r]);
#pragma unroll
    for (int r = 0; r < 4; ++r) s[r] = dpp_add<0x124>(s[r]);
#pragma unroll
    for (int r = 0; r < 4; ++r) s[r] = dpp_add<0x122>(s[r]);
#pragma unroll
    for (int r = 0; r < 4; ++r) s[r] = dpp_add<0x121>(s[r]);
#pragma unroll
    for (int r = 0; r < 4; ++r) {
        const auto a = __builtin_amdgcn_permlane16_swap(__float_as_uint(s[r]), __float_as_uint(s[r]), false, false); s[r] = __uint_as_float(a[0]) + __uint_as_float(a[1]);
        const auto b = __builtin_amdgcn_permlane32_swap(__float_as_uint(s[r]), __float_as_uint(s[r]), false, false); s[r] = __uint_as_float(b[0]) + __uint_as_float(b[1]); }
    const float v = lane == 0 ? s[0] : lane == 1 ? s[1] : lane == 2 ? s[2] : s[3];
    if (lane < 4) __hip_atomic_store((gu32*)(p.ws + WS_GATE) + row0 + lane, __float_as_uint(__builtin_amdgcn_rcpf(1.0f + __builtin_amdgcn_exp2f(-1.4426950408889634f * v))), RLX_AGENT);
}
constexpr int LDS_BYTES = CfgO::LDS > CfgQ::LDS + 256 ? CfgO::LDS : CfgQ::LDS + 256;
static_assert(LDS_TAB + 3072 <= LDS_BYTES && CfgO::LDS <= LDS_BYTES && AT_NS * AT_STAGE <= LDS_BYTES, "LDS map");
__global__ __launch_bounds__(512, 2) void fused(Params pp) {
    extern __shared__ __attribute__((aligned(16))) unsigned char smem[]; ldsp lds = (ldsp)smem;
    const int lane = threadIdx.x & 63, wid = __builtin_amdgcn_readfirstlane(threadIdx.x >> 6);
    const int b = blockIdx.x, x = b & 7, role = (b >> 3) & 1, idx = b >> 4;
    Ctx p; static_cast<Params&>(p) = pp;
    p.tag = (unsigned)__builtin_amdgcn_readfirstlane(__hip_atomic_load((gu32*)(pp.ws + WS_CTL) + C_EPOCH, RLX_AGENT)) + 1u;
    STAMP(0, 0);
    if (role == 0) {
        const int pair = 4 * (x >> 1) + (idx >> 2), panel = 4 * (x & 1) + (idx & 3);
        if (wid >= 4) gate_rows4(p, panel * 256 + pair * 16 + (wid - 4) * 4, lane);
        if (wid < 4) qkv_wave<0>(p, lds, pair, panel, wid, lane); else qkv_wave<1>(p, lds, pair, panel, wid, lane);
        STAMP(0, 6);
        attn_wg(p, lds, 4 * x + (idx >> 2), idx & 3, x, idx, wid, lane);
        STAMP(0, 9);
        {   const int opanel = 8 * (x & 1) + 4 * ((x >> 1) & 1) + (idx >> 2), oj = 4 * (x >> 2) + (idx & 3);
            if (wid < 4) out_part<true, false>(p, lds, opanel, oj, wid, lane); else out_part<false, false>(p, lds, opanel, oj, wid, lane); }
    } else {
        const int panel = 8 * (x & 1) + 4 * ((x >> 1) & 1) + (idx >> 2), j = 4 * (x >> 2) + (idx & 3);
        if (wid < 4) b_wave<true>(p, lds, panel, j, wid, lane); else b_wave<false>(p, lds, panel, j, wid, lane);
        if (panel == 0 && j == 0 && threadIdx.x == 0) __hip_atomic_store((gu32*)(pp.ws + WS_CTL) + C_EPOCH, p.tag, RLX_AGENT);
    }
    STAMP(0, 15);
}
}

extern "C" void kernel_launch(void* const* d_in, const int* in_sizes, int n_in, void* d_out, int out_size, void* d_ws, size_t ws_size, hipStream_t stream) {
    using namespace gm;
    Params p;
    p.ax = (const u16*)d_in[0]; p.wqkv = (const u16*)d_in[1]; p.wo = (const u16*)d_in[2]; p.bx = (const u16*)d_in[3]; p.win = (const u16*)d_in[4];
    p.wsp = (const u16*)d_in[5]; p.bsp = (const u16*)d_in[6]; p.wout = (const u16*)d_in[7]; p.gw = (const u16*)d_in[8];
    p.out = (u16*)d_out; p.ws = (unsigned char*)d_ws;
    (void)hipFuncSetAttribute((const void*)fused, hipFuncAttributeMaxDynamicSharedMemorySize, LDS_BYTES);
    fused<<<256, 512, LDS_BYTES, stream>>>(p);
}
```

```cpp
#include <hip/hip_runtime.h>
#include <hip/hip_bf16.h>
#include <stdint.h>

#define LAS __attribute__((address_space(3)))
typedef short bf16x8 __attribute__((ext_vector_type(8)));
typedef short s16x4 __attribute__((ext_vector_type(4)));
typedef float f32x2 __attribute__((ext_vector_type(2)));
typedef float f32x4 __attribute__((ext_vector_type(4)));
typedef float f32x16 __attribute__((ext_vector_type(16)));
typedef unsigned u32x4 __attribute__((ext_vector_type(4)));
typedef unsigned u32x2 __attribute__((ext_vector_type(2)));
typedef LAS unsigned char* ldsp;
typedef unsigned short u16;

#define OUT_WT
#define NA 16
#define SPD 1
namespace gm {
constexpr int SEQ = 2048, DM = 1024, NH = 32, FH = 1792;
constexpr size_t WS_CTL = 0;
constexpr size_t CTL_BYTES = 16384;
constexpr size_t WS_GATE = 16384;
constexpr size_t WS_STAT = 32768;
constexpr size_t WS_Q = 1u << 20;
constexpr size_t WS_K = WS_Q + (4u << 20);
constexpr size_t WS_V = WS_K + (4u << 20);
constexpr size_t WS_Z = WS_V + (4u << 20);
constexpr size_t WS_ACT = WS_Z + (8u << 20);
constexpr int LDS_PFS = 147456 - 0;
constexpr size_t WS_P = WS_ACT + (12u << 20);
constexpr float QSCALE = 0.17677669529663687f * 1.4426950408889634f;

#define TRACE(t) do {} while (0)
#define DBG_PARAMS
#define DBG_ARGS(id)
#define DBG_FWD
#define STAMP(k, i) do {} while (0)
constexpr int C_EPOCH = 0, C_TMO = 64, F_Q = 128  , F_S = F_Q + 512  , F_Z = F_S + 512  ,
              F_U = F_Z + 256  , F_O = F_U + 512  , F_P = F_O + 128  , C_END = F_P + 512;
static_assert(C_END * 4 <= (int)CTL_BYTES, "control block");
#define GM_VM0() __builtin_amdgcn_s_waitcnt(0x0F70)
#define RLX_AGENT __ATOMIC_RELAXED, __HIP_MEMORY_SCOPE_AGENT
typedef __attribute__((address_space(1))) unsigned gu32;
typedef __attribute__((address_space(1))) unsigned long long gu64;
typedef __amdgpu_buffer_rsrc_t rsrc_t;
__device__ __forceinline__ rsrc_t ws_rsrc(unsigned char* ws) { return __builtin_amdgcn_make_buffer_rsrc(ws, 0, 64u << 20, 0x00020000); }
__device__ __forceinline__ void st16_wt(rsrc_t r, unsigned off, u32x4 v) { __builtin_amdgcn_raw_buffer_store_b128(v, r, off, 0, 16); }
__device__ __forceinline__ void signal(unsigned char* ws, int word, unsigned tag) { __hip_atomic_store((gu32*)(ws + WS_CTL) + word, tag, RLX_AGENT); }
__device__ __forceinline__ void poll_flags(unsigned char* ws, unsigned tag, int w0, int n, bool all128, int wq, int lane) {
    gu32* ctl = (gu32*)(ws + WS_CTL);
    for (unsigned spins = 0;;) {
        bool ok = __hip_atomic_load(ctl + w0 + (lane < n ? lane : 0), RLX_AGENT) == tag;
        if (all128) { const unsigned q0 = __hip_atomic_load(ctl + wq + (lane >> 3) * 32 + (lane & 7), RLX_AGENT), q1 = __hip_atomic_load(ctl + wq + 256 + (lane >> 3) * 32 + (lane & 7), RLX_AGENT); ok = ok && q0 == tag && q1 == tag; }
        if (__all(ok)) break;
        __builtin_amdgcn_s_sleep(2);
        if (++spins > (1u << 19)) { __hip_atomic_store(ctl + C_TMO, tag, RLX_AGENT); break; }
        if ((spins & 255u) == 0u && __builtin_amdgcn_readfirstlane(__hip_atomic_load(ctl + C_TMO, RLX_AGENT)) == tag) break;
    }
}
__device__ __forceinline__ void wait_flags(unsigned char* ws, unsigned tag, int w0, int n, bool all128, int wq, int lane) {
    poll_flags(ws, tag, w0, n, all128, wq, lane);
    GM_VM0();
}
__device__ __forceinline__ u32x4 ld16_dev(rsrc_t r, unsigned off) { return __builtin_amdgcn_raw_buffer_load_b128(r, off, 0, 16); }
enum { X_RAW = 0, X_OWN = 1 };
enum { W_P384 = 0, W_P448 = 1, W_P256 = 2, W_OWN = 3 };

__device__ __forceinline__ unsigned pk_bf16(float lo, float hi) {
    typedef __bf16 bf16x2_t __attribute__((ext_vector_type(2)));
    f32x2 v = {lo, hi}; bf16x2_t b = __builtin_convertvector(v, bf16x2_t); return __builtin_bit_cast(unsigned, b);
}
__device__ __forceinline__ float bf_lo(unsigned w) { return __uint_as_float(w << 16); }
__device__ __forceinline__ float bf_hi(unsigned w) { return __uint_as_float(w & 0xffff0000u); }
__device__ __forceinline__ s16x4 tr_read(ldsp p) { typedef short v4s __attribute__((ext_vector_type(4))); return __builtin_amdgcn_ds_read_tr16_b64_v4i16((LAS v4s*)p); }
template <int I, bool SC = false> __device__ __forceinline__ void glds_run(const char* sbase, unsigned voff) {
    if constexpr (SC) asm volatile("global_load_lds_dwordx4 %0, %1 offset:%2 sc1" ::"v"(voff), "s"(sbase), "n"((I & 3) * 1024) : "memory");
    else asm volatile("global_load_lds_dwordx4 %0, %1 offset:%2" ::"v"(voff), "s"(sbase), "n"((I & 3) * 1024) : "memory");
}
template <int I, bool SC = false> __device__ __forceinline__ void glds_set(const char* sbase, unsigned voff, unsigned dst) {
    if constexpr (SC) asm volatile("s_add_u32 m0, %2, %3\n\ts_nop 0\n\tglobal_load_lds_dwordx4 %0, %1 offset:%4 sc1" ::"v"(voff), "s"(sbase), "s"(dst), "n"((I & ~3) * 1024), "n"((I & 3) * 1024) : "memory", "scc");
    else asm volatile("s_add_u32 m0, %2, %3\n\ts_nop 0\n\tglobal_load_lds_dwordx4 %0, %1 offset:%4" ::"v"(voff), "s"(sbase), "s"(dst), "n"((I & ~3) * 1024), "n"((I & 3) * 1024) : "memory", "scc");
}
__device__ __forceinline__ void pf_lines(const void* base, unsigned voff, unsigned ldsdst) {
    asm volatile("s_mov_b32 m0, %2\n\ts_nop 0\n\tglobal_load_lds_dword %0, %1" ::"v"(voff), "s"(base), "s"(ldsdst) : "memory");
}
#define GM_BAR() do { asm volatile("" ::: "memory"); __builtin_amdgcn_s_barrier(); asm volatile("" ::: "memory"); } while (0)
#define GM_SB() __builtin_amdgcn_sched_barrier(0)
__device__ __forceinline__ bool at_tile(int t, int k) { asm volatile("" : "+s"(t)); return t == k; }

template <int BM_, int BN_, int XI_, int WI_, int NSX_, int NSW_> struct Cfg {
    static constexpr int BM = BM_, BN = BN_, XI = XI_, WI = WI_, NSX = NSX_, NSW = NSW_;
    static constexpr int XB = BM * 128, WB = BN * 128, WOFF = NSX * XB, LDS = NSX * XB + NSW * WB;
    static constexpr int PX = BM / 32, PW = BN / 32;
    static constexpr int WPITCH = (WI == W_P384) ? 384 : (WI == W_P448) ? 448 : (WI == W_P256) ? 256 : 0;
    static constexpr bool XSC = false, WSC = false;
};
template <class C> struct Src { const char* x; const char* w; const char* w2; int ksplit; unsigned xstep, wstep; unsigned xv[C::PX]; unsigned wv[C::PW]; };
template <int WI> __device__ __forceinline__ int w_swz(int r, int x) {
    if (WI == W_P384) return x ^ (((((r >> 1) & 1) | (((r >> 3) & 1) << 1))) << 5);
    if (WI == W_P448) return x ^ (((r >> 3) & 1) << 5);
    if (WI == W_P256) return x ^ (((r & 3) | (((r >> 3) & 1) << 2)) << 5);
    return x;
}
template <class C> __device__ __forceinline__ const char* w_tile(const Src<C>& s, int kt) { return (kt < s.ksplit ? s.w : s.w2) + (unsigned)kt * s.wstep; }
template <class C> __device__ __forceinline__ void set_w2(Src<C>& s, const void* base, int ksplit) { s.ksplit = ksplit; s.w2 = (const char*)base - 4096 - (size_t)ksplit * s.wstep; }
template <class C> __device__ __forceinline__ void src_x_raw(Src<C>& s, const void* base, unsigned pitch, int lid, int lane) {
    s.x = (const char*)base - 4096; s.xstep = 128;
#pragma unroll
    for (int i = 0; i < C::PX; ++i) { const int row = 8 * (lid * C::PX + i) + (lane >> 3); s.xv[i] = row * pitch + (((lane & 7) ^ ((row >> 1) & 7)) * 16) + 4096 - (i & 3) * 1024; }
}
template <class C> __device__ __forceinline__ void src_x_own(Src<C>& s, const void* base, int lid, int lane) {
    s.x = (const char*)base - 4096; s.xstep = C::XB;
#pragma unroll
    for (int i = 0; i < C::PX; ++i) s.xv[i] = (lid * C::PX + i) * 1024 + lane * 16 + 4096 - (i & 3) * 1024;
}
template <class C> __device__ __forceinline__ void src_w_own(Src<C>& s, const void* base, int lid, int lane) {
    s.w = (const char*)base - 4096; s.w2 = s.w; s.ksplit = 1 << 20; s.wstep = C::WB;
#pragma unroll
    for (int i = 0; i < C::PW; ++i) s.wv[i] = (lid * C::PW + i) * 1024 + lane * 16 + 4096 - (i & 3) * 1024;
}
template <class C> __device__ __forceinline__ void src_w_raw(Src<C>& s, const void* base, unsigned pitch, unsigned segstride, int lid, int lane) {
    s.w = (const char*)base - 4096; s.w2 = s.w; s.ksplit = 1 << 20; s.wstep = 64 * pitch;
#pragma unroll
    for (int i = 0; i < C::PW; ++i) { const int o = 1024 * (lid * C::PW + i) + 16 * lane; const int r = o / C::WPITCH, x = w_swz<C::WI>(r, o % C::WPITCH);
        s.wv[i] = r * pitch + (x >> 7) * segstride + (x & 127) + 4096 - (i & 3) * 1024; }
}
template <class C, int I0, int I1, bool FIRST> __device__ __forceinline__ void stage_rng(unsigned dx, unsigned dw, const Src<C>& s, const char* xb, const char* wb) {
    if constexpr (I0 < I1) {
        if constexpr (I0 < C::PW) { if constexpr (FIRST || (I0 & 3) == 0) glds_set<I0, C::WSC>(wb, s.wv[I0], dw); else glds_run<I0, C::WSC>(wb, s.wv[I0]); }
        else { constexpr int j = I0 - C::PW; if constexpr (FIRST || (j & 3) == 0) glds_set<j, C::XSC>(xb, s.xv[j], dx); else glds_run<j, C::XSC>(xb, s.xv[j]); }
        stage_rng<C, I0 + 1, I1, false>(dx, dw, s, xb, wb);
    }
}
template <int MT_, int NT_, int N0_, unsigned SWAP_, bool LD_> struct Wv { static constexpr int MT = MT_, NT = NT_, N0 = N0_; static constexpr unsigned SWAP = SWAP_; static constexpr bool LD = LD_; };
template <class W> struct Frag { bf16x8 x[W::MT]; s16x4 wl[W::NT], wh[W::NT]; };
template <class C, class W> struct RdAddr { unsigned x[2]; unsigned w[(C::WI == W_P384) ? 4 : (C::WI == W_P256) ? W::NT : 2]; };
template <class C, class W> __device__ __forceinline__ void rd_init(RdAddr<C, W>& a, int m0, int n0, int lane) {
    const int fr = lane & 15, fq = lane >> 4, g = lane >> 4, q = (lane & 15) >> 2, p = lane & 3;
    if (C::XI == X_RAW) { for (int kk = 0; kk < 2; ++kk) a.x[kk] = (m0 + fr) * 128 + (((kk * 4 + fq) ^ (fr >> 1)) * 16); }
    else { for (int kk = 0; kk < 2; ++kk) a.x[kk] = (kk * 4 + fq) * (C::BM * 16) + (m0 + fr) * 16; }
    if (C::WI == W_P384) { const int s = ((q >> 1) & 1) | ((g & 1) << 1);
        for (int c = 0; c < 4; ++c) a.w[c] = C::WOFF + (8 * g + q) * 384 + ((c ^ s) * 32) + p * 8; }
    else if (C::WI == W_P448) { const int s = g & 1;
        for (int c = 0; c < 2; ++c) a.w[c] = C::WOFF + (8 * g + q) * 448 + (((c ^ s) - c) * 32) + p * 8; }
    else if (C::WI == W_P256) { const int s = (q & 3) | ((g & 1) << 2);
        for (int t = 0; t < W::NT; ++t) a.w[t] = C::WOFF + (8 * g + q) * 256 + (((n0 + t) ^ s) * 32) + p * 8; }
    else { for (int h = 0; h < 2; ++h) a.w[h] = C::WOFF + (p >> 1) * 1024 + (8 * g + 4 * (h ^ (p >> 1)) + q) * 16 + (p & 1) * 8 + W::N0 * 2048; }
}
template <class C, class W> __device__ __forceinline__ s16x4 rd_w(ldsp b, const RdAddr<C, W>& a, int t, int kk, int h) {
    const int c = W::N0 + t;
    if (C::WI == W_P384) return tr_read(b + a.w[c & 3] + (c >> 2) * 128 + kk * 32 * 384 + h * 4 * 384);
    else if (C::WI == W_P448) return tr_read(b + a.w[c & 1] + c * 32 + kk * 32 * 448 + h * 4 * 448);
    else if (C::WI == W_P256) return tr_read(b + a.w[t] + kk * 32 * 256 + h * 4 * 256);
    else return tr_read(b + a.w[h] + t * 2048 + kk * 512);
}
template <class C, class W> __device__ __forceinline__ void rd_one(Frag<W>& f, ldsp bx, ldsp bw, const RdAddr<C, W>& a, int kk, int r) {
    if (r < 2) { if (r == 0) f.wl[0] = rd_w<C, W>(bw, a, 0, kk, 0); else f.wh[0] = rd_w<C, W>(bw, a, 0, kk, 1); }
    else if (r < 2 + W::MT) { const int m = r - 2; f.x[m] = *(const LAS bf16x8*)(bx + a.x[kk] + m * ((C::XI == X_RAW) ? 2048 : 256)); }
    else { const int u = r - W::MT; const int t = u >> 1; if (u & 1) f.wh[t] = rd_w<C, W>(bw, a, t, kk, 1); else f.wl[t] = rd_w<C, W>(bw, a, t, kk, 0); }
}
template <class W> __device__ __forceinline__ void mma_one(f32x4 (&acc)[W::NT][W::MT], const Frag<W>& f, int i) {
    const int t = i / W::MT, m = i % W::MT;
    const bf16x8 w = __builtin_shufflevector(f.wl[t], f.wh[t], 0, 1, 2, 3, 4, 5, 6, 7);
    acc[t][m] = ((W::SWAP >> t) & 1) ? __builtin_amdgcn_mfma_f32_16x16x32_bf16(f.x[m], w, acc[t][m], 0, 0, 0)
                                     : __builtin_amdgcn_mfma_f32_16x16x32_bf16(w, f.x[m], acc[t][m], 0, 0, 0);
}
template <int RFRONT_, int VA_, int RF1_ = RFRONT_> struct Sch { static constexpr int RFRONT = RFRONT_, VA = VA_, RF1 = RF1_; };
template <class C, class W, class S, int RF, int V0, int V1, int I>
__device__ __forceinline__ void step_slot(f32x4 (&acc)[W::NT][W::MT], const Frag<W>& fc, Frag<W>& fn, ldsp bx, ldsp bw, const RdAddr<C, W>& a, int ksn,
                                          const Src<C>& s, const char* xb, const char* wb, unsigned dx, unsigned dw) {
    constexpr int NM = W::NT * W::MT, NR = 2 * W::NT + W::MT, NV = V1 - V0;
    constexpr int RS = (NM * RF / 16) < 1 ? 1 : (NM * RF / 16);
    if constexpr (I < NM) {
        mma_one<W>(acc, fc, I);
        if constexpr (I < RS) {
#pragma unroll
            for (int r = I * NR / RS; r < (I + 1) * NR / RS; ++r) rd_one<C, W>(fn, bx, bw, a, ksn, r);
        }
        if constexpr (W::LD) {
            constexpr int v0 = V0 + I * NV / NM, v1 = V0 + (I + 1) * NV / NM;
            stage_rng<C, v0, v1, v0 == V0>(dx, dw, s, xb, wb);
        }
        GM_SB();
        step_slot<C, W, S, RF, V0, V1, I + 1>(acc, fc, fn, bx, bw, a, ksn, s, xb, wb, dx, dw);
    }
}
struct NoHook { static constexpr bool PRE = false; __device__ __forceinline__ void pre_x() const {} __device__ __forceinline__ void at(int) const {} };
template <class C, class W, class S, class H>
__device__ __forceinline__ void gemm_loop(ldsp lds, const Src<C>& s, int nt, f32x4 (&acc)[W::NT][W::MT], int lid, int m0, int n0, int lane, H& hook DBG_PARAMS) {
    constexpr int PX = C::PX, PW = C::PW, NSX = C::NSX, NSW = C::NSW, NV = PX + PW;
    static_assert(S::VA >= PW && S::VA <= NV, "all W pieces go out in the step behind the barrier");
    static_assert(NSX >= NSW && NSW >= 2, "ring depths");
    RdAddr<C, W> a; rd_init<C, W>(a, m0, n0, lane);
    const unsigned ldb = (unsigned)(uintptr_t)lds;
    const unsigned lx0 = ldb + lid * PX * 1024, lw0 = ldb + C::WOFF + lid * PW * 1024;
    Frag<W> f0, f1;
    const char* wb = w_tile<C>(s, NSW - 1); const char* xb = s.x + (size_t)(NSX - 1) * s.xstep;
    unsigned dw = lw0 + (NSW - 1) * C::WB, dx = lx0 + (NSX - 1) * C::XB;
    if constexpr (H::PRE) {
        if constexpr (W::LD) {
#pragma unroll
            for (int i = 0; i < NSW; ++i) stage_rng<C, 0, PW, true>(0, lw0 + i * C::WB, s, nullptr, w_tile<C>(s, i));
        }
        hook.pre_x();
        if constexpr (W::LD) {
#pragma unroll
            for (int i = 0; i < NSX - 1; ++i) stage_rng<C, PW, NV, true>(lx0 + i * C::XB, 0, s, s.x + (size_t)i * s.xstep, nullptr);
            if constexpr (S::VA > PW) stage_rng<C, PW, S::VA, true>(dx, dw, s, xb, wb);
            asm volatile("s_waitcnt vmcnt(%0)" ::"n"(S::VA - PW) : "memory");
        }
    } else if constexpr (W::LD) {
#pragma unroll
        for (int i = 0; i < NSX - 1; ++i) {
            if (i < NSW - 1) stage_rng<C, 0, PW, true>(0, lw0 + i * C::WB, s, nullptr, w_tile<C>(s, i));
            stage_rng<C, PW, NV, true>(lx0 + i * C::XB, 0, s, s.x + (size_t)i * s.xstep, nullptr);
        }
        stage_rng<C, 0, S::VA, true>(dx, dw, s, xb, wb);
        constexpr int Y = (NSX - 2) * PX + (NSW - 2) * PW + S::VA;
        asm volatile("s_waitcnt vmcnt(%0)" ::"n"(Y) : "memory");
    }
    GM_BAR();
#pragma unroll
    for (int r = 0; r < 2 * W::NT + W::MT; ++r) rd_one<C, W>(f0, lds, lds, a, 0, r);
    int bx = 0, bw = 0, kx = NSX - 1, kw = NSW - 1;
    __builtin_amdgcn_s_waitcnt(0xC07F);
    for (int t = 0; t < nt; ++t) {
        ldsp cx = lds + bx * C::XB, cw = lds + bw * C::WB;
        const int nbx = (bx + 1 == NSX) ? 0 : bx + 1, nbw = (bw + 1 == NSW) ? 0 : bw + 1;
        ldsp nx = lds + nbx * C::XB, nw = lds + nbw * C::WB;
        TRACE(t);
        GM_SB();
#define GM_STEP(FC, FN, BX, BW, KSN, RF, V0, V1) step_slot<C, W, S, RF, V0, V1, 0>(acc, FC, FN, BX, BW, a, KSN, s, xb, wb, dx, dw)
        GM_STEP(f0, f1, cx, cw, 1, S::RFRONT, S::VA, NV);
        hook.at(t);
        asm volatile("s_waitcnt lgkmcnt(0)" ::: "memory");
        if constexpr (W::LD) {
            constexpr int pw = 1 - NSW, px = 1 - NSX, last = (pw > px) ? pw : px, nper = (-1) - last, Y = nper * NV + ((pw > px) ? PX : 0);
            asm volatile("s_waitcnt vmcnt(%0)" ::"n"(Y) : "memory");
        }
        GM_BAR();
        GM_SB();
        if constexpr (W::LD) {
            kx = (kx + 1 < nt) ? kx + 1 : kx; kw = (kw + 1 < nt) ? kw + 1 : kw;
            xb = s.x + (size_t)kx * s.xstep; wb = w_tile<C>(s, kw);
            dx = lx0 + bx * C::XB; dw = lw0 + bw * C::WB;
        }
        GM_STEP(f1, f0, nx, nw, 0, S::RF1, 0, S::VA);
#undef GM_STEP
        bx = nbx; bw = nbw;
    }
    GM_SB();
    asm volatile("s_waitcnt vmcnt(0) lgkmcnt(0)" ::: "memory");
    GM_BAR();
}
template <class W> __device__ __forceinline__ void acc_zero(f32x4 (&acc)[W::NT][W::MT]) {
#pragma unroll
    for (int t = 0; t < W::NT; ++t)
#pragma unroll
        for (int m = 0; m < W::MT; ++m) acc[t][m] = (f32x4){0.f, 0.f, 0.f, 0.f};
}
__device__ __forceinline__ u32x4 pair16(const f32x4& A, const f32x4& B) {
    const unsigned a0 = pk_bf16(A[0], A[1]), a1 = pk_bf16(A[2], A[3]), b0 = pk_bf16(B[0], B[1]), b1 = pk_bf16(B[2], B[3]);
    const auto r0 = __builtin_amdgcn_permlane16_swap(a0, b0, false, false); const auto r1 = __builtin_amdgcn_permlane16_swap(a1, b1, false, false);
    return (u32x4){r0[0], r1[0], r0[1], r1[1]};
}

namespace g32 {
enum { W_P448 = 1, W_P256 = 2, W_OWNT = 3 };
template <int BM_, int BN_, int XI_, int WI_, int NS_, int NL_ = 4> struct Cfg {
    static constexpr int BM = BM_, BN = BN_, XI = XI_, WI = WI_, NS = NS_, NL = NL_;
    static constexpr int XB = BM * 128, WB = BN * 128, WOFF = NS * XB, LDS = NS * (XB + WB);
    static constexpr int PX = BM / 8 / NL, PW = BN / 8 / NL;
    static constexpr int WPITCH = (WI == W_P448) ? 448 : (WI == W_P256) ? 256 : 16;
    static constexpr bool XSC = (XI == X_OWN), WSC = (WI == W_OWNT);
};
template <class C> __device__ __forceinline__ void src_w_raw(Src<C>& s, const void* base, unsigned pitch, int lid, int lane) {
    s.w = (const char*)base - 4096; s.w2 = s.w; s.ksplit = 1 << 20; s.wstep = 64 * pitch;
#pragma unroll
    for (int i = 0; i < C::PW; ++i) { const int o = 1024 * (lid * C::PW + i) + 16 * lane; const int r = o / C::WPITCH; int x = o % C::WPITCH;
        if (C::WI == W_P256) x ^= (r & 3) << 6;
        s.wv[i] = r * pitch + x + 4096 - (i & 3) * 1024; }
}
template <int MT_, int NT_, bool LD_> struct Wv { static constexpr int MT = MT_, NT = NT_; static constexpr bool LD = LD_; };
template <class C, class W> struct Frag { bf16x8 x[W::MT]; s16x4 wl[W::NT], wh[W::NT]; };
template <class C, class W> struct RdAddr { unsigned x[(C::XI == X_RAW) ? 4 : 1]; unsigned w[(C::WI == W_P256) ? W::NT : 1]; };
template <class C, class W> __device__ __forceinline__ void rd_init(RdAddr<C, W>& a, int m0, int n0  , int lane) {
    const int r = lane & 31, h = lane >> 5, g = lane >> 4, q = (lane & 15) >> 2, p = lane & 3;
    if (C::XI == X_RAW) { for (int ks = 0; ks < 4; ++ks) a.x[ks] = (m0 + r) * 128 + (((2 * ks + h) ^ ((r >> 1) & 7)) * 16); }
    else a.x[0] = h * (C::BM * 16) + (m0 + r) * 16;
    const int rowp = 8 * (g >> 1) + q, colp = (g & 1) * 32 + p * 8;
    if (C::WI == W_P448) a.w[0] = C::WOFF + rowp * 448 + colp + n0 * 64;
    else if (C::WI == W_P256) { for (int t = 0; t < W::NT; ++t) a.w[t] = C::WOFF + rowp * 256 + (((n0 + t) ^ q) * 64) + colp; }
    else a.w[0] = C::WOFF + h * (C::BN * 16) + (n0 * 32 + r) * 16;
}
template <class C, class W> constexpr int n_reads() { return W::MT + (C::WI == W_OWNT ? W::NT : 2 * W::NT); }
template <class C, class W> __device__ __forceinline__ void rd_one(Frag<C, W>& f, ldsp bx, ldsp bw, const RdAddr<C, W>& a, int ks, int r) {
    constexpr int PER = (C::WI == W_OWNT) ? 1 : 2;
    if (r >= PER && r < PER + W::MT) { const int m = r - PER;
        if (C::XI == X_RAW) f.x[m] = *(const LAS bf16x8*)(bx + a.x[ks] + m * 4096);
        else f.x[m] = *(const LAS bf16x8*)(bx + a.x[0] + ks * (2 * C::BM * 16) + m * 512);
        return; }
    const int u = r < PER ? r : r - W::MT, t = u / PER, hh = u % PER;
    if (C::WI == W_OWNT) { const bf16x8 v = *(const LAS bf16x8*)(bw + a.w[0] + ks * (2 * C::BN * 16) + t * 512); f.wl[t] = __builtin_shufflevector(v, v, 0, 1, 2, 3); f.wh[t] = __builtin_shufflevector(v, v, 4, 5, 6, 7); }
    else { s16x4 v;
        if (C::WI == W_P448) v = tr_read(bw + a.w[0] + t * 64 + (ks * 16 + hh * 4) * 448);
        else v = tr_read(bw + a.w[t] + (ks * 16 + hh * 4) * 256);
        if (hh) f.wh[t] = v; else f.wl[t] = v; }
}
template <class C, class W> __device__ __forceinline__ void rd_frags(Frag<C, W>& f, ldsp bx, ldsp bw, const RdAddr<C, W>& a, int ks) {
#pragma unroll
    for (int r = 0; r < n_reads<C, W>(); ++r) rd_one<C, W>(f, bx, bw, a, ks, r);
}
template <class C, class W, bool SWAP  > __device__ __forceinline__ void mma_one(f32x16 (&acc)[W::NT][W::MT], const Frag<C, W>& f, int i) {
    const int t = i / W::MT, m = i % W::MT;
    const bf16x8 w = __builtin_shufflevector(f.wl[t], f.wh[t], 0, 1, 2, 3, 4, 5, 6, 7);
    acc[t][m] = SWAP ? __builtin_amdgcn_mfma_f32_32x32x16_bf16(w, f.x[m], acc[t][m], 0, 0, 0) : __builtin_amdgcn_mfma_f32_32x32x16_bf16(f.x[m], w, acc[t][m], 0, 0, 0);
}
struct NoFill { template <int S> __device__ __forceinline__ void chunk() {} };
template <class C, class W, bool SWAP, int V0, int V1, int BASE, int I, class F>
__device__ __forceinline__ void step_slot(f32x16 (&acc)[W::NT][W::MT], const Frag<C, W>& fc, Frag<C, W>& fn, ldsp bx, ldsp bw, const RdAddr<C, W>& a, int ksn,
                                          const Src<C>& s, const char* xb, const char* wb, unsigned dx, unsigned dw, F& fill) {
    constexpr int NM = W::NT * W::MT, NR = n_reads<C, W>(), NV = V1 - V0;
    if constexpr (I < NM) {
        mma_one<C, W, SWAP>(acc, fc, I);
#pragma unroll
        for (int r = I * NR / NM; r < (I + 1) * NR / NM; ++r) rd_one<C, W>(fn, bx, bw, a, ksn, r);
        if constexpr (W::LD) { constexpr int v0 = V0 + I * NV / NM, v1 = V0 + (I + 1) * NV / NM; stage_rng<C, v0, v1, v0 == V0>(dx, dw, s, xb, wb); }
        fill.template chunk<BASE + I>();
        GM_SB();
        step_slot<C, W, SWAP, V0, V1, BASE, I + 1, F>(acc, fc, fn, bx, bw, a, ksn, s, xb, wb, dx, dw, fill);
    }
}
template <class C, class WF  , int D> struct Pipe {
    static constexpr int PX = C::PX, PW = C::PW, NS = C::NS, NV = PX + PW;
#define QSPLIT 0
    static constexpr int Q1 = QSPLIT == 0 ? (NV + 3) / 4 : QSPLIT == 1 ? (NV + 1) / 2 : PW, Q2 = QSPLIT == 0 ? (NV + 1) / 2 : QSPLIT == 1 ? NV : NV, Q3 = QSPLIT == 0 ? (3 * NV + 3) / 4 : NV;
    static constexpr int PRE = (D == 1) ? Q1 : Q2;
    ldsp lds; int nt, xmod;
    RdAddr<C, WF> a; unsigned lx0, lw0;
    Frag<C, WF> f0, f1, f2, f3;
    __device__ __forceinline__ Pipe(ldsp lds_, const Src<C>& s, int nt_, int xmod_, int lid, int m0, int n0, int lane) : lds(lds_), nt(nt_), xmod(xmod_) {
        rd_init<C, WF>(a, m0, n0, lane);
        const unsigned ldb = (unsigned)(uintptr_t)lds;
        lx0 = ldb + lid * PX * 1024; lw0 = ldb + C::WOFF + lid * PW * 1024;
    }
    __device__ __forceinline__ void issue_x(const Src<C>& s) {
        if constexpr (WF::LD) {
#pragma unroll
            for (int i = 0; i < NS - 1; ++i) stage_rng<C, PW, NV, true>(lx0 + i * C::XB, 0, s, s.x + (size_t)i * s.xstep, nullptr);
        }
    }
    __device__ __forceinline__ void issue_w(const Src<C>& s) {
        static_assert(PRE <= PW, "prologue split");
        if constexpr (WF::LD) {
#pragma unroll
            for (int i = 0; i < NS - 1; ++i) stage_rng<C, 0, PW, true>(0, lw0 + i * C::WB, s, nullptr, w_tile<C>(s, i));
            stage_rng<C, 0, PRE, true>(0, lw0 + (NS - 1) * C::WB, s, nullptr, w_tile<C>(s, NS - 1));
        }
    }
    __device__ __forceinline__ void landed() {
        if constexpr (WF::LD) asm volatile("s_waitcnt vmcnt(0)" ::: "memory");
        GM_BAR();
        rd_frags<C, WF>(f0, lds, lds, a, 0);
        if (D == 2) rd_frags<C, WF>(f1, lds, lds, a, 1);
    }
    __device__ __forceinline__ void begin(const Src<C>& s) {
        if constexpr (WF::LD) {
#pragma unroll
            for (int i = 0; i < NS - 1; ++i) stage_rng<C, 0, NV, true>(lx0 + i * C::XB, lw0 + i * C::WB, s, s.x + (size_t)i * s.xstep, w_tile<C>(s, i));
            stage_rng<C, 0, PRE, true>(0, lw0 + (NS - 1) * C::WB, s, nullptr, w_tile<C>(s, NS - 1));
            asm volatile("s_waitcnt vmcnt(%0)" ::"n"((NS - 2) * NV + PRE) : "memory");
        }
        GM_BAR();
        rd_frags<C, WF>(f0, lds, lds, a, 0);
        if (D == 2) rd_frags<C, WF>(f1, lds, lds, a, 1);
    }
    template <bool SWAP, class H> __device__ __forceinline__ void run(const Src<C>& s, int t0, int t1, f32x16 (&acc)[WF::NT][WF::MT], H& hook DBG_PARAMS) {
        NoFill nf; run_fill<SWAP>(s, t0, t1, t0, acc, hook, nf DBG_FWD);
    }
    template <bool SWAP, class H, class F> __device__ __forceinline__ void run_fill(const Src<C>& s, int t0, int t1, int tb, f32x16 (&acc)[WF::NT][WF::MT], H& hook, F& fill DBG_PARAMS) {
        using W = WF; constexpr int NM = WF::MT * WF::NT;
        int sl = t0 % NS, kn = t0 - 1 + NS; kn = kn < nt ? kn : nt - 1;
        const char* xb = s.x + (unsigned)(kn & (xmod - 1)) * s.xstep; const char* wb = w_tile<C>(s, kn);
        unsigned dx = lx0 + ((sl == 0) ? NS - 1 : sl - 1) * C::XB, dw = lw0 + ((sl == 0) ? NS - 1 : sl - 1) * C::WB;
        __builtin_amdgcn_s_waitcnt(0xC07F);
        for (int t = t0; t < t1; ++t) {
            const int ns = (sl + 1 == NS) ? 0 : sl + 1;
            ldsp cx = lds + sl * C::XB, cw = lds + sl * C::WB, nx = lds + ns * C::XB, nw = lds + ns * C::WB;
            TRACE(t - tb);
            hook.top(t - tb);
            GM_SB();
#define G32_STEP(FC, FN, BX, BW, KSN, V0, V1, ST) step_slot<C, W, SWAP, V0, V1, ST * NM, 0, F>(acc, FC, FN, BX, BW, a, KSN, s, xb, wb, dx, dw, fill)
            if constexpr (D == 1) { G32_STEP(f0, f1, cx, cw, 1, Q1, Q2, 0); G32_STEP(f1, f2, cx, cw, 2, Q2, Q3, 1); G32_STEP(f2, f3, cx, cw, 3, Q3, NV, 2); }
            else { G32_STEP(f0, f2, cx, cw, 2, Q2, Q3, 0); G32_STEP(f1, f3, cx, cw, 3, Q3, NV, 1); }
            hook.at(t - tb);
            asm volatile("s_waitcnt lgkmcnt(0)" ::: "memory");
            if constexpr (W::LD) asm volatile("s_waitcnt vmcnt(%0)" ::"n"((NS - 2) * NV) : "memory");
            GM_BAR();
            GM_SB();
            if constexpr (W::LD) { kn = (kn + 1 < nt) ? kn + 1 : kn; xb = s.x + (unsigned)(kn & (xmod - 1)) * s.xstep; wb = w_tile<C>(s, kn); dx = lx0 + sl * C::XB; dw = lw0 + sl * C::WB; }
            if constexpr (D == 1) { G32_STEP(f3, f0, nx, nw, 0, 0, Q1, 3); }
            else { G32_STEP(f2, f0, nx, nw, 0, 0, Q1, 2); G32_STEP(f3, f1, nx, nw, 1, Q1, Q2, 3); }
#undef G32_STEP
            sl = ns;
        }
        GM_SB();
    }
    __device__ __forceinline__ void end() {
        asm volatile("s_waitcnt vmcnt(0) lgkmcnt(0)" ::: "memory");
        GM_BAR();
    }
};
template <class C, class WF> struct PipeKS {
    static constexpr int PX = C::PX, PW = C::PW, NS = C::NS, NV = PX + PW, NM = WF::MT * WF::NT;
    ldsp lds; int nt, kh;
    RdAddr<C, WF> a; unsigned lx0, lw0;
    Frag<C, WF> a0, a1, b0, b1;
    __device__ __forceinline__ PipeKS(ldsp lds_, int nt_, int kh_, int lid, int m0, int n0, int lane) : lds(lds_), nt(nt_), kh(kh_) {
        rd_init<C, WF>(a, m0, n0, lane);
        static_assert(C::XI == X_OWN && C::WI == W_P256, "layouts of the output GEMM");
        a.x[0] += kh * 2 * (2 * C::BM * 16);
#pragma unroll
        for (int t = 0; t < WF::NT; ++t) a.w[t] += kh * 32 * 256;
        const unsigned ldb = (unsigned)(uintptr_t)lds;
        lx0 = ldb + lid * PX * 1024; lw0 = ldb + C::WOFF + lid * PW * 1024;
    }
    __device__ __forceinline__ void issue_w(const Src<C>& s) {
        if constexpr (WF::LD) {
#pragma unroll
            for (int i = 0; i < NS; ++i) stage_rng<C, 0, PW, true>(0, lw0 + i * C::WB, s, nullptr, w_tile<C>(s, i)); }
    }
    __device__ __forceinline__ void issue_x(const Src<C>& s) {
        if constexpr (WF::LD) {
#pragma unroll
            for (int i = 0; i < NS; ++i) stage_rng<C, PW, NV, true>(lx0 + i * C::XB, 0, s, s.x + (size_t)i * s.xstep, nullptr); }
    }
    __device__ __forceinline__ void landed() {
        if constexpr (WF::LD) asm volatile("s_waitcnt vmcnt(0)" ::: "memory");
        GM_BAR();
        rd_frags<C, WF>(a0, lds, lds, a, 0); rd_frags<C, WF>(a1, lds, lds, a, 1);
    }
    template <class H> __device__ __forceinline__ void run(const Src<C>& s, f32x16 (&acc)[WF::NT][WF::MT], H& hook DBG_PARAMS) {
        using W = WF; NoFill fill; using F = NoFill;
        int sl = 0, kn = NS;
        __builtin_amdgcn_s_waitcnt(0xC07F);
#define KS_PERIOD(T, C0, C1, N0, N1) do { \
            TRACE(T); hook.at(T); \
            asm volatile("s_waitcnt lgkmcnt(0)" ::: "memory"); \
            if constexpr (W::LD) asm volatile("s_waitcnt vmcnt(%0)" ::"n"((NS - 2) * NV) : "memory"); \
            GM_BAR(); GM_SB(); \
            const int ns = (sl + 1 == NS) ? 0 : sl + 1, kc = kn < nt ? kn : nt - 1; \
            const char* xb = s.x + (unsigned)kc * s.xstep; const char* wb = w_tile<C>(s, kc); \
            const unsigned dx = lx0 + sl * C::XB, dw = lw0 + sl * C::WB; \
            ldsp nx = lds + ns * C::XB, nw = lds + ns * C::WB; \
            step_slot<C, W, true, 0, NV / 2, 0, 0, F>(acc, C0, N0, nx, nw, a, 0, s, xb, wb, dx, dw, fill); \
            step_slot<C, W, true, NV / 2, NV, NM, 0, F>(acc, C1, N1, nx, nw, a, 1, s, xb, wb, dx, dw, fill); \
            sl = ns; ++kn; } while (0)
        for (int t = 0; t < nt; t += 2) { KS_PERIOD(t, a0, a1, b0, b1); if (t + 1 >= nt) break; KS_PERIOD(t + 1, b0, b1, a0, a1); }
#undef KS_PERIOD
        GM_SB();
        asm volatile("s_waitcnt vmcnt(0) lgkmcnt(0)" ::: "memory");
        GM_BAR();
    }
};
template <class W> __device__ __forceinline__ void acc_zero(f32x16 (&acc)[W::NT][W::MT]) {
#pragma unroll
    for (int t = 0; t < W::NT; ++t)
#pragma unroll
        for (int m = 0; m < W::MT; ++m) acc[t][m] = (f32x16){};
}
__device__ __forceinline__ u32x4 pair32(unsigned a0, unsigned a1, unsigned b0, unsigned b1) {
    const auto r0 = __builtin_amdgcn_permlane32_swap(a0, b0, false, false); const auto r1 = __builtin_amdgcn_permlane32_swap(a1, b1, false, false);
    return (u32x4){r0[0], r1[0], r0[1], r1[1]};
}
}

struct Params {
    const u16* ax; const u16* wqkv; const u16* wo; const u16* bx; const u16* win; const u16* wsp; const u16* bsp; const u16* wout; const u16* gw;
    u16* out; unsigned char* ws;
};
struct Ctx : Params { unsigned epv; __device__ __forceinline__ unsigned tg() const { return (unsigned)__builtin_amdgcn_readfirstlane(epv) + 1u; } };

using CfgQ = Cfg<256, 192, X_RAW, W_P384, 3, 2>;
struct Warm {
    const char* base; unsigned pitch; int lpr_log2; int cnt; unsigned scratch;
    __device__ __forceinline__ unsigned mask(int t0, int wid) const {
        if (wid < 4 || cnt <= 0) return 0u;
        return ((0x11111111u << (wid - 4)) & (cnt >= 32 ? ~0u : ((1u << cnt) - 1u))) << t0;
    }
    __device__ __forceinline__ void issue(int n, int wid, int lane) const {
        if (n >= 0 && n < cnt && wid == 4 + (n & 3)) { const int idx = n * 64 + lane; pf_lines(base, (unsigned)(idx >> lpr_log2) * pitch + (unsigned)(idx & ((1 << lpr_log2) - 1)) * 128u, scratch); }
    }
};

struct SigHook {
    static constexpr bool PRE = false; int wid, lane; Warm w0, w1; unsigned act;
    __device__ __forceinline__ void pre_x() const {}
    __device__ __forceinline__ void top(int) const {}
    __device__ __forceinline__ void at(int t) const { if ((act >> t) & 1u) { w0.issue(t - 1, wid, lane); w1.issue(t - 1, wid, lane); } }
};
template <int WN> __device__ __forceinline__ void qkv_wave(const Ctx& p, ldsp lds, int pair, int panel, int wid, int lane) {
    using C = CfgQ; using W = Wv<4, 6, WN * 6, WN ? 0x3Cu : 0u, WN == 0>;
    const int lid = wid & 3, m0 = (wid & 3) * 64;
    Src<C> s;
    src_x_raw<C>(s, p.ax + (size_t)panel * 256 * DM, DM * 2, lid, lane);
    src_w_raw<C>(s, p.wqkv + pair * 64, 3 * DM * 2, DM * 2, lid, lane);
    f32x4 acc[6][4]; acc_zero<W>(acc);
    STAMP(0, 1);
    Warm none{nullptr, 0, 0, 0, 0};
    SigHook hk{wid, lane, none, none, 0u};
    gemm_loop<C, W, Sch<12, 7>>(lds, s, DM / 64, acc, lid, m0, 0, lane, hk DBG_ARGS(0));
    STAMP(0, 2);
    const int fr = lane & 15, fq = lane >> 4;
    const int row0 = panel * 256 + m0;
    const rsrc_t rs = ws_rsrc(p.ws);
#pragma unroll
    for (int t = 0; t < 6; t += 2) {
        const int T = WN * 6 + t, kind = T >> 2, head = pair * 2 + ((T >> 1) & 1);
        if (kind < 2) {
            const unsigned base = (unsigned)(kind == 0 ? WS_Q : WS_K);
            const int c = 2 * (fq & 1) + (fq >> 1);
            const float sc = kind == 0 ? QSCALE : 1.0f;
#pragma unroll
            for (int m = 0; m < 4; ++m) { const int row = row0 + m * 16 + fr;
                st16_wt(rs, base + ((head * 64 + (row >> 5)) * 4 + c) * 512 + (row & 31) * 16, pair16(acc[t][m] * sc, acc[t + 1][m] * sc)); }
        } else {
#pragma unroll
            for (int tt = 0; tt < 2; ++tt)
#pragma unroll
                for (int m = 0; m < 4; m += 2) {
                    const f32x4 A = acc[t + tt][m], B = acc[t + tt][m + 1];
                    const unsigned a0 = pk_bf16(A[0], A[1]), a1 = pk_bf16(A[2], A[3]), b0 = pk_bf16(B[0], B[1]), b1 = pk_bf16(B[2], B[3]);
                    const auto r0 = __builtin_amdgcn_permlane32_swap(a0, b0, false, false); const auto r1 = __builtin_amdgcn_permlane32_swap(a1, b1, false, false);
                    const int kb = (row0 >> 4) + m + (fq >> 1), h = fq & 1, d = tt * 16 + fr;
                    st16_wt(rs, (unsigned)WS_V + ((head * 128 + kb) * 2 + h) * 512 + d * 16, (u32x4){r0[0], r1[0], r0[1], r1[1]});
                }
        }
    }
    asm volatile("s_waitcnt vmcnt(0)" ::: "memory");
    GM_BAR();
    if (threadIdx.x == 0) signal(p.ws, F_Q + pair * 32 + panel, p.tg());
}

using CfgZ = g32::Cfg<128, 224, X_RAW, g32::W_P448, 3>;
using CfgS = g32::Cfg<128, 224, X_RAW, g32::W_OWNT, 3>;
#define ONS 5
#define ONL 4
using CfgO = g32::Cfg<128, 128, X_OWN, g32::W_P256, ONS, ONL>;
__device__ __forceinline__ float gelu_tanh(float x) {
    const float t = x * x * (-0.044715f * 2.0f * 0.7978845608028654f * 1.4426950408889634f) + (-2.0f * 0.7978845608028654f * 1.4426950408889634f);
    return x * __builtin_amdgcn_rcpf(1.0f + __builtin_amdgcn_exp2f(x * t));
}
template <int N> __device__ __forceinline__ void gelu_pairs(unsigned* z) {
    constexpr float C1 = -0.044715f * 2.0f * 0.7978845608028654f * 1.4426950408889634f, C2 = -2.0f * 0.7978845608028654f * 1.4426950408889634f;
    f32x2 x[N], e[N];
#pragma unroll
    for (int i = 0; i < N; ++i) x[i] = (f32x2){bf_lo(z[i]), bf_hi(z[i])};
#pragma unroll
    for (int i = 0; i < N; ++i) e[i] = x[i] * (x[i] * x[i] * C1 + C2);
    GM_SB();
#pragma unroll
    for (int i = 0; i < N; ++i) { e[i].x = __builtin_amdgcn_exp2f(e[i].x); e[i].y = __builtin_amdgcn_exp2f(e[i].y); }
    GM_SB();
#pragma unroll
    for (int i = 0; i < N; ++i) e[i] = e[i] + 1.0f;
#pragma unroll
    for (int i = 0; i < N; ++i) { e[i].x = __builtin_amdgcn_rcpf(e[i].x); e[i].y = __builtin_amdgcn_rcpf(e[i].y); }
    GM_SB();
#pragma unroll
    for (int i = 0; i < N; ++i) { const f32x2 y = x[i] * e[i]; z[i] = pk_bf16(y.x, y.y); }
    GM_SB();
}
__device__ __forceinline__ void gelu16(f32x16& a) {
    constexpr float C1 = -0.044715f * 2.0f * 0.7978845608028654f * 1.4426950408889634f, C2 = -2.0f * 0.7978845608028654f * 1.4426950408889634f;
    f32x16 e = a * (a * a * C1 + C2);
    GM_SB();
#pragma unroll
    for (int i = 0; i < 16; ++i) e[i] = __builtin_amdgcn_exp2f(e[i]);
    GM_SB();
    e = e + 1.0f;
#pragma unroll
    for (int i = 0; i < 16; ++i) e[i] = __builtin_amdgcn_rcpf(e[i]);
    GM_SB();
    a = a * e;
}
template <int CTRL> __device__ __forceinline__ float dpp_add(float x) { return x + __int_as_float(__builtin_amdgcn_update_dpp(0, __float_as_int(x), CTRL, 0xf, 0xf, false)); }
__device__ __forceinline__ float red32_pair(float a, float b) {
    const auto r = __builtin_amdgcn_permlane16_swap(__float_as_uint(a), __float_as_uint(b), false, false);
    float x = __uint_as_float(r[0]) + __uint_as_float(r[1]);
    x = dpp_add<0x128>(x); x = dpp_add<0x124>(x); x = dpp_add<0x122>(x); x = dpp_add<0x121>(x);
    return x;
}
constexpr int LDS_TAB = CfgZ::LDS;
template <int NT> struct GeluFill {
    static constexpr int NSLOT = 4 * NT, STRIDE = NSLOT / 4;
    unsigned (&z)[NT][8];
    f32x2 x, e;
    template <int S> __device__ __forceinline__ void chunk() {
        constexpr float C1 = -0.044715f * 2.0f * 0.7978845608028654f * 1.4426950408889634f, C2 = -2.0f * 0.7978845608028654f * 1.4426950408889634f;
        if constexpr (S % STRIDE == 0) { constexpr int pa = S / STRIDE;
            x = (f32x2){bf_lo(z[0][pa]), bf_hi(z[0][pa])};
            const f32x2 w = x * (x * x * C1 + C2);
            e.x = __builtin_amdgcn_exp2f(w.x); e.y = __builtin_amdgcn_exp2f(w.y);
#pragma unroll
            for (int g = 0; g + 1 < 2 * NT; ++g) z[g >> 1][(g & 1) * 4 + pa] = z[(g + 1) >> 1][((g + 1) & 1) * 4 + pa];
            asm volatile("" : "+v"(x), "+v"(e));
        }
        if constexpr (S % STRIDE == 2) { constexpr int pb = S / STRIDE;
            const f32x2 d = e + 1.0f;
            const f32x2 y = x * (f32x2){__builtin_amdgcn_rcpf(d.x), __builtin_amdgcn_rcpf(d.y)};
            z[NT - 1][4 + pb] = pk_bf16(y.x, y.y);
            asm volatile("" : "+v"(z[NT - 1][4 + pb]));
        }
    }
};
template <int NT, bool LD> struct LnHook {
    const Ctx& p; ldsp lds; int panel, j, wid, lane, n0; unsigned (&z2)[NT][8];
    u32x4 w[4];
    __device__ __forceinline__ void normalise(int tt) {
        LAS f32x2* R = (LAS f32x2*)(lds + LDS_TAB + 2048);
        const int r = lane & 31, h = lane >> 5, m0 = (wid & 3) * 32;
        const rsrc_t rs = ws_rsrc(p.ws);
        const int srow = panel * 128 + m0;
#pragma unroll
        for (int i = 0; i < 4; i += 2) {
            const f32x4 ra = *(const LAS f32x4*)(R + m0 + 8 * i + 4 * h), rb = *(const LAS f32x4*)(R + m0 + 8 * i + 4 * h + 2);
            const f32x4 rc = *(const LAS f32x4*)(R + m0 + 8 * i + 8 + 4 * h), rd = *(const LAS f32x4*)(R + m0 + 8 * i + 8 + 4 * h + 2);
            const int k = srow + 8 * (i + h);
            const unsigned base = (unsigned)WS_Z + ((j * 32 + (k >> 6)) * 8 + ((k & 63) >> 3)) * 3584 + (n0 * 32 + r) * 16;
            const unsigned a0 = z2[tt][2 * i], a1 = z2[tt][2 * i + 1], b0 = z2[tt][2 * i + 2], b1 = z2[tt][2 * i + 3];
            st16_wt(rs, base + tt * 512, g32::pair32(pk_bf16(fmaf(bf_lo(a0), ra[1], ra[0]), fmaf(bf_hi(a0), ra[3], ra[2])), pk_bf16(fmaf(bf_lo(a1), rb[1], rb[0]), fmaf(bf_hi(a1), rb[3], rb[2])),
                                                      pk_bf16(fmaf(bf_lo(b0), rc[1], rc[0]), fmaf(bf_hi(b0), rc[3], rc[2])), pk_bf16(fmaf(bf_lo(b1), rd[1], rd[0]), fmaf(bf_hi(b1), rd[3], rd[2]))));
        }
    }
    __device__ __forceinline__ void normalise_at(int t) {
        if (t >= 5 && t < 9) { if (at_tile(t, 5)) normalise(0); if (at_tile(t, 6)) normalise(1); if (at_tile(t, 7)) normalise(2); if constexpr (NT == 4) { if (at_tile(t, 8)) normalise(3); } }
    }
    __device__ __forceinline__ void fetch() {
        const unsigned off = (unsigned)WS_STAT + ((panel * 8 + (lane >> 5) * 4) * 128 + (wid - 4) * 32 + (lane & 31)) * 16;
#pragma unroll
        for (int v = 0; v < 4; ++v) w[v] = ld16_dev(ws_rsrc(p.ws), off + v * 2048);
    }
    __device__ __forceinline__ bool fetched() const { bool ok = true;
#pragma unroll
        for (int v = 0; v < 4; ++v) ok = ok && w[v][1] == p.tg() && w[v][3] == p.tg();
        return __all(ok); }
    __device__ __forceinline__ void top(int t) {
        if constexpr (LD) normalise_at(t);
        else { if (t == 4) fetch(); }
    }
    __device__ __forceinline__ void at(int t) {
        if (t < 4 || t > 11) return;
        if constexpr (!LD) {
            if (at_tile(t, 4)) {
                for (unsigned spins = 0; !fetched();) {
                    __builtin_amdgcn_s_sleep(2); fetch();
                    if (++spins > (1u << 19)) { __hip_atomic_store((gu32*)(p.ws + WS_CTL) + C_TMO, p.tg(), RLX_AGENT); break; }
                }
                LAS f32x2* R = (LAS f32x2*)(lds + LDS_TAB + 2048);
                float ms = 0.f;
#pragma unroll
                for (int v = 0; v < 4; ++v) ms += __uint_as_float(w[v][0]);
                { const auto r = __builtin_amdgcn_permlane32_swap(__float_as_uint(ms), __float_as_uint(ms), false, false); ms = __uint_as_float(r[0]) + __uint_as_float(r[1]); }
                const float mean = ms * 0.125f; float q = 0.f;
#pragma unroll
                for (int v = 0; v < 4; ++v) { const float dm = __uint_as_float(w[v][0]) - mean; q += __uint_as_float(w[v][2]) + 224.0f * dm * dm; }
                { const auto r = __builtin_amdgcn_permlane32_swap(__float_as_uint(q), __float_as_uint(q), false, false); q = __uint_as_float(r[0]) + __uint_as_float(r[1]); }
                { const float rs_ = __builtin_amdgcn_rsqf(q * (1.0f / 1792.0f) + 1e-5f); if (lane < 32) R[(wid - 4) * 32 + lane] = (f32x2){-mean * rs_, rs_}; }
                GM_VM0();
            }
            normalise_at(t);
        }
        if (at_tile(t, 10)) asm volatile("s_waitcnt vmcnt(0)" ::: "memory");
        if constexpr (!LD) { if (at_tile(t, 11)) { if (threadIdx.x == 256) signal(p.ws, F_Z + j * 32 + panel, p.tg()); } }
    }
};
struct SpHook {
    static constexpr bool PRE = false; const Ctx& p; ldsp lds; int panel, wid, lane;
    __device__ __forceinline__ void pre_x() const {}
    __device__ __forceinline__ void top(int) const {}
    __device__ __forceinline__ void at(int t) {
        if (t == 2 && (wid == 4 || wid == 5)) {
            poll_flags(p.ws, p.tg(), F_Q, 1, true, F_Q, lane);
            const int row = (wid - 4) * 64 + lane, trow = panel * 128 + row;
            ((LAS f32x2*)(lds + LDS_TAB + 2048))[row] = (f32x2){__uint_as_float((unsigned)p.bsp[trow] << 16), 1.0f - __uint_as_float(__hip_atomic_load((gu32*)(p.ws + WS_GATE) + trow, RLX_AGENT))};
            GM_VM0();
        }
    }
};
struct WaitHook {
    unsigned char* ws; unsigned tag; int wid, lane, tlate, wl, nl;
    __device__ __forceinline__ void top(int) const {}
    __device__ __forceinline__ void at(int t) const { if (t == tlate && wid == 4) wait_flags(ws, tag, wl, nl, false, 0, lane); }
};
template <bool LD, bool FINAL> __device__ __forceinline__ void out_part(const Ctx& p, ldsp lds, int panel, int j, int wid, int lane) {
    using C = CfgO; using W = g32::Wv<2, 2, LD>;
    const int lid = wid & 3, r = lane & 31, h = lane >> 5;
    const int kh = wid >> 2, mo = ((wid >> 1) & 1) * 64, no = (wid & 1) * 2;
    Src<C> s;
    src_x_own<C>(s, p.ws + WS_ACT + (size_t)panel * 352 * 2048 + (FINAL ? 0 : (FH / 64 + 16 - NA) * C::XB), lid, lane);
    g32::src_w_raw<C>(s, FINAL ? p.wout + j * 128 : p.wo + (size_t)(16 - NA) * 64 * DM + j * 128, DM * 2, lid, lane);
    if (FINAL) set_w2<C>(s, p.wo + j * 128, FH / 64);
    f32x16 acc[2][2]; g32::acc_zero<W>(acc);
    WaitHook hk{p.ws, p.tg(), wid, lane, FINAL && NA < 16 ? FH / 64 - C::NS : -1, F_O + (panel >> 2) * 32, 32};
    constexpr int NKT = FINAL ? FH / 64 + 16 - NA : NA;
    static_assert(NKT % 2 == 0 || true, "");
    g32::PipeKS<C, W> pipe(lds, NKT, kh, lid, mo, no, lane);
    pipe.issue_w(s);
    if (wid == 4) { if (FINAL) wait_flags(p.ws, p.tg(), F_U + panel * 32, 8, false, 0, lane); else wait_flags(p.ws, p.tg(), F_O + (panel >> 2) * 32, 32, false, 0, lane); }
    GM_BAR();
    pipe.issue_x(s);
    pipe.landed();
    STAMP(0, FINAL ? 10 : 7);
    pipe.run(s, acc, hk DBG_ARGS(4));
    STAMP(0, FINAL ? 9 : 8);
    ldsp mine = lds + wid * 8192 + lane * 16, theirs = lds + (wid ^ 4) * 8192 + lane * 16;
#pragma unroll
    for (int t = 0; t < 2; ++t)
#pragma unroll
        for (int i = 0; i < 4; ++i) { const f32x16& A = LD ? acc[t][1] : acc[t][0]; *(LAS f32x4*)(mine + (t * 4 + i) * 1024) = (f32x4){A[4 * i], A[4 * i + 1], A[4 * i + 2], A[4 * i + 3]}; }
    asm volatile("s_waitcnt lgkmcnt(0)" ::: "memory");
    if (FINAL && wid == 4) wait_flags(p.ws, p.tg(), F_P + panel * 32 + j, 1, false, 0, lane);
    GM_BAR();
    const rsrc_t rs = ws_rsrc(p.ws);
    const unsigned pb = (unsigned)WS_P + ((panel * 8 + j) * 8 + wid) * 8192 + lane * 16;
    f32x16 o[2];
    u32x4 pv[8];
    if (FINAL) {
#pragma unroll
        for (int q = 0; q < 8; ++q) pv[q] = ld16_dev(rs, pb + q * 1024);
    }
#pragma unroll
    for (int t = 0; t < 2; ++t) { o[t] = LD ? acc[t][0] : acc[t][1];
#pragma unroll
        for (int i = 0; i < 4; ++i) { const f32x4 v = *(const LAS f32x4*)(theirs + (t * 4 + i) * 1024); o[t][4 * i] += v[0]; o[t][4 * i + 1] += v[1]; o[t][4 * i + 2] += v[2]; o[t][4 * i + 3] += v[3]; } }
    if (!FINAL) {
#pragma unroll
        for (int t = 0; t < 2; ++t)
#pragma unroll
            for (int i = 0; i < 4; ++i) st16_wt(rs, pb + (t * 4 + i) * 1024, (u32x4){__float_as_uint(o[t][4 * i]), __float_as_uint(o[t][4 * i + 1]), __float_as_uint(o[t][4 * i + 2]), __float_as_uint(o[t][4 * i + 3])});
        asm volatile("s_waitcnt vmcnt(0)" ::: "memory");
        GM_BAR();
        if (threadIdx.x == 0) signal(p.ws, F_P + panel * 32 + j, p.tg());
    } else {
#pragma unroll
        for (int t = 0; t < 2; ++t)
#pragma unroll
            for (int i = 0; i < 4; ++i)
#pragma unroll
                for (int e = 0; e < 4; ++e) o[t][4 * i + e] += __uint_as_float(pv[t * 4 + i][e]);
        ldsp st = lds + 65536;
#pragma unroll
        for (int t = 0; t < 2; ++t)
#pragma unroll
            for (int i = 0; i < 4; i += 2) { const f32x16& A = o[t];
                *(LAS u32x4*)(st + (mo + kh * 32 + r) * 272 + (no + t) * 64 + (i + h) * 16) =
                    g32::pair32(pk_bf16(A[4 * i], A[4 * i + 1]), pk_bf16(A[4 * i + 2], A[4 * i + 3]), pk_bf16(A[4 * i + 4], A[4 * i + 5]), pk_bf16(A[4 * i + 6], A[4 * i + 7])); }
        asm volatile("s_waitcnt lgkmcnt(0)" ::: "memory"); GM_BAR();
#pragma unroll
        for (int i = 0; i < 4; ++i) { const int row = wid * 16 + i * 4 + (lane >> 4);
            __builtin_amdgcn_raw_buffer_store_b128(*(const LAS u32x4*)(st + row * 272 + (lane & 15) * 16), __builtin_amdgcn_make_buffer_rsrc(p.out, 0, SEQ * DM * 2, 0x00020000), (unsigned)(((panel * 128 + row) * DM + j * 128 + (lane & 15) * 8) * 2), 0, 16); }
    }
}
template <bool LD> __device__ __forceinline__ void b_wave(const Ctx& p, ldsp lds, int panel, int j, int wid, int lane) {
    constexpr int NT = LD ? 3 : 4;
    const int lid = wid & 3, m0 = lid * 32, n0 = LD ? 0 : 3;
    const int r = lane & 31, h = lane >> 5;
    unsigned z2[NT][8], z1[NT][8];
    const unsigned pfs = (unsigned)(uintptr_t)lds + LDS_PFS;
    {
        using C = CfgZ; using W = g32::Wv<1, NT, LD>; Src<C> s;
        src_x_raw<C>(s, p.ax + (size_t)panel * 128 * DM, DM * 2, lid, lane);
        g32::src_w_raw<C>(s, p.win + FH + j * 224, 2 * FH * 2, lid, lane);
        set_w2<C>(s, p.win + j * 224, DM / 64);
        g32::Pipe<C, W, 1> pipe(lds, s, 2 * DM / 64, DM / 64, lid, m0, n0, lane);
        f32x16 acc[NT][1]; g32::acc_zero<W>(acc);
        STAMP(0, 1);
        Warm wz1{(const char*)p.win + ((j * 224 * 2) & ~127) + (size_t)(panel & 3) * 256 * (2 * FH * 2), 2 * FH * 2, 2, 13, pfs};
        Warm none{nullptr, 0, 0, 0, 0};
        SigHook hk{wid, lane, wz1, none, wz1.mask(1, wid)};
        pipe.begin(s);
        pipe.template run<false>(s, 0, DM / 64, acc, hk DBG_ARGS(1));
        STAMP(0, 2);
        {
            float sm[16], sq[16];
#pragma unroll
            for (int t = 0; t < NT; ++t) {
                gelu16(acc[t][0]);
#pragma unroll
                for (int q = 0; q < 16; ++q) { const float v = acc[t][0][q]; sm[q] = t ? sm[q] + v : v; sq[q] = t ? sq[q] + v * v : v * v; }
#pragma unroll
                for (int q = 0; q < 16; q += 2) z2[t][q >> 1] = pk_bf16(acc[t][0][q], acc[t][0][q + 1]);
                acc[t][0] = (f32x16){};
            }
            float ra[16];
#pragma unroll
            for (int k = 0; k < 8; ++k) {
                const auto a = __builtin_amdgcn_permlane16_swap(__float_as_uint(sm[2 * k]), __float_as_uint(sm[2 * k + 1]), false, false); ra[k] = __uint_as_float(a[0]) + __uint_as_float(a[1]);
                const auto b2 = __builtin_amdgcn_permlane16_swap(__float_as_uint(sq[2 * k]), __float_as_uint(sq[2 * k + 1]), false, false); ra[8 + k] = __uint_as_float(b2[0]) + __uint_as_float(b2[1]); }
#pragma unroll
            for (int k = 0; k < 16; ++k) ra[k] = dpp_add<0x128>(ra[k]);
#pragma unroll
            for (int k = 0; k < 16; ++k) ra[k] = dpp_add<0x124>(ra[k]);
#pragma unroll
            for (int k = 0; k < 16; ++k) ra[k] = dpp_add<0x122>(ra[k]);
#pragma unroll
            for (int k = 0; k < 16; ++k) ra[k] = dpp_add<0x121>(ra[k]);
            float va = ra[0], vb = ra[8];
#pragma unroll
            for (int k = 1; k < 8; ++k) { const bool sel = (lane & 7) == k; va = sel ? ra[k] : va; vb = sel ? ra[8 + k] : vb; }
            LAS f32x2* P = (LAS f32x2*)(lds + LDS_TAB);
            { const int q = 2 * (lane & 7) + ((lane >> 4) & 1), row = m0 + 8 * (q >> 2) + 4 * h + (q & 3);
              if ((lane & 8) == 0) P[row * 2 + (LD ? 0 : 1)] = (f32x2){va, vb}; }
            asm volatile("s_waitcnt lgkmcnt(0)" ::: "memory"); GM_BAR();
            if constexpr (!LD) {
                if (lane < 32) {
                    const int row = lid * 32 + lane;
                    const f32x2 a = P[row * 2], b = P[row * 2 + 1];
                    const float su = a.x + b.x, mt = su * (1.0f / 224.0f), m2 = fmaxf(a.y + b.y - su * mt, 0.f);
                    st16_wt(ws_rsrc(p.ws), (unsigned)WS_STAT + ((panel * 8 + j) * 128 + row) * 16, (u32x4){__float_as_uint(mt), p.tg(), __float_as_uint(m2), p.tg()});
                }
            }
        }
        STAMP(0, 3);
        LnHook<NT, LD> hl{p, lds, panel, j, wid, lane, n0, z2};
        pipe.template run<true>(s, DM / 64, 2 * DM / 64, acc, hl DBG_ARGS(2));
        pipe.end();
        STAMP(0, 4);
#pragma unroll
        for (int t = 0; t < NT; ++t)
#pragma unroll
            for (int q = 0; q < 16; q += 2) z1[t][q >> 1] = pk_bf16(acc[t][0][q], acc[t][0][q + 1]);
    }
    {
        using C = CfgS; using W = g32::Wv<1, NT, LD>; Src<C> s;
        src_x_raw<C>(s, p.wsp + (size_t)panel * 128 * SEQ, SEQ * 2, lid, lane);
        src_w_own<C>(s, p.ws + WS_Z + (size_t)j * 32 * 28672, lid, lane);
        g32::Pipe<C, W, SPD> pipe(lds, s, SEQ / 64, SEQ / 64, lid, m0, n0, lane);
        pipe.issue_x(s);
        if (wid == 4) wait_flags(p.ws, p.tg(), F_Z + j * 32, 16, false, 0, lane);
        GM_BAR();
        pipe.issue_w(s);
        STAMP(0, 5);
#pragma unroll
        for (int t = 0; t < NT; ++t) { gelu_pairs<8>(z1[t]);
#pragma unroll
            for (int q = 0; q < 8; ++q) asm volatile("" : "+v"(z1[t][q])); }
        STAMP(0, 6);
        pipe.landed();
        f32x16 acc[NT][1]; g32::acc_zero<W>(acc);
        SpHook hk{p, lds, panel, wid, lane};
        pipe.template run<true>(s, 0, SEQ / 64, acc, hk DBG_ARGS(3));
        pipe.end();
        STAMP(0, 7);
        const rsrc_t rs = ws_rsrc(p.ws);
        const f32x2 bg = ((const LAS f32x2*)(lds + LDS_TAB + 2048))[m0 + r]; const float b = bg.x, g1 = bg.y;
        const unsigned ub = (unsigned)WS_ACT + (panel * 352 + j * 28 + n0 * 4 + h) * 2048 + (m0 + r) * 16;
#pragma unroll
        for (int t = 0; t < NT; ++t)
#pragma unroll
            for (int i = 0; i < 4; i += 2) {
                unsigned w4[4];
#pragma unroll
                for (int e = 0; e < 4; ++e) { const int q = 4 * i + 2 * e; const unsigned z = z1[t][q >> 1];
                    w4[e] = pk_bf16((acc[t][0][q] + b) * g1 * bf_lo(z), (acc[t][0][q + 1] + b) * g1 * bf_hi(z)); }
                st16_wt(rs, ub + (t * 4 + i) * 2048, g32::pair32(w4[0], w4[1], w4[2], w4[3]));
            }
        asm volatile("s_waitcnt vmcnt(0)" ::: "memory");
        GM_BAR();
        if (threadIdx.x == 256) signal(p.ws, F_U + panel * 32 + j, p.tg());
    }
    STAMP(0, 8);
    out_part<LD, true>(p, lds, panel, j, wid, lane);
}

constexpr int AT_STAGE = 16384, AT_NS = 4;
#define MFMA32(a, b, c) __builtin_amdgcn_mfma_f32_32x32x16_bf16(a, b, c, 0, 0, 0)
#define MFMA16(a, b, c) __builtin_amdgcn_mfma_f32_16x16x32_bf16(a, b, c, 0, 0, 0)
#define PIN(x) asm volatile("" : "+v"(x))
__device__ __forceinline__ void attn_wg(const Ctx& p, ldsp lds, int head, int qb, int x, int a, int wid, int lane) {
    if (wid == 4) wait_flags(p.ws, p.tg(), F_Q + (head >> 1) * 32, 8, false, 0, lane);
    GM_BAR();
    STAMP(0, 3);
    const unsigned char* Kg = p.ws + WS_K + (size_t)head * 131072; const unsigned char* Vg = p.ws + WS_V + (size_t)head * 131072;
    const unsigned ldb = (unsigned)(uintptr_t)lds;
    const unsigned voff = wid * 1024 + lane * 16;
    const char* kb = (const char*)Kg; const char* vb = (const char*)Vg;
    auto stage = [&](int st, int slot) {
        asm volatile("s_add_u32 m0, %2, 0\n\ts_nop 0\n\tglobal_load_lds_dwordx4 %0, %1 sc1" ::"v"(voff), "s"(kb + (size_t)st * 8192), "s"(ldb + slot * AT_STAGE + wid * 1024) : "memory", "scc");
        asm volatile("s_add_u32 m0, %2, 0\n\ts_nop 0\n\tglobal_load_lds_dwordx4 %0, %1 sc1" ::"v"(voff), "s"(vb + (size_t)st * 8192), "s"(ldb + slot * AT_STAGE + 8192 + wid * 1024) : "memory", "scc");
    };
    stage(0, 0); stage(1, 1); stage(2, 2);
    const int q0 = qb * 512 + wid * 64;
    bf16x8 qf[2][2];
#pragma unroll
    for (int a = 0; a < 2; ++a)
#pragma unroll
        for (int d = 0; d < 2; ++d) qf[a][d] = __builtin_bit_cast(bf16x8, ld16_dev(ws_rsrc(p.ws), (unsigned)WS_Q + (head * 64 + (q0 >> 5) + a) * 2048 + d * 1024 + lane * 16));
    bf16x8 sel;
    { const short one = ((lane & 15) == ((lane >> 4) & 1)) ? (short)0x3f80 : (short)0;
#pragma unroll
      for (int i = 0; i < 8; ++i) sel[i] = one; }
    asm volatile("s_waitcnt vmcnt(0)" ::: "memory");
    GM_BAR();
    f32x16 o[2], negm[2]; f32x4 l[2];
    const f32x16 zero16 = {};
    bf16x8 kA[2], kB[2], vA[2], vB[2];
    ldsp lb = lds + lane * 16;
    kA[0] = *(const LAS bf16x8*)(lb); kA[1] = *(const LAS bf16x8*)(lb + 1024);
#pragma unroll
    for (int a = 0; a < 2; ++a) {
        f32x16 s = MFMA32(kA[0], qf[a][0], zero16); s = MFMA32(kA[1], qf[a][1], s);
        float m = fmaxf(fmaxf(s[0], s[1]), fmaxf(s[2], s[3]));
#pragma unroll
        for (int r = 4; r < 16; r += 2) m = fmaxf(m, fmaxf(s[r], s[r + 1]));
        const auto rr = __builtin_amdgcn_permlane32_swap(__float_as_uint(m), __float_as_uint(m), false, false);
        m = -fmaxf(__uint_as_float(rr[0]), __uint_as_float(rr[1]));
#pragma unroll
        for (int r = 0; r < 16; ++r) negm[a][r] = m;
        PIN(negm[a]);
        o[a] = zero16; l[a] = (f32x4){0.f, 0.f, 0.f, 0.f};
    }
    f32x16 S0, S1; u32x4 p0l, p0h, p1l, p1h;
    S0 = MFMA32(kA[0], qf[0][0], negm[0]); S0 = MFMA32(kA[1], qf[0][1], S0);
    p1l = (u32x4){0u, 0u, 0u, 0u}; p1h = p1l;
    vB[0] = (bf16x8){0, 0, 0, 0, 0, 0, 0, 0}; vB[1] = vB[0];
    STAMP(0, 4);
#define EX4(S, i) do { S[i] = __builtin_amdgcn_exp2f(S[i]); S[i + 1] = __builtin_amdgcn_exp2f(S[i + 1]); S[i + 2] = __builtin_amdgcn_exp2f(S[i + 2]); S[i + 3] = __builtin_amdgcn_exp2f(S[i + 3]); } while (0)
#define PK4(P, S, i) do { P[0] = pk_bf16(S[i], S[i + 1]); P[1] = pk_bf16(S[i + 2], S[i + 3]); P[2] = pk_bf16(S[i + 4], S[i + 5]); P[3] = pk_bf16(S[i + 6], S[i + 7]); } while (0)
#define BC(x) __builtin_bit_cast(bf16x8, x)
#define ASTEP(SC, SN, PCl, PCh, PPl, PPh, KF, QN, VF, AP, LOADS) do { \
        SN = MFMA32(KF[0], qf[QN][0], negm[QN]); EX4(SC, 0); PIN(SC); GM_SB(); \
        SN = MFMA32(KF[1], qf[QN][1], SN); EX4(SC, 4); LOADS; PIN(SC); GM_SB(); \
        o[AP] = MFMA32(VF[0], BC(PPl), o[AP]); EX4(SC, 8); PIN(SC); GM_SB(); \
        o[AP] = MFMA32(VF[1], BC(PPh), o[AP]); EX4(SC, 12); PIN(SC); GM_SB(); \
        l[AP] = MFMA16(sel, BC(PPl), l[AP]); PK4(PCl, SC, 0); PIN(PCl); GM_SB(); \
        l[AP] = MFMA16(sel, BC(PPh), l[AP]); PK4(PCh, SC, 8); PIN(PCh); GM_SB(); } while (0)
#define LDK(KF, base, kt) do { KF[0] = *(const LAS bf16x8*)((base) + (kt) * 2048); KF[1] = *(const LAS bf16x8*)((base) + (kt) * 2048 + 1024); } while (0)
#define LDV(VF, base, kt) do { VF[0] = *(const LAS bf16x8*)((base) + 8192 + (kt) * 2048); VF[1] = *(const LAS bf16x8*)((base) + 8192 + (kt) * 2048 + 1024); } while (0)
    float gval = 0.f;
    for (int st = 0; st < 16; ++st) {
        ldsp cb = lb + (st & 3) * AT_STAGE, nb = lb + ((st + 1) & 3) * AT_STAGE;
        if (st < 5) {
            const int n = st * 8 + wid; const unsigned pfs = (unsigned)(uintptr_t)lds + LDS_PFS;
            const int p0 = 8 * (x & 1) + 4 * ((x >> 1) & 1), j0 = 4 * (x >> 2);
            if (n < 16) pf_lines(p.wsp + (size_t)(p0 * 128 + a * 32) * SEQ, (unsigned)(lane & 31) * (SEQ * 2) + (unsigned)(2 * n + (lane >> 5)) * 128u, pfs);
            else if (n < 30) pf_lines(p.wout + (size_t)(a * 112) * DM + j0 * 128, (unsigned)((n - 16) * 8 + (lane >> 3)) * (DM * 2) + (unsigned)(lane & 7) * 128u, pfs);
            else if (n < 38) pf_lines(p.wo + (size_t)(a * 64) * DM + j0 * 128, (unsigned)((n - 30) * 8 + (lane >> 3)) * (DM * 2) + (unsigned)(lane & 7) * 128u, pfs);
        }
        if (st == 8) {
            poll_flags(p.ws, p.tg(), F_Q, 1, true, F_Q, lane);
            gval = __uint_as_float(__hip_atomic_load((gu32*)(p.ws + WS_GATE) + q0 + lane, RLX_AGENT));
        }
        ASTEP(S0, S1, p0l, p0h, p1l, p1h, kA, 1, vB, 1, { LDK(kB, cb, 1); LDV(vA, cb, 0); });
        ASTEP(S1, S0, p1l, p1h, p0l, p0h, kB, 0, vA, 0, {});
        ASTEP(S0, S1, p0l, p0h, p1l, p1h, kB, 1, vA, 1, { LDK(kA, cb, 2); LDV(vB, cb, 1); });
        ASTEP(S1, S0, p1l, p1h, p0l, p0h, kA, 0, vB, 0, {});
        ASTEP(S0, S1, p0l, p0h, p1l, p1h, kA, 1, vB, 1, { LDK(kB, cb, 3); LDV(vA, cb, 2); });
        ASTEP(S1, S0, p1l, p1h, p0l, p0h, kB, 0, vA, 0, {});
        if (st + 1 < 16) {
            if (st + 2 < 16) asm volatile("s_waitcnt vmcnt(2)" ::: "memory"); else asm volatile("s_waitcnt vmcnt(0)" ::: "memory");
            GM_BAR();
            if (st + 3 < 16) stage(st + 3, (st + 3) & 3);
        }
        ASTEP(S0, S1, p0l, p0h, p1l, p1h, kB, 1, vA, 1, { LDK(kA, nb, 0); LDV(vB, cb, 3); });
        ASTEP(S1, S0, p1l, p1h, p0l, p0h, kA, 0, vB, 0, {});
    }
    o[1] = MFMA32(vB[0], BC(p1l), o[1]); o[1] = MFMA32(vB[1], BC(p1h), o[1]);
    l[1] = MFMA16(sel, BC(p1l), l[1]); l[1] = MFMA16(sel, BC(p1h), l[1]);
#undef EX4
#undef PK4
#undef ASTEP
#undef LDK
#undef LDV
    STAMP(0, 5);
    const rsrc_t rs = ws_rsrc(p.ws);
    const float gt0 = __shfl(gval, lane & 31), gt1 = __shfl(gval, 32 + (lane & 31));
#pragma unroll
    for (int a = 0; a < 2; ++a) {
        const int q = lane & 31, hh = lane >> 5;
        const float l0 = __shfl(l[a][0], q & 15), l1 = __shfl(l[a][1], q & 15);
        const float lsum = (q < 16) ? l0 : l1;
        const float sc = (a ? gt1 : gt0) * __builtin_amdgcn_rcpf(lsum);
        const int row = q0 + a * 32 + q, panel = row >> 7;
#pragma unroll
        for (int i = 0; i < 4; i += 2) {
            const unsigned a0 = pk_bf16(o[a][4 * i] * sc, o[a][4 * i + 1] * sc), a1 = pk_bf16(o[a][4 * i + 2] * sc, o[a][4 * i + 3] * sc);
            const unsigned b0 = pk_bf16(o[a][4 * i + 4] * sc, o[a][4 * i + 5] * sc), b1 = pk_bf16(o[a][4 * i + 6] * sc, o[a][4 * i + 7] * sc);
            const auto r0 = __builtin_amdgcn_permlane32_swap(a0, b0, false, false); const auto r1 = __builtin_amdgcn_permlane32_swap(a1, b1, false, false);
            const int c = 224 + head * 4 + i + hh;
            st16_wt(rs, (unsigned)WS_ACT + (panel * 352 + c) * 2048 + (row & 127) * 16, (u32x4){r0[0], r1[0], r0[1], r1[1]});
        }
    }
    asm volatile("s_waitcnt vmcnt(0)" ::: "memory");
    GM_BAR();
    if (threadIdx.x == 0) signal(p.ws, F_O + qb * 32 + head, p.tg());
}

__device__ __forceinline__ void gate_rows4(const Ctx& p, int row0, int lane) {
    u32x4 g[2], xr[4][2];
#pragma unroll
    for (int i = 0; i < 2; ++i) g[i] = ((const u32x4*)p.gw)[lane * 2 + i];
#pragma unroll
    for (int r = 0; r < 4; ++r)
#pragma unroll
        for (int i = 0; i < 2; ++i) xr[r][i] = ((const u32x4*)(p.ax + (size_t)(row0 + r) * DM))[lane * 2 + i];
    float s[4];
#pragma unroll
    for (int r = 0; r < 4; ++r) { float a = 0.f, b = 0.f;
#pragma unroll
        for (int i = 0; i < 2; ++i)
#pragma unroll
            for (int e = 0; e < 4; ++e) { a += bf_lo(xr[r][i][e]) * bf_lo(g[i][e]); b += bf_hi(xr[r][i][e]) * bf_hi(g[i][e]); }
        s[r] = a + b; }
#pragma unroll
    for (int r = 0; r < 4; ++r) s[r] = dpp_add<0x128>(s[r]);
#pragma unroll
    for (int r = 0; r < 4; ++r) s[r] = dpp_add<0x124>(s[r]);
#pragma unroll
    for (int r = 0; r < 4; ++r) s[r] = dpp_add<0x122>(s[r]);
#pragma unroll
    for (int r = 0; r < 4; ++r) s[r] = dpp_add<0x121>(s[r]);
#pragma unroll
    for (int r = 0; r < 4; ++r) {
        const auto a = __builtin_amdgcn_permlane16_swap(__float_as_uint(s[r]), __float_as_uint(s[r]), false, false); s[r] = __uint_as_float(a[0]) + __uint_as_float(a[1]);
        const auto b = __builtin_amdgcn_permlane32_swap(__float_as_uint(s[r]), __float_as_uint(s[r]), false, false); s[r] = __uint_as_float(b[0]) + __uint_as_float(b[1]); }
    const float v = lane == 0 ? s[0] : lane == 1 ? s[1] : lane == 2 ? s[2] : s[3];
    if (lane < 4) __hip_atomic_store((gu32*)(p.ws + WS_GATE) + row0 + lane, __float_as_uint(__builtin_amdgcn_rcpf(1.0f + __builtin_amdgcn_exp2f(-1.4426950408889634f * v))), RLX_AGENT);
}
constexpr int LDS_BYTES = CfgO::LDS > CfgQ::LDS + 256 ? CfgO::LDS : CfgQ::LDS + 256;
static_assert(LDS_TAB + 3072 <= LDS_BYTES && CfgO::LDS <= LDS_BYTES && AT_NS * AT_STAGE <= LDS_BYTES, "LDS map");
__global__ __launch_bounds__(512, 2) void fused(Params pp) {
    extern __shared__ __attribute__((aligned(16))) unsigned char smem[]; ldsp lds = (ldsp)smem;
    const int lane = threadIdx.x & 63, wid = __builtin_amdgcn_readfirstlane(threadIdx.x >> 6);
    const int b = blockIdx.x, x = b & 7, role = (b >> 3) & 1, idx = b >> 4;
    Ctx p; static_cast<Params&>(p) = pp;
    p.epv = __hip_atomic_load((gu32*)(pp.ws + WS_CTL) + C_EPOCH, RLX_AGENT);
    STAMP(0, 0);
    if (role == 0) {
        const int pair = 4 * (x >> 1) + (idx >> 2), panel = 4 * (x & 1) + (idx & 3);
        if (wid >= 4) gate_rows4(p, panel * 256 + pair * 16 + (wid - 4) * 4, lane);
        if (wid < 4) qkv_wave<0>(p, lds, pair, panel, wid, lane); else qkv_wave<1>(p, lds, pair, panel, wid, lane);
        STAMP(0, 6);
        attn_wg(p, lds, 4 * x + (idx >> 2), idx & 3, x, idx, wid, lane);
        STAMP(0, 9);
        {   const int opanel = 8 * (x & 1) + 4 * ((x >> 1) & 1) + (idx >> 2), oj = 4 * (x >> 2) + (idx & 3);
            if (wid < 4) out_part<true, false>(p, lds, opanel, oj, wid, lane); else out_part<false, false>(p, lds, opanel, oj, wid, lane); }
    } else {
        const int panel = 8 * (x & 1) + 4 * ((x >> 1) & 1) + (idx >> 2), j = 4 * (x >> 2) + (idx & 3);
        if (wid < 4) b_wave<true>(p, lds, panel, j, wid, lane); else b_wave<false>(p, lds, panel, j, wid, lane);
        if (panel == 0 && j == 0 && threadIdx.x == 0) __hip_atomic_store((gu32*)(pp.ws + WS_CTL) + C_EPOCH, p.tg(), RLX_AGENT);
    }
    STAMP(0, 15);
}
}

extern "C" void kernel_launch(void* const* d_in, const int* in_sizes, int n_in, void* d_out, int out_size, void* d_ws, size_t ws_size, hipStream_t stream) {
    using namespace gm;
    Params p;
    p.ax = (const u16*)d_in[0]; p.wqkv = (const u16*)d_in[1]; p.wo = (const u16*)d_in[2]; p.bx = (const u16*)d_in[3]; p.win = (const u16*)d_in[4];
    p.wsp = (const u16*)d_in[5]; p.bsp = (const u16*)d_in[6]; p.wout = (const u16*)d_in[7]; p.gw = (const u16*)d_in[8];
    p.out = (u16*)d_out; p.ws = (unsigned char*)d_ws;
    (void)hipFuncSetAttribute((const void*)fused, hipFuncAttributeMaxDynamicSharedMemorySize, LDS_BYTES);
    fused<<<256, 512, LDS_BYTES, stream>>>(p);
}
```

```cpp
#include <hip/hip_runtime.h>
#include <hip/hip_bf16.h>
#include <stdint.h>

#define LAS __attribute__((address_space(3)))
typedef short bf16x8 __attribute__((ext_vector_type(8)));
typedef short s16x4 __attribute__((ext_vector_type(4)));
typedef float f32x2 __attribute__((ext_vector_type(2)));
typedef float f32x4 __attribute__((ext_vector_type(4)));
typedef float f32x16 __attribute__((ext_vector_type(16)));
typedef unsigned u32x4 __attribute__((ext_vector_type(4)));
typedef unsigned u32x2 __attribute__((ext_vector_type(2)));
typedef LAS unsigned char* ldsp;
typedef unsigned short u16;

#define NA 16
#define SPD 1
namespace gm {
constexpr int SEQ = 2048, DM = 1024, NH = 32, FH = 1792;
constexpr size_t WS_CTL = 0;
constexpr size_t CTL_BYTES = 16384;
constexpr size_t WS_GATE = 16384;
constexpr size_t WS_STAT = 32768;
constexpr size_t WS_Q = 1u << 20;
constexpr size_t WS_K = WS_Q + (4u << 20);
constexpr size_t WS_V = WS_K + (4u << 20);
constexpr size_t WS_Z = WS_V + (4u << 20);
constexpr size_t WS_ACT = WS_Z + (8u << 20);
constexpr int LDS_PFS = 147456 - 0;
constexpr size_t WS_P = WS_ACT + (12u << 20);
constexpr float QSCALE = 0.17677669529663687f * 1.4426950408889634f;

#define TRACE(t) do {} while (0)
#define DBG_PARAMS
#define DBG_ARGS(id)
#define DBG_FWD
#define STAMP(k, i) do {} while (0)
constexpr int C_EPOCH = 0, C_TMO = 64, F_Q = 128  , F_S = F_Q + 512  , F_Z = F_S + 512  ,
              F_U = F_Z + 256  , F_O = F_U + 512  , F_P = F_O + 128  , C_END = F_P + 512;
static_assert(C_END * 4 <= (int)CTL_BYTES, "control block");
#define GM_VM0() __builtin_amdgcn_s_waitcnt(0x0F70)
#define RLX_AGENT __ATOMIC_RELAXED, __HIP_MEMORY_SCOPE_AGENT
typedef __attribute__((address_space(1))) unsigned gu32;
typedef __attribute__((address_space(1))) unsigned long long gu64;
typedef __amdgpu_buffer_rsrc_t rsrc_t;
__device__ __forceinline__ rsrc_t ws_rsrc(unsigned char* ws) { return __builtin_amdgcn_make_buffer_rsrc(ws, 0, 64u << 20, 0x00020000); }
__device__ __forceinline__ void st16_wt(rsrc_t r, unsigned off, u32x4 v) { __builtin_amdgcn_raw_buffer_store_b128(v, r, off, 0, 16); }
__device__ __forceinline__ void signal(unsigned char* ws, int word, unsigned tag) { __hip_atomic_store((gu32*)(ws + WS_CTL) + word, tag, RLX_AGENT); }
__device__ __forceinline__ void poll_flags(unsigned char* ws, unsigned tag, int w0, int n, bool all128, int wq, int lane) {
    gu32* ctl = (gu32*)(ws + WS_CTL);
    for (unsigned spins = 0;;) {
        bool ok = __hip_atomic_load(ctl + w0 + (lane < n ? lane : 0), RLX_AGENT) == tag;
        if (all128) { const unsigned q0 = __hip_atomic_load(ctl + wq + (lane >> 3) * 32 + (lane & 7), RLX_AGENT), q1 = __hip_atomic_load(ctl + wq + 256 + (lane >> 3) * 32 + (lane & 7), RLX_AGENT); ok = ok && q0 == tag && q1 == tag; }
        if (__all(ok)) break;
        __builtin_amdgcn_s_sleep(2);
        if (++spins > (1u << 19)) { __hip_atomic_store(ctl + C_TMO, tag, RLX_AGENT); break; }
        if ((spins & 255u) == 0u && __builtin_amdgcn_readfirstlane(__hip_atomic_load(ctl + C_TMO, RLX_AGENT)) == tag) break;
    }
}
__device__ __forceinline__ void wait_flags(unsigned char* ws, unsigned tag, int w0, int n, bool all128, int wq, int lane) {
    poll_flags(ws, tag, w0, n, all128, wq, lane);
    GM_VM0();
}
__device__ __forceinline__ u32x4 ld16_dev(rsrc_t r, unsigned off) { return __builtin_amdgcn_raw_buffer_load_b128(r, off, 0, 16); }
enum { X_RAW = 0, X_OWN = 1 };
enum { W_P384 = 0, W_P448 = 1, W_P256 = 2, W_OWN = 3 };

__device__ __forceinline__ unsigned pk_bf16(float lo, float hi) {
    typedef __bf16 bf16x2_t __attribute__((ext_vector_type(2)));
    f32x2 v = {lo, hi}; bf16x2_t b = __builtin_convertvector(v, bf16x2_t); return __builtin_bit_cast(unsigned, b);
}
__device__ __forceinline__ float bf_lo(unsigned w) { return __uint_as_float(w << 16); }
__device__ __forceinline__ float bf_hi(unsigned w) { return __uint_as_float(w & 0xffff0000u); }
__device__ __forceinline__ s16x4 tr_read(ldsp p) { typedef short v4s __attribute__((ext_vector_type(4))); return __builtin_amdgcn_ds_read_tr16_b64_v4i16((LAS v4s*)p); }
template <int I, bool SC = false> __device__ __forceinline__ void glds_run(const char* sbase, unsigned voff) {
    if constexpr (SC) asm volatile("global_load_lds_dwordx4 %0, %1 offset:%2 sc1" ::"v"(voff), "s"(sbase), "n"((I & 3) * 1024) : "memory");
    else asm volatile("global_load_lds_dwordx4 %0, %1 offset:%2" ::"v"(voff), "s"(sbase), "n"((I & 3) * 1024) : "memory");
}
template <int I, bool SC = false> __device__ __forceinline__ void glds_set(const char* sbase, unsigned voff, unsigned dst) {
    if constexpr (SC) asm volatile("s_add_u32 m0, %2, %3\n\ts_nop 0\n\tglobal_load_lds_dwordx4 %0, %1 offset:%4 sc1" ::"v"(voff), "s"(sbase), "s"(dst), "n"((I & ~3) * 1024), "n"((I & 3) * 1024) : "memory", "scc");
    else asm volatile("s_add_u32 m0, %2, %3\n\ts_nop 0\n\tglobal_load_lds_dwordx4 %0, %1 offset:%4" ::"v"(voff), "s"(sbase), "s"(dst), "n"((I & ~3) * 1024), "n"((I & 3) * 1024) : "memory", "scc");
}
__device__ __forceinline__ void pf_lines(const void* base, unsigned voff, unsigned ldsdst) {
    asm volatile("s_mov_b32 m0, %2\n\ts_nop 0\n\tglobal_load_lds_dword %0, %1" ::"v"(voff), "s"(base), "s"(ldsdst) : "memory");
}
#define GM_BAR() do { asm volatile("" ::: "memory"); __builtin_amdgcn_s_barrier(); asm volatile("" ::: "memory"); } while (0)
#define GM_SB() __builtin_amdgcn_sched_barrier(0)
__device__ __forceinline__ bool at_tile(int t, int k) { asm volatile("" : "+s"(t)); return t == k; }

template <int BM_, int BN_, int XI_, int WI_, int NSX_, int NSW_> struct Cfg {
    static constexpr int BM = BM_, BN = BN_, XI = XI_, WI = WI_, NSX = NSX_, NSW = NSW_;
    static constexpr int XB = BM * 128, WB = BN * 128, WOFF = NSX * XB, LDS = NSX * XB + NSW * WB;
    static constexpr int PX = BM / 32, PW = BN / 32;
    static constexpr int WPITCH = (WI == W_P384) ? 384 : (WI == W_P448) ? 448 : (WI == W_P256) ? 256 : 0;
    static constexpr bool XSC = false, WSC = false;
};
template <class C> struct Src { const char* x; const char* w; const char* w2; int ksplit; unsigned xstep, wstep; unsigned xv[C::PX]; unsigned wv[C::PW]; };
template <int WI> __device__ __forceinline__ int w_swz(int r, int x) {
    if (WI == W_P384) return x ^ (((((r >> 1) & 1) | (((r >> 3) & 1) << 1))) << 5);
    if (WI == W_P448) return x ^ (((r >> 3) & 1) << 5);
    if (WI == W_P256) return x ^ (((r & 3) | (((r >> 3) & 1) << 2)) << 5);
    return x;
}
template <class C> __device__ __forceinline__ const char* w_tile(const Src<C>& s, int kt) { return (kt < s.ksplit ? s.w : s.w2) + (unsigned)kt * s.wstep; }
template <class C> __device__ __forceinline__ void set_w2(Src<C>& s, const void* base, int ksplit) { s.ksplit = ksplit; s.w2 = (const char*)base - 4096 - (size_t)ksplit * s.wstep; }
template <class C> __device__ __forceinline__ void src_x_raw(Src<C>& s, const void* base, unsigned pitch, int lid, int lane) {
    s.x = (const char*)base - 4096; s.xstep = 128;
#pragma unroll
    for (int i = 0; i < C::PX; ++i) { const int row = 8 * (lid * C::PX + i) + (lane >> 3); s.xv[i] = row * pitch + (((lane & 7) ^ ((row >> 1) & 7)) * 16) + 4096 - (i & 3) * 1024; }
}
template <class C> __device__ __forceinline__ void src_x_own(Src<C>& s, const void* base, int lid, int lane) {
    s.x = (const char*)base - 4096; s.xstep = C::XB;
#pragma unroll
    for (int i = 0; i < C::PX; ++i) s.xv[i] = (lid * C::PX + i) * 1024 + lane * 16 + 4096 - (i & 3) * 1024;
}
template <class C> __device__ __forceinline__ void src_w_own(Src<C>& s, const void* base, int lid, int lane) {
    s.w = (const char*)base - 4096; s.w2 = s.w; s.ksplit = 1 << 20; s.wstep = C::WB;
#pragma unroll
    for (int i = 0; i < C::PW; ++i) s.wv[i] = (lid * C::PW + i) * 1024 + lane * 16 + 4096 - (i & 3) * 1024;
}
template <class C> __device__ __forceinline__ void src_w_raw(Src<C>& s, const void* base, unsigned pitch, unsigned segstride, int lid, int lane) {
    s.w = (const char*)base - 4096; s.w2 = s.w; s.ksplit = 1 << 20; s.wstep = 64 * pitch;
#pragma unroll
    for (int i = 0; i < C::PW; ++i) { const int o = 1024 * (lid * C::PW + i) + 16 * lane; const int r = o / C::WPITCH, x = w_swz<C::WI>(r, o % C::WPITCH);
        s.wv[i] = r * pitch + (x >> 7) * segstride + (x & 127) + 4096 - (i & 3) * 1024; }
}
template <class C, int I0, int I1, bool FIRST> __device__ __forceinline__ void stage_rng(unsigned dx, unsigned dw, const Src<C>& s, const char* xb, const char* wb) {
    if constexpr (I0 < I1) {
        if constexpr (I0 < C::PW) { if constexpr (FIRST || (I0 & 3) == 0) glds_set<I0, C::WSC>(wb, s.wv[I0], dw); else glds_run<I0, C::WSC>(wb, s.wv[I0]); }
        else { constexpr int j = I0 - C::PW; if constexpr (FIRST || (j & 3) == 0) glds_set<j, C::XSC>(xb, s.xv[j], dx); else glds_run<j, C::XSC>(xb, s.xv[j]); }
        stage_rng<C, I0 + 1, I1, false>(dx, dw, s, xb, wb);
    }
}
template <int MT_, int NT_, int N0_, unsigned SWAP_, bool LD_> struct Wv { static constexpr int MT = MT_, NT = NT_, N0 = N0_; static constexpr unsigned SWAP = SWAP_; static constexpr bool LD = LD_; };
template <class W> struct Frag { bf16x8 x[W::MT]; s16x4 wl[W::NT], wh[W::NT]; };
template <class C, class W> struct RdAddr { unsigned x[2]; unsigned w[(C::WI == W_P384) ? 4 : (C::WI == W_P256) ? W::NT : 2]; };
template <class C, class W> __device__ __forceinline__ void rd_init(RdAddr<C, W>& a, int m0, int n0, int lane) {
    const int fr = lane & 15, fq = lane >> 4, g = lane >> 4, q = (lane & 15) >> 2, p = lane & 3;
    if (C::XI == X_RAW) { for (int kk = 0; kk < 2; ++kk) a.x[kk] = (m0 + fr) * 128 + (((kk * 4 + fq) ^ (fr >> 1)) * 16); }
    else { for (int kk = 0; kk < 2; ++kk) a.x[kk] = (kk * 4 + fq) * (C::BM * 16) + (m0 + fr) * 16; }
    if (C::WI == W_P384) { const int s = ((q >> 1) & 1) | ((g & 1) << 1);
        for (int c = 0; c < 4; ++c) a.w[c] = C::WOFF + (8 * g + q) * 384 + ((c ^ s) * 32) + p * 8; }
    else if (C::WI == W_P448) { const int s = g & 1;
        for (int c = 0; c < 2; ++c) a.w[c] = C::WOFF + (8 * g + q) * 448 + (((c ^ s) - c) * 32) + p * 8; }
    else if (C::WI == W_P256) { const int s = (q & 3) | ((g & 1) << 2);
        for (int t = 0; t < W::NT; ++t) a.w[t] = C::WOFF + (8 * g + q) * 256 + (((n0 + t) ^ s) * 32) + p * 8; }
    else { for (int h = 0; h < 2; ++h) a.w[h] = C::WOFF + (p >> 1) * 1024 + (8 * g + 4 * (h ^ (p >> 1)) + q) * 16 + (p & 1) * 8 + W::N0 * 2048; }
}
template <class C, class W> __device__ __forceinline__ s16x4 rd_w(ldsp b, const RdAddr<C, W>& a, int t, int kk, int h) {
    const int c = W::N0 + t;
    if (C::WI == W_P384) return tr_read(b + a.w[c & 3] + (c >> 2) * 128 + kk * 32 * 384 + h * 4 * 384);
    else if (C::WI == W_P448) return tr_read(b + a.w[c & 1] + c * 32 + kk * 32 * 448 + h * 4 * 448);
    else if (C::WI == W_P256) return tr_read(b + a.w[t] + kk * 32 * 256 + h * 4 * 256);
    else return tr_read(b + a.w[h] + t * 2048 + kk * 512);
}
template <class C, class W> __device__ __forceinline__ void rd_one(Frag<W>& f, ldsp bx, ldsp bw, const RdAddr<C, W>& a, int kk, int r) {
    if (r < 2) { if (r == 0) f.wl[0] = rd_w<C, W>(bw, a, 0, kk, 0); else f.wh[0] = rd_w<C, W>(bw, a, 0, kk, 1); }
    else if (r < 2 + W::MT) { const int m = r - 2; f.x[m] = *(const LAS bf16x8*)(bx + a.x[kk] + m * ((C::XI == X_RAW) ? 2048 : 256)); }
    else { const int u = r - W::MT; const int t = u >> 1; if (u & 1) f.wh[t] = rd_w<C, W>(bw, a, t, kk, 1); else f.wl[t] = rd_w<C, W>(bw, a, t, kk, 0); }
}
template <class W> __device__ __forceinline__ void mma_one(f32x4 (&acc)[W::NT][W::MT], const Frag<W>& f, int i) {
    const int t = i / W::MT, m = i % W::MT;
    const bf16x8 w = __builtin_shufflevector(f.wl[t], f.wh[t], 0, 1, 2, 3, 4, 5, 6, 7);
    acc[t][m] = ((W::SWAP >> t) & 1) ? __builtin_amdgcn_mfma_f32_16x16x32_bf16(f.x[m], w, acc[t][m], 0, 0, 0)
                                     : __builtin_amdgcn_mfma_f32_16x16x32_bf16(w, f.x[m], acc[t][m], 0, 0, 0);
}
template <int RFRONT_, int VA_, int RF1_ = RFRONT_> struct Sch { static constexpr int RFRONT = RFRONT_, VA = VA_, RF1 = RF1_; };
template <class C, class W, class S, int RF, int V0, int V1, int I>
__device__ __forceinline__ void step_slot(f32x4 (&acc)[W::NT][W::MT], const Frag<W>& fc, Frag<W>& fn, ldsp bx, ldsp bw, const RdAddr<C, W>& a, int ksn,
                                          const Src<C>& s, const char* xb, const char* wb, unsigned dx, unsigned dw) {
    constexpr int NM = W::NT * W::MT, NR = 2 * W::NT + W::MT, NV = V1 - V0;
    constexpr int RS = (NM * RF / 16) < 1 ? 1 : (NM * RF / 16);
    if constexpr (I < NM) {
        mma_one<W>(acc, fc, I);
        if constexpr (I < RS) {
#pragma unroll
            for (int r = I * NR / RS; r < (I + 1) * NR / RS; ++r) rd_one<C, W>(fn, bx, bw, a, ksn, r);
        }
        if constexpr (W::LD) {
            constexpr int v0 = V0 + I * NV / NM, v1 = V0 + (I + 1) * NV / NM;
            stage_rng<C, v0, v1, v0 == V0>(dx, dw, s, xb, wb);
        }
        GM_SB();
        step_slot<C, W, S, RF, V0, V1, I + 1>(acc, fc, fn, bx, bw, a, ksn, s, xb, wb, dx, dw);
    }
}
struct NoHook { static constexpr bool PRE = false; __device__ __forceinline__ void pre_x() const {} __device__ __forceinline__ void at(int) const {} };
template <class C, class W, class S, class H>
__device__ __forceinline__ void gemm_loop(ldsp lds, const Src<C>& s, int nt, f32x4 (&acc)[W::NT][W::MT], int lid, int m0, int n0, int lane, H& hook DBG_PARAMS) {
    constexpr int PX = C::PX, PW = C::PW, NSX = C::NSX, NSW = C::NSW, NV = PX + PW;
    static_assert(S::VA >= PW && S::VA <= NV, "all W pieces go out in the step behind the barrier");
    static_assert(NSX >= NSW && NSW >= 2, "ring depths");
    RdAddr<C, W> a; rd_init<C, W>(a, m0, n0, lane);
    const unsigned ldb = (unsigned)(uintptr_t)lds;
    const unsigned lx0 = ldb + lid * PX * 1024, lw0 = ldb + C::WOFF + lid * PW * 1024;
    Frag<W> f0, f1;
    const char* wb = w_tile<C>(s, NSW - 1); const char* xb = s.x + (size_t)(NSX - 1) * s.xstep;
    unsigned dw = lw0 + (NSW - 1) * C::WB, dx = lx0 + (NSX - 1) * C::XB;
    if constexpr (H::PRE) {
        if constexpr (W::LD) {
#pragma unroll
            for (int i = 0; i < NSW; ++i) stage_rng<C, 0, PW, true>(0, lw0 + i * C::WB, s, nullptr, w_tile<C>(s, i));
        }
        hook.pre_x();
        if constexpr (W::LD) {
#pragma unroll
            for (int i = 0; i < NSX - 1; ++i) stage_rng<C, PW, NV, true>(lx0 + i * C::XB, 0, s, s.x + (size_t)i * s.xstep, nullptr);
            if constexpr (S::VA > PW) stage_rng<C, PW, S::VA, true>(dx, dw, s, xb, wb);
            asm volatile("s_waitcnt vmcnt(%0)" ::"n"(S::VA - PW) : "memory");
        }
    } else if constexpr (W::LD) {
#pragma unroll
        for (int i = 0; i < NSX - 1; ++i) {
            if (i < NSW - 1) stage_rng<C, 0, PW, true>(0, lw0 + i * C::WB, s, nullptr, w_tile<C>(s, i));
            stage_rng<C, PW, NV, true>(lx0 + i * C::XB, 0, s, s.x + (size_t)i * s.xstep, nullptr);
        }
        stage_rng<C, 0, S::VA, true>(dx, dw, s, xb, wb);
        constexpr int Y = (NSX - 2) * PX + (NSW - 2) * PW + S::VA;
        asm volatile("s_waitcnt vmcnt(%0)" ::"n"(Y) : "memory");
    }
    GM_BAR();
#pragma unroll
    for (int r = 0; r < 2 * W::NT + W::MT; ++r) rd_one<C, W>(f0, lds, lds, a, 0, r);
    int bx = 0, bw = 0, kx = NSX - 1, kw = NSW - 1;
    __builtin_amdgcn_s_waitcnt(0xC07F);
    for (int t = 0; t < nt; ++t) {
        ldsp cx = lds + bx * C::XB, cw = lds + bw * C::WB;
        const int nbx = (bx + 1 == NSX) ? 0 : bx + 1, nbw = (bw + 1 == NSW) ? 0 : bw + 1;
        ldsp nx = lds + nbx * C::XB, nw = lds + nbw * C::WB;
        TRACE(t);
        GM_SB();
#define GM_STEP(FC, FN, BX, BW, KSN, RF, V0, V1) step_slot<C, W, S, RF, V0, V1, 0>(acc, FC, FN, BX, BW, a, KSN, s, xb, wb, dx, dw)
        GM_STEP(f0, f1, cx, cw, 1, S::RFRONT, S::VA, NV);
        hook.at(t);
        asm volatile("s_waitcnt lgkmcnt(0)" ::: "memory");
        if constexpr (W::LD) {
            constexpr int pw = 1 - NSW, px = 1 - NSX, last = (pw > px) ? pw : px, nper = (-1) - last, Y = nper * NV + ((pw > px) ? PX : 0);
            asm volatile("s_waitcnt vmcnt(%0)" ::"n"(Y) : "memory");
        }
        GM_BAR();
        GM_SB();
        if constexpr (W::LD) {
            kx = (kx + 1 < nt) ? kx + 1 : kx; kw = (kw + 1 < nt) ? kw + 1 : kw;
            xb = s.x + (size_t)kx * s.xstep; wb = w_tile<C>(s, kw);
            dx = lx0 + bx * C::XB; dw = lw0 + bw * C::WB;
        }
        GM_STEP(f1, f0, nx, nw, 0, S::RF1, 0, S::VA);
#undef GM_STEP
        bx = nbx; bw = nbw;
    }
    GM_SB();
    asm volatile("s_waitcnt vmcnt(0) lgkmcnt(0)" ::: "memory");
    GM_BAR();
}
template <class W> __device__ __forceinline__ void acc_zero(f32x4 (&acc)[W::NT][W::MT]) {
#pragma unroll
    for (int t = 0; t < W::NT; ++t)
#pragma unroll
        for (int m = 0; m < W::MT; ++m) acc[t][m] = (f32x4){0.f, 0.f, 0.f, 0.f};
}
__device__ __forceinline__ u32x4 pair16(const f32x4& A, const f32x4& B) {
    const unsigned a0 = pk_bf16(A[0], A[1]), a1 = pk_bf16(A[2], A[3]), b0 = pk_bf16(B[0], B[1]), b1 = pk_bf16(B[2], B[3]);
    const auto r0 = __builtin_amdgcn_permlane16_swap(a0, b0, false, false); const auto r1 = __builtin_amdgcn_permlane16_swap(a1, b1, false, false);
    return (u32x4){r0[0], r1[0], r0[1], r1[1]};
}

namespace g32 {
enum { W_P448 = 1, W_P256 = 2, W_OWNT = 3 };
template <int BM_, int BN_, int XI_, int WI_, int NS_, int NL_ = 4> struct Cfg {
    static constexpr int BM = BM_, BN = BN_, XI = XI_, WI = WI_, NS = NS_, NL = NL_;
    static constexpr int XB = BM * 128, WB = BN * 128, WOFF = NS * XB, LDS = NS * (XB + WB);
    static constexpr int PX = BM / 8 / NL, PW = BN / 8 / NL;
    static constexpr int WPITCH = (WI == W_P448) ? 448 : (WI == W_P256) ? 256 : 16;
    static constexpr bool XSC = (XI == X_OWN), WSC = (WI == W_OWNT);
};
template <class C> __device__ __forceinline__ void src_w_raw(Src<C>& s, const void* base, unsigned pitch, int lid, int lane) {
    s.w = (const char*)base - 4096; s.w2 = s.w; s.ksplit = 1 << 20; s.wstep = 64 * pitch;
#pragma unroll
    for (int i = 0; i < C::PW; ++i) { const int o = 1024 * (lid * C::PW + i) + 16 * lane; const int r = o / C::WPITCH; int x = o % C::WPITCH;
        if (C::WI == W_P256) x ^= (r & 3) << 6;
        s.wv[i] = r * pitch + x + 4096 - (i & 3) * 1024; }
}
template <int MT_, int NT_, bool LD_> struct Wv { static constexpr int MT = MT_, NT = NT_; static constexpr bool LD = LD_; };
template <class C, class W> struct Frag { bf16x8 x[W::MT]; s16x4 wl[W::NT], wh[W::NT]; };
template <class C, class W> struct RdAddr { unsigned x[(C::XI == X_RAW) ? 4 : 1]; unsigned w[(C::WI == W_P256) ? W::NT : 1]; };
template <class C, class W> __device__ __forceinline__ void rd_init(RdAddr<C, W>& a, int m0, int n0  , int lane) {
    const int r = lane & 31, h = lane >> 5, g = lane >> 4, q = (lane & 15) >> 2, p = lane & 3;
    if (C::XI == X_RAW) { for (int ks = 0; ks < 4; ++ks) a.x[ks] = (m0 + r) * 128 + (((2 * ks + h) ^ ((r >> 1) & 7)) * 16); }
    else a.x[0] = h * (C::BM * 16) + (m0 + r) * 16;
    const int rowp = 8 * (g >> 1) + q, colp = (g & 1) * 32 + p * 8;
    if (C::WI == W_P448) a.w[0] = C::WOFF + rowp * 448 + colp + n0 * 64;
    else if (C::WI == W_P256) { for (int t = 0; t < W::NT; ++t) a.w[t] = C::WOFF + rowp * 256 + (((n0 + t) ^ q) * 64) + colp; }
    else a.w[0] = C::WOFF + h * (C::BN * 16) + (n0 * 32 + r) * 16;
}
template <class C, class W> constexpr int n_reads() { return W::MT + (C::WI == W_OWNT ? W::NT : 2 * W::NT); }
template <class C, class W> __device__ __forceinline__ void rd_one(Frag<C, W>& f, ldsp bx, ldsp bw, const RdAddr<C, W>& a, int ks, int r) {
    constexpr int PER = (C::WI == W_OWNT) ? 1 : 2;
    if (r >= PER && r < PER + W::MT) { const int m = r - PER;
        if (C::XI == X_RAW) f.x[m] = *(const LAS bf16x8*)(bx + a.x[ks] + m * 4096);
        else f.x[m] = *(const LAS bf16x8*)(bx + a.x[0] + ks * (2 * C::BM * 16) + m * 512);
        return; }
    const int u = r < PER ? r : r - W::MT, t = u / PER, hh = u % PER;
    if (C::WI == W_OWNT) { const bf16x8 v = *(const LAS bf16x8*)(bw + a.w[0] + ks * (2 * C::BN * 16) + t * 512); f.wl[t] = __builtin_shufflevector(v, v, 0, 1, 2, 3); f.wh[t] = __builtin_shufflevector(v, v, 4, 5, 6, 7); }
    else { s16x4 v;
        if (C::WI == W_P448) v = tr_read(bw + a.w[0] + t * 64 + (ks * 16 + hh * 4) * 448);
        else v = tr_read(bw + a.w[t] + (ks * 16 + hh * 4) * 256);
        if (hh) f.wh[t] = v; else f.wl[t] = v; }
}
template <class C, class W> __device__ __forceinline__ void rd_frags(Frag<C, W>& f, ldsp bx, ldsp bw, const RdAddr<C, W>& a, int ks) {
#pragma unroll
    for (int r = 0; r < n_reads<C, W>(); ++r) rd_one<C, W>(f, bx, bw, a, ks, r);
}
template <class C, class W, bool SWAP  > __device__ __forceinline__ void mma_one(f32x16 (&acc)[W::NT][W::MT], const Frag<C, W>& f, int i) {
    const int t = i / W::MT, m = i % W::MT;
    const bf16x8 w = __builtin_shufflevector(f.wl[t], f.wh[t], 0, 1, 2, 3, 4, 5, 6, 7);
    acc[t][m] = SWAP ? __builtin_amdgcn_mfma_f32_32x32x16_bf16(w, f.x[m], acc[t][m], 0, 0, 0) : __builtin_amdgcn_mfma_f32_32x32x16_bf16(f.x[m], w, acc[t][m], 0, 0, 0);
}
struct NoFill { template <int S> __device__ __forceinline__ void chunk() {} };
template <class C, class W, bool SWAP, int V0, int V1, int BASE, int I, class F>
__device__ __forceinline__ void step_slot(f32x16 (&acc)[W::NT][W::MT], const Frag<C, W>& fc, Frag<C, W>& fn, ldsp bx, ldsp bw, const RdAddr<C, W>& a, int ksn,
                                          const Src<C>& s, const char* xb, const char* wb, unsigned dx, unsigned dw, F& fill) {
    constexpr int NM = W::NT * W::MT, NR = n_reads<C, W>(), NV = V1 - V0;
    if constexpr (I < NM) {
        mma_one<C, W, SWAP>(acc, fc, I);
#pragma unroll
        for (int r = I * NR / NM; r < (I + 1) * NR / NM; ++r) rd_one<C, W>(fn, bx, bw, a, ksn, r);
        if constexpr (W::LD) { constexpr int v0 = V0 + I * NV / NM, v1 = V0 + (I + 1) * NV / NM; stage_rng<C, v0, v1, v0 == V0>(dx, dw, s, xb, wb); }
        fill.template chunk<BASE + I>();
        GM_SB();
        step_slot<C, W, SWAP, V0, V1, BASE, I + 1, F>(acc, fc, fn, bx, bw, a, ksn, s, xb, wb, dx, dw, fill);
    }
}
template <class C, class WF  , int D> struct Pipe {
    static constexpr int PX = C::PX, PW = C::PW, NS = C::NS, NV = PX + PW;
#define QSPLIT 0
    static constexpr int Q1 = QSPLIT == 0 ? (NV + 3) / 4 : QSPLIT == 1 ? (NV + 1) / 2 : PW, Q2 = QSPLIT == 0 ? (NV + 1) / 2 : QSPLIT == 1 ? NV : NV, Q3 = QSPLIT == 0 ? (3 * NV + 3) / 4 : NV;
    static constexpr int PRE = (D == 1) ? Q1 : Q2;
    ldsp lds; int nt, xmod;
    RdAddr<C, WF> a; unsigned lx0, lw0;
    Frag<C, WF> f0, f1, f2, f3;
    __device__ __forceinline__ Pipe(ldsp lds_, const Src<C>& s, int nt_, int xmod_, int lid, int m0, int n0, int lane) : lds(lds_), nt(nt_), xmod(xmod_) {
        rd_init<C, WF>(a, m0, n0, lane);
        const unsigned ldb = (unsigned)(uintptr_t)lds;
        lx0 = ldb + lid * PX * 1024; lw0 = ldb + C::WOFF + lid * PW * 1024;
    }
    __device__ __forceinline__ void issue_x(const Src<C>& s) {
        if constexpr (WF::LD) {
#pragma unroll
            for (int i = 0; i < NS - 1; ++i) stage_rng<C, PW, NV, true>(lx0 + i * C::XB, 0, s, s.x + (size_t)i * s.xstep, nullptr);
        }
    }
    __device__ __forceinline__ void issue_w(const Src<C>& s) {
        static_assert(PRE <= PW, "prologue split");
        if constexpr (WF::LD) {
#pragma unroll
            for (int i = 0; i < NS - 1; ++i) stage_rng<C, 0, PW, true>(0, lw0 + i * C::WB, s, nullptr, w_tile<C>(s, i));
            stage_rng<C, 0, PRE, true>(0, lw0 + (NS - 1) * C::WB, s, nullptr, w_tile<C>(s, NS - 1));
        }
    }
    __device__ __forceinline__ void landed() {
        if constexpr (WF::LD) asm volatile("s_waitcnt vmcnt(0)" ::: "memory");
        GM_BAR();
        rd_frags<C, WF>(f0, lds, lds, a, 0);
        if (D == 2) rd_frags<C, WF>(f1, lds, lds, a, 1);
    }
    __device__ __forceinline__ void begin(const Src<C>& s) {
        if constexpr (WF::LD) {
#pragma unroll
            for (int i = 0; i < NS - 1; ++i) stage_rng<C, 0, NV, true>(lx0 + i * C::XB, lw0 + i * C::WB, s, s.x + (size_t)i * s.xstep, w_tile<C>(s, i));
            stage_rng<C, 0, PRE, true>(0, lw0 + (NS - 1) * C::WB, s, nullptr, w_tile<C>(s, NS - 1));
            asm volatile("s_waitcnt vmcnt(%0)" ::"n"((NS - 2) * NV + PRE) : "memory");
        }
        GM_BAR();
        rd_frags<C, WF>(f0, lds, lds, a, 0);
        if (D == 2) rd_frags<C, WF>(f1, lds, lds, a, 1);
    }
    template <bool SWAP, class H> __device__ __forceinline__ void run(const Src<C>& s, int t0, int t1, f32x16 (&acc)[WF::NT][WF::MT], H& hook DBG_PARAMS) {
        NoFill nf; run_fill<SWAP>(s, t0, t1, t0, acc, hook, nf DBG_FWD);
    }
    template <bool SWAP, class H, class F> __device__ __forceinline__ void run_fill(const Src<C>& s, int t0, int t1, int tb, f32x16 (&acc)[WF::NT][WF::MT], H& hook, F& fill DBG_PARAMS) {
        using W = WF; constexpr int NM = WF::MT * WF::NT;
        int sl = t0 % NS, kn = t0 - 1 + NS; kn = kn < nt ? kn : nt - 1;
        const char* xb = s.x + (unsigned)(kn & (xmod - 1)) * s.xstep; const char* wb = w_tile<C>(s, kn);
        unsigned dx = lx0 + ((sl == 0) ? NS - 1 : sl - 1) * C::XB, dw = lw0 + ((sl == 0) ? NS - 1 : sl - 1) * C::WB;
        __builtin_amdgcn_s_waitcnt(0xC07F);
        for (int t = t0; t < t1; ++t) {
            const int ns = (sl + 1 == NS) ? 0 : sl + 1;
            ldsp cx = lds + sl * C::XB, cw = lds + sl * C::WB, nx = lds + ns * C::XB, nw = lds + ns * C::WB;
            TRACE(t - tb);
            hook.top(t - tb);
            GM_SB();
#define G32_STEP(FC, FN, BX, BW, KSN, V0, V1, ST) step_slot<C, W, SWAP, V0, V1, ST * NM, 0, F>(acc, FC, FN, BX, BW, a, KSN, s, xb, wb, dx, dw, fill)
            if constexpr (D == 1) { G32_STEP(f0, f1, cx, cw, 1, Q1, Q2, 0); G32_STEP(f1, f2, cx, cw, 2, Q2, Q3, 1); G32_STEP(f2, f3, cx, cw, 3, Q3, NV, 2); }
            else { G32_STEP(f0, f2, cx, cw, 2, Q2, Q3, 0); G32_STEP(f1, f3, cx, cw, 3, Q3, NV, 1); }
            hook.at(t - tb);
            asm volatile("s_waitcnt lgkmcnt(0)" ::: "memory");
            if constexpr (W::LD) asm volatile("s_waitcnt vmcnt(%0)" ::"n"((NS - 2) * NV) : "memory");
            GM_BAR();
            GM_SB();
            if constexpr (W::LD) { kn = (kn + 1 < nt) ? kn + 1 : kn; xb = s.x + (unsigned)(kn & (xmod - 1)) * s.xstep; wb = w_tile<C>(s, kn); dx = lx0 + sl * C::XB; dw = lw0 + sl * C::WB; }
            if constexpr (D == 1) { G32_STEP(f3, f0, nx, nw, 0, 0, Q1, 3); }
            else { G32_STEP(f2, f0, nx, nw, 0, 0, Q1, 2); G32_STEP(f3, f1, nx, nw, 1, Q1, Q2, 3); }
#undef G32_STEP
            sl = ns;
        }
        GM_SB();
    }
    __device__ __forceinline__ void end() {
        asm volatile("s_waitcnt vmcnt(0) lgkmcnt(0)" ::: "memory");
        GM_BAR();
    }
};
template <class C, class WF> struct PipeKS {
    static constexpr int PX = C::PX, PW = C::PW, NS = C::NS, NV = PX + PW, NM = WF::MT * WF::NT;
    ldsp lds; int nt, kh;
    RdAddr<C, WF> a; unsigned lx0, lw0;
    Frag<C, WF> a0, a1, b0, b1;
    __device__ __forceinline__ PipeKS(ldsp lds_, int nt_, int kh_, int lid, int m0, int n0, int lane) : lds(lds_), nt(nt_), kh(kh_) {
        rd_init<C, WF>(a, m0, n0, lane);
        static_assert(C::XI == X_OWN && C::WI == W_P256, "layouts of the output GEMM");
        a.x[0] += kh * 2 * (2 * C::BM * 16);
#pragma unroll
        for (int t = 0; t < WF::NT; ++t) a.w[t] += kh * 32 * 256;
        const unsigned ldb = (unsigned)(uintptr_t)lds;
        lx0 = ldb + lid * PX * 1024; lw0 = ldb + C::WOFF + lid * PW * 1024;
    }
    __device__ __forceinline__ void issue_w(const Src<C>& s) {
        if constexpr (WF::LD) {
#pragma unroll
            for (int i = 0; i < NS; ++i) stage_rng<C, 0, PW, true>(0, lw0 + i * C::WB, s, nullptr, w_tile<C>(s, i)); }
    }
    __device__ __forceinline__ void issue_x(const Src<C>& s) {
        if constexpr (WF::LD) {
#pragma unroll
            for (int i = 0; i < NS; ++i) stage_rng<C, PW, NV, true>(lx0 + i * C::XB, 0, s, s.x + (size_t)i * s.xstep, nullptr); }
    }
    __device__ __forceinline__ void landed() {
        if constexpr (WF::LD) asm volatile("s_waitcnt vmcnt(0)" ::: "memory");
        GM_BAR();
        rd_frags<C, WF>(a0, lds, lds, a, 0); rd_frags<C, WF>(a1, lds, lds, a, 1);
    }
    template <class H> __device__ __forceinline__ void run(const Src<C>& s, f32x16 (&acc)[WF::NT][WF::MT], H& hook DBG_PARAMS) {
        using W = WF; NoFill fill; using F = NoFill;
        int sl = 0, kn = NS;
        __builtin_amdgcn_s_waitcnt(0xC07F);
#define KS_PERIOD(T, C0, C1, N0, N1) do { \
            TRACE(T); hook.at(T); \
            asm volatile("s_waitcnt lgkmcnt(0)" ::: "memory"); \
            if constexpr (W::LD) asm volatile("s_waitcnt vmcnt(%0)" ::"n"((NS - 2) * NV) : "memory"); \
            GM_BAR(); GM_SB(); \
            const int ns = (sl + 1 == NS) ? 0 : sl + 1, kc = kn < nt ? kn : nt - 1; \
            const char* xb = s.x + (unsigned)kc * s.xstep; const char* wb = w_tile<C>(s, kc); \
            const unsigned dx = lx0 + sl * C::XB, dw = lw0 + sl * C::WB; \
            ldsp nx = lds + ns * C::XB, nw = lds + ns * C::WB; \
            step_slot<C, W, true, 0, NV / 2, 0, 0, F>(acc, C0, N0, nx, nw, a, 0, s, xb, wb, dx, dw, fill); \
            step_slot<C, W, true, NV / 2, NV, NM, 0, F>(acc, C1, N1, nx, nw, a, 1, s, xb, wb, dx, dw, fill); \
            sl = ns; ++kn; } while (0)
        for (int t = 0; t < nt; t += 2) { KS_PERIOD(t, a0, a1, b0, b1); if (t + 1 >= nt) break; KS_PERIOD(t + 1, b0, b1, a0, a1); }
#undef KS_PERIOD
        GM_SB();
        asm volatile("s_waitcnt vmcnt(0) lgkmcnt(0)" ::: "memory");
        GM_BAR();
    }
};
template <class W> __device__ __forceinline__ void acc_zero(f32x16 (&acc)[W::NT][W::MT]) {
#pragma unroll
    for (int t = 0; t < W::NT; ++t)
#pragma unroll
        for (int m = 0; m < W::MT; ++m) acc[t][m] = (f32x16){};
}
__device__ __forceinline__ u32x4 pair32(unsigned a0, unsigned a1, unsigned b0, unsigned b1) {
    const auto r0 = __builtin_amdgcn_permlane32_swap(a0, b0, false, false); const auto r1 = __builtin_amdgcn_permlane32_swap(a1, b1, false, false);
    return (u32x4){r0[0], r1[0], r0[1], r1[1]};
}
}

struct Params {
    const u16* ax; const u16* wqkv; const u16* wo; const u16* bx; const u16* win; const u16* wsp; const u16* bsp; const u16* wout; const u16* gw;
    u16* out; unsigned char* ws;
};
struct Ctx : Params { unsigned epv; __device__ __forceinline__ unsigned tg() const { return (unsigned)__builtin_amdgcn_readfirstlane(epv) + 1u; } };

using CfgQ = Cfg<256, 192, X_RAW, W_P384, 3, 2>;
struct Warm {
    const char* base; unsigned pitch; int lpr_log2; int cnt; unsigned scratch;
    __device__ __forceinline__ unsigned mask(int t0, int wid) const {
        if (wid < 4 || cnt <= 0) return 0u;
        return ((0x11111111u << (wid - 4)) & (cnt >= 32 ? ~0u : ((1u << cnt) - 1u))) << t0;
    }
    __device__ __forceinline__ void issue(int n, int wid, int lane) const {
        if (n >= 0 && n < cnt && wid == 4 + (n & 3)) { const int idx = n * 64 + lane; pf_lines(base, (unsigned)(idx >> lpr_log2) * pitch + (unsigned)(idx & ((1 << lpr_log2) - 1)) * 128u, scratch); }
    }
};

struct SigHook {
    static constexpr bool PRE = false; int wid, lane; Warm w0, w1; unsigned act;
    __device__ __forceinline__ void pre_x() const {}
    __device__ __forceinline__ void top(int) const {}
    __device__ __forceinline__ void at(int t) const { if ((act >> t) & 1u) { w0.issue(t - 1, wid, lane); w1.issue(t - 1, wid, lane); } }
};
template <int WN> __device__ __forceinline__ void qkv_wave(const Ctx& p, ldsp lds, int pair, int panel, int wid, int lane) {
    using C = CfgQ; using W = Wv<4, 6, WN * 6, WN ? 0x3Cu : 0u, WN == 0>;
    const int lid = wid & 3, m0 = (wid & 3) * 64;
    Src<C> s;
    src_x_raw<C>(s, p.ax + (size_t)panel * 256 * DM, DM * 2, lid, lane);
    src_w_raw<C>(s, p.wqkv + pair * 64, 3 * DM * 2, DM * 2, lid, lane);
    f32x4 acc[6][4]; acc_zero<W>(acc);
    STAMP(0, 1);
    Warm none{nullptr, 0, 0, 0, 0};
    SigHook hk{wid, lane, none, none, 0u};
    gemm_loop<C, W, Sch<12, 7>>(lds, s, DM / 64, acc, lid, m0, 0, lane, hk DBG_ARGS(0));
    STAMP(0, 2);
    const int fr = lane & 15, fq = lane >> 4;
    const int row0 = panel * 256 + m0;
    const rsrc_t rs = ws_rsrc(p.ws);
#pragma unroll
    for (int t = 0; t < 6; t += 2) {
        const int T = WN * 6 + t, kind = T >> 2, head = pair * 2 + ((T >> 1) & 1);
        if (kind < 2) {
            const unsigned base = (unsigned)(kind == 0 ? WS_Q : WS_K);
            const int c = 2 * (fq & 1) + (fq >> 1);
            const float sc = kind == 0 ? QSCALE : 1.0f;
#pragma unroll
            for (int m = 0; m < 4; ++m) { const int row = row0 + m * 16 + fr;
                st16_wt(rs, base + ((head * 64 + (row >> 5)) * 4 + c) * 512 + (row & 31) * 16, pair16(acc[t][m] * sc, acc[t + 1][m] * sc)); }
        } else {
#pragma unroll
            for (int tt = 0; tt < 2; ++tt)
#pragma unroll
                for (int m = 0; m < 4; m += 2) {
                    const f32x4 A = acc[t + tt][m], B = acc[t + tt][m + 1];
                    const unsigned a0 = pk_bf16(A[0], A[1]), a1 = pk_bf16(A[2], A[3]), b0 = pk_bf16(B[0], B[1]), b1 = pk_bf16(B[2], B[3]);
                    const auto r0 = __builtin_amdgcn_permlane32_swap(a0, b0, false, false); const auto r1 = __builtin_amdgcn_permlane32_swap(a1, b1, false, false);
                    const int kb = (row0 >> 4) + m + (fq >> 1), h = fq & 1, d = tt * 16 + fr;
                    st16_wt(rs, (unsigned)WS_V + ((head * 128 + kb) * 2 + h) * 512 + d * 16, (u32x4){r0[0], r1[0], r0[1], r1[1]});
                }
        }
    }
    asm volatile("s_waitcnt vmcnt(0)" ::: "memory");
    GM_BAR();
    if (threadIdx.x == 0) signal(p.ws, F_Q + pair * 32 + panel, p.tg());
}

using CfgZ = g32::Cfg<128, 224, X_RAW, g32::W_P448, 3>;
using CfgS = g32::Cfg<128, 224, X_RAW, g32::W_OWNT, 3>;
#define ONS 5
#define ONL 4
using CfgO = g32::Cfg<128, 128, X_OWN, g32::W_P256, ONS, ONL>;
__device__ __forceinline__ float gelu_tanh(float x) {
    const float t = x * x * (-0.044715f * 2.0f * 0.7978845608028654f * 1.4426950408889634f) + (-2.0f * 0.7978845608028654f * 1.4426950408889634f);
    return x * __builtin_amdgcn_rcpf(1.0f + __builtin_amdgcn_exp2f(x * t));
}
template <int N> __device__ __forceinline__ void gelu_pairs(unsigned* z) {
    constexpr float C1 = -0.044715f * 2.0f * 0.7978845608028654f * 1.4426950408889634f, C2 = -2.0f * 0.7978845608028654f * 1.4426950408889634f;
    f32x2 x[N], e[N];
#pragma unroll
    for (int i = 0; i < N; ++i) x[i] = (f32x2){bf_lo(z[i]), bf_hi(z[i])};
#pragma unroll
    for (int i = 0; i < N; ++i) e[i] = x[i] * (x[i] * x[i] * C1 + C2);
    GM_SB();
#pragma unroll
    for (int i = 0; i < N; ++i) { e[i].x = __builtin_amdgcn_exp2f(e[i].x); e[i].y = __builtin_amdgcn_exp2f(e[i].y); }
    GM_SB();
#pragma unroll
    for (int i = 0; i < N; ++i) e[i] = e[i] + 1.0f;
#pragma unroll
    for (int i = 0; i < N; ++i) { e[i].x = __builtin_amdgcn_rcpf(e[i].x); e[i].y = __builtin_amdgcn_rcpf(e[i].y); }
    GM_SB();
#pragma unroll
    for (int i = 0; i < N; ++i) { const f32x2 y = x[i] * e[i]; z[i] = pk_bf16(y.x, y.y); }
    GM_SB();
}
__device__ __forceinline__ void gelu16(f32x16& a) {
    constexpr float C1 = -0.044715f * 2.0f * 0.7978845608028654f * 1.4426950408889634f, C2 = -2.0f * 0.7978845608028654f * 1.4426950408889634f;
    f32x16 e = a * (a * a * C1 + C2);
    GM_SB();
#pragma unroll
    for (int i = 0; i < 16; ++i) e[i] = __builtin_amdgcn_exp2f(e[i]);
    GM_SB();
    e = e + 1.0f;
#pragma unroll
    for (int i = 0; i < 16; ++i) e[i] = __builtin_amdgcn_rcpf(e[i]);
    GM_SB();
    a = a * e;
}
template <int CTRL> __device__ __forceinline__ float dpp_add(float x) { return x + __int_as_float(__builtin_amdgcn_update_dpp(0, __float_as_int(x), CTRL, 0xf, 0xf, false)); }
__device__ __forceinline__ float red32_pair(float a, float b) {
    const auto r = __builtin_amdgcn_permlane16_swap(__float_as_uint(a), __float_as_uint(b), false, false);
    float x = __uint_as_float(r[0]) + __uint_as_float(r[1]);
    x = dpp_add<0x128>(x); x = dpp_add<0x124>(x); x = dpp_add<0x122>(x); x = dpp_add<0x121>(x);
    return x;
}
constexpr int LDS_TAB = CfgZ::LDS;
template <int NT> struct GeluFill {
    static constexpr int NSLOT = 4 * NT, STRIDE = NSLOT / 4;
    unsigned (&z)[NT][8];
    f32x2 x, e;
    template <int S> __device__ __forceinline__ void chunk() {
        constexpr float C1 = -0.044715f * 2.0f * 0.7978845608028654f * 1.4426950408889634f, C2 = -2.0f * 0.7978845608028654f * 1.4426950408889634f;
        if constexpr (S % STRIDE == 0) { constexpr int pa = S / STRIDE;
            x = (f32x2){bf_lo(z[0][pa]), bf_hi(z[0][pa])};
            const f32x2 w = x * (x * x * C1 + C2);
            e.x = __builtin_amdgcn_exp2f(w.x); e.y = __builtin_amdgcn_exp2f(w.y);
#pragma unroll
            for (int g = 0; g + 1 < 2 * NT; ++g) z[g >> 1][(g & 1) * 4 + pa] = z[(g + 1) >> 1][((g + 1) & 1) * 4 + pa];
            asm volatile("" : "+v"(x), "+v"(e));
        }
        if constexpr (S % STRIDE == 2) { constexpr int pb = S / STRIDE;
            const f32x2 d = e + 1.0f;
            const f32x2 y = x * (f32x2){__builtin_amdgcn_rcpf(d.x), __builtin_amdgcn_rcpf(d.y)};
            z[NT - 1][4 + pb] = pk_bf16(y.x, y.y);
            asm volatile("" : "+v"(z[NT - 1][4 + pb]));
        }
    }
};
template <int NT, bool LD> struct LnHook {
    const Ctx& p; ldsp lds; int panel, j, wid, lane, n0; unsigned (&z2)[NT][8];
    u32x4 w[4];
    __device__ __forceinline__ void normalise(int tt) {
        LAS f32x2* R = (LAS f32x2*)(lds + LDS_TAB + 2048);
        const int r = lane & 31, h = lane >> 5, m0 = (wid & 3) * 32;
        const rsrc_t rs = ws_rsrc(p.ws);
        const int srow = panel * 128 + m0;
#pragma unroll
        for (int i = 0; i < 4; i += 2) {
            const f32x4 ra = *(const LAS f32x4*)(R + m0 + 8 * i + 4 * h), rb = *(const LAS f32x4*)(R + m0 + 8 * i + 4 * h + 2);
            const f32x4 rc = *(const LAS f32x4*)(R + m0 + 8 * i + 8 + 4 * h), rd = *(const LAS f32x4*)(R + m0 + 8 * i + 8 + 4 * h + 2);
            const int k = srow + 8 * (i + h);
            const unsigned base = (unsigned)WS_Z + ((j * 32 + (k >> 6)) * 8 + ((k & 63) >> 3)) * 3584 + (n0 * 32 + r) * 16;
            const unsigned a0 = z2[tt][2 * i], a1 = z2[tt][2 * i + 1], b0 = z2[tt][2 * i + 2], b1 = z2[tt][2 * i + 3];
            st16_wt(rs, base + tt * 512, g32::pair32(pk_bf16(fmaf(bf_lo(a0), ra[1], ra[0]), fmaf(bf_hi(a0), ra[3], ra[2])), pk_bf16(fmaf(bf_lo(a1), rb[1], rb[0]), fmaf(bf_hi(a1), rb[3], rb[2])),
                                                      pk_bf16(fmaf(bf_lo(b0), rc[1], rc[0]), fmaf(bf_hi(b0), rc[3], rc[2])), pk_bf16(fmaf(bf_lo(b1), rd[1], rd[0]), fmaf(bf_hi(b1), rd[3], rd[2]))));
        }
    }
    __device__ __forceinline__ void normalise_at(int t) {
        if (t >= 5 && t < 9) { if (at_tile(t, 5)) normalise(0); if (at_tile(t, 6)) normalise(1); if (at_tile(t, 7)) normalise(2); if constexpr (NT == 4) { if (at_tile(t, 8)) normalise(3); } }
    }
    __device__ __forceinline__ void fetch() {
        const unsigned off = (unsigned)WS_STAT + ((panel * 8 + (lane >> 5) * 4) * 128 + (wid - 4) * 32 + (lane & 31)) * 16;
#pragma unroll
        for (int v = 0; v < 4; ++v) w[v] = ld16_dev(ws_rsrc(p.ws), off + v * 2048);
    }
    __device__ __forceinline__ bool fetched() const { bool ok = true;
#pragma unroll
        for (int v = 0; v < 4; ++v) ok = ok && w[v][1] == p.tg() && w[v][3] == p.tg();
        return __all(ok); }
    __device__ __forceinline__ void top(int t) {
        if constexpr (LD) normalise_at(t);
        else { if (t == 4) fetch(); }
    }
    __device__ __forceinline__ void at(int t) {
        if (t < 4 || t > 11) return;
        if constexpr (!LD) {
            if (at_tile(t, 4)) {
                for (unsigned spins = 0; !fetched();) {
                    __builtin_amdgcn_s_sleep(2); fetch();
                    if (++spins > (1u << 19)) { __hip_atomic_store((gu32*)(p.ws + WS_CTL) + C_TMO, p.tg(), RLX_AGENT); break; }
                }
                LAS f32x2* R = (LAS f32x2*)(lds + LDS_TAB + 2048);
                float ms = 0.f;
#pragma unroll
                for (int v = 0; v < 4; ++v) ms += __uint_as_float(w[v][0]);
                { const auto r = __builtin_amdgcn_permlane32_swap(__float_as_uint(ms), __float_as_uint(ms), false, false); ms = __uint_as_float(r[0]) + __uint_as_float(r[1]); }
                const float mean = ms * 0.125f; float q = 0.f;
#pragma unroll
                for (int v = 0; v < 4; ++v) { const float dm = __uint_as_float(w[v][0]) - mean; q += __uint_as_float(w[v][2]) + 224.0f * dm * dm; }
                { const auto r = __builtin_amdgcn_permlane32_swap(__float_as_uint(q), __float_as_uint(q), false, false); q = __uint_as_float(r[0]) + __uint_as_float(r[1]); }
                { const float rs_ = __builtin_amdgcn_rsqf(q * (1.0f / 1792.0f) + 1e-5f); if (lane < 32) R[(wid - 4) * 32 + lane] = (f32x2){-mean * rs_, rs_}; }
                GM_VM0();
            }
            normalise_at(t);
        }
        if (at_tile(t, 10)) asm volatile("s_waitcnt vmcnt(0)" ::: "memory");
        if constexpr (!LD) { if (at_tile(t, 11)) { if (threadIdx.x == 256) signal(p.ws, F_Z + j * 32 + panel, p.tg()); } }
    }
};
struct SpHook {
    static constexpr bool PRE = false; const Ctx& p; ldsp lds; int panel, wid, lane;
    __device__ __forceinline__ void pre_x() const {}
    __device__ __forceinline__ void top(int) const {}
    __device__ __forceinline__ void at(int t) {
        if (t == 2 && (wid == 4 || wid == 5)) {
            poll_flags(p.ws, p.tg(), F_Q, 1, true, F_Q, lane);
            const int row = (wid - 4) * 64 + lane, trow = panel * 128 + row;
            ((LAS f32x2*)(lds + LDS_TAB + 2048))[row] = (f32x2){__uint_as_float((unsigned)p.bsp[trow] << 16), 1.0f - __uint_as_float(__hip_atomic_load((gu32*)(p.ws + WS_GATE) + trow, RLX_AGENT))};
            GM_VM0();
        }
    }
};
struct WaitHook {
    unsigned char* ws; unsigned tag; int wid, lane, tlate, wl, nl;
    __device__ __forceinline__ void top(int) const {}
    __device__ __forceinline__ void at(int t) const { if (t == tlate && wid == 4) wait_flags(ws, tag, wl, nl, false, 0, lane); }
};
__device__ __forceinline__ void out_part_early_w(const Ctx& p, ldsp lds, int j, int wid, int lane) {
    using C = CfgO; using W = g32::Wv<2, 2, true>;
    static_assert(C::WOFF >= 65536, "W slots behind the attention ring");
    if (wid < 4) {
        Src<C> s; g32::src_w_raw<C>(s, p.wo + (size_t)(16 - NA) * 64 * DM + j * 128, DM * 2, wid, lane);
        g32::PipeKS<C, W> pipe(lds, NA, 0, wid, 0, 0, lane);
        pipe.issue_w(s);
    }
}
template <bool LD, bool FINAL> __device__ __forceinline__ void out_part(const Ctx& p, ldsp lds, int panel, int j, int wid, int lane) {
    using C = CfgO; using W = g32::Wv<2, 2, LD>;
    const int lid = wid & 3, r = lane & 31, h = lane >> 5;
    const int kh = wid >> 2, mo = ((wid >> 1) & 1) * 64, no = (wid & 1) * 2;
    Src<C> s;
    src_x_own<C>(s, p.ws + WS_ACT + (size_t)panel * 352 * 2048 + (FINAL ? 0 : (FH / 64 + 16 - NA) * C::XB), lid, lane);
    g32::src_w_raw<C>(s, FINAL ? p.wout + j * 128 : p.wo + (size_t)(16 - NA) * 64 * DM + j * 128, DM * 2, lid, lane);
    if (FINAL) set_w2<C>(s, p.wo + j * 128, FH / 64);
    f32x16 acc[2][2]; g32::acc_zero<W>(acc);
    WaitHook hk{p.ws, p.tg(), wid, lane, FINAL && NA < 16 ? FH / 64 - C::NS : -1, F_O + (panel >> 2) * 32, 32};
    constexpr int NKT = FINAL ? FH / 64 + 16 - NA : NA;
    static_assert(NKT % 2 == 0 || true, "");
    g32::PipeKS<C, W> pipe(lds, NKT, kh, lid, mo, no, lane);
    if (FINAL) pipe.issue_w(s);
    if (wid == 4) { if (FINAL) wait_flags(p.ws, p.tg(), F_U + panel * 32, 8, false, 0, lane); else wait_flags(p.ws, p.tg(), F_O + (panel >> 2) * 32, 32, false, 0, lane); }
    GM_BAR();
    pipe.issue_x(s);
    pipe.landed();
    STAMP(0, FINAL ? 10 : 7);
    pipe.run(s, acc, hk DBG_ARGS(4));
    STAMP(0, FINAL ? 9 : 8);
    ldsp mine = lds + wid * 8192 + lane * 16, theirs = lds + (wid ^ 4) * 8192 + lane * 16;
#pragma unroll
    for (int t = 0; t < 2; ++t)
#pragma unroll
        for (int i = 0; i < 4; ++i) { const f32x16& A = LD ? acc[t][1] : acc[t][0]; *(LAS f32x4*)(mine + (t * 4 + i) * 1024) = (f32x4){A[4 * i], A[4 * i + 1], A[4 * i + 2], A[4 * i + 3]}; }
    asm volatile("s_waitcnt lgkmcnt(0)" ::: "memory");
    if (FINAL && wid == 4) wait_flags(p.ws, p.tg(), F_P + panel * 32 + j, 1, false, 0, lane);
    GM_BAR();
    const rsrc_t rs = ws_rsrc(p.ws);
    const unsigned pb = (unsigned)WS_P + ((panel * 8 + j) * 8 + wid) * 8192 + lane * 16;
    f32x16 o[2];
    u32x4 pv[8];
    if (FINAL) {
#pragma unroll
        for (int q = 0; q < 8; ++q) pv[q] = ld16_dev(rs, pb + q * 1024);
    }
#pragma unroll
    for (int t = 0; t < 2; ++t) { o[t] = LD ? acc[t][0] : acc[t][1];
#pragma unroll
        for (int i = 0; i < 4; ++i) { const f32x4 v = *(const LAS f32x4*)(theirs + (t * 4 + i) * 1024); o[t][4 * i] += v[0]; o[t][4 * i + 1] += v[1]; o[t][4 * i + 2] += v[2]; o[t][4 * i + 3] += v[3]; } }
    if (!FINAL) {
#pragma unroll
        for (int t = 0; t < 2; ++t)
#pragma unroll
            for (int i = 0; i < 4; ++i) st16_wt(rs, pb + (t * 4 + i) * 1024, (u32x4){__float_as_uint(o[t][4 * i]), __float_as_uint(o[t][4 * i + 1]), __float_as_uint(o[t][4 * i + 2]), __float_as_uint(o[t][4 * i + 3])});
        asm volatile("s_waitcnt vmcnt(0)" ::: "memory");
        GM_BAR();
        if (threadIdx.x == 0) signal(p.ws, F_P + panel * 32 + j, p.tg());
    } else {
#pragma unroll
        for (int t = 0; t < 2; ++t)
#pragma unroll
            for (int i = 0; i < 4; ++i)
#pragma unroll
                for (int e = 0; e < 4; ++e) o[t][4 * i + e] += __uint_as_float(pv[t * 4 + i][e]);
        ldsp st = lds + 65536;
#pragma unroll
        for (int t = 0; t < 2; ++t)
#pragma unroll
            for (int i = 0; i < 4; i += 2) { const f32x16& A = o[t];
                *(LAS u32x4*)(st + (mo + kh * 32 + r) * 272 + (no + t) * 64 + (i + h) * 16) =
                    g32::pair32(pk_bf16(A[4 * i], A[4 * i + 1]), pk_bf16(A[4 * i + 2], A[4 * i + 3]), pk_bf16(A[4 * i + 4], A[4 * i + 5]), pk_bf16(A[4 * i + 6], A[4 * i + 7])); }
        asm volatile("s_waitcnt lgkmcnt(0)" ::: "memory"); GM_BAR();
#pragma unroll
        for (int i = 0; i < 4; ++i) { const int row = wid * 16 + i * 4 + (lane >> 4);
            __builtin_amdgcn_raw_buffer_store_b128(*(const LAS u32x4*)(st + row * 272 + (lane & 15) * 16), __builtin_amdgcn_make_buffer_rsrc(p.out, 0, SEQ * DM * 2, 0x00020000), (unsigned)(((panel * 128 + row) * DM + j * 128 + (lane & 15) * 8) * 2), 0, 16); }
    }
}
template <bool LD> __device__ __forceinline__ void b_wave(const Ctx& p, ldsp lds, int panel, int j, int wid, int lane) {
    constexpr int NT = LD ? 3 : 4;
    const int lid = wid & 3, m0 = lid * 32, n0 = LD ? 0 : 3;
    const int r = lane & 31, h = lane >> 5;
    unsigned z2[NT][8], z1[NT][8];
    const unsigned pfs = (unsigned)(uintptr_t)lds + LDS_PFS;
    {
        using C = CfgZ; using W = g32::Wv<1, NT, LD>; Src<C> s;
        src_x_raw<C>(s, p.ax + (size_t)panel * 128 * DM, DM * 2, lid, lane);
        g32::src_w_raw<C>(s, p.win + FH + j * 224, 2 * FH * 2, lid, lane);
        set_w2<C>(s, p.win + j * 224, DM / 64);
        g32::Pipe<C, W, 1> pipe(lds, s, 2 * DM / 64, DM / 64, lid, m0, n0, lane);
        f32x16 acc[NT][1]; g32::acc_zero<W>(acc);
        STAMP(0, 1);
        Warm wz1{(const char*)p.win + ((j * 224 * 2) & ~127) + (size_t)(panel & 3) * 256 * (2 * FH * 2), 2 * FH * 2, 2, 13, pfs};
        Warm none{nullptr, 0, 0, 0, 0};
        SigHook hk{wid, lane, wz1, none, wz1.mask(1, wid)};
        pipe.begin(s);
        pipe.template run<false>(s, 0, DM / 64, acc, hk DBG_ARGS(1));
        STAMP(0, 2);
        {
            float sm[16], sq[16];
#pragma unroll
            for (int t = 0; t < NT; ++t) {
                gelu16(acc[t][0]);
#pragma unroll
                for (int q = 0; q < 16; ++q) { const float v = acc[t][0][q]; sm[q] = t ? sm[q] + v : v; sq[q] = t ? sq[q] + v * v : v * v; }
#pragma unroll
                for (int q = 0; q < 16; q += 2) z2[t][q >> 1] = pk_bf16(acc[t][0][q], acc[t][0][q + 1]);
                acc[t][0] = (f32x16){};
            }
            float ra[16];
#pragma unroll
            for (int k = 0; k < 8; ++k) {
                const auto a = __builtin_amdgcn_permlane16_swap(__float_as_uint(sm[2 * k]), __float_as_uint(sm[2 * k + 1]), false, false); ra[k] = __uint_as_float(a[0]) + __uint_as_float(a[1]);
                const auto b2 = __builtin_amdgcn_permlane16_swap(__float_as_uint(sq[2 * k]), __float_as_uint(sq[2 * k + 1]), false, false); ra[8 + k] = __uint_as_float(b2[0]) + __uint_as_float(b2[1]); }
#pragma unroll
            for (int k = 0; k < 16; ++k) ra[k] = dpp_add<0x128>(ra[k]);
#pragma unroll
            for (int k = 0; k < 16; ++k) ra[k] = dpp_add<0x124>(ra[k]);
#pragma unroll
            for (int k = 0; k < 16; ++k) ra[k] = dpp_add<0x122>(ra[k]);
#pragma unroll
            for (int k = 0; k < 16; ++k) ra[k] = dpp_add<0x121>(ra[k]);
            float va = ra[0], vb = ra[8];
#pragma unroll
            for (int k = 1; k < 8; ++k) { const bool sel = (lane & 7) == k; va = sel ? ra[k] : va; vb = sel ? ra[8 + k] : vb; }
            LAS f32x2* P = (LAS f32x2*)(lds + LDS_TAB);
            { const int q = 2 * (lane & 7) + ((lane >> 4) & 1), row = m0 + 8 * (q >> 2) + 4 * h + (q & 3);
              if ((lane & 8) == 0) P[row * 2 + (LD ? 0 : 1)] = (f32x2){va, vb}; }
            asm volatile("s_waitcnt lgkmcnt(0)" ::: "memory"); GM_BAR();
            if constexpr (!LD) {
                if (lane < 32) {
                    const int row = lid * 32 + lane;
                    const f32x2 a = P[row * 2], b = P[row * 2 + 1];
                    const float su = a.x + b.x, mt = su * (1.0f / 224.0f), m2 = fmaxf(a.y + b.y - su * mt, 0.f);
                    st16_wt(ws_rsrc(p.ws), (unsigned)WS_STAT + ((panel * 8 + j) * 128 + row) * 16, (u32x4){__float_as_uint(mt), p.tg(), __float_as_uint(m2), p.tg()});
                }
            }
        }
        STAMP(0, 3);
        LnHook<NT, LD> hl{p, lds, panel, j, wid, lane, n0, z2};
        pipe.template run<true>(s, DM / 64, 2 * DM / 64, acc, hl DBG_ARGS(2));
        pipe.end();
        STAMP(0, 4);
#pragma unroll
        for (int t = 0; t < NT; ++t)
#pragma unroll
            for (int q = 0; q < 16; q += 2) z1[t][q >> 1] = pk_bf16(acc[t][0][q], acc[t][0][q + 1]);
    }
    {
        using C = CfgS; using W = g32::Wv<1, NT, LD>; Src<C> s;
        src_x_raw<C>(s, p.wsp + (size_t)panel * 128 * SEQ, SEQ * 2, lid, lane);
        src_w_own<C>(s, p.ws + WS_Z + (size_t)j * 32 * 28672, lid, lane);
        g32::Pipe<C, W, SPD> pipe(lds, s, SEQ / 64, SEQ / 64, lid, m0, n0, lane);
        pipe.issue_x(s);
        if (wid == 4) wait_flags(p.ws, p.tg(), F_Z + j * 32, 16, false, 0, lane);
        GM_BAR();
        pipe.issue_w(s);
        STAMP(0, 5);
#pragma unroll
        for (int t = 0; t < NT; ++t) { gelu_pairs<8>(z1[t]);
#pragma unroll
            for (int q = 0; q < 8; ++q) asm volatile("" : "+v"(z1[t][q])); }
        STAMP(0, 6);
        pipe.landed();
        f32x16 acc[NT][1]; g32::acc_zero<W>(acc);
        SpHook hk{p, lds, panel, wid, lane};
        pipe.template run<true>(s, 0, SEQ / 64, acc, hk DBG_ARGS(3));
        pipe.end();
        STAMP(0, 7);
        const rsrc_t rs = ws_rsrc(p.ws);
        const f32x2 bg = ((const LAS f32x2*)(lds + LDS_TAB + 2048))[m0 + r]; const float b = bg.x, g1 = bg.y;
        const unsigned ub = (unsigned)WS_ACT + (panel * 352 + j * 28 + n0 * 4 + h) * 2048 + (m0 + r) * 16;
#pragma unroll
        for (int t = 0; t < NT; ++t)
#pragma unroll
            for (int i = 0; i < 4; i += 2) {
                unsigned w4[4];
#pragma unroll
                for (int e = 0; e < 4; ++e) { const int q = 4 * i + 2 * e; const unsigned z = z1[t][q >> 1];
                    w4[e] = pk_bf16((acc[t][0][q] + b) * g1 * bf_lo(z), (acc[t][0][q + 1] + b) * g1 * bf_hi(z)); }
                st16_wt(rs, ub + (t * 4 + i) * 2048, g32::pair32(w4[0], w4[1], w4[2], w4[3]));
            }
        asm volatile("s_waitcnt vmcnt(0)" ::: "memory");
        GM_BAR();
        if (threadIdx.x == 256) signal(p.ws, F_U + panel * 32 + j, p.tg());
    }
    STAMP(0, 8);
    out_part<LD, true>(p, lds, panel, j, wid, lane);
}

constexpr int AT_STAGE = 16384, AT_NS = 4;
#define MFMA32(a, b, c) __builtin_amdgcn_mfma_f32_32x32x16_bf16(a, b, c, 0, 0, 0)
#define MFMA16(a, b, c) __builtin_amdgcn_mfma_f32_16x16x32_bf16(a, b, c, 0, 0, 0)
#define PIN(x) asm volatile("" : "+v"(x))
__device__ __forceinline__ void attn_wg(const Ctx& p, ldsp lds, int head, int qb, int x, int a, int oj, int wid, int lane) {
    if (wid == 4) wait_flags(p.ws, p.tg(), F_Q + (head >> 1) * 32, 8, false, 0, lane);
    GM_BAR();
    STAMP(0, 3);
    const unsigned char* Kg = p.ws + WS_K + (size_t)head * 131072; const unsigned char* Vg = p.ws + WS_V + (size_t)head * 131072;
    const unsigned ldb = (unsigned)(uintptr_t)lds;
    const unsigned voff = wid * 1024 + lane * 16;
    const char* kb = (const char*)Kg; const char* vb = (const char*)Vg;
    auto stage = [&](int st, int slot) {
        asm volatile("s_add_u32 m0, %2, 0\n\ts_nop 0\n\tglobal_load_lds_dwordx4 %0, %1 sc1" ::"v"(voff), "s"(kb + (size_t)st * 8192), "s"(ldb + slot * AT_STAGE + wid * 1024) : "memory", "scc");
        asm volatile("s_add_u32 m0, %2, 0\n\ts_nop 0\n\tglobal_load_lds_dwordx4 %0, %1 sc1" ::"v"(voff), "s"(vb + (size_t)st * 8192), "s"(ldb + slot * AT_STAGE + 8192 + wid * 1024) : "memory", "scc");
    };
    stage(0, 0);
    const int q0 = qb * 512 + wid * 64;
    bf16x8 qf[2][2];
#pragma unroll
    for (int a = 0; a < 2; ++a)
#pragma unroll
        for (int d = 0; d < 2; ++d) qf[a][d] = __builtin_bit_cast(bf16x8, ld16_dev(ws_rsrc(p.ws), (unsigned)WS_Q + (head * 64 + (q0 >> 5) + a) * 2048 + d * 1024 + lane * 16));
    bf16x8 sel;
    { const short one = ((lane & 15) == ((lane >> 4) & 1)) ? (short)0x3f80 : (short)0;
#pragma unroll
      for (int i = 0; i < 8; ++i) sel[i] = one; }
    GM_VM0();
    GM_BAR();
    stage(1, 1); stage(2, 2);
    f32x16 o[2], negm[2]; f32x4 l[2];
    const f32x16 zero16 = {};
    bf16x8 kA[2], kB[2], vA[2], vB[2];
    ldsp lb = lds + lane * 16;
    kA[0] = *(const LAS bf16x8*)(lb); kA[1] = *(const LAS bf16x8*)(lb + 1024);
#pragma unroll
    for (int a = 0; a < 2; ++a) {
        f32x16 s = MFMA32(kA[0], qf[a][0], zero16); s = MFMA32(kA[1], qf[a][1], s);
        float m = fmaxf(fmaxf(s[0], s[1]), fmaxf(s[2], s[3]));
#pragma unroll
        for (int r = 4; r < 16; r += 2) m = fmaxf(m, fmaxf(s[r], s[r + 1]));
        const auto rr = __builtin_amdgcn_permlane32_swap(__float_as_uint(m), __float_as_uint(m), false, false);
        m = -fmaxf(__uint_as_float(rr[0]), __uint_as_float(rr[1]));
#pragma unroll
        for (int r = 0; r < 16; ++r) negm[a][r] = m;
        PIN(negm[a]);
        o[a] = zero16; l[a] = (f32x4){0.f, 0.f, 0.f, 0.f};
    }
    f32x16 S0, S1; u32x4 p0l, p0h, p1l, p1h;
    S0 = MFMA32(kA[0], qf[0][0], negm[0]); S0 = MFMA32(kA[1], qf[0][1], S0);
    p1l = (u32x4){0u, 0u, 0u, 0u}; p1h = p1l;
    vB[0] = (bf16x8){0, 0, 0, 0, 0, 0, 0, 0}; vB[1] = vB[0];
    STAMP(0, 4);
#define EX4(S, i) do { S[i] = __builtin_amdgcn_exp2f(S[i]); S[i + 1] = __builtin_amdgcn_exp2f(S[i + 1]); S[i + 2] = __builtin_amdgcn_exp2f(S[i + 2]); S[i + 3] = __builtin_amdgcn_exp2f(S[i + 3]); } while (0)
#define PK4(P, S, i) do { P[0] = pk_bf16(S[i], S[i + 1]); P[1] = pk_bf16(S[i + 2], S[i + 3]); P[2] = pk_bf16(S[i + 4], S[i + 5]); P[3] = pk_bf16(S[i + 6], S[i + 7]); } while (0)
#define BC(x) __builtin_bit_cast(bf16x8, x)
#define AORD 1
#define ASTEP(SC, SN, PCl, PCh, PPl, PPh, KF, QN, VF, AP, LOADS) do { \
        SN = MFMA32(KF[0], qf[QN][0], negm[QN]); GM_SB(); EX4(SC, 0); PIN(SC); GM_SB(); \
        SN = MFMA32(KF[1], qf[QN][1], SN); GM_SB(); LOADS; EX4(SC, 4); PIN(SC); GM_SB(); \
        o[AP] = MFMA32(VF[0], BC(PPl), o[AP]); GM_SB(); EX4(SC, 8); PIN(SC); GM_SB(); \
        o[AP] = MFMA32(VF[1], BC(PPh), o[AP]); GM_SB(); EX4(SC, 12); PIN(SC); GM_SB(); \
        l[AP] = MFMA16(sel, BC(PPl), l[AP]); GM_SB(); PK4(PCl, SC, 0); PIN(PCl); GM_SB(); \
        l[AP] = MFMA16(sel, BC(PPh), l[AP]); GM_SB(); PK4(PCh, SC, 8); PIN(PCh); GM_SB(); } while (0)
#define LDK(KF, base, kt) do { KF[0] = *(const LAS bf16x8*)((base) + (kt) * 2048); KF[1] = *(const LAS bf16x8*)((base) + (kt) * 2048 + 1024); } while (0)
#define LDV(VF, base, kt) do { VF[0] = *(const LAS bf16x8*)((base) + 8192 + (kt) * 2048); VF[1] = *(const LAS bf16x8*)((base) + 8192 + (kt) * 2048 + 1024); } while (0)
    float gval = 0.f;
    for (int st = 0; st < 16; ++st) {
        ldsp cb = lb + (st & 3) * AT_STAGE, nb = lb + ((st + 1) & 3) * AT_STAGE;
        if (st < 5) {
            const int n = st * 8 + wid; const unsigned pfs = (unsigned)(uintptr_t)lds + LDS_PFS;
            const int p0 = 8 * (x & 1) + 4 * ((x >> 1) & 1), j0 = 4 * (x >> 2);
            if (n < 16) pf_lines(p.wsp + (size_t)(p0 * 128 + a * 32) * SEQ, (unsigned)(lane & 31) * (SEQ * 2) + (unsigned)(2 * n + (lane >> 5)) * 128u, pfs);
            else if (n < 30) pf_lines(p.wout + (size_t)(a * 112) * DM + j0 * 128, (unsigned)((n - 16) * 8 + (lane >> 3)) * (DM * 2) + (unsigned)(lane & 7) * 128u, pfs);
            else if (n < 38) pf_lines(p.wo + (size_t)(a * 64) * DM + j0 * 128, (unsigned)((n - 30) * 8 + (lane >> 3)) * (DM * 2) + (unsigned)(lane & 7) * 128u, pfs);
        }
        if (st == 8) {
            poll_flags(p.ws, p.tg(), F_Q, 1, true, F_Q, lane);
            gval = __uint_as_float(__hip_atomic_load((gu32*)(p.ws + WS_GATE) + q0 + lane, RLX_AGENT));
        }
        ASTEP(S0, S1, p0l, p0h, p1l, p1h, kA, 1, vB, 1, { LDK(kB, cb, 1); LDV(vA, cb, 0); });
        ASTEP(S1, S0, p1l, p1h, p0l, p0h, kB, 0, vA, 0, {});
        ASTEP(S0, S1, p0l, p0h, p1l, p1h, kB, 1, vA, 1, { LDK(kA, cb, 2); LDV(vB, cb, 1); });
        ASTEP(S1, S0, p1l, p1h, p0l, p0h, kA, 0, vB, 0, {});
        ASTEP(S0, S1, p0l, p0h, p1l, p1h, kA, 1, vB, 1, { LDK(kB, cb, 3); LDV(vA, cb, 2); });
        ASTEP(S1, S0, p1l, p1h, p0l, p0h, kB, 0, vA, 0, {});
        if (st + 1 < 16) {
            if (st + 2 < 16) asm volatile("s_waitcnt vmcnt(2)" ::: "memory"); else asm volatile("s_waitcnt vmcnt(0)" ::: "memory");
            GM_BAR();
            if (st + 3 < 16) stage(st + 3, (st + 3) & 3);
            if (st == 14) out_part_early_w(p, lds, oj, wid, lane);
        }
        ASTEP(S0, S1, p0l, p0h, p1l, p1h, kB, 1, vA, 1, { LDK(kA, nb, 0); LDV(vB, cb, 3); });
        ASTEP(S1, S0, p1l, p1h, p0l, p0h, kA, 0, vB, 0, {});
    }
    o[1] = MFMA32(vB[0], BC(p1l), o[1]); o[1] = MFMA32(vB[1], BC(p1h), o[1]);
    l[1] = MFMA16(sel, BC(p1l), l[1]); l[1] = MFMA16(sel, BC(p1h), l[1]);
#undef EX4
#undef PK4
#undef ASTEP
#undef LDK
#undef LDV
    STAMP(0, 5);
    float lsum[2];
    {   bool bad = false;
#pragma unroll
        for (int a = 0; a < 2; ++a) {
            const int q = lane & 31;
            const float l0 = __shfl(l[a][0], q & 15), l1 = __shfl(l[a][1], q & 15);
            lsum[a] = (q < 16) ? l0 : l1;
            float mx = 0.f;
#pragma unroll
            for (int r = 0; r < 16; ++r) mx = fmaxf(mx, fabsf(o[a][r]));
            float sm = 0.f;
#pragma unroll
            for (int r = 0; r < 16; ++r) sm += o[a][r];
            bad = bad || !(lsum[a] < 1.2676506e30f) || !(mx < 3.0e38f) || !(sm == sm);
        }
        if (__builtin_expect(__any(bad), 0)) {
            const rsrc_t rq = ws_rsrc(p.ws);
            float mrun[2] = {-INFINITY, -INFINITY}, lrun[2] = {0.f, 0.f};
            o[0] = zero16; o[1] = zero16;
            for (int kt = 0; kt < 64; ++kt) {
                const unsigned ko = (unsigned)WS_K + head * 131072 + kt * 2048 + lane * 16, vo = (unsigned)WS_V + head * 131072 + kt * 2048 + lane * 16;
                const bf16x8 k0 = __builtin_bit_cast(bf16x8, ld16_dev(rq, ko)), k1 = __builtin_bit_cast(bf16x8, ld16_dev(rq, ko + 1024));
                const bf16x8 v0 = __builtin_bit_cast(bf16x8, ld16_dev(rq, vo)), v1 = __builtin_bit_cast(bf16x8, ld16_dev(rq, vo + 1024));
#pragma unroll
                for (int a = 0; a < 2; ++a) {
                    f32x16 s = MFMA32(k0, qf[a][0], zero16); s = MFMA32(k1, qf[a][1], s);
                    float m = s[0];
#pragma unroll
                    for (int r = 1; r < 16; ++r) m = fmaxf(m, s[r]);
                    { const auto rr = __builtin_amdgcn_permlane32_swap(__float_as_uint(m), __float_as_uint(m), false, false); m = fmaxf(__uint_as_float(rr[0]), __uint_as_float(rr[1])); }
                    const float mn = fmaxf(mrun[a], m), alpha = __builtin_amdgcn_exp2f(mrun[a] - mn);
                    mrun[a] = mn;
                    u32x4 pl, ph; float ls = 0.f;
#pragma unroll
                    for (int i = 0; i < 4; ++i) {
                        pl[i] = pk_bf16(__builtin_amdgcn_exp2f(s[2 * i] - mn), __builtin_amdgcn_exp2f(s[2 * i + 1] - mn));
                        ph[i] = pk_bf16(__builtin_amdgcn_exp2f(s[8 + 2 * i] - mn), __builtin_amdgcn_exp2f(s[9 + 2 * i] - mn));
                        ls += (bf_lo(pl[i]) + bf_hi(pl[i])) + (bf_lo(ph[i]) + bf_hi(ph[i])); }
                    lrun[a] = lrun[a] * alpha + ls;
                    o[a] = o[a] * alpha;
                    o[a] = MFMA32(v0, BC(pl), o[a]); o[a] = MFMA32(v1, BC(ph), o[a]);
                }
            }
#pragma unroll
            for (int a = 0; a < 2; ++a) { const auto rr = __builtin_amdgcn_permlane32_swap(__float_as_uint(lrun[a]), __float_as_uint(lrun[a]), false, false); lsum[a] = __uint_as_float(rr[0]) + __uint_as_float(rr[1]); }
        }
    }
    const rsrc_t rs = ws_rsrc(p.ws);
    const float gt0 = __shfl(gval, lane & 31), gt1 = __shfl(gval, 32 + (lane & 31));
#pragma unroll
    for (int a = 0; a < 2; ++a) {
        const int q = lane & 31, hh = lane >> 5;
        const float sc = (a ? gt1 : gt0) * __builtin_amdgcn_rcpf(lsum[a]);
        const int row = q0 + a * 32 + q, panel = row >> 7;
#pragma unroll
        for (int i = 0; i < 4; i += 2) {
            const unsigned a0 = pk_bf16(o[a][4 * i] * sc, o[a][4 * i + 1] * sc), a1 = pk_bf16(o[a][4 * i + 2] * sc, o[a][4 * i + 3] * sc);
            const unsigned b0 = pk_bf16(o[a][4 * i + 4] * sc, o[a][4 * i + 5] * sc), b1 = pk_bf16(o[a][4 * i + 6] * sc, o[a][4 * i + 7] * sc);
            const auto r0 = __builtin_amdgcn_permlane32_swap(a0, b0, false, false); const auto r1 = __builtin_amdgcn_permlane32_swap(a1, b1, false, false);
            const int c = 224 + head * 4 + i + hh;
            st16_wt(rs, (unsigned)WS_ACT + (panel * 352 + c) * 2048 + (row & 127) * 16, (u32x4){r0[0], r1[0], r0[1], r1[1]});
        }
    }
    asm volatile("s_waitcnt vmcnt(0)" ::: "memory");
    GM_BAR();
    if (threadIdx.x == 0) signal(p.ws, F_O + qb * 32 + head, p.tg());
}

__device__ __forceinline__ void gate_rows4(const Ctx& p, int row0, int lane) {
    u32x4 g[2], xr[4][2];
#pragma unroll
    for (int i = 0; i < 2; ++i) g[i] = ((const u32x4*)p.gw)[lane * 2 + i];
#pragma unroll
    for (int r = 0; r < 4; ++r)
#pragma unroll
        for (int i = 0; i < 2; ++i) xr[r][i] = ((const u32x4*)(p.ax + (size_t)(row0 + r) * DM))[lane * 2 + i];
    float s[4];
#pragma unroll
    for (int r = 0; r < 4; ++r) { float a = 0.f, b = 0.f;
#pragma unroll
        for (int i = 0; i < 2; ++i)
#pragma unroll
            for (int e = 0; e < 4; ++e) { a += bf_lo(xr[r][i][e]) * bf_lo(g[i][e]); b += bf_hi(xr[r][i][e]) * bf_hi(g[i][e]); }
        s[r] = a + b; }
#pragma unroll
    for (int r = 0; r < 4; ++r) s[r] = dpp_add<0x128>(s[r]);
#pragma unroll
    for (int r = 0; r < 4; ++r) s[r] = dpp_add<0x124>(s[r]);
#pragma unroll
    for (int r = 0; r < 4; ++r) s[r] = dpp_add<0x122>(s[r]);
#pragma unroll
    for (int r = 0; r < 4; ++r) s[r] = dpp_add<0x121>(s[r]);
#pragma unroll
    for (int r = 0; r < 4; ++r) {
        const auto a = __builtin_amdgcn_permlane16_swap(__float_as_uint(s[r]), __float_as_uint(s[r]), false, false); s[r] = __uint_as_float(a[0]) + __uint_as_float(a[1]);
        const auto b = __builtin_amdgcn_permlane32_swap(__float_as_uint(s[r]), __float_as_uint(s[r]), false, false); s[r] = __uint_as_float(b[0]) + __uint_as_float(b[1]); }
    const float v = lane == 0 ? s[0] : lane == 1 ? s[1] : lane == 2 ? s[2] : s[3];
    if (lane < 4) __hip_atomic_store((gu32*)(p.ws + WS_GATE) + row0 + lane, __float_as_uint(__builtin_amdgcn_rcpf(1.0f + __builtin_amdgcn_exp2f(-1.4426950408889634f * v))), RLX_AGENT);
}
constexpr int LDS_BYTES = CfgO::LDS > CfgQ::LDS + 256 ? CfgO::LDS : CfgQ::LDS + 256;
static_assert(LDS_TAB + 3072 <= LDS_BYTES && CfgO::LDS <= LDS_BYTES && AT_NS * AT_STAGE <= LDS_BYTES, "LDS map");
__global__ __launch_bounds__(512, 2) void fused(Params pp) {
    extern __shared__ __attribute__((aligned(16))) unsigned char smem[]; ldsp lds = (ldsp)smem;
    const int lane = threadIdx.x & 63, wid = __builtin_amdgcn_readfirstlane(threadIdx.x >> 6);
    const int b = blockIdx.x, x = b & 7, role = (b >> 3) & 1, idx = b >> 4;
    Ctx p; static_cast<Params&>(p) = pp;
    p.epv = __hip_atomic_load((gu32*)(pp.ws + WS_CTL) + C_EPOCH, RLX_AGENT);
    STAMP(0, 0);
    if (role == 0) {
        const int pair = 4 * (x >> 1) + (idx >> 2), panel = 4 * (x & 1) + (idx & 3);
        if (wid >= 4) gate_rows4(p, panel * 256 + pair * 16 + (wid - 4) * 4, lane);
        if (wid < 4) qkv_wave<0>(p, lds, pair, panel, wid, lane); else qkv_wave<1>(p, lds, pair, panel, wid, lane);
        STAMP(0, 6);
        const int opanel = 8 * (x & 1) + 4 * ((x >> 1) & 1) + (idx >> 2), oj = 4 * (x >> 2) + (idx & 3);
        attn_wg(p, lds, 4 * x + (idx >> 2), idx & 3, x, idx, oj, wid, lane);
        STAMP(0, 9);
        {
            if (wid < 4) out_part<true, false>(p, lds, opanel, oj, wid, lane); else out_part<false, false>(p, lds, opanel, oj, wid, lane); }
    } else {
        const int panel = 8 * (x & 1) + 4 * ((x >> 1) & 1) + (idx >> 2), j = 4 * (x >> 2) + (idx & 3);
        if (wid < 4) b_wave<true>(p, lds, panel, j, wid, lane); else b_wave<false>(p, lds, panel, j, wid, lane);
        if (panel == 0 && j == 0 && threadIdx.x == 0) __hip_atomic_store((gu32*)(pp.ws + WS_CTL) + C_EPOCH, p.tg(), RLX_AGENT);
    }
    STAMP(0, 15);
}
}

extern "C" void kernel_launch(void* const* d_in, const int* in_sizes, int n_in, void* d_out, int out_size, void* d_ws, size_t ws_size, hipStream_t stream) {
    using namespace gm;
    Params p;
    p.ax = (const u16*)d_in[0]; p.wqkv = (const u16*)d_in[1]; p.wo = (const u16*)d_in[2]; p.bx = (const u16*)d_in[3]; p.win = (const u16*)d_in[4];
    p.wsp = (const u16*)d_in[5]; p.bsp = (const u16*)d_in[6]; p.wout = (const u16*)d_in[7]; p.gw = (const u16*)d_in[8];
    p.out = (u16*)d_out; p.ws = (unsigned char*)d_ws;
    (void)hipFuncSetAttribute((const void*)fused, hipFuncAttributeMaxDynamicSharedMemorySize, LDS_BYTES);
    fused<<<256, 512, LDS_BYTES, stream>>>(p);
}
```

```cpp
#include <hip/hip_runtime.h>
#include <hip/hip_bf16.h>
#include <stdint.h>

#define LAS __attribute__((address_space(3)))
typedef short bf16x8 __attribute__((ext_vector_type(8)));
typedef short s16x4 __attribute__((ext_vector_type(4)));
typedef float f32x2 __attribute__((ext_vector_type(2)));
typedef float f32x4 __attribute__((ext_vector_type(4)));
typedef float f32x16 __attribute__((ext_vector_type(16)));
typedef unsigned u32x4 __attribute__((ext_vector_type(4)));
typedef unsigned u32x2 __attribute__((ext_vector_type(2)));
typedef LAS unsigned char* ldsp;
typedef unsigned short u16;

#define NA 16
#define TF 5
#define SPD 1
namespace gm {
constexpr int SEQ = 2048, DM = 1024, NH = 32, FH = 1792;
constexpr size_t WS_CTL = 0;
constexpr size_t CTL_BYTES = 16384;
constexpr size_t WS_GATE = 16384;
constexpr size_t WS_STAT = 32768;
constexpr size_t WS_Q = 1u << 20;
constexpr size_t WS_K = WS_Q + (4u << 20);
constexpr size_t WS_V = WS_K + (4u << 20);
constexpr size_t WS_Z = WS_V + (4u << 20);
constexpr size_t WS_ACT = WS_Z + (8u << 20);
constexpr int LDS_PFS = 147456 - 0;
constexpr size_t WS_P = WS_ACT + (12u << 20);
constexpr float QSCALE = 0.17677669529663687f * 1.4426950408889634f;

#define TRACE(t) do {} while (0)
#define DBG_PARAMS
#define DBG_ARGS(id)
#define DBG_FWD
#define STAMP(k, i) do {} while (0)
constexpr int C_EPOCH = 0, C_TMO = 64, F_Q = 128  , F_S = F_Q + 512  , F_Z = F_S + 512  ,
              F_U = F_Z + 256  , F_O = F_U + 512  , F_P = F_O + 128  , F_G = F_P + 512  , C_END = F_G + 512;
static_assert(C_END * 4 <= (int)CTL_BYTES, "control block");
#define GM_VM0() __builtin_amdgcn_s_waitcnt(0x0F70)
#define RLX_AGENT __ATOMIC_RELAXED, __HIP_MEMORY_SCOPE_AGENT
typedef __attribute__((address_space(1))) unsigned gu32;
typedef __attribute__((address_space(1))) unsigned long long gu64;
typedef __amdgpu_buffer_rsrc_t rsrc_t;
__device__ __forceinline__ rsrc_t ws_rsrc(unsigned char* ws) { return __builtin_amdgcn_make_buffer_rsrc(ws, 0, 64u << 20, 0x00020000); }
__device__ __forceinline__ void st16_wt(rsrc_t r, unsigned off, u32x4 v) { __builtin_amdgcn_raw_buffer_store_b128(v, r, off, 0, 16); }
__device__ __forceinline__ void signal(unsigned char* ws, int word, unsigned tag) { __hip_atomic_store((gu32*)(ws + WS_CTL) + word, tag, RLX_AGENT); }
__device__ __forceinline__ void poll_flags(unsigned char* ws, unsigned tag, int w0, int n, bool all128, int wq, int lane) {
    gu32* ctl = (gu32*)(ws + WS_CTL);
    for (unsigned spins = 0;;) {
        bool ok = __hip_atomic_load(ctl + w0 + (lane < n ? lane : 0), RLX_AGENT) == tag;
        if (all128) { const unsigned q0 = __hip_atomic_load(ctl + wq + (lane >> 3) * 32 + (lane & 7), RLX_AGENT), q1 = __hip_atomic_load(ctl + wq + 256 + (lane >> 3) * 32 + (lane & 7), RLX_AGENT); ok = ok && q0 == tag && q1 == tag; }
        if (__all(ok)) break;
        __builtin_amdgcn_s_sleep(2);
        if (++spins > (1u << 19)) { __hip_atomic_store(ctl + C_TMO, tag, RLX_AGENT); break; }
        if ((spins & 255u) == 0u && __builtin_amdgcn_readfirstlane(__hip_atomic_load(ctl + C_TMO, RLX_AGENT)) == tag) break;
    }
}
__device__ __forceinline__ void wait_flags(unsigned char* ws, unsigned tag, int w0, int n, bool all128, int wq, int lane) {
    poll_flags(ws, tag, w0, n, all128, wq, lane);
    GM_VM0();
}
__device__ __forceinline__ u32x4 ld16_dev(rsrc_t r, unsigned off) { return __builtin_amdgcn_raw_buffer_load_b128(r, off, 0, 16); }
enum { X_RAW = 0, X_OWN = 1 };
enum { W_P384 = 0, W_P448 = 1, W_P256 = 2, W_OWN = 3 };

__device__ __forceinline__ unsigned pk_bf16(float lo, float hi) {
    typedef __bf16 bf16x2_t __attribute__((ext_vector_type(2)));
    f32x2 v = {lo, hi}; bf16x2_t b = __builtin_convertvector(v, bf16x2_t); return __builtin_bit_cast(unsigned, b);
}
__device__ __forceinline__ float bf_lo(unsigned w) { return __uint_as_float(w << 16); }
__device__ __forceinline__ float bf_hi(unsigned w) { return __uint_as_float(w & 0xffff0000u); }
__device__ __forceinline__ s16x4 tr_read(ldsp p) { typedef short v4s __attribute__((ext_vector_type(4))); return __builtin_amdgcn_ds_read_tr16_b64_v4i16((LAS v4s*)p); }
template <int I, bool SC = false> __device__ __forceinline__ void glds_run(const char* sbase, unsigned voff) {
    if constexpr (SC) asm volatile("global_load_lds_dwordx4 %0, %1 offset:%2 sc1" ::"v"(voff), "s"(sbase), "n"((I & 3) * 1024) : "memory");
    else asm volatile("global_load_lds_dwordx4 %0, %1 offset:%2" ::"v"(voff), "s"(sbase), "n"((I & 3) * 1024) : "memory");
}
template <int I, bool SC = false> __device__ __forceinline__ void glds_set(const char* sbase, unsigned voff, unsigned dst) {
    if constexpr (SC) asm volatile("s_add_u32 m0, %2, %3\n\ts_nop 0\n\tglobal_load_lds_dwordx4 %0, %1 offset:%4 sc1" ::"v"(voff), "s"(sbase), "s"(dst), "n"((I & ~3) * 1024), "n"((I & 3) * 1024) : "memory", "scc");
    else asm volatile("s_add_u32 m0, %2, %3\n\ts_nop 0\n\tglobal_load_lds_dwordx4 %0, %1 offset:%4" ::"v"(voff), "s"(sbase), "s"(dst), "n"((I & ~3) * 1024), "n"((I & 3) * 1024) : "memory", "scc");
}
__device__ __forceinline__ void pf_lines(const void* base, unsigned voff, unsigned ldsdst) {
    asm volatile("s_mov_b32 m0, %2\n\ts_nop 0\n\tglobal_load_lds_dword %0, %1" ::"v"(voff), "s"(base), "s"(ldsdst) : "memory");
}
#define GM_BAR() do { asm volatile("" ::: "memory"); __builtin_amdgcn_s_barrier(); asm volatile("" ::: "memory"); } while (0)
#define GM_SB() __builtin_amdgcn_sched_barrier(0)
__device__ __forceinline__ bool at_tile(int t, int k) { asm volatile("" : "+s"(t)); return t == k; }

template <int BM_, int BN_, int XI_, int WI_, int NSX_, int NSW_> struct Cfg {
    static constexpr int BM = BM_, BN = BN_, XI = XI_, WI = WI_, NSX = NSX_, NSW = NSW_;
    static constexpr int XB = BM * 128, WB = BN * 128, WOFF = NSX * XB, LDS = NSX * XB + NSW * WB;
    static constexpr int PX = BM / 32, PW = BN / 32;
    static constexpr int WPITCH = (WI == W_P384) ? 384 : (WI == W_P448) ? 448 : (WI == W_P256) ? 256 : 0;
    static constexpr bool XSC = false, WSC = false;
};
template <class C> struct Src { const char* x; const char* w; const char* w2; int ksplit; unsigned xstep, wstep; unsigned xv[C::PX]; unsigned wv[C::PW]; };
template <int WI> __device__ __forceinline__ int w_swz(int r, int x) {
    if (WI == W_P384) return x ^ (((((r >> 1) & 1) | (((r >> 3) & 1) << 1))) << 5);
    if (WI == W_P448) return x ^ (((r >> 3) & 1) << 5);
    if (WI == W_P256) return x ^ (((r & 3) | (((r >> 3) & 1) << 2)) << 5);
    return x;
}
template <class C> __device__ __forceinline__ const char* w_tile(const Src<C>& s, int kt) { return (kt < s.ksplit ? s.w : s.w2) + (unsigned)kt * s.wstep; }
template <class C> __device__ __forceinline__ void set_w2(Src<C>& s, const void* base, int ksplit) { s.ksplit = ksplit; s.w2 = (const char*)base - 4096 - (size_t)ksplit * s.wstep; }
template <class C> __device__ __forceinline__ void src_x_raw(Src<C>& s, const void* base, unsigned pitch, int lid, int lane) {
    s.x = (const char*)base - 4096; s.xstep = 128;
#pragma unroll
    for (int i = 0; i < C::PX; ++i) { const int row = 8 * (lid * C::PX + i) + (lane >> 3); s.xv[i] = row * pitch + (((lane & 7) ^ ((row >> 1) & 7)) * 16) + 4096 - (i & 3) * 1024; }
}
template <class C> __device__ __forceinline__ void src_x_own(Src<C>& s, const void* base, int lid, int lane) {
    s.x = (const char*)base - 4096; s.xstep = C::XB;
#pragma unroll
    for (int i = 0; i < C::PX; ++i) s.xv[i] = (lid * C::PX + i) * 1024 + lane * 16 + 4096 - (i & 3) * 1024;
}
template <class C> __device__ __forceinline__ void src_w_own(Src<C>& s, const void* base, int lid, int lane) {
    s.w = (const char*)base - 4096; s.w2 = s.w; s.ksplit = 1 << 20; s.wstep = C::WB;
#pragma unroll
    for (int i = 0; i < C::PW; ++i) s.wv[i] = (lid * C::PW + i) * 1024 + lane * 16 + 4096 - (i & 3) * 1024;
}
template <class C> __device__ __forceinline__ void src_w_raw(Src<C>& s, const void* base, unsigned pitch, unsigned segstride, int lid, int lane) {
    s.w = (const char*)base - 4096; s.w2 = s.w; s.ksplit = 1 << 20; s.wstep = 64 * pitch;
#pragma unroll
    for (int i = 0; i < C::PW; ++i) { const int o = 1024 * (lid * C::PW + i) + 16 * lane; const int r = o / C::WPITCH, x = w_swz<C::WI>(r, o % C::WPITCH);
        s.wv[i] = r * pitch + (x >> 7) * segstride + (x & 127) + 4096 - (i & 3) * 1024; }
}
template <class C, int I0, int I1, bool FIRST> __device__ __forceinline__ void stage_rng(unsigned dx, unsigned dw, const Src<C>& s, const char* xb, const char* wb) {
    if constexpr (I0 < I1) {
        if constexpr (I0 < C::PW) { if constexpr (FIRST || (I0 & 3) == 0) glds_set<I0, C::WSC>(wb, s.wv[I0], dw); else glds_run<I0, C::WSC>(wb, s.wv[I0]); }
        else { constexpr int j = I0 - C::PW; if constexpr (FIRST || (j & 3) == 0) glds_set<j, C::XSC>(xb, s.xv[j], dx); else glds_run<j, C::XSC>(xb, s.xv[j]); }
        stage_rng<C, I0 + 1, I1, false>(dx, dw, s, xb, wb);
    }
}
template <int MT_, int NT_, int N0_, unsigned SWAP_, bool LD_> struct Wv { static constexpr int MT = MT_, NT = NT_, N0 = N0_; static constexpr unsigned SWAP = SWAP_; static constexpr bool LD = LD_; };
template <class W> struct Frag { bf16x8 x[W::MT]; s16x4 wl[W::NT], wh[W::NT]; };
template <class C, class W> struct RdAddr { unsigned x[2]; unsigned w[(C::WI == W_P384) ? 4 : (C::WI == W_P256) ? W::NT : 2]; };
template <class C, class W> __device__ __forceinline__ void rd_init(RdAddr<C, W>& a, int m0, int n0, int lane) {
    const int fr = lane & 15, fq = lane >> 4, g = lane >> 4, q = (lane & 15) >> 2, p = lane & 3;
    if (C::XI == X_RAW) { for (int kk = 0; kk < 2; ++kk) a.x[kk] = (m0 + fr) * 128 + (((kk * 4 + fq) ^ (fr >> 1)) * 16); }
    else { for (int kk = 0; kk < 2; ++kk) a.x[kk] = (kk * 4 + fq) * (C::BM * 16) + (m0 + fr) * 16; }
    if (C::WI == W_P384) { const int s = ((q >> 1) & 1) | ((g & 1) << 1);
        for (int c = 0; c < 4; ++c) a.w[c] = C::WOFF + (8 * g + q) * 384 + ((c ^ s) * 32) + p * 8; }
    else if (C::WI == W_P448) { const int s = g & 1;
        for (int c = 0; c < 2; ++c) a.w[c] = C::WOFF + (8 * g + q) * 448 + (((c ^ s) - c) * 32) + p * 8; }
    else if (C::WI == W_P256) { const int s = (q & 3) | ((g & 1) << 2);
        for (int t = 0; t < W::NT; ++t) a.w[t] = C::WOFF + (8 * g + q) * 256 + (((n0 + t) ^ s) * 32) + p * 8; }
    else { for (int h = 0; h < 2; ++h) a.w[h] = C::WOFF + (p >> 1) * 1024 + (8 * g + 4 * (h ^ (p >> 1)) + q) * 16 + (p & 1) * 8 + W::N0 * 2048; }
}
template <class C, class W> __device__ __forceinline__ s16x4 rd_w(ldsp b, const RdAddr<C, W>& a, int t, int kk, int h) {
    const int c = W::N0 + t;
    if (C::WI == W_P384) return tr_read(b + a.w[c & 3] + (c >> 2) * 128 + kk * 32 * 384 + h * 4 * 384);
    else if (C::WI == W_P448) return tr_read(b + a.w[c & 1] + c * 32 + kk * 32 * 448 + h * 4 * 448);
    else if (C::WI == W_P256) return tr_read(b + a.w[t] + kk * 32 * 256 + h * 4 * 256);
    else return tr_read(b + a.w[h] + t * 2048 + kk * 512);
}
template <class C, class W> __device__ __forceinline__ void rd_one(Frag<W>& f, ldsp bx, ldsp bw, const RdAddr<C, W>& a, int kk, int r) {
    if (r < 2) { if (r == 0) f.wl[0] = rd_w<C, W>(bw, a, 0, kk, 0); else f.wh[0] = rd_w<C, W>(bw, a, 0, kk, 1); }
    else if (r < 2 + W::MT) { const int m = r - 2; f.x[m] = *(const LAS bf16x8*)(bx + a.x[kk] + m * ((C::XI == X_RAW) ? 2048 : 256)); }
    else { const int u = r - W::MT; const int t = u >> 1; if (u & 1) f.wh[t] = rd_w<C, W>(bw, a, t, kk, 1); else f.wl[t] = rd_w<C, W>(bw, a, t, kk, 0); }
}
template <class W> __device__ __forceinline__ void mma_one(f32x4 (&acc)[W::NT][W::MT], const Frag<W>& f, int i) {
    const int t = i / W::MT, m = i % W::MT;
    const bf16x8 w = __builtin_shufflevector(f.wl[t], f.wh[t], 0, 1, 2, 3, 4, 5, 6, 7);
    acc[t][m] = ((W::SWAP >> t) & 1) ? __builtin_amdgcn_mfma_f32_16x16x32_bf16(f.x[m], w, acc[t][m], 0, 0, 0)
                                     : __builtin_amdgcn_mfma_f32_16x16x32_bf16(w, f.x[m], acc[t][m], 0, 0, 0);
}
template <int RFRONT_, int VA_, int RF1_ = RFRONT_> struct Sch { static constexpr int RFRONT = RFRONT_, VA = VA_, RF1 = RF1_; };
template <class C, class W, class S, int RF, int V0, int V1, int I>
__device__ __forceinline__ void step_slot(f32x4 (&acc)[W::NT][W::MT], const Frag<W>& fc, Frag<W>& fn, ldsp bx, ldsp bw, const RdAddr<C, W>& a, int ksn,
                                          const Src<C>& s, const char* xb, const char* wb, unsigned dx, unsigned dw) {
    constexpr int NM = W::NT * W::MT, NR = 2 * W::NT + W::MT, NV = V1 - V0;
    constexpr int RS = (NM * RF / 16) < 1 ? 1 : (NM * RF / 16);
    if constexpr (I < NM) {
        mma_one<W>(acc, fc, I);
        if constexpr (I < RS) {
#pragma unroll
            for (int r = I * NR / RS; r < (I + 1) * NR / RS; ++r) rd_one<C, W>(fn, bx, bw, a, ksn, r);
        }
        if constexpr (W::LD) {
            constexpr int v0 = V0 + I * NV / NM, v1 = V0 + (I + 1) * NV / NM;
            stage_rng<C, v0, v1, v0 == V0>(dx, dw, s, xb, wb);
        }
        GM_SB();
        step_slot<C, W, S, RF, V0, V1, I + 1>(acc, fc, fn, bx, bw, a, ksn, s, xb, wb, dx, dw);
    }
}
struct NoHook { static constexpr bool PRE = false; __device__ __forceinline__ void pre_x() const {} __device__ __forceinline__ void at(int) const {} };
template <class C, class W, class S, class H>
__device__ __forceinline__ void gemm_loop(ldsp lds, const Src<C>& s, int nt, f32x4 (&acc)[W::NT][W::MT], int lid, int m0, int n0, int lane, H& hook DBG_PARAMS) {
    constexpr int PX = C::PX, PW = C::PW, NSX = C::NSX, NSW = C::NSW, NV = PX + PW;
    static_assert(S::VA >= PW && S::VA <= NV, "all W pieces go out in the step behind the barrier");
    static_assert(NSX >= NSW && NSW >= 2, "ring depths");
    RdAddr<C, W> a; rd_init<C, W>(a, m0, n0, lane);
    const unsigned ldb = (unsigned)(uintptr_t)lds;
    const unsigned lx0 = ldb + lid * PX * 1024, lw0 = ldb + C::WOFF + lid * PW * 1024;
    Frag<W> f0, f1;
    const char* wb = w_tile<C>(s, NSW - 1); const char* xb = s.x + (size_t)(NSX - 1) * s.xstep;
    unsigned dw = lw0 + (NSW - 1) * C::WB, dx = lx0 + (NSX - 1) * C::XB;
    if constexpr (H::PRE) {
        if constexpr (W::LD) {
#pragma unroll
            for (int i = 0; i < NSW; ++i) stage_rng<C, 0, PW, true>(0, lw0 + i * C::WB, s, nullptr, w_tile<C>(s, i));
        }
        hook.pre_x();
        if constexpr (W::LD) {
#pragma unroll
            for (int i = 0; i < NSX - 1; ++i) stage_rng<C, PW, NV, true>(lx0 + i * C::XB, 0, s, s.x + (size_t)i * s.xstep, nullptr);
            if constexpr (S::VA > PW) stage_rng<C, PW, S::VA, true>(dx, dw, s, xb, wb);
            asm volatile("s_waitcnt vmcnt(%0)" ::"n"(S::VA - PW) : "memory");
        }
    } else if constexpr (W::LD) {
#pragma unroll
        for (int i = 0; i < NSX - 1; ++i) {
            if (i < NSW - 1) stage_rng<C, 0, PW, true>(0, lw0 + i * C::WB, s, nullptr, w_tile<C>(s, i));
            stage_rng<C, PW, NV, true>(lx0 + i * C::XB, 0, s, s.x + (size_t)i * s.xstep, nullptr);
        }
        stage_rng<C, 0, S::VA, true>(dx, dw, s, xb, wb);
        constexpr int Y = (NSX - 2) * PX + (NSW - 2) * PW + S::VA;
        asm volatile("s_waitcnt vmcnt(%0)" ::"n"(Y) : "memory");
    }
    GM_BAR();
#pragma unroll
    for (int r = 0; r < 2 * W::NT + W::MT; ++r) rd_one<C, W>(f0, lds, lds, a, 0, r);
    int bx = 0, bw = 0, kx = NSX - 1, kw = NSW - 1;
    __builtin_amdgcn_s_waitcnt(0xC07F);
    for (int t = 0; t < nt; ++t) {
        ldsp cx = lds + bx * C::XB, cw = lds + bw * C::WB;
        const int nbx = (bx + 1 == NSX) ? 0 : bx + 1, nbw = (bw + 1 == NSW) ? 0 : bw + 1;
        ldsp nx = lds + nbx * C::XB, nw = lds + nbw * C::WB;
        TRACE(t);
        GM_SB();
#define GM_STEP(FC, FN, BX, BW, KSN, RF, V0, V1) step_slot<C, W, S, RF, V0, V1, 0>(acc, FC, FN, BX, BW, a, KSN, s, xb, wb, dx, dw)
        GM_STEP(f0, f1, cx, cw, 1, S::RFRONT, S::VA, NV);
        hook.at(t);
        asm volatile("s_waitcnt lgkmcnt(0)" ::: "memory");
        if constexpr (W::LD) {
            constexpr int pw = 1 - NSW, px = 1 - NSX, last = (pw > px) ? pw : px, nper = (-1) - last, Y = nper * NV + ((pw > px) ? PX : 0);
            asm volatile("s_waitcnt vmcnt(%0)" ::"n"(Y) : "memory");
        }
        GM_BAR();
        GM_SB();
        if constexpr (W::LD) {
            kx = (kx + 1 < nt) ? kx + 1 : kx; kw = (kw + 1 < nt) ? kw + 1 : kw;
            xb = s.x + (size_t)kx * s.xstep; wb = w_tile<C>(s, kw);
            dx = lx0 + bx * C::XB; dw = lw0 + bw * C::WB;
        }
        GM_STEP(f1, f0, nx, nw, 0, S::RF1, 0, S::VA);
#undef GM_STEP
        bx = nbx; bw = nbw;
    }
    GM_SB();
    asm volatile("s_waitcnt vmcnt(0) lgkmcnt(0)" ::: "memory");
    GM_BAR();
}
template <class W> __device__ __forceinline__ void acc_zero(f32x4 (&acc)[W::NT][W::MT]) {
#pragma unroll
    for (int t = 0; t < W::NT; ++t)
#pragma unroll
        for (int m = 0; m < W::MT; ++m) acc[t][m] = (f32x4){0.f, 0.f, 0.f, 0.f};
}
__device__ __forceinline__ u32x4 pair16(const f32x4& A, const f32x4& B) {
    const unsigned a0 = pk_bf16(A[0], A[1]), a1 = pk_bf16(A[2], A[3]), b0 = pk_bf16(B[0], B[1]), b1 = pk_bf16(B[2], B[3]);
    const auto r0 = __builtin_amdgcn_permlane16_swap(a0, b0, false, false); const auto r1 = __builtin_amdgcn_permlane16_swap(a1, b1, false, false);
    return (u32x4){r0[0], r1[0], r0[1], r1[1]};
}

namespace g32 {
enum { W_P448 = 1, W_P256 = 2, W_OWNT = 3 };
template <int BM_, int BN_, int XI_, int WI_, int NS_, int NL_ = 4> struct Cfg {
    static constexpr int BM = BM_, BN = BN_, XI = XI_, WI = WI_, NS = NS_, NL = NL_;
    static constexpr int XB = BM * 128, WB = BN * 128, WOFF = NS * XB, LDS = NS * (XB + WB);
    static constexpr int PX = BM / 8 / NL, PW = BN / 8 / NL;
    static constexpr int WPITCH = (WI == W_P448) ? 448 : (WI == W_P256) ? 256 : 16;
    static constexpr bool XSC = (XI == X_OWN), WSC = (WI == W_OWNT);
};
template <class C> __device__ __forceinline__ void src_w_raw(Src<C>& s, const void* base, unsigned pitch, int lid, int lane) {
    s.w = (const char*)base - 4096; s.w2 = s.w; s.ksplit = 1 << 20; s.wstep = 64 * pitch;
#pragma unroll
    for (int i = 0; i < C::PW; ++i) { const int o = 1024 * (lid * C::PW + i) + 16 * lane; const int r = o / C::WPITCH; int x = o % C::WPITCH;
        if (C::WI == W_P256) x ^= (r & 3) << 6;
        s.wv[i] = r * pitch + x + 4096 - (i & 3) * 1024; }
}
template <int MT_, int NT_, bool LD_> struct Wv { static constexpr int MT = MT_, NT = NT_; static constexpr bool LD = LD_; };
template <class C, class W> struct Frag { bf16x8 x[W::MT]; s16x4 wl[W::NT], wh[W::NT]; };
template <class C, class W> struct RdAddr { unsigned x[(C::XI == X_RAW) ? 4 : 1]; unsigned w[(C::WI == W_P256) ? W::NT : 1]; };
template <class C, class W> __device__ __forceinline__ void rd_init(RdAddr<C, W>& a, int m0, int n0  , int lane) {
    const int r = lane & 31, h = lane >> 5, g = lane >> 4, q = (lane & 15) >> 2, p = lane & 3;
    if (C::XI == X_RAW) { for (int ks = 0; ks < 4; ++ks) a.x[ks] = (m0 + r) * 128 + (((2 * ks + h) ^ ((r >> 1) & 7)) * 16); }
    else a.x[0] = h * (C::BM * 16) + (m0 + r) * 16;
    const int rowp = 8 * (g >> 1) + q, colp = (g & 1) * 32 + p * 8;
    if (C::WI == W_P448) a.w[0] = C::WOFF + rowp * 448 + colp + n0 * 64;
    else if (C::WI == W_P256) { for (int t = 0; t < W::NT; ++t) a.w[t] = C::WOFF + rowp * 256 + (((n0 + t) ^ q) * 64) + colp; }
    else a.w[0] = C::WOFF + h * (C::BN * 16) + (n0 * 32 + r) * 16;
}
template <class C, class W> constexpr int n_reads() { return W::MT + (C::WI == W_OWNT ? W::NT : 2 * W::NT); }
template <class C, class W> __device__ __forceinline__ void rd_one(Frag<C, W>& f, ldsp bx, ldsp bw, const RdAddr<C, W>& a, int ks, int r) {
    constexpr int PER = (C::WI == W_OWNT) ? 1 : 2;
    if (r >= PER && r < PER + W::MT) { const int m = r - PER;
        if (C::XI == X_RAW) f.x[m] = *(const LAS bf16x8*)(bx + a.x[ks] + m * 4096);
        else f.x[m] = *(const LAS bf16x8*)(bx + a.x[0] + ks * (2 * C::BM * 16) + m * 512);
        return; }
    const int u = r < PER ? r : r - W::MT, t = u / PER, hh = u % PER;
    if (C::WI == W_OWNT) { const bf16x8 v = *(const LAS bf16x8*)(bw + a.w[0] + ks * (2 * C::BN * 16) + t * 512); f.wl[t] = __builtin_shufflevector(v, v, 0, 1, 2, 3); f.wh[t] = __builtin_shufflevector(v, v, 4, 5, 6, 7); }
    else { s16x4 v;
        if (C::WI == W_P448) v = tr_read(bw + a.w[0] + t * 64 + (ks * 16 + hh * 4) * 448);
        else v = tr_read(bw + a.w[t] + (ks * 16 + hh * 4) * 256);
        if (hh) f.wh[t] = v; else f.wl[t] = v; }
}
template <class C, class W> __device__ __forceinline__ void rd_frags(Frag<C, W>& f, ldsp bx, ldsp bw, const RdAddr<C, W>& a, int ks) {
#pragma unroll
    for (int r = 0; r < n_reads<C, W>(); ++r) rd_one<C, W>(f, bx, bw, a, ks, r);
}
template <class C, class W, bool SWAP  > __device__ __forceinline__ void mma_one(f32x16 (&acc)[W::NT][W::MT], const Frag<C, W>& f, int i) {
    const int t = i / W::MT, m = i % W::MT;
    const bf16x8 w = __builtin_shufflevector(f.wl[t], f.wh[t], 0, 1, 2, 3, 4, 5, 6, 7);
    acc[t][m] = SWAP ? __builtin_amdgcn_mfma_f32_32x32x16_bf16(w, f.x[m], acc[t][m], 0, 0, 0) : __builtin_amdgcn_mfma_f32_32x32x16_bf16(f.x[m], w, acc[t][m], 0, 0, 0);
}
struct NoFill { template <int S> __device__ __forceinline__ void chunk() {} };
template <class C, class W, bool SWAP, int V0, int V1, int BASE, int I, class F>
__device__ __forceinline__ void step_slot(f32x16 (&acc)[W::NT][W::MT], const Frag<C, W>& fc, Frag<C, W>& fn, ldsp bx, ldsp bw, const RdAddr<C, W>& a, int ksn,
                                          const Src<C>& s, const char* xb, const char* wb, unsigned dx, unsigned dw, F& fill) {
    constexpr int NM = W::NT * W::MT, NR = n_reads<C, W>(), NV = V1 - V0;
    if constexpr (I < NM) {
        mma_one<C, W, SWAP>(acc, fc, I);
#pragma unroll
        for (int r = I * NR / NM; r < (I + 1) * NR / NM; ++r) rd_one<C, W>(fn, bx, bw, a, ksn, r);
        if constexpr (W::LD) { constexpr int v0 = V0 + I * NV / NM, v1 = V0 + (I + 1) * NV / NM; stage_rng<C, v0, v1, v0 == V0>(dx, dw, s, xb, wb); }
        fill.template chunk<BASE + I>();
        GM_SB();
        step_slot<C, W, SWAP, V0, V1, BASE, I + 1, F>(acc, fc, fn, bx, bw, a, ksn, s, xb, wb, dx, dw, fill);
    }
}
template <class C, class WF  , int D> struct Pipe {
    static constexpr int PX = C::PX, PW = C::PW, NS = C::NS, NV = PX + PW;
#define QSPLIT 0
    static constexpr int Q1 = QSPLIT == 0 ? (NV + 3) / 4 : QSPLIT == 1 ? (NV + 1) / 2 : PW, Q2 = QSPLIT == 0 ? (NV + 1) / 2 : QSPLIT == 1 ? NV : NV, Q3 = QSPLIT == 0 ? (3 * NV + 3) / 4 : NV;
    static constexpr int PRE = (D == 1) ? Q1 : Q2;
    ldsp lds; int nt, xmod;
    RdAddr<C, WF> a; unsigned lx0, lw0;
    Frag<C, WF> f0, f1, f2, f3;
    __device__ __forceinline__ Pipe(ldsp lds_, const Src<C>& s, int nt_, int xmod_, int lid, int m0, int n0, int lane) : lds(lds_), nt(nt_), xmod(xmod_) {
        rd_init<C, WF>(a, m0, n0, lane);
        const unsigned ldb = (unsigned)(uintptr_t)lds;
        lx0 = ldb + lid * PX * 1024; lw0 = ldb + C::WOFF + lid * PW * 1024;
    }
    __device__ __forceinline__ void issue_x(const Src<C>& s) {
        if constexpr (WF::LD) {
#pragma unroll
            for (int i = 0; i < NS - 1; ++i) stage_rng<C, PW, NV, true>(lx0 + i * C::XB, 0, s, s.x + (size_t)i * s.xstep, nullptr);
        }
    }
    __device__ __forceinline__ void issue_w(const Src<C>& s) {
        static_assert(PRE <= PW, "prologue split");
        if constexpr (WF::LD) {
#pragma unroll
            for (int i = 0; i < NS - 1; ++i) stage_rng<C, 0, PW, true>(0, lw0 + i * C::WB, s, nullptr, w_tile<C>(s, i));
            stage_rng<C, 0, PRE, true>(0, lw0 + (NS - 1) * C::WB, s, nullptr, w_tile<C>(s, NS - 1));
        }
    }
    __device__ __forceinline__ void landed() {
        if constexpr (WF::LD) asm volatile("s_waitcnt vmcnt(0)" ::: "memory");
        GM_BAR();
        rd_frags<C, WF>(f0, lds, lds, a, 0);
        if (D == 2) rd_frags<C, WF>(f1, lds, lds, a, 1);
    }
    __device__ __forceinline__ void begin(const Src<C>& s) {
        if constexpr (WF::LD) {
#pragma unroll
            for (int i = 0; i < NS - 1; ++i) stage_rng<C, 0, NV, true>(lx0 + i * C::XB, lw0 + i * C::WB, s, s.x + (size_t)i * s.xstep, w_tile<C>(s, i));
            stage_rng<C, 0, PRE, true>(0, lw0 + (NS - 1) * C::WB, s, nullptr, w_tile<C>(s, NS - 1));
            asm volatile("s_waitcnt vmcnt(%0)" ::"n"((NS - 2) * NV + PRE) : "memory");
        }
        GM_BAR();
        rd_frags<C, WF>(f0, lds, lds, a, 0);
        if (D == 2) rd_frags<C, WF>(f1, lds, lds, a, 1);
    }
    template <bool SWAP, class H> __device__ __forceinline__ void run(const Src<C>& s, int t0, int t1, f32x16 (&acc)[WF::NT][WF::MT], H& hook DBG_PARAMS) {
        NoFill nf; run_fill<SWAP>(s, t0, t1, t0, acc, hook, nf DBG_FWD);
    }
    template <bool SWAP, class H, class F> __device__ __forceinline__ void run_fill(const Src<C>& s, int t0, int t1, int tb, f32x16 (&acc)[WF::NT][WF::MT], H& hook, F& fill DBG_PARAMS) {
        using W = WF; constexpr int NM = WF::MT * WF::NT;
        int sl = t0 % NS, kn = t0 - 1 + NS; kn = kn < nt ? kn : nt - 1;
        const char* xb = s.x + (unsigned)(kn & (xmod - 1)) * s.xstep; const char* wb = w_tile<C>(s, kn);
        unsigned dx = lx0 + ((sl == 0) ? NS - 1 : sl - 1) * C::XB, dw = lw0 + ((sl == 0) ? NS - 1 : sl - 1) * C::WB;
        __builtin_amdgcn_s_waitcnt(0xC07F);
        for (int t = t0; t < t1; ++t) {
            const int ns = (sl + 1 == NS) ? 0 : sl + 1;
            ldsp cx = lds + sl * C::XB, cw = lds + sl * C::WB, nx = lds + ns * C::XB, nw = lds + ns * C::WB;
            TRACE(t - tb);
            hook.top(t - tb);
            GM_SB();
#define G32_STEP(FC, FN, BX, BW, KSN, V0, V1, ST) step_slot<C, W, SWAP, V0, V1, ST * NM, 0, F>(acc, FC, FN, BX, BW, a, KSN, s, xb, wb, dx, dw, fill)
            if constexpr (D == 1) { G32_STEP(f0, f1, cx, cw, 1, Q1, Q2, 0); G32_STEP(f1, f2, cx, cw, 2, Q2, Q3, 1); G32_STEP(f2, f3, cx, cw, 3, Q3, NV, 2); }
            else { G32_STEP(f0, f2, cx, cw, 2, Q2, Q3, 0); G32_STEP(f1, f3, cx, cw, 3, Q3, NV, 1); }
            hook.at(t - tb);
            asm volatile("s_waitcnt lgkmcnt(0)" ::: "memory");
            if constexpr (W::LD) asm volatile("s_waitcnt vmcnt(%0)" ::"n"((NS - 2) * NV) : "memory");
            GM_BAR();
            GM_SB();
            if constexpr (W::LD) { kn = (kn + 1 < nt) ? kn + 1 : kn; xb = s.x + (unsigned)(kn & (xmod - 1)) * s.xstep; wb = w_tile<C>(s, kn); dx = lx0 + sl * C::XB; dw = lw0 + sl * C::WB; }
            if constexpr (D == 1) { G32_STEP(f3, f0, nx, nw, 0, 0, Q1, 3); }
            else { G32_STEP(f2, f0, nx, nw, 0, 0, Q1, 2); G32_STEP(f3, f1, nx, nw, 1, Q1, Q2, 3); }
#undef G32_STEP
            sl = ns;
        }
        GM_SB();
    }
    __device__ __forceinline__ void end() {
        asm volatile("s_waitcnt vmcnt(0) lgkmcnt(0)" ::: "memory");
        GM_BAR();
    }
};
template <class C, class WF> struct PipeKS {
    static constexpr int PX = C::PX, PW = C::PW, NS = C::NS, NV = PX + PW, NM = WF::MT * WF::NT;
    ldsp lds; int nt, kh;
    RdAddr<C, WF> a; unsigned lx0, lw0;
    Frag<C, WF> a0, a1, b0, b1;
    __device__ __forceinline__ PipeKS(ldsp lds_, int nt_, int kh_, int lid, int m0, int n0, int lane) : lds(lds_), nt(nt_), kh(kh_) {
        rd_init<C, WF>(a, m0, n0, lane);
        static_assert(C::XI == X_OWN && C::WI == W_P256, "layouts of the output GEMM");
        a.x[0] += kh * 2 * (2 * C::BM * 16);
#pragma unroll
        for (int t = 0; t < WF::NT; ++t) a.w[t] += kh * 32 * 256;
        const unsigned ldb = (unsigned)(uintptr_t)lds;
        lx0 = ldb + lid * PX * 1024; lw0 = ldb + C::WOFF + lid * PW * 1024;
    }
    __device__ __forceinline__ void issue_w(const Src<C>& s) {
        if constexpr (WF::LD) {
#pragma unroll
            for (int i = 0; i < NS; ++i) stage_rng<C, 0, PW, true>(0, lw0 + i * C::WB, s, nullptr, w_tile<C>(s, i)); }
    }
    __device__ __forceinline__ void issue_x(const Src<C>& s) {
        if constexpr (WF::LD) {
#pragma unroll
            for (int i = 0; i < NS; ++i) stage_rng<C, PW, NV, true>(lx0 + i * C::XB, 0, s, s.x + (size_t)i * s.xstep, nullptr); }
    }
    __device__ __forceinline__ void landed() {
        if constexpr (WF::LD) asm volatile("s_waitcnt vmcnt(0)" ::: "memory");
        GM_BAR();
        rd_frags<C, WF>(a0, lds, lds, a, 0); rd_frags<C, WF>(a1, lds, lds, a, 1);
    }
    template <class H> __device__ __forceinline__ void run(const Src<C>& s, f32x16 (&acc)[WF::NT][WF::MT], H& hook DBG_PARAMS) {
        using W = WF; NoFill fill; using F = NoFill;
        int sl = 0, kn = NS;
        __builtin_amdgcn_s_waitcnt(0xC07F);
#define KS_PERIOD(T, C0, C1, N0, N1) do { \
            TRACE(T); hook.at(T); \
            asm volatile("s_waitcnt lgkmcnt(0)" ::: "memory"); \
            if constexpr (W::LD) asm volatile("s_waitcnt vmcnt(%0)" ::"n"((NS - 2) * NV) : "memory"); \
            GM_BAR(); GM_SB(); \
            const int ns = (sl + 1 == NS) ? 0 : sl + 1, kc = kn < nt ? kn : nt - 1; \
            const char* xb = s.x + (unsigned)kc * s.xstep; const char* wb = w_tile<C>(s, kc); \
            const unsigned dx = lx0 + sl * C::XB, dw = lw0 + sl * C::WB; \
            ldsp nx = lds + ns * C::XB, nw = lds + ns * C::WB; \
            step_slot<C, W, true, 0, NV / 2, 0, 0, F>(acc, C0, N0, nx, nw, a, 0, s, xb, wb, dx, dw, fill); \
            step_slot<C, W, true, NV / 2, NV, NM, 0, F>(acc, C1, N1, nx, nw, a, 1, s, xb, wb, dx, dw, fill); \
            sl = ns; ++kn; } while (0)
        for (int t = 0; t < nt; t += 2) { KS_PERIOD(t, a0, a1, b0, b1); if (t + 1 >= nt) break; KS_PERIOD(t + 1, b0, b1, a0, a1); }
#undef KS_PERIOD
        GM_SB();
        asm volatile("s_waitcnt vmcnt(0) lgkmcnt(0)" ::: "memory");
        GM_BAR();
    }
};
template <class W> __device__ __forceinline__ void acc_zero(f32x16 (&acc)[W::NT][W::MT]) {
#pragma unroll
    for (int t = 0; t < W::NT; ++t)
#pragma unroll
        for (int m = 0; m < W::MT; ++m) acc[t][m] = (f32x16){};
}
__device__ __forceinline__ u32x4 pair32(unsigned a0, unsigned a1, unsigned b0, unsigned b1) {
    const auto r0 = __builtin_amdgcn_permlane32_swap(a0, b0, false, false); const auto r1 = __builtin_amdgcn_permlane32_swap(a1, b1, false, false);
    return (u32x4){r0[0], r1[0], r0[1], r1[1]};
}
}

struct Params {
    const u16* ax; const u16* wqkv; const u16* wo; const u16* bx; const u16* win; const u16* wsp; const u16* bsp; const u16* wout; const u16* gw;
    u16* out; unsigned char* ws;
};
struct Ctx : Params { unsigned epv; __device__ __forceinline__ unsigned tg() const { return (unsigned)__builtin_amdgcn_readfirstlane(epv) + 1u; } };

using CfgQ = Cfg<256, 192, X_RAW, W_P384, 3, 2>;
struct Warm {
    const char* base; unsigned pitch; int lpr_log2; int cnt; unsigned scratch;
    __device__ __forceinline__ unsigned mask(int t0, int wid) const {
        if (wid < 4 || cnt <= 0) return 0u;
        return ((0x11111111u << (wid - 4)) & (cnt >= 32 ? ~0u : ((1u << cnt) - 1u))) << t0;
    }
    __device__ __forceinline__ void issue(int n, int wid, int lane) const {
        if (n >= 0 && n < cnt && wid == 4 + (n & 3)) { const int idx = n * 64 + lane; pf_lines(base, (unsigned)(idx >> lpr_log2) * pitch + (unsigned)(idx & ((1 << lpr_log2) - 1)) * 128u, scratch); }
    }
};

struct SigHook {
    static constexpr bool PRE = false; int wid, lane; Warm w0, w1; unsigned act;
    __device__ __forceinline__ void pre_x() const {}
    __device__ __forceinline__ void top(int) const {}
    __device__ __forceinline__ void at(int t) const { if ((act >> t) & 1u) { w0.issue(t - 1, wid, lane); w1.issue(t - 1, wid, lane); } }
};
template <int WN> __device__ __forceinline__ void qkv_wave(const Ctx& p, ldsp lds, int pair, int panel, int wid, int lane) {
    using C = CfgQ; using W = Wv<4, 6, WN * 6, WN ? 0x3Cu : 0u, WN == 0>;
    const int lid = wid & 3, m0 = (wid & 3) * 64;
    Src<C> s;
    src_x_raw<C>(s, p.ax + (size_t)panel * 256 * DM, DM * 2, lid, lane);
    src_w_raw<C>(s, p.wqkv + pair * 64, 3 * DM * 2, DM * 2, lid, lane);
    f32x4 acc[6][4]; acc_zero<W>(acc);
    STAMP(0, 1);
    Warm none{nullptr, 0, 0, 0, 0};
    SigHook hk{wid, lane, none, none, 0u};
    gemm_loop<C, W, Sch<12, 7>>(lds, s, DM / 64, acc, lid, m0, 0, lane, hk DBG_ARGS(0));
    STAMP(0, 2);
    const int fr = lane & 15, fq = lane >> 4;
    const int row0 = panel * 256 + m0;
    const rsrc_t rs = ws_rsrc(p.ws);
#pragma unroll
    for (int t = 0; t < 6; t += 2) {
        const int T = WN * 6 + t, kind = T >> 2, head = pair * 2 + ((T >> 1) & 1);
        if (kind < 2) {
            const unsigned base = (unsigned)(kind == 0 ? WS_Q : WS_K);
            const int c = 2 * (fq & 1) + (fq >> 1);
            const float sc = kind == 0 ? QSCALE : 1.0f;
#pragma unroll
            for (int m = 0; m < 4; ++m) { const int row = row0 + m * 16 + fr;
                st16_wt(rs, base + ((head * 64 + (row >> 5)) * 4 + c) * 512 + (row & 31) * 16, pair16(acc[t][m] * sc, acc[t + 1][m] * sc)); }
        } else {
#pragma unroll
            for (int tt = 0; tt < 2; ++tt)
#pragma unroll
                for (int m = 0; m < 4; m += 2) {
                    const f32x4 A = acc[t + tt][m], B = acc[t + tt][m + 1];
                    const unsigned a0 = pk_bf16(A[0], A[1]), a1 = pk_bf16(A[2], A[3]), b0 = pk_bf16(B[0], B[1]), b1 = pk_bf16(B[2], B[3]);
                    const auto r0 = __builtin_amdgcn_permlane32_swap(a0, b0, false, false); const auto r1 = __builtin_amdgcn_permlane32_swap(a1, b1, false, false);
                    const int kb = (row0 >> 4) + m + (fq >> 1), h = fq & 1, d = tt * 16 + fr;
                    st16_wt(rs, (unsigned)WS_V + ((head * 128 + kb) * 2 + h) * 512 + d * 16, (u32x4){r0[0], r1[0], r0[1], r1[1]});
                }
        }
    }
    asm volatile("s_waitcnt vmcnt(0)" ::: "memory");
    GM_BAR();
    if (threadIdx.x == 0) signal(p.ws, F_Q + pair * 32 + panel, p.tg());
}

using CfgZ = g32::Cfg<128, 224, X_RAW, g32::W_P448, 3>;
using CfgS = g32::Cfg<128, 224, X_RAW, g32::W_OWNT, 3>;
#define ONS 5
#define ONL 4
using CfgO = g32::Cfg<128, 128, X_OWN, g32::W_P256, ONS, ONL>;
__device__ __forceinline__ float gelu_tanh(float x) {
    const float t = x * x * (-0.044715f * 2.0f * 0.7978845608028654f * 1.4426950408889634f) + (-2.0f * 0.7978845608028654f * 1.4426950408889634f);
    return x * __builtin_amdgcn_rcpf(1.0f + __builtin_amdgcn_exp2f(x * t));
}
template <int N> __device__ __forceinline__ void gelu_pairs(unsigned* z) {
    constexpr float C1 = -0.044715f * 2.0f * 0.7978845608028654f * 1.4426950408889634f, C2 = -2.0f * 0.7978845608028654f * 1.4426950408889634f;
    f32x2 x[N], e[N];
#pragma unroll
    for (int i = 0; i < N; ++i) x[i] = (f32x2){bf_lo(z[i]), bf_hi(z[i])};
#pragma unroll
    for (int i = 0; i < N; ++i) e[i] = x[i] * (x[i] * x[i] * C1 + C2);
    GM_SB();
#pragma unroll
    for (int i = 0; i < N; ++i) { e[i].x = __builtin_amdgcn_exp2f(e[i].x); e[i].y = __builtin_amdgcn_exp2f(e[i].y); }
    GM_SB();
#pragma unroll
    for (int i = 0; i < N; ++i) e[i] = e[i] + 1.0f;
#pragma unroll
    for (int i = 0; i < N; ++i) { e[i].x = __builtin_amdgcn_rcpf(e[i].x); e[i].y = __builtin_amdgcn_rcpf(e[i].y); }
    GM_SB();
#pragma unroll
    for (int i = 0; i < N; ++i) { const f32x2 y = x[i] * e[i]; z[i] = pk_bf16(y.x, y.y); }
    GM_SB();
}
__device__ __forceinline__ void gelu16(f32x16& a) {
    constexpr float C1 = -0.044715f * 2.0f * 0.7978845608028654f * 1.4426950408889634f, C2 = -2.0f * 0.7978845608028654f * 1.4426950408889634f;
    f32x16 e = a * (a * a * C1 + C2);
    GM_SB();
#pragma unroll
    for (int i = 0; i < 16; ++i) e[i] = __builtin_amdgcn_exp2f(e[i]);
    GM_SB();
    e = e + 1.0f;
#pragma unroll
    for (int i = 0; i < 16; ++i) e[i] = __builtin_amdgcn_rcpf(e[i]);
    GM_SB();
    a = a * e;
}
template <int CTRL> __device__ __forceinline__ float dpp_add(float x) { return x + __int_as_float(__builtin_amdgcn_update_dpp(0, __float_as_int(x), CTRL, 0xf, 0xf, false)); }
__device__ __forceinline__ float red32_pair(float a, float b) {
    const auto r = __builtin_amdgcn_permlane16_swap(__float_as_uint(a), __float_as_uint(b), false, false);
    float x = __uint_as_float(r[0]) + __uint_as_float(r[1]);
    x = dpp_add<0x128>(x); x = dpp_add<0x124>(x); x = dpp_add<0x122>(x); x = dpp_add<0x121>(x);
    return x;
}
constexpr int LDS_TAB = CfgZ::LDS;
template <int NT> struct GeluFill {
    static constexpr int NSLOT = 4 * NT, STRIDE = NSLOT / 4;
    unsigned (&z)[NT][8];
    f32x2 x, e;
    template <int S> __device__ __forceinline__ void chunk() {
        constexpr float C1 = -0.044715f * 2.0f * 0.7978845608028654f * 1.4426950408889634f, C2 = -2.0f * 0.7978845608028654f * 1.4426950408889634f;
        if constexpr (S % STRIDE == 0) { constexpr int pa = S / STRIDE;
            x = (f32x2){bf_lo(z[0][pa]), bf_hi(z[0][pa])};
            const f32x2 w = x * (x * x * C1 + C2);
            e.x = __builtin_amdgcn_exp2f(w.x); e.y = __builtin_amdgcn_exp2f(w.y);
#pragma unroll
            for (int g = 0; g + 1 < 2 * NT; ++g) z[g >> 1][(g & 1) * 4 + pa] = z[(g + 1) >> 1][((g + 1) & 1) * 4 + pa];
            asm volatile("" : "+v"(x), "+v"(e));
        }
        if constexpr (S % STRIDE == 2) { constexpr int pb = S / STRIDE;
            const f32x2 d = e + 1.0f;
            const f32x2 y = x * (f32x2){__builtin_amdgcn_rcpf(d.x), __builtin_amdgcn_rcpf(d.y)};
            z[NT - 1][4 + pb] = pk_bf16(y.x, y.y);
            asm volatile("" : "+v"(z[NT - 1][4 + pb]));
        }
    }
};
template <int NT, bool LD> struct LnHook {
    const Ctx& p; ldsp lds; int panel, j, wid, lane, n0; unsigned (&z2)[NT][8];
    u32x4 w[4];
    __device__ __forceinline__ void normalise(int tt) {
        LAS f32x2* R = (LAS f32x2*)(lds + LDS_TAB + 2048);
        const int r = lane & 31, h = lane >> 5, m0 = (wid & 3) * 32;
        const rsrc_t rs = ws_rsrc(p.ws);
        const int srow = panel * 128 + m0;
#pragma unroll
        for (int i = 0; i < 4; i += 2) {
            const f32x4 ra = *(const LAS f32x4*)(R + m0 + 8 * i + 4 * h), rb = *(const LAS f32x4*)(R + m0 + 8 * i + 4 * h + 2);
            const f32x4 rc = *(const LAS f32x4*)(R + m0 + 8 * i + 8 + 4 * h), rd = *(const LAS f32x4*)(R + m0 + 8 * i + 8 + 4 * h + 2);
            const int k = srow + 8 * (i + h);
            const unsigned base = (unsigned)WS_Z + ((j * 32 + (k >> 6)) * 8 + ((k & 63) >> 3)) * 3584 + (n0 * 32 + r) * 16;
            const unsigned a0 = z2[tt][2 * i], a1 = z2[tt][2 * i + 1], b0 = z2[tt][2 * i + 2], b1 = z2[tt][2 * i + 3];
            st16_wt(rs, base + tt * 512, g32::pair32(pk_bf16(fmaf(bf_lo(a0), ra[1], ra[0]), fmaf(bf_hi(a0), ra[3], ra[2])), pk_bf16(fmaf(bf_lo(a1), rb[1], rb[0]), fmaf(bf_hi(a1), rb[3], rb[2])),
                                                      pk_bf16(fmaf(bf_lo(b0), rc[1], rc[0]), fmaf(bf_hi(b0), rc[3], rc[2])), pk_bf16(fmaf(bf_lo(b1), rd[1], rd[0]), fmaf(bf_hi(b1), rd[3], rd[2]))));
        }
    }
    __device__ __forceinline__ void normalise_at(int t) {
        if (t >= TF + 1 && t < TF + 5) { if (at_tile(t, TF + 1)) normalise(0); if (at_tile(t, TF + 2)) normalise(1); if (at_tile(t, TF + 3)) normalise(2); if constexpr (NT == 4) { if (at_tile(t, TF + 4)) normalise(3); } }
    }
    __device__ __forceinline__ void fetch() {
        const unsigned off = (unsigned)WS_STAT + ((panel * 8 + (lane >> 5) * 4) * 128 + (wid - 4) * 32 + (lane & 31)) * 16;
#pragma unroll
        for (int v = 0; v < 4; ++v) w[v] = ld16_dev(ws_rsrc(p.ws), off + v * 2048);
    }
    __device__ __forceinline__ bool fetched() const { bool ok = true;
#pragma unroll
        for (int v = 0; v < 4; ++v) ok = ok && w[v][1] == p.tg() && w[v][3] == p.tg();
        return __all(ok); }
    __device__ __forceinline__ void top(int t) {
        if constexpr (LD) normalise_at(t);
        else { if (t == TF) fetch(); }
    }
    __device__ __forceinline__ void at(int t) {
        if (t < TF || t > TF + 7) return;
        if constexpr (!LD) {
            if (at_tile(t, TF)) {
                for (unsigned spins = 0; !fetched();) {
                    __builtin_amdgcn_s_sleep(2); fetch();
                    if (++spins > (1u << 19)) { __hip_atomic_store((gu32*)(p.ws + WS_CTL) + C_TMO, p.tg(), RLX_AGENT); break; }
                }
                LAS f32x2* R = (LAS f32x2*)(lds + LDS_TAB + 2048);
                float ms = 0.f;
#pragma unroll
                for (int v = 0; v < 4; ++v) ms += __uint_as_float(w[v][0]);
                { const auto r = __builtin_amdgcn_permlane32_swap(__float_as_uint(ms), __float_as_uint(ms), false, false); ms = __uint_as_float(r[0]) + __uint_as_float(r[1]); }
                const float mean = ms * 0.125f; float q = 0.f;
#pragma unroll
                for (int v = 0; v < 4; ++v) { const float dm = __uint_as_float(w[v][0]) - mean; q += __uint_as_float(w[v][2]) + 224.0f * dm * dm; }
                { const auto r = __builtin_amdgcn_permlane32_swap(__float_as_uint(q), __float_as_uint(q), false, false); q = __uint_as_float(r[0]) + __uint_as_float(r[1]); }
                { const float rs_ = __builtin_amdgcn_rsqf(q * (1.0f / 1792.0f) + 1e-5f); if (lane < 32) R[(wid - 4) * 32 + lane] = (f32x2){-mean * rs_, rs_}; }
                GM_VM0();
            }
            normalise_at(t);
        }
        if (at_tile(t, TF + 6)) asm volatile("s_waitcnt vmcnt(0)" ::: "memory");
        if constexpr (!LD) { if (at_tile(t, TF + 7)) { if (threadIdx.x == 256) signal(p.ws, F_Z + j * 32 + panel, p.tg()); } }
    }
};
struct SpHook {
    static constexpr bool PRE = false; const Ctx& p; ldsp lds; int panel, wid, lane;
    __device__ __forceinline__ void pre_x() const {}
    __device__ __forceinline__ void top(int) const {}
    __device__ __forceinline__ void at(int t) {
        if (t == 2 && (wid == 4 || wid == 5)) {
            poll_flags(p.ws, p.tg(), F_G, 1, true, F_G, lane);
            const int row = (wid - 4) * 64 + lane, trow = panel * 128 + row;
            ((LAS f32x2*)(lds + LDS_TAB + 2048))[row] = (f32x2){__uint_as_float((unsigned)p.bsp[trow] << 16), 1.0f - __uint_as_float(__hip_atomic_load((gu32*)(p.ws + WS_GATE) + trow, RLX_AGENT))};
            GM_VM0();
        }
    }
};
struct WaitHook {
    unsigned char* ws; unsigned tag; int wid, lane, tlate, wl, nl;
    __device__ __forceinline__ void top(int) const {}
    __device__ __forceinline__ void at(int t) const { if (t == tlate && wid == 4) wait_flags(ws, tag, wl, nl, false, 0, lane); }
};
__device__ __forceinline__ void out_part_early_w(const Ctx& p, ldsp lds, int j, int wid, int lane) {
    using C = CfgO; using W = g32::Wv<2, 2, true>;
    static_assert(C::WOFF >= 65536, "W slots behind the attention ring");
    if (wid < 4) {
        Src<C> s; g32::src_w_raw<C>(s, p.wo + (size_t)(16 - NA) * 64 * DM + j * 128, DM * 2, wid, lane);
        g32::PipeKS<C, W> pipe(lds, NA, 0, wid, 0, 0, lane);
        pipe.issue_w(s);
    }
}
template <bool LD, bool FINAL> __device__ __forceinline__ void out_part(const Ctx& p, ldsp lds, int panel, int j, int wid, int lane) {
    using C = CfgO; using W = g32::Wv<2, 2, LD>;
    const int lid = wid & 3, r = lane & 31, h = lane >> 5;
    const int kh = wid >> 2, mo = ((wid >> 1) & 1) * 64, no = (wid & 1) * 2;
    Src<C> s;
    src_x_own<C>(s, p.ws + WS_ACT + (size_t)panel * 352 * 2048 + (FINAL ? 0 : (FH / 64 + 16 - NA) * C::XB), lid, lane);
    g32::src_w_raw<C>(s, FINAL ? p.wout + j * 128 : p.wo + (size_t)(16 - NA) * 64 * DM + j * 128, DM * 2, lid, lane);
    if (FINAL) set_w2<C>(s, p.wo + j * 128, FH / 64);
    f32x16 acc[2][2]; g32::acc_zero<W>(acc);
    WaitHook hk{p.ws, p.tg(), wid, lane, FINAL && NA < 16 ? FH / 64 - C::NS : -1, F_O + (panel >> 2) * 32, 32};
    constexpr int NKT = FINAL ? FH / 64 + 16 - NA : NA;
    static_assert(NKT % 2 == 0 || true, "");
    g32::PipeKS<C, W> pipe(lds, NKT, kh, lid, mo, no, lane);
    if (FINAL) pipe.issue_w(s);
    if (wid == 4) { if (FINAL) wait_flags(p.ws, p.tg(), F_U + panel * 32, 8, false, 0, lane); else wait_flags(p.ws, p.tg(), F_O + (panel >> 2) * 32, 32, false, 0, lane); }
    GM_BAR();
    pipe.issue_x(s);
    pipe.landed();
    STAMP(0, FINAL ? 10 : 7);
    pipe.run(s, acc, hk DBG_ARGS(4));
    STAMP(0, FINAL ? 9 : 8);
    ldsp mine = lds + wid * 8192 + lane * 16, theirs = lds + (wid ^ 4) * 8192 + lane * 16;
#pragma unroll
    for (int t = 0; t < 2; ++t)
#pragma unroll
        for (int i = 0; i < 4; ++i) { const f32x16& A = LD ? acc[t][1] : acc[t][0]; *(LAS f32x4*)(mine + (t * 4 + i) * 1024) = (f32x4){A[4 * i], A[4 * i + 1], A[4 * i + 2], A[4 * i + 3]}; }
    asm volatile("s_waitcnt lgkmcnt(0)" ::: "memory");
    if (FINAL && wid == 4) wait_flags(p.ws, p.tg(), F_P + panel * 32 + j, 1, false, 0, lane);
    GM_BAR();
    const rsrc_t rs = ws_rsrc(p.ws);
    const unsigned pb = (unsigned)WS_P + ((panel * 8 + j) * 8 + wid) * 8192 + lane * 16;
    f32x16 o[2];
    u32x4 pv[8];
    if (FINAL) {
#pragma unroll
        for (int q = 0; q < 8; ++q) pv[q] = ld16_dev(rs, pb + q * 1024);
    }
#pragma unroll
    for (int t = 0; t < 2; ++t) { o[t] = LD ? acc[t][0] : acc[t][1];
#pragma unroll
        for (int i = 0; i < 4; ++i) { const f32x4 v = *(const LAS f32x4*)(theirs + (t * 4 + i) * 1024); o[t][4 * i] += v[0]; o[t][4 * i + 1] += v[1]; o[t][4 * i + 2] += v[2]; o[t][4 * i + 3] += v[3]; } }
    if (!FINAL) {
#pragma unroll
        for (int t = 0; t < 2; ++t)
#pragma unroll
            for (int i = 0; i < 4; ++i) st16_wt(rs, pb + (t * 4 + i) * 1024, (u32x4){__float_as_uint(o[t][4 * i]), __float_as_uint(o[t][4 * i + 1]), __float_as_uint(o[t][4 * i + 2]), __float_as_uint(o[t][4 * i + 3])});
        asm volatile("s_waitcnt vmcnt(0)" ::: "memory");
        GM_BAR();
        if (threadIdx.x == 0) signal(p.ws, F_P + panel * 32 + j, p.tg());
    } else {
#pragma unroll
        for (int t = 0; t < 2; ++t)
#pragma unroll
            for (int i = 0; i < 4; ++i)
#pragma unroll
                for (int e = 0; e < 4; ++e) o[t][4 * i + e] += __uint_as_float(pv[t * 4 + i][e]);
        ldsp st = lds + 65536;
#pragma unroll
        for (int t = 0; t < 2; ++t)
#pragma unroll
            for (int i = 0; i < 4; i += 2) { const f32x16& A = o[t];
                *(LAS u32x4*)(st + (mo + kh * 32 + r) * 272 + (no + t) * 64 + (i + h) * 16) =
                    g32::pair32(pk_bf16(A[4 * i], A[4 * i + 1]), pk_bf16(A[4 * i + 2], A[4 * i + 3]), pk_bf16(A[4 * i + 4], A[4 * i + 5]), pk_bf16(A[4 * i + 6], A[4 * i + 7])); }
        asm volatile("s_waitcnt lgkmcnt(0)" ::: "memory"); GM_BAR();
#pragma unroll
        for (int i = 0; i < 4; ++i) { const int row = wid * 16 + i * 4 + (lane >> 4);
            __builtin_amdgcn_raw_buffer_store_b128(*(const LAS u32x4*)(st + row * 272 + (lane & 15) * 16), __builtin_amdgcn_make_buffer_rsrc(p.out, 0, SEQ * DM * 2, 0x00020000), (unsigned)(((panel * 128 + row) * DM + j * 128 + (lane & 15) * 8) * 2), 0, 16); }
    }
}
template <bool LD> __device__ __forceinline__ void b_wave(const Ctx& p, ldsp lds, int panel, int j, int wid, int lane) {
    constexpr int NT = LD ? 3 : 4;
    const int lid = wid & 3, m0 = lid * 32, n0 = LD ? 0 : 3;
    const int r = lane & 31, h = lane >> 5;
    unsigned z2[NT][8], z1[NT][8];
    const unsigned pfs = (unsigned)(uintptr_t)lds + LDS_PFS;
    {
        using C = CfgZ; using W = g32::Wv<1, NT, LD>; Src<C> s;
        src_x_raw<C>(s, p.ax + (size_t)panel * 128 * DM, DM * 2, lid, lane);
        g32::src_w_raw<C>(s, p.win + FH + j * 224, 2 * FH * 2, lid, lane);
        set_w2<C>(s, p.win + j * 224, DM / 64);
        g32::Pipe<C, W, 1> pipe(lds, s, 2 * DM / 64, DM / 64, lid, m0, n0, lane);
        f32x16 acc[NT][1]; g32::acc_zero<W>(acc);
        STAMP(0, 1);
        Warm wz1{(const char*)p.win + ((j * 224 * 2) & ~127) + (size_t)(panel & 3) * 256 * (2 * FH * 2), 2 * FH * 2, 2, 13, pfs};
        Warm none{nullptr, 0, 0, 0, 0};
        SigHook hk{wid, lane, wz1, none, wz1.mask(1, wid)};
        pipe.begin(s);
        pipe.template run<false>(s, 0, DM / 64, acc, hk DBG_ARGS(1));
        STAMP(0, 2);
        {
            float sm[16], sq[16];
#pragma unroll
            for (int t = 0; t < NT; ++t) {
                gelu16(acc[t][0]);
#pragma unroll
                for (int q = 0; q < 16; ++q) { const float v = acc[t][0][q]; sm[q] = t ? sm[q] + v : v; sq[q] = t ? sq[q] + v * v : v * v; }
#pragma unroll
                for (int q = 0; q < 16; q += 2) z2[t][q >> 1] = pk_bf16(acc[t][0][q], acc[t][0][q + 1]);
                acc[t][0] = (f32x16){};
            }
            float ra[16];
#pragma unroll
            for (int k = 0; k < 8; ++k) {
                const auto a = __builtin_amdgcn_permlane16_swap(__float_as_uint(sm[2 * k]), __float_as_uint(sm[2 * k + 1]), false, false); ra[k] = __uint_as_float(a[0]) + __uint_as_float(a[1]);
                const auto b2 = __builtin_amdgcn_permlane16_swap(__float_as_uint(sq[2 * k]), __float_as_uint(sq[2 * k + 1]), false, false); ra[8 + k] = __uint_as_float(b2[0]) + __uint_as_float(b2[1]); }
#pragma unroll
            for (int k = 0; k < 16; ++k) ra[k] = dpp_add<0x128>(ra[k]);
#pragma unroll
            for (int k = 0; k < 16; ++k) ra[k] = dpp_add<0x124>(ra[k]);
#pragma unroll
            for (int k = 0; k < 16; ++k) ra[k] = dpp_add<0x122>(ra[k]);
#pragma unroll
            for (int k = 0; k < 16; ++k) ra[k] = dpp_add<0x121>(ra[k]);
            float va = ra[0], vb = ra[8];
#pragma unroll
            for (int k = 1; k < 8; ++k) { const bool sel = (lane & 7) == k; va = sel ? ra[k] : va; vb = sel ? ra[8 + k] : vb; }
            LAS f32x2* P = (LAS f32x2*)(lds + LDS_TAB);
            { const int q = 2 * (lane & 7) + ((lane >> 4) & 1), row = m0 + 8 * (q >> 2) + 4 * h + (q & 3);
              if ((lane & 8) == 0) P[row * 2 + (LD ? 0 : 1)] = (f32x2){va, vb}; }
            asm volatile("s_waitcnt lgkmcnt(0)" ::: "memory"); GM_BAR();
            if constexpr (!LD) {
                if (lane < 32) {
                    const int row = lid * 32 + lane;
                    const f32x2 a = P[row * 2], b = P[row * 2 + 1];
                    const float su = a.x + b.x, mt = su * (1.0f / 224.0f), m2 = fmaxf(a.y + b.y - su * mt, 0.f);
                    st16_wt(ws_rsrc(p.ws), (unsigned)WS_STAT + ((panel * 8 + j) * 128 + row) * 16, (u32x4){__float_as_uint(mt), p.tg(), __float_as_uint(m2), p.tg()});
                }
            }
        }
        STAMP(0, 3);
        LnHook<NT, LD> hl{p, lds, panel, j, wid, lane, n0, z2};
        pipe.template run<true>(s, DM / 64, 2 * DM / 64, acc, hl DBG_ARGS(2));
        pipe.end();
        STAMP(0, 4);
#pragma unroll
        for (int t = 0; t < NT; ++t)
#pragma unroll
            for (int q = 0; q < 16; q += 2) z1[t][q >> 1] = pk_bf16(acc[t][0][q], acc[t][0][q + 1]);
    }
    {
        using C = CfgS; using W = g32::Wv<1, NT, LD>; Src<C> s;
        src_x_raw<C>(s, p.wsp + (size_t)panel * 128 * SEQ, SEQ * 2, lid, lane);
        src_w_own<C>(s, p.ws + WS_Z + (size_t)j * 32 * 28672, lid, lane);
        g32::Pipe<C, W, SPD> pipe(lds, s, SEQ / 64, SEQ / 64, lid, m0, n0, lane);
        pipe.issue_x(s);
        if (wid == 4) wait_flags(p.ws, p.tg(), F_Z + j * 32, 16, false, 0, lane);
        GM_BAR();
        pipe.issue_w(s);
        STAMP(0, 5);
#pragma unroll
        for (int t = 0; t < NT; ++t) { gelu_pairs<8>(z1[t]);
#pragma unroll
            for (int q = 0; q < 8; ++q) asm volatile("" : "+v"(z1[t][q])); }
        STAMP(0, 6);
        pipe.landed();
        f32x16 acc[NT][1]; g32::acc_zero<W>(acc);
        SpHook hk{p, lds, panel, wid, lane};
        pipe.template run<true>(s, 0, SEQ / 64, acc, hk DBG_ARGS(3));
        pipe.end();
        STAMP(0, 7);
        const rsrc_t rs = ws_rsrc(p.ws);
        const f32x2 bg = ((const LAS f32x2*)(lds + LDS_TAB + 2048))[m0 + r]; const float b = bg.x, g1 = bg.y;
        const unsigned ub = (unsigned)WS_ACT + (panel * 352 + j * 28 + n0 * 4 + h) * 2048 + (m0 + r) * 16;
#pragma unroll
        for (int t = 0; t < NT; ++t)
#pragma unroll
            for (int i = 0; i < 4; i += 2) {
                unsigned w4[4];
#pragma unroll
                for (int e = 0; e < 4; ++e) { const int q = 4 * i + 2 * e; const unsigned z = z1[t][q >> 1];
                    w4[e] = pk_bf16((acc[t][0][q] + b) * g1 * bf_lo(z), (acc[t][0][q + 1] + b) * g1 * bf_hi(z)); }
                st16_wt(rs, ub + (t * 4 + i) * 2048, g32::pair32(w4[0], w4[1], w4[2], w4[3]));
            }
        asm volatile("s_waitcnt vmcnt(0)" ::: "memory");
        GM_BAR();
        if (threadIdx.x == 256) signal(p.ws, F_U + panel * 32 + j, p.tg());
    }
    STAMP(0, 8);
    out_part<LD, true>(p, lds, panel, j, wid, lane);
}

constexpr int AT_STAGE = 16384, AT_NS = 4;
#define MFMA32(a, b, c) __builtin_amdgcn_mfma_f32_32x32x16_bf16(a, b, c, 0, 0, 0)
#define MFMA16(a, b, c) __builtin_amdgcn_mfma_f32_16x16x32_bf16(a, b, c, 0, 0, 0)
#define PIN(x) asm volatile("" : "+v"(x))
__device__ __forceinline__ void gate_rows4(const Ctx& p, int row0, int lane);
__device__ __forceinline__ void attn_wg(const Ctx& p, ldsp lds, int head, int qb, int x, int a, int oj, int gpair, int gpanel, int wid, int lane) {
    if (wid == 4) wait_flags(p.ws, p.tg(), F_Q + (head >> 1) * 32, 8, false, 0, lane);
    if (wid < 4) { gate_rows4(p, gpanel * 256 + gpair * 16 + wid * 4, lane); asm volatile("s_waitcnt vmcnt(0)" ::: "memory"); }
    GM_BAR();
    if (threadIdx.x == 0) signal(p.ws, F_G + gpair * 32 + gpanel, p.tg());
    STAMP(0, 3);
    const unsigned char* Kg = p.ws + WS_K + (size_t)head * 131072; const unsigned char* Vg = p.ws + WS_V + (size_t)head * 131072;
    const unsigned ldb = (unsigned)(uintptr_t)lds;
    const unsigned voff = wid * 1024 + lane * 16;
    const char* kb = (const char*)Kg; const char* vb = (const char*)Vg;
    auto stage = [&](int st, int slot) {
        asm volatile("s_add_u32 m0, %2, 0\n\ts_nop 0\n\tglobal_load_lds_dwordx4 %0, %1 sc1" ::"v"(voff), "s"(kb + (size_t)st * 8192), "s"(ldb + slot * AT_STAGE + wid * 1024) : "memory", "scc");
        asm volatile("s_add_u32 m0, %2, 0\n\ts_nop 0\n\tglobal_load_lds_dwordx4 %0, %1 sc1" ::"v"(voff), "s"(vb + (size_t)st * 8192), "s"(ldb + slot * AT_STAGE + 8192 + wid * 1024) : "memory", "scc");
    };
    stage(0, 0);
    const int q0 = qb * 512 + wid * 64;
    bf16x8 qf[2][2];
#pragma unroll
    for (int a = 0; a < 2; ++a)
#pragma unroll
        for (int d = 0; d < 2; ++d) qf[a][d] = __builtin_bit_cast(bf16x8, ld16_dev(ws_rsrc(p.ws), (unsigned)WS_Q + (head * 64 + (q0 >> 5) + a) * 2048 + d * 1024 + lane * 16));
    bf16x8 sel;
    { const short one = ((lane & 15) == ((lane >> 4) & 1)) ? (short)0x3f80 : (short)0;
#pragma unroll
      for (int i = 0; i < 8; ++i) sel[i] = one; }
    GM_VM0();
    GM_BAR();
    stage(1, 1); stage(2, 2);
    f32x16 o[2], negm[2]; f32x4 l[2];
    const f32x16 zero16 = {};
    bf16x8 kA[2], kB[2], vA[2], vB[2];
    ldsp lb = lds + lane * 16;
    kA[0] = *(const LAS bf16x8*)(lb); kA[1] = *(const LAS bf16x8*)(lb + 1024);
#pragma unroll
    for (int a = 0; a < 2; ++a) {
        f32x16 s = MFMA32(kA[0], qf[a][0], zero16); s = MFMA32(kA[1], qf[a][1], s);
        float m = fmaxf(fmaxf(s[0], s[1]), fmaxf(s[2], s[3]));
#pragma unroll
        for (int r = 4; r < 16; r += 2) m = fmaxf(m, fmaxf(s[r], s[r + 1]));
        const auto rr = __builtin_amdgcn_permlane32_swap(__float_as_uint(m), __float_as_uint(m), false, false);
        m = -fmaxf(__uint_as_float(rr[0]), __uint_as_float(rr[1]));
#pragma unroll
        for (int r = 0; r < 16; ++r) negm[a][r] = m;
        PIN(negm[a]);
        o[a] = zero16; l[a] = (f32x4){0.f, 0.f, 0.f, 0.f};
    }
    f32x16 S0, S1; u32x4 p0l, p0h, p1l, p1h;
    S0 = MFMA32(kA[0], qf[0][0], negm[0]); S0 = MFMA32(kA[1], qf[0][1], S0);
    p1l = (u32x4){0u, 0u, 0u, 0u}; p1h = p1l;
    vB[0] = (bf16x8){0, 0, 0, 0, 0, 0, 0, 0}; vB[1] = vB[0];
    STAMP(0, 4);
#define EX4(S, i) do { S[i] = __builtin_amdgcn_exp2f(S[i]); S[i + 1] = __builtin_amdgcn_exp2f(S[i + 1]); S[i + 2] = __builtin_amdgcn_exp2f(S[i + 2]); S[i + 3] = __builtin_amdgcn_exp2f(S[i + 3]); } while (0)
#define PK4(P, S, i) do { P[0] = pk_bf16(S[i], S[i + 1]); P[1] = pk_bf16(S[i + 2], S[i + 3]); P[2] = pk_bf16(S[i + 4], S[i + 5]); P[3] = pk_bf16(S[i + 6], S[i + 7]); } while (0)
#define BC(x) __builtin_bit_cast(bf16x8, x)
#define AORD 1
#define ASTEP(SC, SN, PCl, PCh, PPl, PPh, KF, QN, VF, AP, LOADS) do { \
        SN = MFMA32(KF[0], qf[QN][0], negm[QN]); GM_SB(); EX4(SC, 0); PIN(SC); GM_SB(); \
        SN = MFMA32(KF[1], qf[QN][1], SN); GM_SB(); LOADS; EX4(SC, 4); PIN(SC); GM_SB(); \
        o[AP] = MFMA32(VF[0], BC(PPl), o[AP]); GM_SB(); EX4(SC, 8); PIN(SC); GM_SB(); \
        o[AP] = MFMA32(VF[1], BC(PPh), o[AP]); GM_SB(); EX4(SC, 12); PIN(SC); GM_SB(); \
        l[AP] = MFMA16(sel, BC(PPl), l[AP]); GM_SB(); PK4(PCl, SC, 0); PIN(PCl); GM_SB(); \
        l[AP] = MFMA16(sel, BC(PPh), l[AP]); GM_SB(); PK4(PCh, SC, 8); PIN(PCh); GM_SB(); } while (0)
#define LDK(KF, base, kt) do { KF[0] = *(const LAS bf16x8*)((base) + (kt) * 2048); KF[1] = *(const LAS bf16x8*)((base) + (kt) * 2048 + 1024); } while (0)
#define LDV(VF, base, kt) do { VF[0] = *(const LAS bf16x8*)((base) + 8192 + (kt) * 2048); VF[1] = *(const LAS bf16x8*)((base) + 8192 + (kt) * 2048 + 1024); } while (0)
    float gval = 0.f;
    for (int st = 0; st < 16; ++st) {
        ldsp cb = lb + (st & 3) * AT_STAGE, nb = lb + ((st + 1) & 3) * AT_STAGE;
        if (st < 5) {
            const int n = st * 8 + wid; const unsigned pfs = (unsigned)(uintptr_t)lds + LDS_PFS;
            const int p0 = 8 * (x & 1) + 4 * ((x >> 1) & 1), j0 = 4 * (x >> 2);
            if (n < 16) pf_lines(p.wsp + (size_t)(p0 * 128 + a * 32) * SEQ, (unsigned)(lane & 31) * (SEQ * 2) + (unsigned)(2 * n + (lane >> 5)) * 128u, pfs);
            else if (n < 30) pf_lines(p.wout + (size_t)(a * 112) * DM + j0 * 128, (unsigned)((n - 16) * 8 + (lane >> 3)) * (DM * 2) + (unsigned)(lane & 7) * 128u, pfs);
            else if (n < 38) pf_lines(p.wo + (size_t)(a * 64) * DM + j0 * 128, (unsigned)((n - 30) * 8 + (lane >> 3)) * (DM * 2) + (unsigned)(lane & 7) * 128u, pfs);
        }
        if (st == 8) {
            poll_flags(p.ws, p.tg(), F_G, 1, true, F_G, lane);
            gval = __uint_as_float(__hip_atomic_load((gu32*)(p.ws + WS_GATE) + q0 + lane, RLX_AGENT));
        }
        ASTEP(S0, S1, p0l, p0h, p1l, p1h, kA, 1, vB, 1, { LDK(kB, cb, 1); LDV(vA, cb, 0); });
        ASTEP(S1, S0, p1l, p1h, p0l, p0h, kB, 0, vA, 0, {});
        ASTEP(S0, S1, p0l, p0h, p1l, p1h, kB, 1, vA, 1, { LDK(kA, cb, 2); LDV(vB, cb, 1); });
        ASTEP(S1, S0, p1l, p1h, p0l, p0h, kA, 0, vB, 0, {});
        ASTEP(S0, S1, p0l, p0h, p1l, p1h, kA, 1, vB, 1, { LDK(kB, cb, 3); LDV(vA, cb, 2); });
        ASTEP(S1, S0, p1l, p1h, p0l, p0h, kB, 0, vA, 0, {});
        if (st + 1 < 16) {
            if (st + 2 < 16) asm volatile("s_waitcnt vmcnt(2)" ::: "memory"); else asm volatile("s_waitcnt vmcnt(0)" ::: "memory");
            GM_BAR();
            if (st + 3 < 16) stage(st + 3, (st + 3) & 3);
            if (st == 14) out_part_early_w(p, lds, oj, wid, lane);
        }
        ASTEP(S0, S1, p0l, p0h, p1l, p1h, kB, 1, vA, 1, { LDK(kA, nb, 0); LDV(vB, cb, 3); });
        ASTEP(S1, S0, p1l, p1h, p0l, p0h, kA, 0, vB, 0, {});
    }
    o[1] = MFMA32(vB[0], BC(p1l), o[1]); o[1] = MFMA32(vB[1], BC(p1h), o[1]);
    l[1] = MFMA16(sel, BC(p1l), l[1]); l[1] = MFMA16(sel, BC(p1h), l[1]);
#undef EX4
#undef PK4
#undef ASTEP
#undef LDK
#undef LDV
    STAMP(0, 5);
    float lsum[2];
    {   bool bad = false;
#pragma unroll
        for (int a = 0; a < 2; ++a) {
            const int q = lane & 31;
            const float l0 = __shfl(l[a][0], q & 15), l1 = __shfl(l[a][1], q & 15);
            lsum[a] = (q < 16) ? l0 : l1;
            float mx = 0.f;
#pragma unroll
            for (int r = 0; r < 16; ++r) mx = fmaxf(mx, fabsf(o[a][r]));
            float sm = 0.f;
#pragma unroll
            for (int r = 0; r < 16; ++r) sm += o[a][r];
            bad = bad || !(lsum[a] < 1.2676506e30f) || !(mx < 3.0e38f) || !(sm == sm);
        }
        if (__builtin_expect(__any(bad), 0)) {
            const rsrc_t rq = ws_rsrc(p.ws);
            float mrun[2] = {-INFINITY, -INFINITY}, lrun[2] = {0.f, 0.f};
            o[0] = zero16; o[1] = zero16;
            for (int kt = 0; kt < 64; ++kt) {
                const unsigned ko = (unsigned)WS_K + head * 131072 + kt * 2048 + lane * 16, vo = (unsigned)WS_V + head * 131072 + kt * 2048 + lane * 16;
                const bf16x8 k0 = __builtin_bit_cast(bf16x8, ld16_dev(rq, ko)), k1 = __builtin_bit_cast(bf16x8, ld16_dev(rq, ko + 1024));
                const bf16x8 v0 = __builtin_bit_cast(bf16x8, ld16_dev(rq, vo)), v1 = __builtin_bit_cast(bf16x8, ld16_dev(rq, vo + 1024));
#pragma unroll
                for (int a = 0; a < 2; ++a) {
                    f32x16 s = MFMA32(k0, qf[a][0], zero16); s = MFMA32(k1, qf[a][1], s);
                    float m = s[0];
#pragma unroll
                    for (int r = 1; r < 16; ++r) m = fmaxf(m, s[r]);
                    { const auto rr = __builtin_amdgcn_permlane32_swap(__float_as_uint(m), __float_as_uint(m), false, false); m = fmaxf(__uint_as_float(rr[0]), __uint_as_float(rr[1])); }
                    const float mn = fmaxf(mrun[a], m), alpha = __builtin_amdgcn_exp2f(mrun[a] - mn);
                    mrun[a] = mn;
                    u32x4 pl, ph; float ls = 0.f;
#pragma unroll
                    for (int i = 0; i < 4; ++i) {
                        pl[i] = pk_bf16(__builtin_amdgcn_exp2f(s[2 * i] - mn), __builtin_amdgcn_exp2f(s[2 * i + 1] - mn));
                        ph[i] = pk_bf16(__builtin_amdgcn_exp2f(s[8 + 2 * i] - mn), __builtin_amdgcn_exp2f(s[9 + 2 * i] - mn));
                        ls += (bf_lo(pl[i]) + bf_hi(pl[i])) + (bf_lo(ph[i]) + bf_hi(ph[i])); }
                    lrun[a] = lrun[a] * alpha + ls;
                    o[a] = o[a] * alpha;
                    o[a] = MFMA32(v0, BC(pl), o[a]); o[a] = MFMA32(v1, BC(ph), o[a]);
                }
            }
#pragma unroll
            for (int a = 0; a < 2; ++a) { const auto rr = __builtin_amdgcn_permlane32_swap(__float_as_uint(lrun[a]), __float_as_uint(lrun[a]), false, false); lsum[a] = __uint_as_float(rr[0]) + __uint_as_float(rr[1]); }
        }
    }
    const rsrc_t rs = ws_rsrc(p.ws);
    const float gt0 = __shfl(gval, lane & 31), gt1 = __shfl(gval, 32 + (lane & 31));
#pragma unroll
    for (int a = 0; a < 2; ++a) {
        const int q = lane & 31, hh = lane >> 5;
        const float sc = (a ? gt1 : gt0) * __builtin_amdgcn_rcpf(lsum[a]);
        const int row = q0 + a * 32 + q, panel = row >> 7;
#pragma unroll
        for (int i = 0; i < 4; i += 2) {
            const unsigned a0 = pk_bf16(o[a][4 * i] * sc, o[a][4 * i + 1] * sc), a1 = pk_bf16(o[a][4 * i + 2] * sc, o[a][4 * i + 3] * sc);
            const unsigned b0 = pk_bf16(o[a][4 * i + 4] * sc, o[a][4 * i + 5] * sc), b1 = pk_bf16(o[a][4 * i + 6] * sc, o[a][4 * i + 7] * sc);
            const auto r0 = __builtin_amdgcn_permlane32_swap(a0, b0, false, false); const auto r1 = __builtin_amdgcn_permlane32_swap(a1, b1, false, false);
            const int c = 224 + head * 4 + i + hh;
            st16_wt(rs, (unsigned)WS_ACT + (panel * 352 + c) * 2048 + (row & 127) * 16, (u32x4){r0[0], r1[0], r0[1], r1[1]});
        }
    }
    asm volatile("s_waitcnt vmcnt(0)" ::: "memory");
    GM_BAR();
    if (threadIdx.x == 0) signal(p.ws, F_O + qb * 32 + head, p.tg());
}

__device__ __forceinline__ void gate_rows4(const Ctx& p, int row0, int lane) {
    u32x4 g[2], xr[4][2];
#pragma unroll
    for (int i = 0; i < 2; ++i) g[i] = ((const u32x4*)p.gw)[lane * 2 + i];
#pragma unroll
    for (int r = 0; r < 4; ++r)
#pragma unroll
        for (int i = 0; i < 2; ++i) xr[r][i] = ((const u32x4*)(p.ax + (size_t)(row0 + r) * DM))[lane * 2 + i];
    float s[4];
#pragma unroll
    for (int r = 0; r < 4; ++r) { float a = 0.f, b = 0.f;
#pragma unroll
        for (int i = 0; i < 2; ++i)
#pragma unroll
            for (int e = 0; e < 4; ++e) { a += bf_lo(xr[r][i][e]) * bf_lo(g[i][e]); b += bf_hi(xr[r][i][e]) * bf_hi(g[i][e]); }
        s[r] = a + b; }
#pragma unroll
    for (int r = 0; r < 4; ++r) s[r] = dpp_add<0x128>(s[r]);
#pragma unroll
    for (int r = 0; r < 4; ++r) s[r] = dpp_add<0x124>(s[r]);
#pragma unroll
    for (int r = 0; r < 4; ++r) s[r] = dpp_add<0x122>(s[r]);
#pragma unroll
    for (int r = 0; r < 4; ++r) s[r] = dpp_add<0x121>(s[r]);
#pragma unroll
    for (int r = 0; r < 4; ++r) {
        const auto a = __builtin_amdgcn_permlane16_swap(__float_as_uint(s[r]), __float_as_uint(s[r]), false, false); s[r] = __uint_as_float(a[0]) + __uint_as_float(a[1]);
        const auto b = __builtin_amdgcn_permlane32_swap(__float_as_uint(s[r]), __float_as_uint(s[r]), false, false); s[r] = __uint_as_float(b[0]) + __uint_as_float(b[1]); }
    const float v = lane == 0 ? s[0] : lane == 1 ? s[1] : lane == 2 ? s[2] : s[3];
    if (lane < 4) __hip_atomic_store((gu32*)(p.ws + WS_GATE) + row0 + lane, __float_as_uint(__builtin_amdgcn_rcpf(1.0f + __builtin_amdgcn_exp2f(-1.4426950408889634f * v))), RLX_AGENT);
}
constexpr int LDS_BYTES = CfgO::LDS > CfgQ::LDS + 256 ? CfgO::LDS : CfgQ::LDS + 256;
static_assert(LDS_TAB + 3072 <= LDS_BYTES && CfgO::LDS <= LDS_BYTES && AT_NS * AT_STAGE <= LDS_BYTES, "LDS map");
__global__ __launch_bounds__(512, 2) void fused(Params pp) {
    extern __shared__ __attribute__((aligned(16))) unsigned char smem[]; ldsp lds = (ldsp)smem;
    const int lane = threadIdx.x & 63, wid = __builtin_amdgcn_readfirstlane(threadIdx.x >> 6);
    const int b = blockIdx.x, x = b & 7, role = (b >> 3) & 1, idx = b >> 4;
    Ctx p; static_cast<Params&>(p) = pp;
    p.epv = __hip_atomic_load((gu32*)(pp.ws + WS_CTL) + C_EPOCH, RLX_AGENT);
    STAMP(0, 0);
    if (role == 0) {
        const int pair = 4 * (x >> 1) + (idx >> 2), panel = 4 * (x & 1) + (idx & 3);
        if (wid < 4) qkv_wave<0>(p, lds, pair, panel, wid, lane); else qkv_wave<1>(p, lds, pair, panel, wid, lane);
        STAMP(0, 6);
        const int opanel = 8 * (x & 1) + 4 * ((x >> 1) & 1) + (idx >> 2), oj = 4 * (x >> 2) + (idx & 3);
        attn_wg(p, lds, 4 * x + (idx >> 2), idx & 3, x, idx, oj, pair, panel, wid, lane);
        STAMP(0, 9);
        {
            if (wid < 4) out_part<true, false>(p, lds, opanel, oj, wid, lane); else out_part<false, false>(p, lds, opanel, oj, wid, lane); }
    } else {
        const int panel = 8 * (x & 1) + 4 * ((x >> 1) & 1) + (idx >> 2), j = 4 * (x >> 2) + (idx & 3);
        if (wid < 4) b_wave<true>(p, lds, panel, j, wid, lane); else b_wave<false>(p, lds, panel, j, wid, lane);
        if (panel == 0 && j == 0 && threadIdx.x == 0) __hip_atomic_store((gu32*)(pp.ws + WS_CTL) + C_EPOCH, p.tg(), RLX_AGENT);
    }
    STAMP(0, 15);
}
}

extern "C" void kernel_launch(void* const* d_in, const int* in_sizes, int n_in, void* d_out, int out_size, void* d_ws, size_t ws_size, hipStream_t stream) {
    using namespace gm;
    Params p;
    p.ax = (const u16*)d_in[0]; p.wqkv = (const u16*)d_in[1]; p.wo = (const u16*)d_in[2]; p.bx = (const u16*)d_in[3]; p.win = (const u16*)d_in[4];
    p.wsp = (const u16*)d_in[5]; p.bsp = (const u16*)d_in[6]; p.wout = (const u16*)d_in[7]; p.gw = (const u16*)d_in[8];
    p.out = (u16*)d_out; p.ws = (unsigned char*)d_ws;
    (void)hipFuncSetAttribute((const void*)fused, hipFuncAttributeMaxDynamicSharedMemorySize, LDS_BYTES);
    fused<<<256, 512, LDS_BYTES, stream>>>(p);
}
```

```cpp
#include <hip/hip_runtime.h>
#include <hip/hip_bf16.h>
#include <stdint.h>

#define LAS __attribute__((address_space(3)))
typedef short bf16x8 __attribute__((ext_vector_type(8)));
typedef short s16x4 __attribute__((ext_vector_type(4)));
typedef float f32x2 __attribute__((ext_vector_type(2)));
typedef float f32x4 __attribute__((ext_vector_type(4)));
typedef float f32x16 __attribute__((ext_vector_type(16)));
typedef unsigned u32x4 __attribute__((ext_vector_type(4)));
typedef unsigned u32x2 __attribute__((ext_vector_type(2)));
typedef LAS unsigned char* ldsp;
typedef unsigned short u16;

#define NA 16
#define TF 5
#define SPD 1
namespace gm {
constexpr int SEQ = 2048, DM = 1024, NH = 32, FH = 1792;
constexpr size_t WS_CTL = 0;
constexpr size_t CTL_BYTES = 16384;
constexpr size_t WS_GATE = 16384;
constexpr size_t WS_STAT = 32768;
constexpr size_t WS_Q = 1u << 20;
constexpr size_t WS_K = WS_Q + (4u << 20);
constexpr size_t WS_V = WS_K + (4u << 20);
constexpr size_t WS_Z = WS_V + (4u << 20);
constexpr size_t WS_ACT = WS_Z + (8u << 20);
constexpr int LDS_PFS = 147456 - 0;
constexpr size_t WS_P = WS_ACT + (12u << 20);
constexpr float QSCALE = 0.17677669529663687f * 1.4426950408889634f;

#define TRACE(t) do {} while (0)
#define DBG_PARAMS
#define DBG_ARGS(id)
#define DBG_FWD
#define STAMP(k, i) do {} while (0)
constexpr int C_EPOCH = 0, C_TMO = 64, F_Q = 128  , F_S = F_Q + 512  , F_Z = F_S + 512  ,
              F_U = F_Z + 256  , F_O = F_U + 512  , F_P = F_O + 128  , F_G = F_P + 512  , C_END = F_G + 512;
static_assert(C_END * 4 <= (int)CTL_BYTES, "control block");
#define GM_VM0() __builtin_amdgcn_s_waitcnt(0x0F70)
#define RLX_AGENT __ATOMIC_RELAXED, __HIP_MEMORY_SCOPE_AGENT
typedef __attribute__((address_space(1))) unsigned gu32;
typedef __attribute__((address_space(1))) unsigned long long gu64;
typedef __amdgpu_buffer_rsrc_t rsrc_t;
__device__ __forceinline__ rsrc_t ws_rsrc(unsigned char* ws) { return __builtin_amdgcn_make_buffer_rsrc(ws, 0, 64u << 20, 0x00020000); }
__device__ __forceinline__ void st16_wt(rsrc_t r, unsigned off, u32x4 v) { __builtin_amdgcn_raw_buffer_store_b128(v, r, off, 0, 16); }
__device__ __forceinline__ void signal(unsigned char* ws, int word, unsigned tag) { __hip_atomic_store((gu32*)(ws + WS_CTL) + word, tag, RLX_AGENT); }
__device__ __forceinline__ void poll_flags(unsigned char* ws, unsigned tag, int w0, int n, bool all128, int wq, int lane) {
    gu32* ctl = (gu32*)(ws + WS_CTL);
    for (unsigned spins = 0;;) {
        bool ok = __hip_atomic_load(ctl + w0 + (lane < n ? lane : 0), RLX_AGENT) == tag;
        if (all128) { const unsigned q0 = __hip_atomic_load(ctl + wq + (lane >> 3) * 32 + (lane & 7), RLX_AGENT), q1 = __hip_atomic_load(ctl + wq + 256 + (lane >> 3) * 32 + (lane & 7), RLX_AGENT); ok = ok && q0 == tag && q1 == tag; }
        if (__all(ok)) break;
        __builtin_amdgcn_s_sleep(2);
        if (++spins > (1u << 19)) { __hip_atomic_store(ctl + C_TMO, tag, RLX_AGENT); break; }
        if ((spins & 255u) == 0u && __builtin_amdgcn_readfirstlane(__hip_atomic_load(ctl + C_TMO, RLX_AGENT)) == tag) break;
    }
}
__device__ __forceinline__ void wait_flags(unsigned char* ws, unsigned tag, int w0, int n, bool all128, int wq, int lane) {
    poll_flags(ws, tag, w0, n, all128, wq, lane);
    GM_VM0();
}
__device__ __forceinline__ u32x4 ld16_dev(rsrc_t r, unsigned off) { return __builtin_amdgcn_raw_buffer_load_b128(r, off, 0, 16); }
enum { X_RAW = 0, X_OWN = 1 };
enum { W_P384 = 0, W_P448 = 1, W_P256 = 2, W_OWN = 3 };

__device__ __forceinline__ unsigned pk_bf16(float lo, float hi) {
    typedef __bf16 bf16x2_t __attribute__((ext_vector_type(2)));
    f32x2 v = {lo, hi}; bf16x2_t b = __builtin_convertvector(v, bf16x2_t); return __builtin_bit_cast(unsigned, b);
}
__device__ __forceinline__ float bf_lo(unsigned w) { return __uint_as_float(w << 16); }
__device__ __forceinline__ float bf_hi(unsigned w) { return __uint_as_float(w & 0xffff0000u); }
__device__ __forceinline__ s16x4 tr_read(ldsp p) { typedef short v4s __attribute__((ext_vector_type(4))); return __builtin_amdgcn_ds_read_tr16_b64_v4i16((LAS v4s*)p); }
template <int I, bool SC = false> __device__ __forceinline__ void glds_run(const char* sbase, unsigned voff) {
    if constexpr (SC) asm volatile("global_load_lds_dwordx4 %0, %1 offset:%2 sc1" ::"v"(voff), "s"(sbase), "n"((I & 3) * 1024) : "memory");
    else asm volatile("global_load_lds_dwordx4 %0, %1 offset:%2" ::"v"(voff), "s"(sbase), "n"((I & 3) * 1024) : "memory");
}
template <int I, bool SC = false> __device__ __forceinline__ void glds_set(const char* sbase, unsigned voff, unsigned dst) {
    if constexpr (SC) asm volatile("s_add_u32 m0, %2, %3\n\ts_nop 0\n\tglobal_load_lds_dwordx4 %0, %1 offset:%4 sc1" ::"v"(voff), "s"(sbase), "s"(dst), "n"((I & ~3) * 1024), "n"((I & 3) * 1024) : "memory", "scc");
    else asm volatile("s_add_u32 m0, %2, %3\n\ts_nop 0\n\tglobal_load_lds_dwordx4 %0, %1 offset:%4" ::"v"(voff), "s"(sbase), "s"(dst), "n"((I & ~3) * 1024), "n"((I & 3) * 1024) : "memory", "scc");
}
__device__ __forceinline__ void pf_lines(const void* base, unsigned voff, unsigned ldsdst) {
    asm volatile("s_mov_b32 m0, %2\n\ts_nop 0\n\tglobal_load_lds_dword %0, %1" ::"v"(voff), "s"(base), "s"(ldsdst) : "memory");
}
#define GM_BAR() do { asm volatile("" ::: "memory"); __builtin_amdgcn_s_barrier(); asm volatile("" ::: "memory"); } while (0)
#define GM_SB() __builtin_amdgcn_sched_barrier(0)
__device__ __forceinline__ bool at_tile(int t, int k) { asm volatile("" : "+s"(t)); return t == k; }

template <int BM_, int BN_, int XI_, int WI_, int NSX_, int NSW_> struct Cfg {
    static constexpr int BM = BM_, BN = BN_, XI = XI_, WI = WI_, NSX = NSX_, NSW = NSW_;
    static constexpr int XB = BM * 128, WB = BN * 128, WOFF = NSX * XB, LDS = NSX * XB + NSW * WB;
    static constexpr int PX = BM / 32, PW = BN / 32;
    static constexpr int WPITCH = (WI == W_P384) ? 384 : (WI == W_P448) ? 448 : (WI == W_P256) ? 256 : 0;
    static constexpr bool XSC = false, WSC = false;
};
template <class C> struct Src { const char* x; const char* w; const char* w2; int ksplit; unsigned xstep, wstep; unsigned xv[C::PX]; unsigned wv[C::PW]; };
template <int WI> __device__ __forceinline__ int w_swz(int r, int x) {
    if (WI == W_P384) return x ^ (((((r >> 1) & 1) | (((r >> 3) & 1) << 1))) << 5);
    if (WI == W_P448) return x ^ (((r >> 3) & 1) << 5);
    if (WI == W_P256) return x ^ (((r & 3) | (((r >> 3) & 1) << 2)) << 5);
    return x;
}
template <class C> __device__ __forceinline__ const char* w_tile(const Src<C>& s, int kt) { return (kt < s.ksplit ? s.w : s.w2) + (unsigned)kt * s.wstep; }
template <class C> __device__ __forceinline__ void set_w2(Src<C>& s, const void* base, int ksplit) { s.ksplit = ksplit; s.w2 = (const char*)base - 4096 - (size_t)ksplit * s.wstep; }
template <class C> __device__ __forceinline__ void src_x_raw(Src<C>& s, const void* base, unsigned pitch, int lid, int lane) {
    s.x = (const char*)base - 4096; s.xstep = 128;
#pragma unroll
    for (int i = 0; i < C::PX; ++i) { const int row = 8 * (lid * C::PX + i) + (lane >> 3); s.xv[i] = row * pitch + (((lane & 7) ^ ((row >> 1) & 7)) * 16) + 4096 - (i & 3) * 1024; }
}
template <class C> __device__ __forceinline__ void src_x_own(Src<C>& s, const void* base, int lid, int lane) {
    s.x = (const char*)base - 4096; s.xstep = C::XB;
#pragma unroll
    for (int i = 0; i < C::PX; ++i) s.xv[i] = (lid * C::PX + i) * 1024 + lane * 16 + 4096 - (i & 3) * 1024;
}
template <class C> __device__ __forceinline__ void src_w_own(Src<C>& s, const void* base, int lid, int lane) {
    s.w = (const char*)base - 4096; s.w2 = s.w; s.ksplit = 1 << 20; s.wstep = C::WB;
#pragma unroll
    for (int i = 0; i < C::PW; ++i) s.wv[i] = (lid * C::PW + i) * 1024 + lane * 16 + 4096 - (i & 3) * 1024;
}
template <class C> __device__ __forceinline__ void src_w_raw(Src<C>& s, const void* base, unsigned pitch, unsigned segstride, int lid, int lane) {
    s.w = (const char*)base - 4096; s.w2 = s.w; s.ksplit = 1 << 20; s.wstep = 64 * pitch;
#pragma unroll
    for (int i = 0; i < C::PW; ++i) { const int o = 1024 * (lid * C::PW + i) + 16 * lane; const int r = o / C::WPITCH, x = w_swz<C::WI>(r, o % C::WPITCH);
        s.wv[i] = r * pitch + (x >> 7) * segstride + (x & 127) + 4096 - (i & 3) * 1024; }
}
template <class C, int I0, int I1, bool FIRST> __device__ __forceinline__ void stage_rng(unsigned dx, unsigned dw, const Src<C>& s, const char* xb, const char* wb) {
    if constexpr (I0 < I1) {
        if constexpr (I0 < C::PW) { if constexpr (FIRST || (I0 & 3) == 0) glds_set<I0, C::WSC>(wb, s.wv[I0], dw); else glds_run<I0, C::WSC>(wb, s.wv[I0]); }
        else { constexpr int j = I0 - C::PW; if constexpr (FIRST || (j & 3) == 0) glds_set<j, C::XSC>(xb, s.xv[j], dx); else glds_run<j, C::XSC>(xb, s.xv[j]); }
        stage_rng<C, I0 + 1, I1, false>(dx, dw, s, xb, wb);
    }
}
template <int MT_, int NT_, int N0_, unsigned SWAP_, bool LD_> struct Wv { static constexpr int MT = MT_, NT = NT_, N0 = N0_; static constexpr unsigned SWAP = SWAP_; static constexpr bool LD = LD_; };
template <class W> struct Frag { bf16x8 x[W::MT]; s16x4 wl[W::NT], wh[W::NT]; };
template <class C, class W> struct RdAddr { unsigned x[2]; unsigned w[(C::WI == W_P384) ? 4 : (C::WI == W_P256) ? W::NT : 2]; };
template <class C, class W> __device__ __forceinline__ void rd_init(RdAddr<C, W>& a, int m0, int n0, int lane) {
    const int fr = lane & 15, fq = lane >> 4, g = lane >> 4, q = (lane & 15) >> 2, p = lane & 3;
    if (C::XI == X_RAW) { for (int kk = 0; kk < 2; ++kk) a.x[kk] = (m0 + fr) * 128 + (((kk * 4 + fq) ^ (fr >> 1)) * 16); }
    else { for (int kk = 0; kk < 2; ++kk) a.x[kk] = (kk * 4 + fq) * (C::BM * 16) + (m0 + fr) * 16; }
    if (C::WI == W_P384) { const int s = ((q >> 1) & 1) | ((g & 1) << 1);
        for (int c = 0; c < 4; ++c) a.w[c] = C::WOFF + (8 * g + q) * 384 + ((c ^ s) * 32) + p * 8; }
    else if (C::WI == W_P448) { const int s = g & 1;
        for (int c = 0; c < 2; ++c) a.w[c] = C::WOFF + (8 * g + q) * 448 + (((c ^ s) - c) * 32) + p * 8; }
    else if (C::WI == W_P256) { const int s = (q & 3) | ((g & 1) << 2);
        for (int t = 0; t < W::NT; ++t) a.w[t] = C::WOFF + (8 * g + q) * 256 + (((n0 + t) ^ s) * 32) + p * 8; }
    else { for (int h = 0; h < 2; ++h) a.w[h] = C::WOFF + (p >> 1) * 1024 + (8 * g + 4 * (h ^ (p >> 1)) + q) * 16 + (p & 1) * 8 + W::N0 * 2048; }
}
template <class C, class W> __device__ __forceinline__ s16x4 rd_w(ldsp b, const RdAddr<C, W>& a, int t, int kk, int h) {
    const int c = W::N0 + t;
    if (C::WI == W_P384) return tr_read(b + a.w[c & 3] + (c >> 2) * 128 + kk * 32 * 384 + h * 4 * 384);
    else if (C::WI == W_P448) return tr_read(b + a.w[c & 1] + c * 32 + kk * 32 * 448 + h * 4 * 448);
    else if (C::WI == W_P256) return tr_read(b + a.w[t] + kk * 32 * 256 + h * 4 * 256);
    else return tr_read(b + a.w[h] + t * 2048 + kk * 512);
}
template <class C, class W> __device__ __forceinline__ void rd_one(Frag<W>& f, ldsp bx, ldsp bw, const RdAddr<C, W>& a, int kk, int r) {
    if (r < 2) { if (r == 0) f.wl[0] = rd_w<C, W>(bw, a, 0, kk, 0); else f.wh[0] = rd_w<C, W>(bw, a, 0, kk, 1); }
    else if (r < 2 + W::MT) { const int m = r - 2; f.x[m] = *(const LAS bf16x8*)(bx + a.x[kk] + m * ((C::XI == X_RAW) ? 2048 : 256)); }
    else { const int u = r - W::MT; const int t = u >> 1; if (u & 1) f.wh[t] = rd_w<C, W>(bw, a, t, kk, 1); else f.wl[t] = rd_w<C, W>(bw, a, t, kk, 0); }
}
template <class W> __device__ __forceinline__ void mma_one(f32x4 (&acc)[W::NT][W::MT], const Frag<W>& f, int i) {
    const int t = i / W::MT, m = i % W::MT;
    const bf16x8 w = __builtin_shufflevector(f.wl[t], f.wh[t], 0, 1, 2, 3, 4, 5, 6, 7);
    acc[t][m] = ((W::SWAP >> t) & 1) ? __builtin_amdgcn_mfma_f32_16x16x32_bf16(f.x[m], w, acc[t][m], 0, 0, 0)
                                     : __builtin_amdgcn_mfma_f32_16x16x32_bf16(w, f.x[m], acc[t][m], 0, 0, 0);
}
template <int RFRONT_, int VA_, int RF1_ = RFRONT_> struct Sch { static constexpr int RFRONT = RFRONT_, VA = VA_, RF1 = RF1_; };
template <class C, class W, class S, int RF, int V0, int V1, int I>
__device__ __forceinline__ void step_slot(f32x4 (&acc)[W::NT][W::MT], const Frag<W>& fc, Frag<W>& fn, ldsp bx, ldsp bw, const RdAddr<C, W>& a, int ksn,
                                          const Src<C>& s, const char* xb, const char* wb, unsigned dx, unsigned dw) {
    constexpr int NM = W::NT * W::MT, NR = 2 * W::NT + W::MT, NV = V1 - V0;
    constexpr int RS = (NM * RF / 16) < 1 ? 1 : (NM * RF / 16);
    if constexpr (I < NM) {
        mma_one<W>(acc, fc, I);
        if constexpr (I < RS) {
#pragma unroll
            for (int r = I * NR / RS; r < (I + 1) * NR / RS; ++r) rd_one<C, W>(fn, bx, bw, a, ksn, r);
        }
        if constexpr (W::LD) {
            constexpr int v0 = V0 + I * NV / NM, v1 = V0 + (I + 1) * NV / NM;
            stage_rng<C, v0, v1, v0 == V0>(dx, dw, s, xb, wb);
        }
        GM_SB();
        step_slot<C, W, S, RF, V0, V1, I + 1>(acc, fc, fn, bx, bw, a, ksn, s, xb, wb, dx, dw);
    }
}
struct NoHook { static constexpr bool PRE = false; __device__ __forceinline__ void pre_x() const {} __device__ __forceinline__ void at(int) const {} };
template <class C, class W, class S, class H>
__device__ __forceinline__ void gemm_loop(ldsp lds, const Src<C>& s, int nt, f32x4 (&acc)[W::NT][W::MT], int lid, int m0, int n0, int lane, H& hook DBG_PARAMS) {
    constexpr int PX = C::PX, PW = C::PW, NSX = C::NSX, NSW = C::NSW, NV = PX + PW;
    static_assert(S::VA >= PW && S::VA <= NV, "all W pieces go out in the step behind the barrier");
    static_assert(NSX >= NSW && NSW >= 2, "ring depths");
    RdAddr<C, W> a; rd_init<C, W>(a, m0, n0, lane);
    const unsigned ldb = (unsigned)(uintptr_t)lds;
    const unsigned lx0 = ldb + lid * PX * 1024, lw0 = ldb + C::WOFF + lid * PW * 1024;
    Frag<W> f0, f1;
    const char* wb = w_tile<C>(s, NSW - 1); const char* xb = s.x + (size_t)(NSX - 1) * s.xstep;
    unsigned dw = lw0 + (NSW - 1) * C::WB, dx = lx0 + (NSX - 1) * C::XB;
    if constexpr (H::PRE) {
        if constexpr (W::LD) {
#pragma unroll
            for (int i = 0; i < NSW; ++i) stage_rng<C, 0, PW, true>(0, lw0 + i * C::WB, s, nullptr, w_tile<C>(s, i));
        }
        hook.pre_x();
        if constexpr (W::LD) {
#pragma unroll
            for (int i = 0; i < NSX - 1; ++i) stage_rng<C, PW, NV, true>(lx0 + i * C::XB, 0, s, s.x + (size_t)i * s.xstep, nullptr);
            if constexpr (S::VA > PW) stage_rng<C, PW, S::VA, true>(dx, dw, s, xb, wb);
            asm volatile("s_waitcnt vmcnt(%0)" ::"n"(S::VA - PW) : "memory");
        }
    } else if constexpr (W::LD) {
#pragma unroll
        for (int i = 0; i < NSX - 1; ++i) {
            if (i < NSW - 1) stage_rng<C, 0, PW, true>(0, lw0 + i * C::WB, s, nullptr, w_tile<C>(s, i));
            stage_rng<C, PW, NV, true>(lx0 + i * C::XB, 0, s, s.x + (size_t)i * s.xstep, nullptr);
        }
        stage_rng<C, 0, S::VA, true>(dx, dw, s, xb, wb);
        constexpr int Y = (NSX - 2) * PX + (NSW - 2) * PW + S::VA;
        asm volatile("s_waitcnt vmcnt(%0)" ::"n"(Y) : "memory");
    }
    GM_BAR();
#pragma unroll
    for (int r = 0; r < 2 * W::NT + W::MT; ++r) rd_one<C, W>(f0, lds, lds, a, 0, r);
    int bx = 0, bw = 0, kx = NSX - 1, kw = NSW - 1;
    __builtin_amdgcn_s_waitcnt(0xC07F);
    for (int t = 0; t < nt; ++t) {
        ldsp cx = lds + bx * C::XB, cw = lds + bw * C::WB;
        const int nbx = (bx + 1 == NSX) ? 0 : bx + 1, nbw = (bw + 1 == NSW) ? 0 : bw + 1;
        ldsp nx = lds + nbx * C::XB, nw = lds + nbw * C::WB;
        TRACE(t);
        GM_SB();
#define GM_STEP(FC, FN, BX, BW, KSN, RF, V0, V1) step_slot<C, W, S, RF, V0, V1, 0>(acc, FC, FN, BX, BW, a, KSN, s, xb, wb, dx, dw)
        GM_STEP(f0, f1, cx, cw, 1, S::RFRONT, S::VA, NV);
        hook.at(t);
        asm volatile("s_waitcnt lgkmcnt(0)" ::: "memory");
        if constexpr (W::LD) {
            constexpr int pw = 1 - NSW, px = 1 - NSX, last = (pw > px) ? pw : px, nper = (-1) - last, Y = nper * NV + ((pw > px) ? PX : 0);
            asm volatile("s_waitcnt vmcnt(%0)" ::"n"(Y) : "memory");
        }
        GM_BAR();
        GM_SB();
        if constexpr (W::LD) {
            kx = (kx + 1 < nt) ? kx + 1 : kx; kw = (kw + 1 < nt) ? kw + 1 : kw;
            xb = s.x + (size_t)kx * s.xstep; wb = w_tile<C>(s, kw);
            dx = lx0 + bx * C::XB; dw = lw0 + bw * C::WB;
        }
        GM_STEP(f1, f0, nx, nw, 0, S::RF1, 0, S::VA);
#undef GM_STEP
        bx = nbx; bw = nbw;
    }
    GM_SB();
    asm volatile("s_waitcnt vmcnt(0) lgkmcnt(0)" ::: "memory");
    GM_BAR();
}
template <class W> __device__ __forceinline__ void acc_zero(f32x4 (&acc)[W::NT][W::MT]) {
#pragma unroll
    for (int t = 0; t < W::NT; ++t)
#pragma unroll
        for (int m = 0; m < W::MT; ++m) acc[t][m] = (f32x4){0.f, 0.f, 0.f, 0.f};
}
__device__ __forceinline__ u32x4 pair16(const f32x4& A, const f32x4& B) {
    const unsigned a0 = pk_bf16(A[0], A[1]), a1 = pk_bf16(A[2], A[3]), b0 = pk_bf16(B[0], B[1]), b1 = pk_bf16(B[2], B[3]);
    const auto r0 = __builtin_amdgcn_permlane16_swap(a0, b0, false, false); const auto r1 = __builtin_amdgcn_permlane16_swap(a1, b1, false, false);
    return (u32x4){r0[0], r1[0], r0[1], r1[1]};
}

namespace g32 {
enum { W_P448 = 1, W_P256 = 2, W_OWNT = 3 };
template <int BM_, int BN_, int XI_, int WI_, int NS_, int NL_ = 4> struct Cfg {
    static constexpr int BM = BM_, BN = BN_, XI = XI_, WI = WI_, NS = NS_, NL = NL_;
    static constexpr int XB = BM * 128, WB = BN * 128, WOFF = NS * XB, LDS = NS * (XB + WB);
    static constexpr int PX = BM / 8 / NL, PW = BN / 8 / NL;
    static constexpr int WPITCH = (WI == W_P448) ? 448 : (WI == W_P256) ? 256 : 16;
    static constexpr bool XSC = (XI == X_OWN), WSC = (WI == W_OWNT);
};
template <class C> __device__ __forceinline__ void src_w_raw(Src<C>& s, const void* base, unsigned pitch, int lid, int lane) {
    s.w = (const char*)base - 4096; s.w2 = s.w; s.ksplit = 1 << 20; s.wstep = 64 * pitch;
#pragma unroll
    for (int i = 0; i < C::PW; ++i) { const int o = 1024 * (lid * C::PW + i) + 16 * lane; const int r = o / C::WPITCH; int x = o % C::WPITCH;
        if (C::WI == W_P256) x ^= (r & 3) << 6;
        s.wv[i] = r * pitch + x + 4096 - (i & 3) * 1024; }
}
template <int MT_, int NT_, bool LD_> struct Wv { static constexpr int MT = MT_, NT = NT_; static constexpr bool LD = LD_; };
template <class C, class W> struct Frag { bf16x8 x[W::MT]; s16x4 wl[W::NT], wh[W::NT]; };
template <class C, class W> struct RdAddr { unsigned x[(C::XI == X_RAW) ? 4 : 1]; unsigned w[(C::WI == W_P256) ? W::NT : 1]; };
template <class C, class W> __device__ __forceinline__ void rd_init(RdAddr<C, W>& a, int m0, int n0  , int lane) {
    const int r = lane & 31, h = lane >> 5, g = lane >> 4, q = (lane & 15) >> 2, p = lane & 3;
    if (C::XI == X_RAW) { for (int ks = 0; ks < 4; ++ks) a.x[ks] = (m0 + r) * 128 + (((2 * ks + h) ^ ((r >> 1) & 7)) * 16); }
    else a.x[0] = h * (C::BM * 16) + (m0 + r) * 16;
    const int rowp = 8 * (g >> 1) + q, colp = (g & 1) * 32 + p * 8;
    if (C::WI == W_P448) a.w[0] = C::WOFF + rowp * 448 + colp + n0 * 64;
    else if (C::WI == W_P256) { for (int t = 0; t < W::NT; ++t) a.w[t] = C::WOFF + rowp * 256 + (((n0 + t) ^ q) * 64) + colp; }
    else a.w[0] = C::WOFF + h * (C::BN * 16) + (n0 * 32 + r) * 16;
}
template <class C, class W> constexpr int n_reads() { return W::MT + (C::WI == W_OWNT ? W::NT : 2 * W::NT); }
template <class C, class W> __device__ __forceinline__ void rd_one(Frag<C, W>& f, ldsp bx, ldsp bw, const RdAddr<C, W>& a, int ks, int r) {
    constexpr int PER = (C::WI == W_OWNT) ? 1 : 2;
    if (r >= PER && r < PER + W::MT) { const int m = r - PER;
        if (C::XI == X_RAW) f.x[m] = *(const LAS bf16x8*)(bx + a.x[ks] + m * 4096);
        else f.x[m] = *(const LAS bf16x8*)(bx + a.x[0] + ks * (2 * C::BM * 16) + m * 512);
        return; }
    const int u = r < PER ? r : r - W::MT, t = u / PER, hh = u % PER;
    if (C::WI == W_OWNT) { const bf16x8 v = *(const LAS bf16x8*)(bw + a.w[0] + ks * (2 * C::BN * 16) + t * 512); f.wl[t] = __builtin_shufflevector(v, v, 0, 1, 2, 3); f.wh[t] = __builtin_shufflevector(v, v, 4, 5, 6, 7); }
    else { s16x4 v;
        if (C::WI == W_P448) v = tr_read(bw + a.w[0] + t * 64 + (ks * 16 + hh * 4) * 448);
        else v = tr_read(bw + a.w[t] + (ks * 16 + hh * 4) * 256);
        if (hh) f.wh[t] = v; else f.wl[t] = v; }
}
template <class C, class W> __device__ __forceinline__ void rd_frags(Frag<C, W>& f, ldsp bx, ldsp bw, const RdAddr<C, W>& a, int ks) {
#pragma unroll
    for (int r = 0; r < n_reads<C, W>(); ++r) rd_one<C, W>(f, bx, bw, a, ks, r);
}
template <class C, class W, bool SWAP  > __device__ __forceinline__ void mma_one(f32x16 (&acc)[W::NT][W::MT], const Frag<C, W>& f, int i) {
    const int t = i / W::MT, m = i % W::MT;
    const bf16x8 w = __builtin_shufflevector(f.wl[t], f.wh[t], 0, 1, 2, 3, 4, 5, 6, 7);
    acc[t][m] = SWAP ? __builtin_amdgcn_mfma_f32_32x32x16_bf16(w, f.x[m], acc[t][m], 0, 0, 0) : __builtin_amdgcn_mfma_f32_32x32x16_bf16(f.x[m], w, acc[t][m], 0, 0, 0);
}
struct NoFill { template <int S> __device__ __forceinline__ void chunk() {} };
template <class C, class W, bool SWAP, int V0, int V1, int BASE, int I, class F>
__device__ __forceinline__ void step_slot(f32x16 (&acc)[W::NT][W::MT], const Frag<C, W>& fc, Frag<C, W>& fn, ldsp bx, ldsp bw, const RdAddr<C, W>& a, int ksn,
                                          const Src<C>& s, const char* xb, const char* wb, unsigned dx, unsigned dw, F& fill) {
    constexpr int NM = W::NT * W::MT, NR = n_reads<C, W>(), NV = V1 - V0;
    if constexpr (I < NM) {
        mma_one<C, W, SWAP>(acc, fc, I);
#pragma unroll
        for (int r = I * NR / NM; r < (I + 1) * NR / NM; ++r) rd_one<C, W>(fn, bx, bw, a, ksn, r);
        if constexpr (W::LD) { constexpr int v0 = V0 + I * NV / NM, v1 = V0 + (I + 1) * NV / NM; stage_rng<C, v0, v1, v0 == V0>(dx, dw, s, xb, wb); }
        fill.template chunk<BASE + I>();
        GM_SB();
        step_slot<C, W, SWAP, V0, V1, BASE, I + 1, F>(acc, fc, fn, bx, bw, a, ksn, s, xb, wb, dx, dw, fill);
    }
}
template <class C, class WF  , int D> struct Pipe {
    static constexpr int PX = C::PX, PW = C::PW, NS = C::NS, NV = PX + PW;
#define QSPLIT 0
    static constexpr int Q1 = QSPLIT == 0 ? (NV + 3) / 4 : QSPLIT == 1 ? (NV + 1) / 2 : PW, Q2 = QSPLIT == 0 ? (NV + 1) / 2 : QSPLIT == 1 ? NV : NV, Q3 = QSPLIT == 0 ? (3 * NV + 3) / 4 : NV;
    static constexpr int PRE = (D == 1) ? Q1 : Q2;
    ldsp lds; int nt, xmod;
    RdAddr<C, WF> a; unsigned lx0, lw0;
    Frag<C, WF> f0, f1, f2, f3;
    __device__ __forceinline__ Pipe(ldsp lds_, const Src<C>& s, int nt_, int xmod_, int lid, int m0, int n0, int lane) : lds(lds_), nt(nt_), xmod(xmod_) {
        rd_init<C, WF>(a, m0, n0, lane);
        const unsigned ldb = (unsigned)(uintptr_t)lds;
        lx0 = ldb + lid * PX * 1024; lw0 = ldb + C::WOFF + lid * PW * 1024;
    }
    __device__ __forceinline__ void issue_x(const Src<C>& s) {
        if constexpr (WF::LD) {
#pragma unroll
            for (int i = 0; i < NS - 1; ++i) stage_rng<C, PW, NV, true>(lx0 + i * C::XB, 0, s, s.x + (size_t)i * s.xstep, nullptr);
        }
    }
    __device__ __forceinline__ void issue_w(const Src<C>& s) {
        static_assert(PRE <= PW, "prologue split");
        if constexpr (WF::LD) {
#pragma unroll
            for (int i = 0; i < NS - 1; ++i) stage_rng<C, 0, PW, true>(0, lw0 + i * C::WB, s, nullptr, w_tile<C>(s, i));
            stage_rng<C, 0, PRE, true>(0, lw0 + (NS - 1) * C::WB, s, nullptr, w_tile<C>(s, NS - 1));
        }
    }
    __device__ __forceinline__ void landed() {
        if constexpr (WF::LD) asm volatile("s_waitcnt vmcnt(0)" ::: "memory");
        GM_BAR();
        rd_frags<C, WF>(f0, lds, lds, a, 0);
        if (D == 2) rd_frags<C, WF>(f1, lds, lds, a, 1);
    }
    __device__ __forceinline__ void begin(const Src<C>& s) {
        if constexpr (WF::LD) {
#pragma unroll
            for (int i = 0; i < NS - 1; ++i) stage_rng<C, 0, NV, true>(lx0 + i * C::XB, lw0 + i * C::WB, s, s.x + (size_t)i * s.xstep, w_tile<C>(s, i));
            stage_rng<C, 0, PRE, true>(0, lw0 + (NS - 1) * C::WB, s, nullptr, w_tile<C>(s, NS - 1));
            asm volatile("s_waitcnt vmcnt(%0)" ::"n"((NS - 2) * NV + PRE) : "memory");
        }
        GM_BAR();
        rd_frags<C, WF>(f0, lds, lds, a, 0);
        if (D == 2) rd_frags<C, WF>(f1, lds, lds, a, 1);
    }
    template <bool SWAP, class H> __device__ __forceinline__ void run(const Src<C>& s, int t0, int t1, f32x16 (&acc)[WF::NT][WF::MT], H& hook DBG_PARAMS) {
        NoFill nf; run_fill<SWAP>(s, t0, t1, t0, acc, hook, nf DBG_FWD);
    }
    template <bool SWAP, class H, class F> __device__ __forceinline__ void run_fill(const Src<C>& s, int t0, int t1, int tb, f32x16 (&acc)[WF::NT][WF::MT], H& hook, F& fill DBG_PARAMS) {
        using W = WF; constexpr int NM = WF::MT * WF::NT;
        int sl = t0 % NS, kn = t0 - 1 + NS; kn = kn < nt ? kn : nt - 1;
        const char* xb = s.x + (unsigned)(kn & (xmod - 1)) * s.xstep; const char* wb = w_tile<C>(s, kn);
        unsigned dx = lx0 + ((sl == 0) ? NS - 1 : sl - 1) * C::XB, dw = lw0 + ((sl == 0) ? NS - 1 : sl - 1) * C::WB;
        __builtin_amdgcn_s_waitcnt(0xC07F);
        for (int t = t0; t < t1; ++t) {
            const int ns = (sl + 1 == NS) ? 0 : sl + 1;
            ldsp cx = lds + sl * C::XB, cw = lds + sl * C::WB, nx = lds + ns * C::XB, nw = lds + ns * C::WB;
            TRACE(t - tb);
            hook.top(t - tb);
            GM_SB();
#define G32_STEP(FC, FN, BX, BW, KSN, V0, V1, ST) step_slot<C, W, SWAP, V0, V1, ST * NM, 0, F>(acc, FC, FN, BX, BW, a, KSN, s, xb, wb, dx, dw, fill)
            if constexpr (D == 1) { G32_STEP(f0, f1, cx, cw, 1, Q1, Q2, 0); G32_STEP(f1, f2, cx, cw, 2, Q2, Q3, 1); G32_STEP(f2, f3, cx, cw, 3, Q3, NV, 2); }
            else { G32_STEP(f0, f2, cx, cw, 2, Q2, Q3, 0); G32_STEP(f1, f3, cx, cw, 3, Q3, NV, 1); }
            hook.at(t - tb);
            asm volatile("s_waitcnt lgkmcnt(0)" ::: "memory");
            if constexpr (W::LD) asm volatile("s_waitcnt vmcnt(%0)" ::"n"((NS - 2) * NV) : "memory");
            GM_BAR();
            GM_SB();
            if constexpr (W::LD) { kn = (kn + 1 < nt) ? kn + 1 : kn; xb = s.x + (unsigned)(kn & (xmod - 1)) * s.xstep; wb = w_tile<C>(s, kn); dx = lx0 + sl * C::XB; dw = lw0 + sl * C::WB; }
            if constexpr (D == 1) { G32_STEP(f3, f0, nx, nw, 0, 0, Q1, 3); }
            else { G32_STEP(f2, f0, nx, nw, 0, 0, Q1, 2); G32_STEP(f3, f1, nx, nw, 1, Q1, Q2, 3); }
#undef G32_STEP
            sl = ns;
        }
        GM_SB();
    }
    __device__ __forceinline__ void end() {
        asm volatile("s_waitcnt vmcnt(0) lgkmcnt(0)" ::: "memory");
        GM_BAR();
    }
};
template <class C, class WF> struct PipeKS {
    static constexpr int PX = C::PX, PW = C::PW, NS = C::NS, NV = PX + PW, NM = WF::MT * WF::NT;
    ldsp lds; int nt, kh;
    RdAddr<C, WF> a; unsigned lx0, lw0;
    Frag<C, WF> a0, a1, b0, b1;
    __device__ __forceinline__ PipeKS(ldsp lds_, int nt_, int kh_, int lid, int m0, int n0, int lane) : lds(lds_), nt(nt_), kh(kh_) {
        rd_init<C, WF>(a, m0, n0, lane);
        static_assert(C::XI == X_OWN && C::WI == W_P256, "layouts of the output GEMM");
        a.x[0] += kh * 2 * (2 * C::BM * 16);
#pragma unroll
        for (int t = 0; t < WF::NT; ++t) a.w[t] += kh * 32 * 256;
        const unsigned ldb = (unsigned)(uintptr_t)lds;
        lx0 = ldb + lid * PX * 1024; lw0 = ldb + C::WOFF + lid * PW * 1024;
    }
    __device__ __forceinline__ void issue_w(const Src<C>& s) {
        if constexpr (WF::LD) {
#pragma unroll
            for (int i = 0; i < NS; ++i) stage_rng<C, 0, PW, true>(0, lw0 + i * C::WB, s, nullptr, w_tile<C>(s, i)); }
    }
    __device__ __forceinline__ void issue_x(const Src<C>& s) {
        if constexpr (WF::LD) {
#pragma unroll
            for (int i = 0; i < NS; ++i) stage_rng<C, PW, NV, true>(lx0 + i * C::XB, 0, s, s.x + (size_t)i * s.xstep, nullptr); }
    }
    __device__ __forceinline__ void landed() {
        if constexpr (WF::LD) asm volatile("s_waitcnt vmcnt(0)" ::: "memory");
        GM_BAR();
        rd_frags<C, WF>(a0, lds, lds, a, 0); rd_frags<C, WF>(a1, lds, lds, a, 1);
    }
    template <class H> __device__ __forceinline__ void run(const Src<C>& s, f32x16 (&acc)[WF::NT][WF::MT], H& hook DBG_PARAMS) {
        using W = WF; NoFill fill; using F = NoFill;
        int sl = 0, kn = NS;
        __builtin_amdgcn_s_waitcnt(0xC07F);
#define KS_PERIOD(T, C0, C1, N0, N1) do { \
            TRACE(T); hook.at(T); \
            asm volatile("s_waitcnt lgkmcnt(0)" ::: "memory"); \
            if constexpr (W::LD) asm volatile("s_waitcnt vmcnt(%0)" ::"n"((NS - 2) * NV) : "memory"); \
            GM_BAR(); GM_SB(); \
            const int ns = (sl + 1 == NS) ? 0 : sl + 1, kc = kn < nt ? kn : nt - 1; \
            const char* xb = s.x + (unsigned)kc * s.xstep; const char* wb = w_tile<C>(s, kc); \
            const unsigned dx = lx0 + sl * C::XB, dw = lw0 + sl * C::WB; \
            ldsp nx = lds + ns * C::XB, nw = lds + ns * C::WB; \
            step_slot<C, W, true, 0, NV / 2, 0, 0, F>(acc, C0, N0, nx, nw, a, 0, s, xb, wb, dx, dw, fill); \
            step_slot<C, W, true, NV / 2, NV, NM, 0, F>(acc, C1, N1, nx, nw, a, 1, s, xb, wb, dx, dw, fill); \
            sl = ns; ++kn; } while (0)
        for (int t = 0; t < nt; t += 2) { KS_PERIOD(t, a0, a1, b0, b1); if (t + 1 >= nt) break; KS_PERIOD(t + 1, b0, b1, a0, a1); }
#undef KS_PERIOD
        GM_SB();
        asm volatile("s_waitcnt vmcnt(0) lgkmcnt(0)" ::: "memory");
        GM_BAR();
    }
};
template <class W> __device__ __forceinline__ void acc_zero(f32x16 (&acc)[W::NT][W::MT]) {
#pragma unroll
    for (int t = 0; t < W::NT; ++t)
#pragma unroll
        for (int m = 0; m < W::MT; ++m) acc[t][m] = (f32x16){};
}
__device__ __forceinline__ u32x4 pair32(unsigned a0, unsigned a1, unsigned b0, unsigned b1) {
    const auto r0 = __builtin_amdgcn_permlane32_swap(a0, b0, false, false); const auto r1 = __builtin_amdgcn_permlane32_swap(a1, b1, false, false);
    return (u32x4){r0[0], r1[0], r0[1], r1[1]};
}
}

struct Params {
    const u16* ax; const u16* wqkv; const u16* wo; const u16* bx; const u16* win; const u16* wsp; const u16* bsp; const u16* wout; const u16* gw;
    u16* out; unsigned char* ws;
};
struct Ctx : Params { unsigned epv; __device__ __forceinline__ unsigned tg() const { return (unsigned)__builtin_amdgcn_readfirstlane(epv) + 1u; } };

using CfgQ = Cfg<256, 192, X_RAW, W_P384, 3, 2>;
struct Warm {
    const char* base; unsigned pitch; int lpr_log2; int cnt; unsigned scratch;
    __device__ __forceinline__ unsigned mask(int t0, int wid) const {
        if (wid < 4 || cnt <= 0) return 0u;
        return ((0x11111111u << (wid - 4)) & (cnt >= 32 ? ~0u : ((1u << cnt) - 1u))) << t0;
    }
    __device__ __forceinline__ void issue(int n, int wid, int lane) const {
        if (n >= 0 && n < cnt && wid == 4 + (n & 3)) { const int idx = n * 64 + lane; pf_lines(base, (unsigned)(idx >> lpr_log2) * pitch + (unsigned)(idx & ((1 << lpr_log2) - 1)) * 128u, scratch); }
    }
};

struct SigHook {
    static constexpr bool PRE = false; int wid, lane; Warm w0, w1; unsigned act;
    __device__ __forceinline__ void pre_x() const {}
    __device__ __forceinline__ void top(int) const {}
    __device__ __forceinline__ void at(int t) const { if ((act >> t) & 1u) { w0.issue(t - 1, wid, lane); w1.issue(t - 1, wid, lane); } }
};
template <int WN> __device__ __forceinline__ void qkv_wave(const Ctx& p, ldsp lds, int pair, int panel, int wid, int lane) {
    using C = CfgQ; using W = Wv<4, 6, WN * 6, WN ? 0x3Cu : 0u, WN == 0>;
    const int lid = wid & 3, m0 = (wid & 3) * 64;
    Src<C> s;
    src_x_raw<C>(s, p.ax + (size_t)panel * 256 * DM, DM * 2, lid, lane);
    src_w_raw<C>(s, p.wqkv + pair * 64, 3 * DM * 2, DM * 2, lid, lane);
    f32x4 acc[6][4]; acc_zero<W>(acc);
    STAMP(0, 1);
    Warm none{nullptr, 0, 0, 0, 0};
    SigHook hk{wid, lane, none, none, 0u};
    gemm_loop<C, W, Sch<12, 7>>(lds, s, DM / 64, acc, lid, m0, 0, lane, hk DBG_ARGS(0));
    STAMP(0, 2);
    const int fr = lane & 15, fq = lane >> 4;
    const int row0 = panel * 256 + m0;
    const rsrc_t rs = ws_rsrc(p.ws);
#pragma unroll
    for (int t = 0; t < 6; t += 2) {
        const int T = WN * 6 + t, kind = T >> 2, head = pair * 2 + ((T >> 1) & 1);
        if (kind < 2) {
            const unsigned base = (unsigned)(kind == 0 ? WS_Q : WS_K);
            const int c = 2 * (fq & 1) + (fq >> 1);
            const float sc = kind == 0 ? QSCALE : 1.0f;
#pragma unroll
            for (int m = 0; m < 4; ++m) { const int row = row0 + m * 16 + fr;
                st16_wt(rs, base + ((head * 64 + (row >> 5)) * 4 + c) * 512 + (row & 31) * 16, pair16(acc[t][m] * sc, acc[t + 1][m] * sc)); }
        } else {
#pragma unroll
            for (int tt = 0; tt < 2; ++tt)
#pragma unroll
                for (int m = 0; m < 4; m += 2) {
                    const f32x4 A = acc[t + tt][m], B = acc[t + tt][m + 1];
                    const unsigned a0 = pk_bf16(A[0], A[1]), a1 = pk_bf16(A[2], A[3]), b0 = pk_bf16(B[0], B[1]), b1 = pk_bf16(B[2], B[3]);
                    const auto r0 = __builtin_amdgcn_permlane32_swap(a0, b0, false, false); const auto r1 = __builtin_amdgcn_permlane32_swap(a1, b1, false, false);
                    const int kb = (row0 >> 4) + m + (fq >> 1), h = fq & 1, d = tt * 16 + fr;
                    st16_wt(rs, (unsigned)WS_V + ((head * 128 + kb) * 2 + h) * 512 + d * 16, (u32x4){r0[0], r1[0], r0[1], r1[1]});
                }
        }
    }
    asm volatile("s_waitcnt vmcnt(0)" ::: "memory");
    GM_BAR();
    if (threadIdx.x == 0) signal(p.ws, F_Q + pair * 32 + panel, p.tg());
}

using CfgZ = g32::Cfg<128, 224, X_RAW, g32::W_P448, 3>;
using CfgS = g32::Cfg<128, 224, X_RAW, g32::W_OWNT, 3>;
#define ONS 5
#define ONL 4
using CfgO = g32::Cfg<128, 128, X_OWN, g32::W_P256, ONS, ONL>;
__device__ __forceinline__ float gelu_tanh(float x) {
    const float t = x * x * (-0.044715f * 2.0f * 0.7978845608028654f * 1.4426950408889634f) + (-2.0f * 0.7978845608028654f * 1.4426950408889634f);
    return x * __builtin_amdgcn_rcpf(1.0f + __builtin_amdgcn_exp2f(x * t));
}
template <int N> __device__ __forceinline__ void gelu_pairs(unsigned* z) {
    constexpr float C1 = -0.044715f * 2.0f * 0.7978845608028654f * 1.4426950408889634f, C2 = -2.0f * 0.7978845608028654f * 1.4426950408889634f;
    f32x2 x[N], e[N];
#pragma unroll
    for (int i = 0; i < N; ++i) x[i] = (f32x2){bf_lo(z[i]), bf_hi(z[i])};
#pragma unroll
    for (int i = 0; i < N; ++i) e[i] = x[i] * (x[i] * x[i] * C1 + C2);
    GM_SB();
#pragma unroll
    for (int i = 0; i < N; ++i) { e[i].x = __builtin_amdgcn_exp2f(e[i].x); e[i].y = __builtin_amdgcn_exp2f(e[i].y); }
    GM_SB();
#pragma unroll
    for (int i = 0; i < N; ++i) e[i] = e[i] + 1.0f;
#pragma unroll
    for (int i = 0; i < N; ++i) { e[i].x = __builtin_amdgcn_rcpf(e[i].x); e[i].y = __builtin_amdgcn_rcpf(e[i].y); }
    GM_SB();
#pragma unroll
    for (int i = 0; i < N; ++i) { const f32x2 y = x[i] * e[i]; z[i] = pk_bf16(y.x, y.y); }
    GM_SB();
}
__device__ __forceinline__ void gelu16(f32x16& a) {
    constexpr float C1 = -0.044715f * 2.0f * 0.7978845608028654f * 1.4426950408889634f, C2 = -2.0f * 0.7978845608028654f * 1.4426950408889634f;
    f32x16 e = a * (a * a * C1 + C2);
    GM_SB();
#pragma unroll
    for (int i = 0; i < 16; ++i) e[i] = __builtin_amdgcn_exp2f(e[i]);
    GM_SB();
    e = e + 1.0f;
#pragma unroll
    for (int i = 0; i < 16; ++i) e[i] = __builtin_amdgcn_rcpf(e[i]);
    GM_SB();
    a = a * e;
}
template <int CTRL> __device__ __forceinline__ float dpp_add(float x) { return x + __int_as_float(__builtin_amdgcn_update_dpp(0, __float_as_int(x), CTRL, 0xf, 0xf, false)); }
__device__ __forceinline__ float red32_pair(float a, float b) {
    const auto r = __builtin_amdgcn_permlane16_swap(__float_as_uint(a), __float_as_uint(b), false, false);
    float x = __uint_as_float(r[0]) + __uint_as_float(r[1]);
    x = dpp_add<0x128>(x); x = dpp_add<0x124>(x); x = dpp_add<0x122>(x); x = dpp_add<0x121>(x);
    return x;
}
constexpr int LDS_TAB = CfgZ::LDS;
template <int NT> struct GeluFill {
    static constexpr int NSLOT = 4 * NT, STRIDE = NSLOT / 4;
    unsigned (&z)[NT][8];
    f32x2 x, e;
    template <int S> __device__ __forceinline__ void chunk() {
        constexpr float C1 = -0.044715f * 2.0f * 0.7978845608028654f * 1.4426950408889634f, C2 = -2.0f * 0.7978845608028654f * 1.4426950408889634f;
        if constexpr (S % STRIDE == 0) { constexpr int pa = S / STRIDE;
            x = (f32x2){bf_lo(z[0][pa]), bf_hi(z[0][pa])};
            const f32x2 w = x * (x * x * C1 + C2);
            e.x = __builtin_amdgcn_exp2f(w.x); e.y = __builtin_amdgcn_exp2f(w.y);
#pragma unroll
            for (int g = 0; g + 1 < 2 * NT; ++g) z[g >> 1][(g & 1) * 4 + pa] = z[(g + 1) >> 1][((g + 1) & 1) * 4 + pa];
            asm volatile("" : "+v"(x), "+v"(e));
        }
        if constexpr (S % STRIDE == 2) { constexpr int pb = S / STRIDE;
            const f32x2 d = e + 1.0f;
            const f32x2 y = x * (f32x2){__builtin_amdgcn_rcpf(d.x), __builtin_amdgcn_rcpf(d.y)};
            z[NT - 1][4 + pb] = pk_bf16(y.x, y.y);
            asm volatile("" : "+v"(z[NT - 1][4 + pb]));
        }
    }
};
template <int NT, bool LD> struct LnHook {
    const Ctx& p; ldsp lds; int panel, j, wid, lane, n0; unsigned (&z2)[NT][8];
    u32x4 w[4];
    __device__ __forceinline__ void normalise(int tt) {
        LAS f32x2* R = (LAS f32x2*)(lds + LDS_TAB + 2048);
        const int r = lane & 31, h = lane >> 5, m0 = (wid & 3) * 32;
        const rsrc_t rs = ws_rsrc(p.ws);
        const int srow = panel * 128 + m0;
#pragma unroll
        for (int i = 0; i < 4; i += 2) {
            const f32x4 ra = *(const LAS f32x4*)(R + m0 + 8 * i + 4 * h), rb = *(const LAS f32x4*)(R + m0 + 8 * i + 4 * h + 2);
            const f32x4 rc = *(const LAS f32x4*)(R + m0 + 8 * i + 8 + 4 * h), rd = *(const LAS f32x4*)(R + m0 + 8 * i + 8 + 4 * h + 2);
            const int k = srow + 8 * (i + h);
            const unsigned base = (unsigned)WS_Z + ((j * 32 + (k >> 6)) * 8 + ((k & 63) >> 3)) * 3584 + (n0 * 32 + r) * 16;
            const unsigned a0 = z2[tt][2 * i], a1 = z2[tt][2 * i + 1], b0 = z2[tt][2 * i + 2], b1 = z2[tt][2 * i + 3];
            st16_wt(rs, base + tt * 512, g32::pair32(pk_bf16(fmaf(bf_lo(a0), ra[1], ra[0]), fmaf(bf_hi(a0), ra[3], ra[2])), pk_bf16(fmaf(bf_lo(a1), rb[1], rb[0]), fmaf(bf_hi(a1), rb[3], rb[2])),
                                                      pk_bf16(fmaf(bf_lo(b0), rc[1], rc[0]), fmaf(bf_hi(b0), rc[3], rc[2])), pk_bf16(fmaf(bf_lo(b1), rd[1], rd[0]), fmaf(bf_hi(b1), rd[3], rd[2]))));
        }
    }
    __device__ __forceinline__ void normalise_at(int t) {
        if (t >= TF + 1 && t < TF + 5) { if (at_tile(t, TF + 1)) normalise(0); if (at_tile(t, TF + 2)) normalise(1); if (at_tile(t, TF + 3)) normalise(2); if constexpr (NT == 4) { if (at_tile(t, TF + 4)) normalise(3); } }
    }
    __device__ __forceinline__ void fetch() {
        const unsigned off = (unsigned)WS_STAT + ((panel * 8 + (lane >> 5) * 4) * 128 + (wid - 4) * 32 + (lane & 31)) * 16;
#pragma unroll
        for (int v = 0; v < 4; ++v) w[v] = ld16_dev(ws_rsrc(p.ws), off + v * 2048);
    }
    __device__ __forceinline__ bool fetched() const { bool ok = true;
#pragma unroll
        for (int v = 0; v < 4; ++v) ok = ok && w[v][1] == p.tg() && w[v][3] == p.tg();
        return __all(ok); }
    __device__ __forceinline__ void top(int t) {
        if constexpr (LD) normalise_at(t);
        else { if (t == TF) fetch(); }
    }
    __device__ __forceinline__ void at(int t) {
        if (t < TF || t > TF + 7) return;
        if constexpr (!LD) {
            if (at_tile(t, TF)) {
                for (unsigned spins = 0; !fetched();) {
                    __builtin_amdgcn_s_sleep(2); fetch();
                    if (++spins > (1u << 19)) { __hip_atomic_store((gu32*)(p.ws + WS_CTL) + C_TMO, p.tg(), RLX_AGENT); break; }
                }
                LAS f32x2* R = (LAS f32x2*)(lds + LDS_TAB + 2048);
                float ms = 0.f;
#pragma unroll
                for (int v = 0; v < 4; ++v) ms += __uint_as_float(w[v][0]);
                { const auto r = __builtin_amdgcn_permlane32_swap(__float_as_uint(ms), __float_as_uint(ms), false, false); ms = __uint_as_float(r[0]) + __uint_as_float(r[1]); }
                const float mean = ms * 0.125f; float q = 0.f;
#pragma unroll
                for (int v = 0; v < 4; ++v) { const float dm = __uint_as_float(w[v][0]) - mean; q += __uint_as_float(w[v][2]) + 224.0f * dm * dm; }
                { const auto r = __builtin_amdgcn_permlane32_swap(__float_as_uint(q), __float_as_uint(q), false, false); q = __uint_as_float(r[0]) + __uint_as_float(r[1]); }
                { const float rs_ = __builtin_amdgcn_rsqf(q * (1.0f / 1792.0f) + 1e-5f); if (lane < 32) R[(wid - 4) * 32 + lane] = (f32x2){-mean * rs_, rs_}; }
                GM_VM0();
            }
            normalise_at(t);
        }
        if (at_tile(t, TF + 6)) asm volatile("s_waitcnt vmcnt(0)" ::: "memory");
        if constexpr (!LD) { if (at_tile(t, TF + 7)) { if (threadIdx.x == 256) signal(p.ws, F_Z + j * 32 + panel, p.tg()); } }
    }
};
struct SpHook {
    static constexpr bool PRE = false; const Ctx& p; ldsp lds; int panel, wid, lane;
    __device__ __forceinline__ void pre_x() const {}
    __device__ __forceinline__ void top(int) const {}
    __device__ __forceinline__ void at(int t) {
        if (t == 2 && (wid == 4 || wid == 5)) {
            poll_flags(p.ws, p.tg(), F_G, 1, true, F_G, lane);
            const int row = (wid - 4) * 64 + lane, trow = panel * 128 + row;
            ((LAS f32x2*)(lds + LDS_TAB + 2048))[row] = (f32x2){__uint_as_float((unsigned)p.bsp[trow] << 16), 1.0f - __uint_as_float(__hip_atomic_load((gu32*)(p.ws + WS_GATE) + trow, RLX_AGENT))};
            GM_VM0();
        }
    }
};
struct WaitHook {
    unsigned char* ws; unsigned tag; int wid, lane, tlate, wl, nl;
    __device__ __forceinline__ void top(int) const {}
    __device__ __forceinline__ void at(int t) const { if (t == tlate && wid == 4) wait_flags(ws, tag, wl, nl, false, 0, lane); }
};
template <class C> __device__ __forceinline__ void out_src_w(Src<C>& s, const Ctx& p, int k0, int j, int lid, int lane) {
    if (k0 < FH / 64) { g32::src_w_raw<C>(s, p.wout + (size_t)k0 * 64 * DM + j * 128, DM * 2, lid, lane); set_w2<C>(s, p.wo + j * 128, FH / 64 - k0); }
    else g32::src_w_raw<C>(s, p.wo + (size_t)(k0 - FH / 64) * 64 * DM + j * 128, DM * 2, lid, lane);
}
__device__ __forceinline__ void out_part_early_w(const Ctx& p, ldsp lds, int j, int wid, int lane) {
    using C = CfgO; using W = g32::Wv<2, 2, true>;
    static_assert(C::WOFF >= 65536, "W slots behind the attention ring");
    if (wid < 4) {
        Src<C> s; out_src_w<C>(s, p, 44 - NA, j, wid, lane);
        g32::PipeKS<C, W> pipe(lds, NA, 0, wid, 0, 0, lane);
        pipe.issue_w(s);
    }
}
template <bool LD, bool FINAL> __device__ __forceinline__ void out_part(const Ctx& p, ldsp lds, int panel, int j, int wid, int lane) {
    using C = CfgO; using W = g32::Wv<2, 2, LD>;
    const int lid = wid & 3, r = lane & 31, h = lane >> 5;
    const int kh = wid >> 2, mo = ((wid >> 1) & 1) * 64, no = (wid & 1) * 2;
    Src<C> s;
    constexpr int K0 = FINAL ? 0 : 44 - NA, NKT = FINAL ? 44 - NA : NA, NU = FH / 64 - K0 < NKT ? FH / 64 - K0 : NKT;
    src_x_own<C>(s, p.ws + WS_ACT + (size_t)panel * 352 * 2048 + K0 * C::XB, lid, lane);
    out_src_w<C>(s, p, K0, j, lid, lane);
    f32x16 acc[2][2]; g32::acc_zero<W>(acc);
    constexpr bool LATE = NU >= C::NS && NU < NKT;
    WaitHook hk{p.ws, p.tg(), wid, lane, LATE ? NU - C::NS : -1, F_O + (panel >> 2) * 32, 32};
    g32::PipeKS<C, W> pipe(lds, NKT, kh, lid, mo, no, lane);
    if (FINAL) pipe.issue_w(s);
    if (wid == 4) {
        if (NU > 0) wait_flags(p.ws, p.tg(), F_U + panel * 32, 8, false, 0, lane);
        if (NU < NKT && !LATE) wait_flags(p.ws, p.tg(), F_O + (panel >> 2) * 32, 32, false, 0, lane);
    }
    GM_BAR();
    pipe.issue_x(s);
    pipe.landed();
    STAMP(0, FINAL ? 10 : 7);
    pipe.run(s, acc, hk DBG_ARGS(4));
    STAMP(0, FINAL ? 9 : 8);
    ldsp mine = lds + wid * 8192 + lane * 16, theirs = lds + (wid ^ 4) * 8192 + lane * 16;
#pragma unroll
    for (int t = 0; t < 2; ++t)
#pragma unroll
        for (int i = 0; i < 4; ++i) { const f32x16& A = LD ? acc[t][1] : acc[t][0]; *(LAS f32x4*)(mine + (t * 4 + i) * 1024) = (f32x4){A[4 * i], A[4 * i + 1], A[4 * i + 2], A[4 * i + 3]}; }
    asm volatile("s_waitcnt lgkmcnt(0)" ::: "memory");
    if (FINAL && wid == 4) wait_flags(p.ws, p.tg(), F_P + panel * 32 + j, 1, false, 0, lane);
    GM_BAR();
    const rsrc_t rs = ws_rsrc(p.ws);
    const unsigned pb = (unsigned)WS_P + ((panel * 8 + j) * 8 + wid) * 8192 + lane * 16;
    f32x16 o[2];
    u32x4 pv[8];
    if (FINAL) {
#pragma unroll
        for (int q = 0; q < 8; ++q) pv[q] = ld16_dev(rs, pb + q * 1024);
    }
#pragma unroll
    for (int t = 0; t < 2; ++t) { o[t] = LD ? acc[t][0] : acc[t][1];
#pragma unroll
        for (int i = 0; i < 4; ++i) { const f32x4 v = *(const LAS f32x4*)(theirs + (t * 4 + i) * 1024); o[t][4 * i] += v[0]; o[t][4 * i + 1] += v[1]; o[t][4 * i + 2] += v[2]; o[t][4 * i + 3] += v[3]; } }
    if (!FINAL) {
#pragma unroll
        for (int t = 0; t < 2; ++t)
#pragma unroll
            for (int i = 0; i < 4; ++i) st16_wt(rs, pb + (t * 4 + i) * 1024, (u32x4){__float_as_uint(o[t][4 * i]), __float_as_uint(o[t][4 * i + 1]), __float_as_uint(o[t][4 * i + 2]), __float_as_uint(o[t][4 * i + 3])});
        asm volatile("s_waitcnt vmcnt(0)" ::: "memory");
        GM_BAR();
        if (threadIdx.x == 0) signal(p.ws, F_P + panel * 32 + j, p.tg());
    } else {
#pragma unroll
        for (int t = 0; t < 2; ++t)
#pragma unroll
            for (int i = 0; i < 4; ++i)
#pragma unroll
                for (int e = 0; e < 4; ++e) o[t][4 * i + e] += __uint_as_float(pv[t * 4 + i][e]);
        ldsp st = lds + 65536;
#pragma unroll
        for (int t = 0; t < 2; ++t)
#pragma unroll
            for (int i = 0; i < 4; i += 2) { const f32x16& A = o[t];
                *(LAS u32x4*)(st + (mo + kh * 32 + r) * 272 + (no + t) * 64 + (i + h) * 16) =
                    g32::pair32(pk_bf16(A[4 * i], A[4 * i + 1]), pk_bf16(A[4 * i + 2], A[4 * i + 3]), pk_bf16(A[4 * i + 4], A[4 * i + 5]), pk_bf16(A[4 * i + 6], A[4 * i + 7])); }
        asm volatile("s_waitcnt lgkmcnt(0)" ::: "memory"); GM_BAR();
#pragma unroll
        for (int i = 0; i < 4; ++i) { const int row = wid * 16 + i * 4 + (lane >> 4);
            __builtin_amdgcn_raw_buffer_store_b128(*(const LAS u32x4*)(st + row * 272 + (lane & 15) * 16), __builtin_amdgcn_make_buffer_rsrc(p.out, 0, SEQ * DM * 2, 0x00020000), (unsigned)(((panel * 128 + row) * DM + j * 128 + (lane & 15) * 8) * 2), 0, 16); }
    }
}
template <bool LD> __device__ __forceinline__ void b_wave(const Ctx& p, ldsp lds, int panel, int j, int wid, int lane) {
    constexpr int NT = LD ? 3 : 4;
    const int lid = wid & 3, m0 = lid * 32, n0 = LD ? 0 : 3;
    const int r = lane & 31, h = lane >> 5;
    unsigned z2[NT][8], z1[NT][8];
    const unsigned pfs = (unsigned)(uintptr_t)lds + LDS_PFS;
    {
        using C = CfgZ; using W = g32::Wv<1, NT, LD>; Src<C> s;
        src_x_raw<C>(s, p.ax + (size_t)panel * 128 * DM, DM * 2, lid, lane);
        g32::src_w_raw<C>(s, p.win + FH + j * 224, 2 * FH * 2, lid, lane);
        set_w2<C>(s, p.win + j * 224, DM / 64);
        g32::Pipe<C, W, 1> pipe(lds, s, 2 * DM / 64, DM / 64, lid, m0, n0, lane);
        f32x16 acc[NT][1]; g32::acc_zero<W>(acc);
        STAMP(0, 1);
        Warm wz1{(const char*)p.win + ((j * 224 * 2) & ~127) + (size_t)(panel & 3) * 256 * (2 * FH * 2), 2 * FH * 2, 2, 13, pfs};
        Warm none{nullptr, 0, 0, 0, 0};
        SigHook hk{wid, lane, wz1, none, wz1.mask(1, wid)};
        pipe.begin(s);
        pipe.template run<false>(s, 0, DM / 64, acc, hk DBG_ARGS(1));
        STAMP(0, 2);
        {
            float sm[16], sq[16];
#pragma unroll
            for (int t = 0; t < NT; ++t) {
                gelu16(acc[t][0]);
#pragma unroll
                for (int q = 0; q < 16; ++q) { const float v = acc[t][0][q]; sm[q] = t ? sm[q] + v : v; sq[q] = t ? sq[q] + v * v : v * v; }
#pragma unroll
                for (int q = 0; q < 16; q += 2) z2[t][q >> 1] = pk_bf16(acc[t][0][q], acc[t][0][q + 1]);
                acc[t][0] = (f32x16){};
            }
            float ra[16];
#pragma unroll
            for (int k = 0; k < 8; ++k) {
                const auto a = __builtin_amdgcn_permlane16_swap(__float_as_uint(sm[2 * k]), __float_as_uint(sm[2 * k + 1]), false, false); ra[k] = __uint_as_float(a[0]) + __uint_as_float(a[1]);
                const auto b2 = __builtin_amdgcn_permlane16_swap(__float_as_uint(sq[2 * k]), __float_as_uint(sq[2 * k + 1]), false, false); ra[8 + k] = __uint_as_float(b2[0]) + __uint_as_float(b2[1]); }
#pragma unroll
            for (int k = 0; k < 16; ++k) ra[k] = dpp_add<0x128>(ra[k]);
#pragma unroll
            for (int k = 0; k < 16; ++k) ra[k] = dpp_add<0x124>(ra[k]);
#pragma unroll
            for (int k = 0; k < 16; ++k) ra[k] = dpp_add<0x122>(ra[k]);
#pragma unroll
            for (int k = 0; k < 16; ++k) ra[k] = dpp_add<0x121>(ra[k]);
            float va = ra[0], vb = ra[8];
#pragma unroll
            for (int k = 1; k < 8; ++k) { const bool sel = (lane & 7) == k; va = sel ? ra[k] : va; vb = sel ? ra[8 + k] : vb; }
            LAS f32x2* P = (LAS f32x2*)(lds + LDS_TAB);
            { const int q = 2 * (lane & 7) + ((lane >> 4) & 1), row = m0 + 8 * (q >> 2) + 4 * h + (q & 3);
              if ((lane & 8) == 0) P[row * 2 + (LD ? 0 : 1)] = (f32x2){va, vb}; }
            asm volatile("s_waitcnt lgkmcnt(0)" ::: "memory"); GM_BAR();
            if constexpr (!LD) {
                if (lane < 32) {
                    const int row = lid * 32 + lane;
                    const f32x2 a = P[row * 2], b = P[row * 2 + 1];
                    const float su = a.x + b.x, mt = su * (1.0f / 224.0f), m2 = fmaxf(a.y + b.y - su * mt, 0.f);
                    st16_wt(ws_rsrc(p.ws), (unsigned)WS_STAT + ((panel * 8 + j) * 128 + row) * 16, (u32x4){__float_as_uint(mt), p.tg(), __float_as_uint(m2), p.tg()});
                }
            }
        }
        STAMP(0, 3);
        LnHook<NT, LD> hl{p, lds, panel, j, wid, lane, n0, z2};
        pipe.template run<true>(s, DM / 64, 2 * DM / 64, acc, hl DBG_ARGS(2));
        pipe.end();
        STAMP(0, 4);
#pragma unroll
        for (int t = 0; t < NT; ++t)
#pragma unroll
            for (int q = 0; q < 16; q += 2) z1[t][q >> 1] = pk_bf16(acc[t][0][q], acc[t][0][q + 1]);
    }
    {
        using C = CfgS; using W = g32::Wv<1, NT, LD>; Src<C> s;
        src_x_raw<C>(s, p.wsp + (size_t)panel * 128 * SEQ, SEQ * 2, lid, lane);
        src_w_own<C>(s, p.ws + WS_Z + (size_t)j * 32 * 28672, lid, lane);
        g32::Pipe<C, W, SPD> pipe(lds, s, SEQ / 64, SEQ / 64, lid, m0, n0, lane);
        pipe.issue_x(s);
        if (wid == 4) wait_flags(p.ws, p.tg(), F_Z + j * 32, 16, false, 0, lane);
        GM_BAR();
        pipe.issue_w(s);
        STAMP(0, 5);
#pragma unroll
        for (int t = 0; t < NT; ++t) { gelu_pairs<8>(z1[t]);
#pragma unroll
            for (int q = 0; q < 8; ++q) asm volatile("" : "+v"(z1[t][q])); }
        STAMP(0, 6);
        pipe.landed();
        f32x16 acc[NT][1]; g32::acc_zero<W>(acc);
        SpHook hk{p, lds, panel, wid, lane};
        pipe.template run<true>(s, 0, SEQ / 64, acc, hk DBG_ARGS(3));
        pipe.end();
        STAMP(0, 7);
        const rsrc_t rs = ws_rsrc(p.ws);
        const f32x2 bg = ((const LAS f32x2*)(lds + LDS_TAB + 2048))[m0 + r]; const float b = bg.x, g1 = bg.y;
        const unsigned ub = (unsigned)WS_ACT + (panel * 352 + j * 28 + n0 * 4 + h) * 2048 + (m0 + r) * 16;
#pragma unroll
        for (int t = 0; t < NT; ++t)
#pragma unroll
            for (int i = 0; i < 4; i += 2) {
                unsigned w4[4];
#pragma unroll
                for (int e = 0; e < 4; ++e) { const int q = 4 * i + 2 * e; const unsigned z = z1[t][q >> 1];
                    w4[e] = pk_bf16((acc[t][0][q] + b) * g1 * bf_lo(z), (acc[t][0][q + 1] + b) * g1 * bf_hi(z)); }
                st16_wt(rs, ub + (t * 4 + i) * 2048, g32::pair32(w4[0], w4[1], w4[2], w4[3]));
            }
        asm volatile("s_waitcnt vmcnt(0)" ::: "memory");
        GM_BAR();
        if (threadIdx.x == 256) signal(p.ws, F_U + panel * 32 + j, p.tg());
    }
    STAMP(0, 8);
    out_part<LD, true>(p, lds, panel, j, wid, lane);
}

constexpr int AT_STAGE = 16384, AT_NS = 4;
#define MFMA32(a, b, c) __builtin_amdgcn_mfma_f32_32x32x16_bf16(a, b, c, 0, 0, 0)
#define MFMA16(a, b, c) __builtin_amdgcn_mfma_f32_16x16x32_bf16(a, b, c, 0, 0, 0)
#define PIN(x) asm volatile("" : "+v"(x))
__device__ __forceinline__ void gate_rows4(const Ctx& p, int row0, int lane);
__device__ __forceinline__ void attn_wg(const Ctx& p, ldsp lds, int head, int qb, int x, int a, int oj, int gpair, int gpanel, int wid, int lane) {
    if (wid == 4) wait_flags(p.ws, p.tg(), F_Q + (head >> 1) * 32, 8, false, 0, lane);
    if (wid < 4) { gate_rows4(p, gpanel * 256 + gpair * 16 + wid * 4, lane); asm volatile("s_waitcnt vmcnt(0)" ::: "memory"); }
    GM_BAR();
    if (threadIdx.x == 0) signal(p.ws, F_G + gpair * 32 + gpanel, p.tg());
    STAMP(0, 3);
    const unsigned char* Kg = p.ws + WS_K + (size_t)head * 131072; const unsigned char* Vg = p.ws + WS_V + (size_t)head * 131072;
    const unsigned ldb = (unsigned)(uintptr_t)lds;
    const unsigned voff = wid * 1024 + lane * 16;
    const char* kb = (const char*)Kg; const char* vb = (const char*)Vg;
    auto stage = [&](int st, int slot) {
        asm volatile("s_add_u32 m0, %2, 0\n\ts_nop 0\n\tglobal_load_lds_dwordx4 %0, %1 sc1" ::"v"(voff), "s"(kb + (size_t)st * 8192), "s"(ldb + slot * AT_STAGE + wid * 1024) : "memory", "scc");
        asm volatile("s_add_u32 m0, %2, 0\n\ts_nop 0\n\tglobal_load_lds_dwordx4 %0, %1 sc1" ::"v"(voff), "s"(vb + (size_t)st * 8192), "s"(ldb + slot * AT_STAGE + 8192 + wid * 1024) : "memory", "scc");
    };
    stage(0, 0);
    const int q0 = qb * 512 + wid * 64;
    bf16x8 qf[2][2];
#pragma unroll
    for (int a = 0; a < 2; ++a)
#pragma unroll
        for (int d = 0; d < 2; ++d) qf[a][d] = __builtin_bit_cast(bf16x8, ld16_dev(ws_rsrc(p.ws), (unsigned)WS_Q + (head * 64 + (q0 >> 5) + a) * 2048 + d * 1024 + lane * 16));
    bf16x8 sel;
    { const short one = ((lane & 15) == ((lane >> 4) & 1)) ? (short)0x3f80 : (short)0;
#pragma unroll
      for (int i = 0; i < 8; ++i) sel[i] = one; }
    GM_VM0();
    GM_BAR();
    stage(1, 1); stage(2, 2);
    f32x16 o[2], negm[2]; f32x4 l[2];
    const f32x16 zero16 = {};
    bf16x8 kA[2], kB[2], vA[2], vB[2];
    ldsp lb = lds + lane * 16;
    kA[0] = *(const LAS bf16x8*)(lb); kA[1] = *(const LAS bf16x8*)(lb + 1024);
#pragma unroll
    for (int a = 0; a < 2; ++a) {
        f32x16 s = MFMA32(kA[0], qf[a][0], zero16); s = MFMA32(kA[1], qf[a][1], s);
        float m = fmaxf(fmaxf(s[0], s[1]), fmaxf(s[2], s[3]));
#pragma unroll
        for (int r = 4; r < 16; r += 2) m = fmaxf(m, fmaxf(s[r], s[r + 1]));
        const auto rr = __builtin_amdgcn_permlane32_swap(__float_as_uint(m), __float_as_uint(m), false, false);
        m = -fmaxf(__uint_as_float(rr[0]), __uint_as_float(rr[1]));
#pragma unroll
        for (int r = 0; r < 16; ++r) negm[a][r] = m;
        PIN(negm[a]);
        o[a] = zero16; l[a] = (f32x4){0.f, 0.f, 0.f, 0.f};
    }
    f32x16 S0, S1; u32x4 p0l, p0h, p1l, p1h;
    S0 = MFMA32(kA[0], qf[0][0], negm[0]); S0 = MFMA32(kA[1], qf[0][1], S0);
    p1l = (u32x4){0u, 0u, 0u, 0u}; p1h = p1l;
    vB[0] = (bf16x8){0, 0, 0, 0, 0, 0, 0, 0}; vB[1] = vB[0];
    STAMP(0, 4);
#define EX4(S, i) do { S[i] = __builtin_amdgcn_exp2f(S[i]); S[i + 1] = __builtin_amdgcn_exp2f(S[i + 1]); S[i + 2] = __builtin_amdgcn_exp2f(S[i + 2]); S[i + 3] = __builtin_amdgcn_exp2f(S[i + 3]); } while (0)
#define PK4(P, S, i) do { P[0] = pk_bf16(S[i], S[i + 1]); P[1] = pk_bf16(S[i + 2], S[i + 3]); P[2] = pk_bf16(S[i + 4], S[i + 5]); P[3] = pk_bf16(S[i + 6], S[i + 7]); } while (0)
#define BC(x) __builtin_bit_cast(bf16x8, x)
#define AORD 1
#define ASTEP(SC, SN, PCl, PCh, PPl, PPh, KF, QN, VF, AP, LOADS) do { \
        SN = MFMA32(KF[0], qf[QN][0], negm[QN]); GM_SB(); EX4(SC, 0); PIN(SC); GM_SB(); \
        SN = MFMA32(KF[1], qf[QN][1], SN); GM_SB(); LOADS; EX4(SC, 4); PIN(SC); GM_SB(); \
        o[AP] = MFMA32(VF[0], BC(PPl), o[AP]); GM_SB(); EX4(SC, 8); PIN(SC); GM_SB(); \
        o[AP] = MFMA32(VF[1], BC(PPh), o[AP]); GM_SB(); EX4(SC, 12); PIN(SC); GM_SB(); \
        l[AP] = MFMA16(sel, BC(PPl), l[AP]); GM_SB(); PK4(PCl, SC, 0); PIN(PCl); GM_SB(); \
        l[AP] = MFMA16(sel, BC(PPh), l[AP]); GM_SB(); PK4(PCh, SC, 8); PIN(PCh); GM_SB(); } while (0)
#define LDK(KF, base, kt) do { KF[0] = *(const LAS bf16x8*)((base) + (kt) * 2048); KF[1] = *(const LAS bf16x8*)((base) + (kt) * 2048 + 1024); } while (0)
#define LDV(VF, base, kt) do { VF[0] = *(const LAS bf16x8*)((base) + 8192 + (kt) * 2048); VF[1] = *(const LAS bf16x8*)((base) + 8192 + (kt) * 2048 + 1024); } while (0)
    float gval = 0.f;
    for (int st = 0; st < 16; ++st) {
        ldsp cb = lb + (st & 3) * AT_STAGE, nb = lb + ((st + 1) & 3) * AT_STAGE;
        if (st < 5) {
            const int n = st * 8 + wid; const unsigned pfs = (unsigned)(uintptr_t)lds + LDS_PFS;
            const int p0 = 8 * (x & 1) + 4 * ((x >> 1) & 1), j0 = 4 * (x >> 2);
            if (n < 16) pf_lines(p.wsp + (size_t)(p0 * 128 + a * 32) * SEQ, (unsigned)(lane & 31) * (SEQ * 2) + (unsigned)(2 * n + (lane >> 5)) * 128u, pfs);
            else if (n < 30) pf_lines(p.wout + (size_t)(a * 112) * DM + j0 * 128, (unsigned)((n - 16) * 8 + (lane >> 3)) * (DM * 2) + (unsigned)(lane & 7) * 128u, pfs);
            else if (n < 38) pf_lines(p.wo + (size_t)(a * 64) * DM + j0 * 128, (unsigned)((n - 30) * 8 + (lane >> 3)) * (DM * 2) + (unsigned)(lane & 7) * 128u, pfs);
        }
        if (st == 8) {
            poll_flags(p.ws, p.tg(), F_G, 1, true, F_G, lane);
            gval = __uint_as_float(__hip_atomic_load((gu32*)(p.ws + WS_GATE) + q0 + lane, RLX_AGENT));
        }
        ASTEP(S0, S1, p0l, p0h, p1l, p1h, kA, 1, vB, 1, { LDK(kB, cb, 1); LDV(vA, cb, 0); });
        ASTEP(S1, S0, p1l, p1h, p0l, p0h, kB, 0, vA, 0, {});
        ASTEP(S0, S1, p0l, p0h, p1l, p1h, kB, 1, vA, 1, { LDK(kA, cb, 2); LDV(vB, cb, 1); });
        ASTEP(S1, S0, p1l, p1h, p0l, p0h, kA, 0, vB, 0, {});
        ASTEP(S0, S1, p0l, p0h, p1l, p1h, kA, 1, vB, 1, { LDK(kB, cb, 3); LDV(vA, cb, 2); });
        ASTEP(S1, S0, p1l, p1h, p0l, p0h, kB, 0, vA, 0, {});
        if (st + 1 < 16) {
            if (st + 2 < 16) asm volatile("s_waitcnt vmcnt(2)" ::: "memory"); else asm volatile("s_waitcnt vmcnt(0)" ::: "memory");
            GM_BAR();
            if (st + 3 < 16) stage(st + 3, (st + 3) & 3);
            if (st == 14) out_part_early_w(p, lds, oj, wid, lane);
        }
        ASTEP(S0, S1, p0l, p0h, p1l, p1h, kB, 1, vA, 1, { LDK(kA, nb, 0); LDV(vB, cb, 3); });
        ASTEP(S1, S0, p1l, p1h, p0l, p0h, kA, 0, vB, 0, {});
    }
    o[1] = MFMA32(vB[0], BC(p1l), o[1]); o[1] = MFMA32(vB[1], BC(p1h), o[1]);
    l[1] = MFMA16(sel, BC(p1l), l[1]); l[1] = MFMA16(sel, BC(p1h), l[1]);
#undef EX4
#undef PK4
#undef ASTEP
#undef LDK
#undef LDV
    STAMP(0, 5);
    float lsum[2];
    {   bool bad = false;
#pragma unroll
        for (int a = 0; a < 2; ++a) {
            const int q = lane & 31;
            const float l0 = __shfl(l[a][0], q & 15), l1 = __shfl(l[a][1], q & 15);
            lsum[a] = (q < 16) ? l0 : l1;
            float mx = 0.f;
#pragma unroll
            for (int r = 0; r < 16; ++r) mx = fmaxf(mx, fabsf(o[a][r]));
            float sm = 0.f;
#pragma unroll
            for (int r = 0; r < 16; ++r) sm += o[a][r];
            bad = bad || !(lsum[a] < 1.2676506e30f) || !(mx < 3.0e38f) || !(sm == sm);
        }
        if (__builtin_expect(__any(bad), 0)) {
            const rsrc_t rq = ws_rsrc(p.ws);
            float mrun[2] = {-INFINITY, -INFINITY}, lrun[2] = {0.f, 0.f};
            o[0] = zero16; o[1] = zero16;
            for (int kt = 0; kt < 64; ++kt) {
                const unsigned ko = (unsigned)WS_K + head * 131072 + kt * 2048 + lane * 16, vo = (unsigned)WS_V + head * 131072 + kt * 2048 + lane * 16;
                const bf16x8 k0 = __builtin_bit_cast(bf16x8, ld16_dev(rq, ko)), k1 = __builtin_bit_cast(bf16x8, ld16_dev(rq, ko + 1024));
                const bf16x8 v0 = __builtin_bit_cast(bf16x8, ld16_dev(rq, vo)), v1 = __builtin_bit_cast(bf16x8, ld16_dev(rq, vo + 1024));
#pragma unroll
                for (int a = 0; a < 2; ++a) {
                    f32x16 s = MFMA32(k0, qf[a][0], zero16); s = MFMA32(k1, qf[a][1], s);
                    float m = s[0];
#pragma unroll
                    for (int r = 1; r < 16; ++r) m = fmaxf(m, s[r]);
                    { const auto rr = __builtin_amdgcn_permlane32_swap(__float_as_uint(m), __float_as_uint(m), false, false); m = fmaxf(__uint_as_float(rr[0]), __uint_as_float(rr[1])); }
                    const float mn = fmaxf(mrun[a], m), alpha = __builtin_amdgcn_exp2f(mrun[a] - mn);
                    mrun[a] = mn;
                    u32x4 pl, ph; float ls = 0.f;
#pragma unroll
                    for (int i = 0; i < 4; ++i) {
                        pl[i] = pk_bf16(__builtin_amdgcn_exp2f(s[2 * i] - mn), __builtin_amdgcn_exp2f(s[2 * i + 1] - mn));
                        ph[i] = pk_bf16(__builtin_amdgcn_exp2f(s[8 + 2 * i] - mn), __builtin_amdgcn_exp2f(s[9 + 2 * i] - mn));
                        ls += (bf_lo(pl[i]) + bf_hi(pl[i])) + (bf_lo(ph[i]) + bf_hi(ph[i])); }
                    lrun[a] = lrun[a] * alpha + ls;
                    o[a] = o[a] * alpha;
                    o[a] = MFMA32(v0, BC(pl), o[a]); o[a] = MFMA32(v1, BC(ph), o[a]);
                }
            }
#pragma unroll
            for (int a = 0; a < 2; ++a) { const auto rr = __builtin_amdgcn_permlane32_swap(__float_as_uint(lrun[a]), __float_as_uint(lrun[a]), false, false); lsum[a] = __uint_as_float(rr[0]) + __uint_as_float(rr[1]); }
        }
    }
    const rsrc_t rs = ws_rsrc(p.ws);
    const float gt0 = __shfl(gval, lane & 31), gt1 = __shfl(gval, 32 + (lane & 31));
#pragma unroll
    for (int a = 0; a < 2; ++a) {
        const int q = lane & 31, hh = lane >> 5;
        const float sc = (a ? gt1 : gt0) * __builtin_amdgcn_rcpf(lsum[a]);
        const int row = q0 + a * 32 + q, panel = row >> 7;
#pragma unroll
        for (int i = 0; i < 4; i += 2) {
            const unsigned a0 = pk_bf16(o[a][4 * i] * sc, o[a][4 * i + 1] * sc), a1 = pk_bf16(o[a][4 * i + 2] * sc, o[a][4 * i + 3] * sc);
            const unsigned b0 = pk_bf16(o[a][4 * i + 4] * sc, o[a][4 * i + 5] * sc), b1 = pk_bf16(o[a][4 * i + 6] * sc, o[a][4 * i + 7] * sc);
            const auto r0 = __builtin_amdgcn_permlane32_swap(a0, b0, false, false); const auto r1 = __builtin_amdgcn_permlane32_swap(a1, b1, false, false);
            const int c = 224 + head * 4 + i + hh;
            st16_wt(rs, (unsigned)WS_ACT + (panel * 352 + c) * 2048 + (row & 127) * 16, (u32x4){r0[0], r1[0], r0[1], r1[1]});
        }
    }
    asm volatile("s_waitcnt vmcnt(0)" ::: "memory");
    GM_BAR();
    if (threadIdx.x == 0) signal(p.ws, F_O + qb * 32 + head, p.tg());
}

__device__ __forceinline__ void gate_rows4(const Ctx& p, int row0, int lane) {
    u32x4 g[2], xr[4][2];
#pragma unroll
    for (int i = 0; i < 2; ++i) g[i] = ((const u32x4*)p.gw)[lane * 2 + i];
#pragma unroll
    for (int r = 0; r < 4; ++r)
#pragma unroll
        for (int i = 0; i < 2; ++i) xr[r][i] = ((const u32x4*)(p.ax + (size_t)(row0 + r) * DM))[lane * 2 + i];
    float s[4];
#pragma unroll
    for (int r = 0; r < 4; ++r) { float a = 0.f, b = 0.f;
#pragma unroll
        for (int i = 0; i < 2; ++i)
#pragma unroll
            for (int e = 0; e < 4; ++e) { a += bf_lo(xr[r][i][e]) * bf_lo(g[i][e]); b += bf_hi(xr[r][i][e]) * bf_hi(g[i][e]); }
        s[r] = a + b; }
#pragma unroll
    for (int r = 0; r < 4; ++r) s[r] = dpp_add<0x128>(s[r]);
#pragma unroll
    for (int r = 0; r < 4; ++r) s[r] = dpp_add<0x124>(s[r]);
#pragma unroll
    for (int r = 0; r < 4; ++r) s[r] = dpp_add<0x122>(s[r]);
#pragma unroll
    for (int r = 0; r < 4; ++r) s[r] = dpp_add<0x121>(s[r]);
#pragma unroll
    for (int r = 0; r < 4; ++r) {
        const auto a = __builtin_amdgcn_permlane16_swap(__float_as_uint(s[r]), __float_as_uint(s[r]), false, false); s[r] = __uint_as_float(a[0]) + __uint_as_float(a[1]);
        const auto b = __builtin_amdgcn_permlane32_swap(__float_as_uint(s[r]), __float_as_uint(s[r]), false, false); s[r] = __uint_as_float(b[0]) + __uint_as_float(b[1]); }
    const float v = lane == 0 ? s[0] : lane == 1 ? s[1] : lane == 2 ? s[2] : s[3];
    if (lane < 4) __hip_atomic_store((gu32*)(p.ws + WS_GATE) + row0 + lane, __float_as_uint(__builtin_amdgcn_rcpf(1.0f + __builtin_amdgcn_exp2f(-1.4426950408889634f * v))), RLX_AGENT);
}
constexpr int LDS_BYTES = CfgO::LDS > CfgQ::LDS + 256 ? CfgO::LDS : CfgQ::LDS + 256;
static_assert(LDS_TAB + 3072 <= LDS_BYTES && CfgO::LDS <= LDS_BYTES && AT_NS * AT_STAGE <= LDS_BYTES, "LDS map");
__global__ __launch_bounds__(512, 2) void fused(Params pp) {
    extern __shared__ __attribute__((aligned(16))) unsigned char smem[]; ldsp lds = (ldsp)smem;
    const int lane = threadIdx.x & 63, wid = __builtin_amdgcn_readfirstlane(threadIdx.x >> 6);
    const int b = blockIdx.x, x = b & 7, role = (b >> 3) & 1, idx = b >> 4;
    Ctx p; static_cast<Params&>(p) = pp;
    p.epv = __hip_atomic_load((gu32*)(pp.ws + WS_CTL) + C_EPOCH, RLX_AGENT);
    STAMP(0, 0);
    if (role == 0) {
        const int pair = 4 * (x >> 1) + (idx >> 2), panel = 4 * (x & 1) + (idx & 3);
        if (wid < 4) qkv_wave<0>(p, lds, pair, panel, wid, lane); else qkv_wave<1>(p, lds, pair, panel, wid, lane);
        STAMP(0, 6);
        const int opanel = 8 * (x & 1) + 4 * ((x >> 1) & 1) + (idx >> 2), oj = 4 * (x >> 2) + (idx & 3);
        attn_wg(p, lds, 4 * x + (idx >> 2), idx & 3, x, idx, oj, pair, panel, wid, lane);
        STAMP(0, 9);
        {
            if (wid < 4) out_part<true, false>(p, lds, opanel, oj, wid, lane); else out_part<false, false>(p, lds, opanel, oj, wid, lane); }
    } else {
        const int panel = 8 * (x & 1) + 4 * ((x >> 1) & 1) + (idx >> 2), j = 4 * (x >> 2) + (idx & 3);
        if (wid < 4) b_wave<true>(p, lds, panel, j, wid, lane); else b_wave<false>(p, lds, panel, j, wid, lane);
        if (panel == 0 && j == 0 && threadIdx.x == 0) __hip_atomic_store((gu32*)(pp.ws + WS_CTL) + C_EPOCH, p.tg(), RLX_AGENT);
    }
    STAMP(0, 15);
}
}

extern "C" void kernel_launch(void* const* d_in, const int* in_sizes, int n_in, void* d_out, int out_size, void* d_ws, size_t ws_size, hipStream_t stream) {
    using namespace gm;
    Params p;
    p.ax = (const u16*)d_in[0]; p.wqkv = (const u16*)d_in[1]; p.wo = (const u16*)d_in[2]; p.bx = (const u16*)d_in[3]; p.win = (const u16*)d_in[4];
    p.wsp = (const u16*)d_in[5]; p.bsp = (const u16*)d_in[6]; p.wout = (const u16*)d_in[7]; p.gw = (const u16*)d_in[8];
    p.out = (u16*)d_out; p.ws = (unsigned char*)d_ws;
    (void)hipFuncSetAttribute((const void*)fused, hipFuncAttributeMaxDynamicSharedMemorySize, LDS_BYTES);
    fused<<<256, 512, LDS_BYTES, stream>>>(p);
}
```

```cpp
#include <hip/hip_runtime.h>
#include <hip/hip_bf16.h>
#include <stdint.h>

#define LAS __attribute__((address_space(3)))
typedef short bf16x8 __attribute__((ext_vector_type(8)));
typedef short s16x4 __attribute__((ext_vector_type(4)));
typedef float f32x2 __attribute__((ext_vector_type(2)));
typedef float f32x4 __attribute__((ext_vector_type(4)));
typedef float f32x16 __attribute__((ext_vector_type(16)));
typedef unsigned u32x4 __attribute__((ext_vector_type(4)));
typedef unsigned u32x2 __attribute__((ext_vector_type(2)));
typedef LAS unsigned char* ldsp;
typedef unsigned short u16;

#define NA 16
#define TF 5
#define SPD 1
namespace gm {
constexpr int SEQ = 2048, DM = 1024, NH = 32, FH = 1792;
constexpr size_t WS_CTL = 0;
constexpr size_t CTL_BYTES = 16384;
constexpr size_t WS_GATE = 16384;
constexpr size_t WS_STAT = 32768;
constexpr size_t WS_Q = 1u << 20;
constexpr size_t WS_K = WS_Q + (4u << 20);
constexpr size_t WS_V = WS_K + (4u << 20);
constexpr size_t WS_Z = WS_V + (4u << 20);
constexpr size_t WS_ACT = WS_Z + (8u << 20);
constexpr int LDS_PFS = 147456 - 0;
constexpr size_t WS_P = WS_ACT + (12u << 20);
constexpr float QSCALE = 0.17677669529663687f * 1.4426950408889634f;

#define TRACE(t) do {} while (0)
#define DBG_PARAMS
#define DBG_ARGS(id)
#define DBG_FWD
#define STAMP(k, i) do {} while (0)
constexpr int C_EPOCH = 0, C_TMO = 64, F_Q = 128  , F_S = F_Q + 512  , F_Z = F_S + 512  ,
              F_U = F_Z + 256  , F_O = F_U + 512  , F_P = F_O + 128  , F_G = F_P + 512  , C_END = F_G + 512;
static_assert(C_END * 4 <= (int)CTL_BYTES, "control block");
#define GM_VM0() __builtin_amdgcn_s_waitcnt(0x0F70)
#define RLX_AGENT __ATOMIC_RELAXED, __HIP_MEMORY_SCOPE_AGENT
typedef __attribute__((address_space(1))) unsigned gu32;
typedef __attribute__((address_space(1))) unsigned long long gu64;
typedef __amdgpu_buffer_rsrc_t rsrc_t;
__device__ __forceinline__ rsrc_t uni_rsrc(const void* base, unsigned bytes) {
    const unsigned long long a = (unsigned long long)(uintptr_t)base;
    const unsigned lo = __builtin_amdgcn_readfirstlane((unsigned)a), hi = __builtin_amdgcn_readfirstlane((unsigned)(a >> 32));
    return __builtin_amdgcn_make_buffer_rsrc((void*)(uintptr_t)(((unsigned long long)hi << 32) | lo), 0, bytes, 0x00020000);
}
__device__ __forceinline__ rsrc_t ws_rsrc(unsigned char* ws) { return uni_rsrc(ws, 64u << 20); }
__device__ __forceinline__ void st16_wt(rsrc_t r, unsigned off, u32x4 v) { __builtin_amdgcn_raw_buffer_store_b128(v, r, off, 0, 16); }
__device__ __forceinline__ void signal(unsigned char* ws, int word, unsigned tag) { __hip_atomic_store((gu32*)(ws + WS_CTL) + word, tag, RLX_AGENT); }
__device__ __forceinline__ void poll_flags(unsigned char* ws, unsigned tag, int w0, int n, bool all128, int wq, int lane) {
    gu32* ctl = (gu32*)(ws + WS_CTL);
    for (unsigned spins = 0;;) {
        bool ok = __hip_atomic_load(ctl + w0 + (lane < n ? lane : 0), RLX_AGENT) == tag;
        if (all128) { const unsigned q0 = __hip_atomic_load(ctl + wq + (lane >> 3) * 32 + (lane & 7), RLX_AGENT), q1 = __hip_atomic_load(ctl + wq + 256 + (lane >> 3) * 32 + (lane & 7), RLX_AGENT); ok = ok && q0 == tag && q1 == tag; }
        if (__all(ok)) break;
        __builtin_amdgcn_s_sleep(2);
        if (++spins > (1u << 19)) { __hip_atomic_store(ctl + C_TMO, tag, RLX_AGENT); break; }
        if ((spins & 255u) == 0u && __builtin_amdgcn_readfirstlane(__hip_atomic_load(ctl + C_TMO, RLX_AGENT)) == tag) break;
    }
}
__device__ __forceinline__ void wait_flags(unsigned char* ws, unsigned tag, int w0, int n, bool all128, int wq, int lane) {
    poll_flags(ws, tag, w0, n, all128, wq, lane);
    GM_VM0();
}
__device__ __forceinline__ u32x4 ld16_dev(rsrc_t r, unsigned off) { return __builtin_amdgcn_raw_buffer_load_b128(r, off, 0, 16); }
enum { X_RAW = 0, X_OWN = 1 };
enum { W_P384 = 0, W_P448 = 1, W_P256 = 2, W_OWN = 3 };

__device__ __forceinline__ unsigned pk_bf16(float lo, float hi) {
    typedef __bf16 bf16x2_t __attribute__((ext_vector_type(2)));
    f32x2 v = {lo, hi}; bf16x2_t b = __builtin_convertvector(v, bf16x2_t); return __builtin_bit_cast(unsigned, b);
}
__device__ __forceinline__ float bf_lo(unsigned w) { return __uint_as_float(w << 16); }
__device__ __forceinline__ float bf_hi(unsigned w) { return __uint_as_float(w & 0xffff0000u); }
__device__ __forceinline__ s16x4 tr_read(ldsp p) { typedef short v4s __attribute__((ext_vector_type(4))); return __builtin_amdgcn_ds_read_tr16_b64_v4i16((LAS v4s*)p); }
template <int I, bool SC = false> __device__ __forceinline__ void glds_run(const char* sbase, unsigned voff) {
    if constexpr (SC) asm volatile("global_load_lds_dwordx4 %0, %1 offset:%2 sc1" ::"v"(voff), "s"(sbase), "n"((I & 3) * 1024) : "memory");
    else asm volatile("global_load_lds_dwordx4 %0, %1 offset:%2" ::"v"(voff), "s"(sbase), "n"((I & 3) * 1024) : "memory");
}
template <int I, bool SC = false> __device__ __forceinline__ void glds_set(const char* sbase, unsigned voff, unsigned dst) {
    if constexpr (SC) asm volatile("s_add_u32 m0, %2, %3\n\ts_nop 0\n\tglobal_load_lds_dwordx4 %0, %1 offset:%4 sc1" ::"v"(voff), "s"(sbase), "s"(dst), "n"((I & ~3) * 1024), "n"((I & 3) * 1024) : "memory", "scc");
    else asm volatile("s_add_u32 m0, %2, %3\n\ts_nop 0\n\tglobal_load_lds_dwordx4 %0, %1 offset:%4" ::"v"(voff), "s"(sbase), "s"(dst), "n"((I & ~3) * 1024), "n"((I & 3) * 1024) : "memory", "scc");
}
__device__ __forceinline__ void pf_lines(const void* base, unsigned voff, unsigned ldsdst) {
    asm volatile("s_mov_b32 m0, %2\n\ts_nop 0\n\tglobal_load_lds_dword %0, %1" ::"v"(voff), "s"(base), "s"(ldsdst) : "memory");
}
#define GM_BAR() do { asm volatile("" ::: "memory"); __builtin_amdgcn_s_barrier(); asm volatile("" ::: "memory"); } while (0)
#define GM_SB() __builtin_amdgcn_sched_barrier(0)
__device__ __forceinline__ bool at_tile(int t, int k) { asm volatile("" : "+s"(t)); return t == k; }

template <int BM_, int BN_, int XI_, int WI_, int NSX_, int NSW_> struct Cfg {
    static constexpr int BM = BM_, BN = BN_, XI = XI_, WI = WI_, NSX = NSX_, NSW = NSW_;
    static constexpr int XB = BM * 128, WB = BN * 128, WOFF = NSX * XB, LDS = NSX * XB + NSW * WB;
    static constexpr int PX = BM / 32, PW = BN / 32;
    static constexpr int WPITCH = (WI == W_P384) ? 384 : (WI == W_P448) ? 448 : (WI == W_P256) ? 256 : 0;
    static constexpr bool XSC = false, WSC = false;
};
template <class C> struct Src { const char* x; const char* w; const char* w2; int ksplit; unsigned xstep, wstep; unsigned xv[C::PX]; unsigned wv[C::PW]; };
template <int WI> __device__ __forceinline__ int w_swz(int r, int x) {
    if (WI == W_P384) return x ^ (((((r >> 1) & 1) | (((r >> 3) & 1) << 1))) << 5);
    if (WI == W_P448) return x ^ (((r >> 3) & 1) << 5);
    if (WI == W_P256) return x ^ (((r & 3) | (((r >> 3) & 1) << 2)) << 5);
    return x;
}
template <class C> __device__ __forceinline__ const char* w_tile(const Src<C>& s, int kt) { return (kt < s.ksplit ? s.w : s.w2) + (unsigned)kt * s.wstep; }
template <class C> __device__ __forceinline__ void set_w2(Src<C>& s, const void* base, int ksplit) { s.ksplit = ksplit; s.w2 = (const char*)base - 4096 - (size_t)ksplit * s.wstep; }
template <class C> __device__ __forceinline__ void src_x_raw(Src<C>& s, const void* base, unsigned pitch, int lid, int lane) {
    s.x = (const char*)base - 4096; s.xstep = 128;
#pragma unroll
    for (int i = 0; i < C::PX; ++i) { const int row = 8 * (lid * C::PX + i) + (lane >> 3); s.xv[i] = row * pitch + (((lane & 7) ^ ((row >> 1) & 7)) * 16) + 4096 - (i & 3) * 1024; }
}
template <class C> __device__ __forceinline__ void src_x_own(Src<C>& s, const void* base, int lid, int lane) {
    s.x = (const char*)base - 4096; s.xstep = C::XB;
#pragma unroll
    for (int i = 0; i < C::PX; ++i) s.xv[i] = (lid * C::PX + i) * 1024 + lane * 16 + 4096 - (i & 3) * 1024;
}
template <class C> __device__ __forceinline__ void src_w_own(Src<C>& s, const void* base, int lid, int lane) {
    s.w = (const char*)base - 4096; s.w2 = s.w; s.ksplit = 1 << 20; s.wstep = C::WB;
#pragma unroll
    for (int i = 0; i < C::PW; ++i) s.wv[i] = (lid * C::PW + i) * 1024 + lane * 16 + 4096 - (i & 3) * 1024;
}
template <class C> __device__ __forceinline__ void src_w_raw(Src<C>& s, const void* base, unsigned pitch, unsigned segstride, int lid, int lane) {
    s.w = (const char*)base - 4096; s.w2 = s.w; s.ksplit = 1 << 20; s.wstep = 64 * pitch;
#pragma unroll
    for (int i = 0; i < C::PW; ++i) { const int o = 1024 * (lid * C::PW + i) + 16 * lane; const int r = o / C::WPITCH, x = w_swz<C::WI>(r, o % C::WPITCH);
        s.wv[i] = r * pitch + (x >> 7) * segstride + (x & 127) + 4096 - (i & 3) * 1024; }
}
template <class C, int I0, int I1, bool FIRST> __device__ __forceinline__ void stage_rng(unsigned dx, unsigned dw, const Src<C>& s, const char* xb, const char* wb) {
    if constexpr (I0 < I1) {
        if constexpr (I0 < C::PW) { if constexpr (FIRST || (I0 & 3) == 0) glds_set<I0, C::WSC>(wb, s.wv[I0], dw); else glds_run<I0, C::WSC>(wb, s.wv[I0]); }
        else { constexpr int j = I0 - C::PW; if constexpr (FIRST || (j & 3) == 0) glds_set<j, C::XSC>(xb, s.xv[j], dx); else glds_run<j, C::XSC>(xb, s.xv[j]); }
        stage_rng<C, I0 + 1, I1, false>(dx, dw, s, xb, wb);
    }
}
template <int MT_, int NT_, int N0_, unsigned SWAP_, bool LD_> struct Wv { static constexpr int MT = MT_, NT = NT_, N0 = N0_; static constexpr unsigned SWAP = SWAP_; static constexpr bool LD = LD_; };
template <class W> struct Frag { bf16x8 x[W::MT]; s16x4 wl[W::NT], wh[W::NT]; };
template <class C, class W> struct RdAddr { unsigned x[2]; unsigned w[(C::WI == W_P384) ? 4 : (C::WI == W_P256) ? W::NT : 2]; };
template <class C, class W> __device__ __forceinline__ void rd_init(RdAddr<C, W>& a, int m0, int n0, int lane) {
    const int fr = lane & 15, fq = lane >> 4, g = lane >> 4, q = (lane & 15) >> 2, p = lane & 3;
    if (C::XI == X_RAW) { for (int kk = 0; kk < 2; ++kk) a.x[kk] = (m0 + fr) * 128 + (((kk * 4 + fq) ^ (fr >> 1)) * 16); }
    else { for (int kk = 0; kk < 2; ++kk) a.x[kk] = (kk * 4 + fq) * (C::BM * 16) + (m0 + fr) * 16; }
    if (C::WI == W_P384) { const int s = ((q >> 1) & 1) | ((g & 1) << 1);
        for (int c = 0; c < 4; ++c) a.w[c] = C::WOFF + (8 * g + q) * 384 + ((c ^ s) * 32) + p * 8; }
    else if (C::WI == W_P448) { const int s = g & 1;
        for (int c = 0; c < 2; ++c) a.w[c] = C::WOFF + (8 * g + q) * 448 + (((c ^ s) - c) * 32) + p * 8; }
    else if (C::WI == W_P256) { const int s = (q & 3) | ((g & 1) << 2);
        for (int t = 0; t < W::NT; ++t) a.w[t] = C::WOFF + (8 * g + q) * 256 + (((n0 + t) ^ s) * 32) + p * 8; }
    else { for (int h = 0; h < 2; ++h) a.w[h] = C::WOFF + (p >> 1) * 1024 + (8 * g + 4 * (h ^ (p >> 1)) + q) * 16 + (p & 1) * 8 + W::N0 * 2048; }
}
template <class C, class W> __device__ __forceinline__ s16x4 rd_w(ldsp b, const RdAddr<C, W>& a, int t, int kk, int h) {
    const int c = W::N0 + t;
    if (C::WI == W_P384) return tr_read(b + a.w[c & 3] + (c >> 2) * 128 + kk * 32 * 384 + h * 4 * 384);
    else if (C::WI == W_P448) return tr_read(b + a.w[c & 1] + c * 32 + kk * 32 * 448 + h * 4 * 448);
    else if (C::WI == W_P256) return tr_read(b + a.w[t] + kk * 32 * 256 + h * 4 * 256);
    else return tr_read(b + a.w[h] + t * 2048 + kk * 512);
}
template <class C, class W> __device__ __forceinline__ void rd_one(Frag<W>& f, ldsp bx, ldsp bw, const RdAddr<C, W>& a, int kk, int r) {
    if (r < 2) { if (r == 0) f.wl[0] = rd_w<C, W>(bw, a, 0, kk, 0); else f.wh[0] = rd_w<C, W>(bw, a, 0, kk, 1); }
    else if (r < 2 + W::MT) { const int m = r - 2; f.x[m] = *(const LAS bf16x8*)(bx + a.x[kk] + m * ((C::XI == X_RAW) ? 2048 : 256)); }
    else { const int u = r - W::MT; const int t = u >> 1; if (u & 1) f.wh[t] = rd_w<C, W>(bw, a, t, kk, 1); else f.wl[t] = rd_w<C, W>(bw, a, t, kk, 0); }
}
template <class W> __device__ __forceinline__ void mma_one(f32x4 (&acc)[W::NT][W::MT], const Frag<W>& f, int i) {
    const int t = i / W::MT, m = i % W::MT;
    const bf16x8 w = __builtin_shufflevector(f.wl[t], f.wh[t], 0, 1, 2, 3, 4, 5, 6, 7);
    acc[t][m] = ((W::SWAP >> t) & 1) ? __builtin_amdgcn_mfma_f32_16x16x32_bf16(f.x[m], w, acc[t][m], 0, 0, 0)
                                     : __builtin_amdgcn_mfma_f32_16x16x32_bf16(w, f.x[m], acc[t][m], 0, 0, 0);
}
template <int RFRONT_, int VA_, int RF1_ = RFRONT_> struct Sch { static constexpr int RFRONT = RFRONT_, VA = VA_, RF1 = RF1_; };
template <class C, class W, class S, int RF, int V0, int V1, int I>
__device__ __forceinline__ void step_slot(f32x4 (&acc)[W::NT][W::MT], const Frag<W>& fc, Frag<W>& fn, ldsp bx, ldsp bw, const RdAddr<C, W>& a, int ksn,
                                          const Src<C>& s, const char* xb, const char* wb, unsigned dx, unsigned dw) {
    constexpr int NM = W::NT * W::MT, NR = 2 * W::NT + W::MT, NV = V1 - V0;
    constexpr int RS = (NM * RF / 16) < 1 ? 1 : (NM * RF / 16);
    if constexpr (I < NM) {
        mma_one<W>(acc, fc, I);
        if constexpr (I < RS) {
#pragma unroll
            for (int r = I * NR / RS; r < (I + 1) * NR / RS; ++r) rd_one<C, W>(fn, bx, bw, a, ksn, r);
        }
        if constexpr (W::LD) {
            constexpr int v0 = V0 + I * NV / NM, v1 = V0 + (I + 1) * NV / NM;
            stage_rng<C, v0, v1, v0 == V0>(dx, dw, s, xb, wb);
        }
        GM_SB();
        step_slot<C, W, S, RF, V0, V1, I + 1>(acc, fc, fn, bx, bw, a, ksn, s, xb, wb, dx, dw);
    }
}
struct NoHook { static constexpr bool PRE = false; __device__ __forceinline__ void pre_x() const {} __device__ __forceinline__ void at(int) const {} };
template <class C, class W, class S, class H>
__device__ __forceinline__ void gemm_loop(ldsp lds, const Src<C>& s, int nt, f32x4 (&acc)[W::NT][W::MT], int lid, int m0, int n0, int lane, H& hook DBG_PARAMS) {
    constexpr int PX = C::PX, PW = C::PW, NSX = C::NSX, NSW = C::NSW, NV = PX + PW;
    static_assert(S::VA >= PW && S::VA <= NV, "all W pieces go out in the step behind the barrier");
    static_assert(NSX >= NSW && NSW >= 2, "ring depths");
    RdAddr<C, W> a; rd_init<C, W>(a, m0, n0, lane);
    const unsigned ldb = (unsigned)(uintptr_t)lds;
    const unsigned lx0 = ldb + lid * PX * 1024, lw0 = ldb + C::WOFF + lid * PW * 1024;
    Frag<W> f0, f1;
    const char* wb = w_tile<C>(s, NSW - 1); const char* xb = s.x + (size_t)(NSX - 1) * s.xstep;
    unsigned dw = lw0 + (NSW - 1) * C::WB, dx = lx0 + (NSX - 1) * C::XB;
    if constexpr (H::PRE) {
        if constexpr (W::LD) {
#pragma unroll
            for (int i = 0; i < NSW; ++i) stage_rng<C, 0, PW, true>(0, lw0 + i * C::WB, s, nullptr, w_tile<C>(s, i));
        }
        hook.pre_x();
        if constexpr (W::LD) {
#pragma unroll
            for (int i = 0; i < NSX - 1; ++i) stage_rng<C, PW, NV, true>(lx0 + i * C::XB, 0, s, s.x + (size_t)i * s.xstep, nullptr);
            if constexpr (S::VA > PW) stage_rng<C, PW, S::VA, true>(dx, dw, s, xb, wb);
            asm volatile("s_waitcnt vmcnt(%0)" ::"n"(S::VA - PW) : "memory");
        }
    } else if constexpr (W::LD) {
#pragma unroll
        for (int i = 0; i < NSX - 1; ++i) {
            if (i < NSW - 1) stage_rng<C, 0, PW, true>(0, lw0 + i * C::WB, s, nullptr, w_tile<C>(s, i));
            stage_rng<C, PW, NV, true>(lx0 + i * C::XB, 0, s, s.x + (size_t)i * s.xstep, nullptr);
        }
        stage_rng<C, 0, S::VA, true>(dx, dw, s, xb, wb);
        constexpr int Y = (NSX - 2) * PX + (NSW - 2) * PW + S::VA;
        asm volatile("s_waitcnt vmcnt(%0)" ::"n"(Y) : "memory");
    }
    GM_BAR();
#pragma unroll
    for (int r = 0; r < 2 * W::NT + W::MT; ++r) rd_one<C, W>(f0, lds, lds, a, 0, r);
    int bx = 0, bw = 0, kx = NSX - 1, kw = NSW - 1;
    __builtin_amdgcn_s_waitcnt(0xC07F);
    for (int t = 0; t < nt; ++t) {
        ldsp cx = lds + bx * C::XB, cw = lds + bw * C::WB;
        const int nbx = (bx + 1 == NSX) ? 0 : bx + 1, nbw = (bw + 1 == NSW) ? 0 : bw + 1;
        ldsp nx = lds + nbx * C::XB, nw = lds + nbw * C::WB;
        TRACE(t);
        GM_SB();
#define GM_STEP(FC, FN, BX, BW, KSN, RF, V0, V1) step_slot<C, W, S, RF, V0, V1, 0>(acc, FC, FN, BX, BW, a, KSN, s, xb, wb, dx, dw)
        GM_STEP(f0, f1, cx, cw, 1, S::RFRONT, S::VA, NV);
        hook.at(t);
        asm volatile("s_waitcnt lgkmcnt(0)" ::: "memory");
        if constexpr (W::LD) {
            constexpr int pw = 1 - NSW, px = 1 - NSX, last = (pw > px) ? pw : px, nper = (-1) - last, Y = nper * NV + ((pw > px) ? PX : 0);
            asm volatile("s_waitcnt vmcnt(%0)" ::"n"(Y) : "memory");
        }
        GM_BAR();
        GM_SB();
        if constexpr (W::LD) {
            kx = (kx + 1 < nt) ? kx + 1 : kx; kw = (kw + 1 < nt) ? kw + 1 : kw;
            xb = s.x + (size_t)kx * s.xstep; wb = w_tile<C>(s, kw);
            dx = lx0 + bx * C::XB; dw = lw0 + bw * C::WB;
        }
        GM_STEP(f1, f0, nx, nw, 0, S::RF1, 0, S::VA);
#undef GM_STEP
        bx = nbx; bw = nbw;
    }
    GM_SB();
    asm volatile("s_waitcnt vmcnt(0) lgkmcnt(0)" ::: "memory");
    GM_BAR();
}
template <class W> __device__ __forceinline__ void acc_zero(f32x4 (&acc)[W::NT][W::MT]) {
#pragma unroll
    for (int t = 0; t < W::NT; ++t)
#pragma unroll
        for (int m = 0; m < W::MT; ++m) acc[t][m] = (f32x4){0.f, 0.f, 0.f, 0.f};
}
__device__ __forceinline__ u32x4 pair16(const f32x4& A, const f32x4& B) {
    const unsigned a0 = pk_bf16(A[0], A[1]), a1 = pk_bf16(A[2], A[3]), b0 = pk_bf16(B[0], B[1]), b1 = pk_bf16(B[2], B[3]);
    const auto r0 = __builtin_amdgcn_permlane16_swap(a0, b0, false, false); const auto r1 = __builtin_amdgcn_permlane16_swap(a1, b1, false, false);
    return (u32x4){r0[0], r1[0], r0[1], r1[1]};
}

namespace g32 {
enum { W_P448 = 1, W_P256 = 2, W_OWNT = 3 };
template <int BM_, int BN_, int XI_, int WI_, int NS_, int NL_ = 4> struct Cfg {
    static constexpr int BM = BM_, BN = BN_, XI = XI_, WI = WI_, NS = NS_, NL = NL_;
    static constexpr int XB = BM * 128, WB = BN * 128, WOFF = NS * XB, LDS = NS * (XB + WB);
    static constexpr int PX = BM / 8 / NL, PW = BN / 8 / NL;
    static constexpr int WPITCH = (WI == W_P448) ? 448 : (WI == W_P256) ? 256 : 16;
    static constexpr bool XSC = (XI == X_OWN), WSC = (WI == W_OWNT);
};
template <class C> __device__ __forceinline__ void src_w_raw(Src<C>& s, const void* base, unsigned pitch, int lid, int lane) {
    s.w = (const char*)base - 4096; s.w2 = s.w; s.ksplit = 1 << 20; s.wstep = 64 * pitch;
#pragma unroll
    for (int i = 0; i < C::PW; ++i) { const int o = 1024 * (lid * C::PW + i) + 16 * lane; const int r = o / C::WPITCH; int x = o % C::WPITCH;
        if (C::WI == W_P256) x ^= (r & 3) << 6;
        s.wv[i] = r * pitch + x + 4096 - (i & 3) * 1024; }
}
template <int MT_, int NT_, bool LD_> struct Wv { static constexpr int MT = MT_, NT = NT_; static constexpr bool LD = LD_; };
template <class C, class W> struct Frag { bf16x8 x[W::MT]; s16x4 wl[W::NT], wh[W::NT]; };
template <class C, class W> struct RdAddr { unsigned x[(C::XI == X_RAW) ? 4 : 1]; unsigned w[(C::WI == W_P256) ? W::NT : 1]; };
template <class C, class W> __device__ __forceinline__ void rd_init(RdAddr<C, W>& a, int m0, int n0  , int lane) {
    const int r = lane & 31, h = lane >> 5, g = lane >> 4, q = (lane & 15) >> 2, p = lane & 3;
    if (C::XI == X_RAW) { for (int ks = 0; ks < 4; ++ks) a.x[ks] = (m0 + r) * 128 + (((2 * ks + h) ^ ((r >> 1) & 7)) * 16); }
    else a.x[0] = h * (C::BM * 16) + (m0 + r) * 16;
    const int rowp = 8 * (g >> 1) + q, colp = (g & 1) * 32 + p * 8;
    if (C::WI == W_P448) a.w[0] = C::WOFF + rowp * 448 + colp + n0 * 64;
    else if (C::WI == W_P256) { for (int t = 0; t < W::NT; ++t) a.w[t] = C::WOFF + rowp * 256 + (((n0 + t) ^ q) * 64) + colp; }
    else a.w[0] = C::WOFF + h * (C::BN * 16) + (n0 * 32 + r) * 16;
}
template <class C, class W> constexpr int n_reads() { return W::MT + (C::WI == W_OWNT ? W::NT : 2 * W::NT); }
template <class C, class W> __device__ __forceinline__ void rd_one(Frag<C, W>& f, ldsp bx, ldsp bw, const RdAddr<C, W>& a, int ks, int r) {
    constexpr int PER = (C::WI == W_OWNT) ? 1 : 2;
    if (r >= PER && r < PER + W::MT) { const int m = r - PER;
        if (C::XI == X_RAW) f.x[m] = *(const LAS bf16x8*)(bx + a.x[ks] + m * 4096);
        else f.x[m] = *(const LAS bf16x8*)(bx + a.x[0] + ks * (2 * C::BM * 16) + m * 512);
        return; }
    const int u = r < PER ? r : r - W::MT, t = u / PER, hh = u % PER;
    if (C::WI == W_OWNT) { const bf16x8 v = *(const LAS bf16x8*)(bw + a.w[0] + ks * (2 * C::BN * 16) + t * 512); f.wl[t] = __builtin_shufflevector(v, v, 0, 1, 2, 3); f.wh[t] = __builtin_shufflevector(v, v, 4, 5, 6, 7); }
    else { s16x4 v;
        if (C::WI == W_P448) v = tr_read(bw + a.w[0] + t * 64 + (ks * 16 + hh * 4) * 448);
        else v = tr_read(bw + a.w[t] + (ks * 16 + hh * 4) * 256);
        if (hh) f.wh[t] = v; else f.wl[t] = v; }
}
template <class C, class W> __device__ __forceinline__ void rd_frags(Frag<C, W>& f, ldsp bx, ldsp bw, const RdAddr<C, W>& a, int ks) {
#pragma unroll
    for (int r = 0; r < n_reads<C, W>(); ++r) rd_one<C, W>(f, bx, bw, a, ks, r);
}
template <class C, class W, bool SWAP  > __device__ __forceinline__ void mma_one(f32x16 (&acc)[W::NT][W::MT], const Frag<C, W>& f, int i) {
    const int t = i / W::MT, m = i % W::MT;
    const bf16x8 w = __builtin_shufflevector(f.wl[t], f.wh[t], 0, 1, 2, 3, 4, 5, 6, 7);
    acc[t][m] = SWAP ? __builtin_amdgcn_mfma_f32_32x32x16_bf16(w, f.x[m], acc[t][m], 0, 0, 0) : __builtin_amdgcn_mfma_f32_32x32x16_bf16(f.x[m], w, acc[t][m], 0, 0, 0);
}
struct NoFill { template <int S> __device__ __forceinline__ void chunk() {} };
template <class C, class W, bool SWAP, int V0, int V1, int BASE, int I, class F>
__device__ __forceinline__ void step_slot(f32x16 (&acc)[W::NT][W::MT], const Frag<C, W>& fc, Frag<C, W>& fn, ldsp bx, ldsp bw, const RdAddr<C, W>& a, int ksn,
                                          const Src<C>& s, const char* xb, const char* wb, unsigned dx, unsigned dw, F& fill) {
    constexpr int NM = W::NT * W::MT, NR = n_reads<C, W>(), NV = V1 - V0;
    if constexpr (I < NM) {
        mma_one<C, W, SWAP>(acc, fc, I);
#pragma unroll
        for (int r = I * NR / NM; r < (I + 1) * NR / NM; ++r) rd_one<C, W>(fn, bx, bw, a, ksn, r);
        if constexpr (W::LD) { constexpr int v0 = V0 + I * NV / NM, v1 = V0 + (I + 1) * NV / NM; stage_rng<C, v0, v1, v0 == V0>(dx, dw, s, xb, wb); }
        fill.template chunk<BASE + I>();
        GM_SB();
        step_slot<C, W, SWAP, V0, V1, BASE, I + 1, F>(acc, fc, fn, bx, bw, a, ksn, s, xb, wb, dx, dw, fill);
    }
}
template <class C, class WF  , int D> struct Pipe {
    static constexpr int PX = C::PX, PW = C::PW, NS = C::NS, NV = PX + PW;
#define QSPLIT 0
    static constexpr int Q1 = QSPLIT == 0 ? (NV + 3) / 4 : QSPLIT == 1 ? (NV + 1) / 2 : PW, Q2 = QSPLIT == 0 ? (NV + 1) / 2 : QSPLIT == 1 ? NV : NV, Q3 = QSPLIT == 0 ? (3 * NV + 3) / 4 : NV;
    static constexpr int PRE = (D == 1) ? Q1 : Q2;
    ldsp lds; int nt, xmod;
    RdAddr<C, WF> a; unsigned lx0, lw0;
    Frag<C, WF> f0, f1, f2, f3;
    __device__ __forceinline__ Pipe(ldsp lds_, const Src<C>& s, int nt_, int xmod_, int lid, int m0, int n0, int lane) : lds(lds_), nt(nt_), xmod(xmod_) {
        rd_init<C, WF>(a, m0, n0, lane);
        const unsigned ldb = (unsigned)(uintptr_t)lds;
        lx0 = ldb + lid * PX * 1024; lw0 = ldb + C::WOFF + lid * PW * 1024;
    }
    __device__ __forceinline__ void issue_x(const Src<C>& s) {
        if constexpr (WF::LD) {
#pragma unroll
            for (int i = 0; i < NS - 1; ++i) stage_rng<C, PW, NV, true>(lx0 + i * C::XB, 0, s, s.x + (size_t)i * s.xstep, nullptr);
        }
    }
    __device__ __forceinline__ void issue_w(const Src<C>& s) {
        static_assert(PRE <= PW, "prologue split");
        if constexpr (WF::LD) {
#pragma unroll
            for (int i = 0; i < NS - 1; ++i) stage_rng<C, 0, PW, true>(0, lw0 + i * C::WB, s, nullptr, w_tile<C>(s, i));
            stage_rng<C, 0, PRE, true>(0, lw0 + (NS - 1) * C::WB, s, nullptr, w_tile<C>(s, NS - 1));
        }
    }
    __device__ __forceinline__ void landed() {
        if constexpr (WF::LD) asm volatile("s_waitcnt vmcnt(0)" ::: "memory");
        GM_BAR();
        rd_frags<C, WF>(f0, lds, lds, a, 0);
        if (D == 2) rd_frags<C, WF>(f1, lds, lds, a, 1);
    }
    __device__ __forceinline__ void begin(const Src<C>& s) {
        if constexpr (WF::LD) {
#pragma unroll
            for (int i = 0; i < NS - 1; ++i) stage_rng<C, 0, NV, true>(lx0 + i * C::XB, lw0 + i * C::WB, s, s.x + (size_t)i * s.xstep, w_tile<C>(s, i));
            stage_rng<C, 0, PRE, true>(0, lw0 + (NS - 1) * C::WB, s, nullptr, w_tile<C>(s, NS - 1));
            asm volatile("s_waitcnt vmcnt(%0)" ::"n"((NS - 2) * NV + PRE) : "memory");
        }
        GM_BAR();
        rd_frags<C, WF>(f0, lds, lds, a, 0);
        if (D == 2) rd_frags<C, WF>(f1, lds, lds, a, 1);
    }
    template <bool SWAP, class H> __device__ __forceinline__ void run(const Src<C>& s, int t0, int t1, f32x16 (&acc)[WF::NT][WF::MT], H& hook DBG_PARAMS) {
        NoFill nf; run_fill<SWAP>(s, t0, t1, t0, acc, hook, nf DBG_FWD);
    }
    template <bool SWAP, class H, class F> __device__ __forceinline__ void run_fill(const Src<C>& s, int t0, int t1, int tb, f32x16 (&acc)[WF::NT][WF::MT], H& hook, F& fill DBG_PARAMS) {
        using W = WF; constexpr int NM = WF::MT * WF::NT;
        int sl = t0 % NS, kn = t0 - 1 + NS; kn = kn < nt ? kn : nt - 1;
        const char* xb = s.x + (unsigned)(kn & (xmod - 1)) * s.xstep; const char* wb = w_tile<C>(s, kn);
        unsigned dx = lx0 + ((sl == 0) ? NS - 1 : sl - 1) * C::XB, dw = lw0 + ((sl == 0) ? NS - 1 : sl - 1) * C::WB;
        __builtin_amdgcn_s_waitcnt(0xC07F);
        for (int t = t0; t < t1; ++t) {
            const int ns = (sl + 1 == NS) ? 0 : sl + 1;
            ldsp cx = lds + sl * C::XB, cw = lds + sl * C::WB, nx = lds + ns * C::XB, nw = lds + ns * C::WB;
            TRACE(t - tb);
            hook.top(t - tb);
            GM_SB();
#define G32_STEP(FC, FN, BX, BW, KSN, V0, V1, ST) step_slot<C, W, SWAP, V0, V1, ST * NM, 0, F>(acc, FC, FN, BX, BW, a, KSN, s, xb, wb, dx, dw, fill)
            if constexpr (D == 1) { G32_STEP(f0, f1, cx, cw, 1, Q1, Q2, 0); G32_STEP(f1, f2, cx, cw, 2, Q2, Q3, 1); G32_STEP(f2, f3, cx, cw, 3, Q3, NV, 2); }
            else { G32_STEP(f0, f2, cx, cw, 2, Q2, Q3, 0); G32_STEP(f1, f3, cx, cw, 3, Q3, NV, 1); }
            hook.at(t - tb);
            asm volatile("s_waitcnt lgkmcnt(0)" ::: "memory");
            if constexpr (W::LD) asm volatile("s_waitcnt vmcnt(%0)" ::"n"((NS - 2) * NV) : "memory");
            GM_BAR();
            GM_SB();
            if constexpr (W::LD) { kn = (kn + 1 < nt) ? kn + 1 : kn; xb = s.x + (unsigned)(kn & (xmod - 1)) * s.xstep; wb = w_tile<C>(s, kn); dx = lx0 + sl * C::XB; dw = lw0 + sl * C::WB; }
            if constexpr (D == 1) { G32_STEP(f3, f0, nx, nw, 0, 0, Q1, 3); }
            else { G32_STEP(f2, f0, nx, nw, 0, 0, Q1, 2); G32_STEP(f3, f1, nx, nw, 1, Q1, Q2, 3); }
#undef G32_STEP
            sl = ns;
        }
        GM_SB();
    }
    __device__ __forceinline__ void end() {
        asm volatile("s_waitcnt vmcnt(0) lgkmcnt(0)" ::: "memory");
        GM_BAR();
    }
};
template <class C, class WF> struct PipeKS {
    static constexpr int PX = C::PX, PW = C::PW, NS = C::NS, NV = PX + PW, NM = WF::MT * WF::NT;
    ldsp lds; int nt, kh;
    RdAddr<C, WF> a; unsigned lx0, lw0;
    Frag<C, WF> a0, a1, b0, b1;
    __device__ __forceinline__ PipeKS(ldsp lds_, int nt_, int kh_, int lid, int m0, int n0, int lane) : lds(lds_), nt(nt_), kh(kh_) {
        rd_init<C, WF>(a, m0, n0, lane);
        static_assert(C::XI == X_OWN && C::WI == W_P256, "layouts of the output GEMM");
        a.x[0] += kh * 2 * (2 * C::BM * 16);
#pragma unroll
        for (int t = 0; t < WF::NT; ++t) a.w[t] += kh * 32 * 256;
        const unsigned ldb = (unsigned)(uintptr_t)lds;
        lx0 = ldb + lid * PX * 1024; lw0 = ldb + C::WOFF + lid * PW * 1024;
    }
    __device__ __forceinline__ void issue_w(const Src<C>& s) {
        if constexpr (WF::LD) {
#pragma unroll
            for (int i = 0; i < NS; ++i) stage_rng<C, 0, PW, true>(0, lw0 + i * C::WB, s, nullptr, w_tile<C>(s, i)); }
    }
    __device__ __forceinline__ void issue_x(const Src<C>& s) {
        if constexpr (WF::LD) {
#pragma unroll
            for (int i = 0; i < NS; ++i) stage_rng<C, PW, NV, true>(lx0 + i * C::XB, 0, s, s.x + (size_t)i * s.xstep, nullptr); }
    }
    __device__ __forceinline__ void landed() {
        if constexpr (WF::LD) asm volatile("s_waitcnt vmcnt(0)" ::: "memory");
        GM_BAR();
        rd_frags<C, WF>(a0, lds, lds, a, 0); rd_frags<C, WF>(a1, lds, lds, a, 1);
    }
    template <class H> __device__ __forceinline__ void run(const Src<C>& s, f32x16 (&acc)[WF::NT][WF::MT], H& hook DBG_PARAMS) {
        using W = WF; NoFill fill; using F = NoFill;
        int sl = 0, kn = NS;
        __builtin_amdgcn_s_waitcnt(0xC07F);
#define KS_PERIOD(T, C0, C1, N0, N1) do { \
            TRACE(T); hook.at(T); \
            asm volatile("s_waitcnt lgkmcnt(0)" ::: "memory"); \
            if constexpr (W::LD) asm volatile("s_waitcnt vmcnt(%0)" ::"n"((NS - 2) * NV) : "memory"); \
            GM_BAR(); GM_SB(); \
            const int ns = (sl + 1 == NS) ? 0 : sl + 1, kc = kn < nt ? kn : nt - 1; \
            const char* xb = s.x + (unsigned)kc * s.xstep; const char* wb = w_tile<C>(s, kc); \
            const unsigned dx = lx0 + sl * C::XB, dw = lw0 + sl * C::WB; \
            ldsp nx = lds + ns * C::XB, nw = lds + ns * C::WB; \
            step_slot<C, W, true, 0, NV / 2, 0, 0, F>(acc, C0, N0, nx, nw, a, 0, s, xb, wb, dx, dw, fill); \
            step_slot<C, W, true, NV / 2, NV, NM, 0, F>(acc, C1, N1, nx, nw, a, 1, s, xb, wb, dx, dw, fill); \
            sl = ns; ++kn; } while (0)
        for (int t = 0; t < nt; t += 2) { KS_PERIOD(t, a0, a1, b0, b1); if (t + 1 >= nt) break; KS_PERIOD(t + 1, b0, b1, a0, a1); }
#undef KS_PERIOD
        GM_SB();
        asm volatile("s_waitcnt vmcnt(0) lgkmcnt(0)" ::: "memory");
        GM_BAR();
    }
};
template <class W> __device__ __forceinline__ void acc_zero(f32x16 (&acc)[W::NT][W::MT]) {
#pragma unroll
    for (int t = 0; t < W::NT; ++t)
#pragma unroll
        for (int m = 0; m < W::MT; ++m) acc[t][m] = (f32x16){};
}
__device__ __forceinline__ u32x4 pair32(unsigned a0, unsigned a1, unsigned b0, unsigned b1) {
    const auto r0 = __builtin_amdgcn_permlane32_swap(a0, b0, false, false); const auto r1 = __builtin_amdgcn_permlane32_swap(a1, b1, false, false);
    return (u32x4){r0[0], r1[0], r0[1], r1[1]};
}
}

struct Params {
    const u16* ax; const u16* wqkv; const u16* wo; const u16* bx; const u16* win; const u16* wsp; const u16* bsp; const u16* wout; const u16* gw;
    u16* out; unsigned char* ws;
};
struct Ctx : Params { unsigned epv; __device__ __forceinline__ unsigned tg() const { return (unsigned)__builtin_amdgcn_readfirstlane(epv) + 1u; } };

using CfgQ = Cfg<256, 192, X_RAW, W_P384, 3, 2>;
struct Warm {
    const char* base; unsigned pitch; int lpr_log2; int cnt; unsigned scratch;
    __device__ __forceinline__ unsigned mask(int t0, int wid) const {
        if (wid < 4 || cnt <= 0) return 0u;
        return ((0x11111111u << (wid - 4)) & (cnt >= 32 ? ~0u : ((1u << cnt) - 1u))) << t0;
    }
    __device__ __forceinline__ void issue(int n, int wid, int lane) const {
        if (n >= 0 && n < cnt && wid == 4 + (n & 3)) { const int idx = n * 64 + lane; pf_lines(base, (unsigned)(idx >> lpr_log2) * pitch + (unsigned)(idx & ((1 << lpr_log2) - 1)) * 128u, scratch); }
    }
};

struct SigHook {
    static constexpr bool PRE = false; int wid, lane; Warm w0, w1; unsigned act;
    __device__ __forceinline__ void pre_x() const {}
    __device__ __forceinline__ void top(int) const {}
    __device__ __forceinline__ void at(int t) const { if ((act >> t) & 1u) { w0.issue(t - 1, wid, lane); w1.issue(t - 1, wid, lane); } }
};
template <int WN> __device__ __forceinline__ void qkv_wave(const Ctx& p, ldsp lds, int pair, int panel, int wid, int lane) {
    using C = CfgQ; using W = Wv<4, 6, WN * 6, WN ? 0x3Cu : 0u, WN == 0>;
    const int lid = wid & 3, m0 = (wid & 3) * 64;
    Src<C> s;
    src_x_raw<C>(s, p.ax + (size_t)panel * 256 * DM, DM * 2, lid, lane);
    src_w_raw<C>(s, p.wqkv + pair * 64, 3 * DM * 2, DM * 2, lid, lane);
    f32x4 acc[6][4]; acc_zero<W>(acc);
    STAMP(0, 1);
    Warm none{nullptr, 0, 0, 0, 0};
    SigHook hk{wid, lane, none, none, 0u};
    gemm_loop<C, W, Sch<12, 7>>(lds, s, DM / 64, acc, lid, m0, 0, lane, hk DBG_ARGS(0));
    STAMP(0, 2);
    const int fr = lane & 15, fq = lane >> 4;
    const int row0 = panel * 256 + m0;
    const rsrc_t rs = ws_rsrc(p.ws);
#pragma unroll
    for (int t = 0; t < 6; t += 2) {
        const int T = WN * 6 + t, kind = T >> 2, head = pair * 2 + ((T >> 1) & 1);
        if (kind < 2) {
            const unsigned base = (unsigned)(kind == 0 ? WS_Q : WS_K);
            const int c = 2 * (fq & 1) + (fq >> 1);
            const float sc = kind == 0 ? QSCALE : 1.0f;
#pragma unroll
            for (int m = 0; m < 4; ++m) { const int row = row0 + m * 16 + fr;
                st16_wt(rs, base + ((head * 64 + (row >> 5)) * 4 + c) * 512 + (row & 31) * 16, pair16(acc[t][m] * sc, acc[t + 1][m] * sc)); }
        } else {
#pragma unroll
            for (int tt = 0; tt < 2; ++tt)
#pragma unroll
                for (int m = 0; m < 4; m += 2) {
                    const f32x4 A = acc[t + tt][m], B = acc[t + tt][m + 1];
                    const unsigned a0 = pk_bf16(A[0], A[1]), a1 = pk_bf16(A[2], A[3]), b0 = pk_bf16(B[0], B[1]), b1 = pk_bf16(B[2], B[3]);
                    const auto r0 = __builtin_amdgcn_permlane32_swap(a0, b0, false, false); const auto r1 = __builtin_amdgcn_permlane32_swap(a1, b1, false, false);
                    const int kb = (row0 >> 4) + m + (fq >> 1), h = fq & 1, d = tt * 16 + fr;
                    st16_wt(rs, (unsigned)WS_V + ((head * 128 + kb) * 2 + h) * 512 + d * 16, (u32x4){r0[0], r1[0], r0[1], r1[1]});
                }
        }
    }
    asm volatile("s_waitcnt vmcnt(0)" ::: "memory");
    GM_BAR();
    if (threadIdx.x == 0) signal(p.ws, F_Q + pair * 32 + panel, p.tg());
}

using CfgZ = g32::Cfg<128, 224, X_RAW, g32::W_P448, 3>;
using CfgS = g32::Cfg<128, 224, X_RAW, g32::W_OWNT, 3>;
#define ONS 5
#define ONL 4
using CfgO = g32::Cfg<128, 128, X_OWN, g32::W_P256, ONS, ONL>;
__device__ __forceinline__ float gelu_tanh(float x) {
    const float t = x * x * (-0.044715f * 2.0f * 0.7978845608028654f * 1.4426950408889634f) + (-2.0f * 0.7978845608028654f * 1.4426950408889634f);
    return x * __builtin_amdgcn_rcpf(1.0f + __builtin_amdgcn_exp2f(x * t));
}
template <int N> __device__ __forceinline__ void gelu_pairs(unsigned* z) {
    constexpr float C1 = -0.044715f * 2.0f * 0.7978845608028654f * 1.4426950408889634f, C2 = -2.0f * 0.7978845608028654f * 1.4426950408889634f;
    f32x2 x[N], e[N];
#pragma unroll
    for (int i = 0; i < N; ++i) x[i] = (f32x2){bf_lo(z[i]), bf_hi(z[i])};
#pragma unroll
    for (int i = 0; i < N; ++i) e[i] = x[i] * (x[i] * x[i] * C1 + C2);
    GM_SB();
#pragma unroll
    for (int i = 0; i < N; ++i) { e[i].x = __builtin_amdgcn_exp2f(e[i].x); e[i].y = __builtin_amdgcn_exp2f(e[i].y); }
    GM_SB();
#pragma unroll
    for (int i = 0; i < N; ++i) e[i] = e[i] + 1.0f;
#pragma unroll
    for (int i = 0; i < N; ++i) { e[i].x = __builtin_amdgcn_rcpf(e[i].x); e[i].y = __builtin_amdgcn_rcpf(e[i].y); }
    GM_SB();
#pragma unroll
    for (int i = 0; i < N; ++i) { const f32x2 y = x[i] * e[i]; z[i] = pk_bf16(y.x, y.y); }
    GM_SB();
}
__device__ __forceinline__ void gelu16(f32x16& a) {
    constexpr float C1 = -0.044715f * 2.0f * 0.7978845608028654f * 1.4426950408889634f, C2 = -2.0f * 0.7978845608028654f * 1.4426950408889634f;
    f32x16 e = a * (a * a * C1 + C2);
    GM_SB();
#pragma unroll
    for (int i = 0; i < 16; ++i) e[i] = __builtin_amdgcn_exp2f(e[i]);
    GM_SB();
    e = e + 1.0f;
#pragma unroll
    for (int i = 0; i < 16; ++i) e[i] = __builtin_amdgcn_rcpf(e[i]);
    GM_SB();
    a = a * e;
}
__device__ __forceinline__ float add1(float a, float b) { float r; asm("v_add_f32_e32 %0, %1, %2" : "=v"(r) : "v"(a), "v"(b)); return r; }
__device__ __forceinline__ float fma1(float a, float b, float c) { float r; asm("v_fma_f32 %0, %1, %2, %3" : "=v"(r) : "v"(a), "v"(b), "v"(c)); return r; }
#define GM_ROR16(x, N) do { asm volatile("s_nop 1" ::: "memory"); _Pragma("unroll") for (int k_ = 0; k_ < 16; ++k_) \
        asm volatile("v_add_f32_dpp %0, %0, %0 row_ror:" #N " row_mask:0xf bank_mask:0xf" : "+v"(x[k_])); } while (0)
template <int CTRL> __device__ __forceinline__ float dpp_add(float x) { return x + __int_as_float(__builtin_amdgcn_update_dpp(0, __float_as_int(x), CTRL, 0xf, 0xf, true)); }
__device__ __forceinline__ float red32_pair(float a, float b) {
    const auto r = __builtin_amdgcn_permlane16_swap(__float_as_uint(a), __float_as_uint(b), false, false);
    float x = __uint_as_float(r[0]) + __uint_as_float(r[1]);
    x = dpp_add<0x128>(x); x = dpp_add<0x124>(x); x = dpp_add<0x122>(x); x = dpp_add<0x121>(x);
    return x;
}
constexpr int LDS_TAB = CfgZ::LDS;
template <int NT> struct GeluFill {
    static constexpr int NSLOT = 4 * NT, STRIDE = NSLOT / 4;
    unsigned (&z)[NT][8];
    f32x2 x, e;
    template <int S> __device__ __forceinline__ void chunk() {
        constexpr float C1 = -0.044715f * 2.0f * 0.7978845608028654f * 1.4426950408889634f, C2 = -2.0f * 0.7978845608028654f * 1.4426950408889634f;
        if constexpr (S % STRIDE == 0) { constexpr int pa = S / STRIDE;
            x = (f32x2){bf_lo(z[0][pa]), bf_hi(z[0][pa])};
            const f32x2 w = x * (x * x * C1 + C2);
            e.x = __builtin_amdgcn_exp2f(w.x); e.y = __builtin_amdgcn_exp2f(w.y);
#pragma unroll
            for (int g = 0; g + 1 < 2 * NT; ++g) z[g >> 1][(g & 1) * 4 + pa] = z[(g + 1) >> 1][((g + 1) & 1) * 4 + pa];
            asm volatile("" : "+v"(x), "+v"(e));
        }
        if constexpr (S % STRIDE == 2) { constexpr int pb = S / STRIDE;
            const f32x2 d = e + 1.0f;
            const f32x2 y = x * (f32x2){__builtin_amdgcn_rcpf(d.x), __builtin_amdgcn_rcpf(d.y)};
            z[NT - 1][4 + pb] = pk_bf16(y.x, y.y);
            asm volatile("" : "+v"(z[NT - 1][4 + pb]));
        }
    }
};
template <int NT, bool LD> struct LnHook {
    const Ctx& p; ldsp lds; int panel, j, wid, lane, n0; unsigned (&z2)[NT][8];
    u32x4 w[4];
    __device__ __forceinline__ void normalise(int tt) {
        LAS f32x2* R = (LAS f32x2*)(lds + LDS_TAB + 2048);
        const int r = lane & 31, h = lane >> 5, m0 = (wid & 3) * 32;
        const rsrc_t rs = ws_rsrc(p.ws);
        const int srow = panel * 128 + m0;
#pragma unroll
        for (int i = 0; i < 4; i += 2) {
            const f32x4 ra = *(const LAS f32x4*)(R + m0 + 8 * i + 4 * h), rb = *(const LAS f32x4*)(R + m0 + 8 * i + 4 * h + 2);
            const f32x4 rc = *(const LAS f32x4*)(R + m0 + 8 * i + 8 + 4 * h), rd = *(const LAS f32x4*)(R + m0 + 8 * i + 8 + 4 * h + 2);
            const int k = srow + 8 * (i + h);
            const unsigned base = (unsigned)WS_Z + ((j * 32 + (k >> 6)) * 8 + ((k & 63) >> 3)) * 3584 + (n0 * 32 + r) * 16;
            const unsigned a0 = z2[tt][2 * i], a1 = z2[tt][2 * i + 1], b0 = z2[tt][2 * i + 2], b1 = z2[tt][2 * i + 3];
            st16_wt(rs, base + tt * 512, g32::pair32(pk_bf16(fma1(bf_lo(a0), ra[1], ra[0]), fma1(bf_hi(a0), ra[3], ra[2])), pk_bf16(fma1(bf_lo(a1), rb[1], rb[0]), fma1(bf_hi(a1), rb[3], rb[2])),
                                                      pk_bf16(fma1(bf_lo(b0), rc[1], rc[0]), fma1(bf_hi(b0), rc[3], rc[2])), pk_bf16(fma1(bf_lo(b1), rd[1], rd[0]), fma1(bf_hi(b1), rd[3], rd[2]))));
        }
    }
    __device__ __forceinline__ void normalise_at(int t) {
        if (t >= TF + 1 && t < TF + 5) { if (at_tile(t, TF + 1)) normalise(0); if (at_tile(t, TF + 2)) normalise(1); if (at_tile(t, TF + 3)) normalise(2); if constexpr (NT == 4) { if (at_tile(t, TF + 4)) normalise(3); } }
    }
    __device__ __forceinline__ void fetch() {
        const unsigned off = (unsigned)WS_STAT + ((panel * 8 + (lane >> 5) * 4) * 128 + (wid - 4) * 32 + (lane & 31)) * 16;
#pragma unroll
        for (int v = 0; v < 4; ++v) w[v] = ld16_dev(ws_rsrc(p.ws), off + v * 2048);
    }
    __device__ __forceinline__ bool fetched() const { bool ok = true;
#pragma unroll
        for (int v = 0; v < 4; ++v) ok = ok && w[v][1] == p.tg() && w[v][3] == p.tg();
        return __all(ok); }
    __device__ __forceinline__ void top(int t) {
        if constexpr (LD) normalise_at(t);
        else { if (t == TF) fetch(); }
    }
    __device__ __forceinline__ void at(int t) {
        if (t < TF || t > TF + 7) return;
        if constexpr (!LD) {
            if (at_tile(t, TF)) {
                for (unsigned spins = 0; !fetched();) {
                    __builtin_amdgcn_s_sleep(2); fetch();
                    if (++spins > (1u << 19)) { __hip_atomic_store((gu32*)(p.ws + WS_CTL) + C_TMO, p.tg(), RLX_AGENT); break; }
                }
                LAS f32x2* R = (LAS f32x2*)(lds + LDS_TAB + 2048);
                float ms = 0.f;
#pragma unroll
                for (int v = 0; v < 4; ++v) ms += __uint_as_float(w[v][0]);
                { const auto r = __builtin_amdgcn_permlane32_swap(__float_as_uint(ms), __float_as_uint(ms), false, false); ms = __uint_as_float(r[0]) + __uint_as_float(r[1]); }
                const float mean = ms * 0.125f; float q = 0.f;
#pragma unroll
                for (int v = 0; v < 4; ++v) { const float dm = __uint_as_float(w[v][0]) - mean; q += __uint_as_float(w[v][2]) + 224.0f * dm * dm; }
                { const auto r = __builtin_amdgcn_permlane32_swap(__float_as_uint(q), __float_as_uint(q), false, false); q = __uint_as_float(r[0]) + __uint_as_float(r[1]); }
                { const float rs_ = __builtin_amdgcn_rsqf(q * (1.0f / 1792.0f) + 1e-5f); if (lane < 32) R[(wid - 4) * 32 + lane] = (f32x2){-mean * rs_, rs_}; }
                GM_VM0();
            }
            normalise_at(t);
        }
        if (at_tile(t, TF + 6)) asm volatile("s_waitcnt vmcnt(0)" ::: "memory");
        if constexpr (!LD) { if (at_tile(t, TF + 7)) { if (threadIdx.x == 256) signal(p.ws, F_Z + j * 32 + panel, p.tg()); } }
    }
};
struct SpHook {
    static constexpr bool PRE = false; const Ctx& p; ldsp lds; int panel, wid, lane;
    __device__ __forceinline__ void pre_x() const {}
    __device__ __forceinline__ void top(int) const {}
    __device__ __forceinline__ void at(int t) {
        if (t == 2 && (wid == 4 || wid == 5)) {
            poll_flags(p.ws, p.tg(), F_G, 1, true, F_G, lane);
            const int row = (wid - 4) * 64 + lane, trow = panel * 128 + row;
            ((LAS f32x2*)(lds + LDS_TAB + 2048))[row] = (f32x2){__uint_as_float((unsigned)p.bsp[trow] << 16), 1.0f - __uint_as_float(__hip_atomic_load((gu32*)(p.ws + WS_GATE) + trow, RLX_AGENT))};
            GM_VM0();
        }
    }
};
struct WaitHook {
    unsigned char* ws; unsigned tag; int wid, lane, tlate, wl, nl;
    __device__ __forceinline__ void top(int) const {}
    __device__ __forceinline__ void at(int t) const { if (t == tlate && wid == 4) wait_flags(ws, tag, wl, nl, false, 0, lane); }
};
template <class C> __device__ __forceinline__ void out_src_w(Src<C>& s, const Ctx& p, int k0, int j, int lid, int lane) {
    if (k0 < FH / 64) { g32::src_w_raw<C>(s, p.wout + (size_t)k0 * 64 * DM + j * 128, DM * 2, lid, lane); set_w2<C>(s, p.wo + j * 128, FH / 64 - k0); }
    else g32::src_w_raw<C>(s, p.wo + (size_t)(k0 - FH / 64) * 64 * DM + j * 128, DM * 2, lid, lane);
}
__device__ __forceinline__ void out_part_early_w(const Ctx& p, ldsp lds, int j, int wid, int lane) {
    using C = CfgO; using W = g32::Wv<2, 2, true>;
    static_assert(C::WOFF >= 65536, "W slots behind the attention ring");
    if (wid < 4) {
        Src<C> s; out_src_w<C>(s, p, 44 - NA, j, wid, lane);
        g32::PipeKS<C, W> pipe(lds, NA, 0, wid, 0, 0, lane);
        pipe.issue_w(s);
    }
}
template <bool LD, bool FINAL> __device__ __forceinline__ void out_part(const Ctx& p, ldsp lds, int panel, int j, int wid, int lane) {
    using C = CfgO; using W = g32::Wv<2, 2, LD>;
    const int lid = wid & 3, r = lane & 31, h = lane >> 5;
    const int kh = wid >> 2, mo = ((wid >> 1) & 1) * 64, no = (wid & 1) * 2;
    Src<C> s;
    constexpr int K0 = FINAL ? 0 : 44 - NA, NKT = FINAL ? 44 - NA : NA, NU = FH / 64 - K0 < NKT ? FH / 64 - K0 : NKT;
    src_x_own<C>(s, p.ws + WS_ACT + (size_t)panel * 352 * 2048 + K0 * C::XB, lid, lane);
    out_src_w<C>(s, p, K0, j, lid, lane);
    f32x16 acc[2][2]; g32::acc_zero<W>(acc);
    constexpr bool LATE = NU >= C::NS && NU < NKT;
    WaitHook hk{p.ws, p.tg(), wid, lane, LATE ? NU - C::NS : -1, F_O + (panel >> 2) * 32, 32};
    g32::PipeKS<C, W> pipe(lds, NKT, kh, lid, mo, no, lane);
    if (FINAL) pipe.issue_w(s);
    if (wid == 4) {
        if (NU > 0) wait_flags(p.ws, p.tg(), F_U + panel * 32, 8, false, 0, lane);
        if (NU < NKT && !LATE) wait_flags(p.ws, p.tg(), F_O + (panel >> 2) * 32, 32, false, 0, lane);
    }
    GM_BAR();
    pipe.issue_x(s);
    pipe.landed();
    STAMP(0, FINAL ? 10 : 7);
    pipe.run(s, acc, hk DBG_ARGS(4));
    STAMP(0, FINAL ? 9 : 8);
    ldsp mine = lds + wid * 8192 + lane * 16, theirs = lds + (wid ^ 4) * 8192 + lane * 16;
#pragma unroll
    for (int t = 0; t < 2; ++t)
#pragma unroll
        for (int i = 0; i < 4; ++i) { const f32x16& A = LD ? acc[t][1] : acc[t][0]; *(LAS f32x4*)(mine + (t * 4 + i) * 1024) = (f32x4){A[4 * i], A[4 * i + 1], A[4 * i + 2], A[4 * i + 3]}; }
    asm volatile("s_waitcnt lgkmcnt(0)" ::: "memory");
    if (FINAL && wid == 4) wait_flags(p.ws, p.tg(), F_P + panel * 32 + j, 1, false, 0, lane);
    GM_BAR();
    const rsrc_t rs = ws_rsrc(p.ws);
    const unsigned pb = (unsigned)WS_P + ((panel * 8 + j) * 8 + wid) * 8192 + lane * 16;
    f32x16 o[2];
    u32x4 pv[8];
    if (FINAL) {
#pragma unroll
        for (int q = 0; q < 8; ++q) pv[q] = ld16_dev(rs, pb + q * 1024);
    }
#pragma unroll
    for (int t = 0; t < 2; ++t) { o[t] = LD ? acc[t][0] : acc[t][1];
#pragma unroll
        for (int i = 0; i < 4; ++i) { const f32x4 v = *(const LAS f32x4*)(theirs + (t * 4 + i) * 1024); o[t][4 * i] += v[0]; o[t][4 * i + 1] += v[1]; o[t][4 * i + 2] += v[2]; o[t][4 * i + 3] += v[3]; } }
    if (!FINAL) {
#pragma unroll
        for (int t = 0; t < 2; ++t)
#pragma unroll
            for (int i = 0; i < 4; ++i) st16_wt(rs, pb + (t * 4 + i) * 1024, (u32x4){__float_as_uint(o[t][4 * i]), __float_as_uint(o[t][4 * i + 1]), __float_as_uint(o[t][4 * i + 2]), __float_as_uint(o[t][4 * i + 3])});
        asm volatile("s_waitcnt vmcnt(0)" ::: "memory");
        GM_BAR();
        if (threadIdx.x == 0) signal(p.ws, F_P + panel * 32 + j, p.tg());
    } else {
#pragma unroll
        for (int t = 0; t < 2; ++t)
#pragma unroll
            for (int i = 0; i < 4; ++i)
#pragma unroll
                for (int e = 0; e < 4; ++e) o[t][4 * i + e] += __uint_as_float(pv[t * 4 + i][e]);
        ldsp st = lds + 65536;
#pragma unroll
        for (int t = 0; t < 2; ++t)
#pragma unroll
            for (int i = 0; i < 4; i += 2) { const f32x16& A = o[t];
                *(LAS u32x4*)(st + (mo + kh * 32 + r) * 272 + (no + t) * 64 + (i + h) * 16) =
                    g32::pair32(pk_bf16(A[4 * i], A[4 * i + 1]), pk_bf16(A[4 * i + 2], A[4 * i + 3]), pk_bf16(A[4 * i + 4], A[4 * i + 5]), pk_bf16(A[4 * i + 6], A[4 * i + 7])); }
        asm volatile("s_waitcnt lgkmcnt(0)" ::: "memory"); GM_BAR();
#pragma unroll
        for (int i = 0; i < 4; ++i) { const int row = wid * 16 + i * 4 + (lane >> 4);
            __builtin_amdgcn_raw_buffer_store_b128(*(const LAS u32x4*)(st + row * 272 + (lane & 15) * 16), uni_rsrc(p.out, SEQ * DM * 2), (unsigned)(((panel * 128 + row) * DM + j * 128 + (lane & 15) * 8) * 2), 0, 16); }
    }
}
template <bool LD> __device__ __forceinline__ void b_wave(const Ctx& p, ldsp lds, int panel, int j, int wid, int lane) {
    constexpr int NT = LD ? 3 : 4;
    const int lid = wid & 3, m0 = lid * 32, n0 = LD ? 0 : 3;
    const int r = lane & 31, h = lane >> 5;
    unsigned z2[NT][8], z1[NT][8];
    const unsigned pfs = (unsigned)(uintptr_t)lds + LDS_PFS;
    {
        using C = CfgZ; using W = g32::Wv<1, NT, LD>; Src<C> s;
        src_x_raw<C>(s, p.ax + (size_t)panel * 128 * DM, DM * 2, lid, lane);
        g32::src_w_raw<C>(s, p.win + FH + j * 224, 2 * FH * 2, lid, lane);
        set_w2<C>(s, p.win + j * 224, DM / 64);
        g32::Pipe<C, W, 1> pipe(lds, s, 2 * DM / 64, DM / 64, lid, m0, n0, lane);
        f32x16 acc[NT][1]; g32::acc_zero<W>(acc);
        STAMP(0, 1);
        Warm wz1{(const char*)p.win + ((j * 224 * 2) & ~127) + (size_t)(panel & 3) * 256 * (2 * FH * 2), 2 * FH * 2, 2, 13, pfs};
        Warm none{nullptr, 0, 0, 0, 0};
        SigHook hk{wid, lane, wz1, none, wz1.mask(1, wid)};
        pipe.begin(s);
        pipe.template run<false>(s, 0, DM / 64, acc, hk DBG_ARGS(1));
        STAMP(0, 2);
        {
            float sm[16], sq[16];
#pragma unroll
            for (int t = 0; t < NT; ++t) {
                gelu16(acc[t][0]);
#pragma unroll
                for (int q = 0; q < 16; ++q) { const float v = acc[t][0][q]; sm[q] = t ? add1(sm[q], v) : v; sq[q] = t ? fma1(v, v, sq[q]) : v * v; }
#pragma unroll
                for (int q = 0; q < 16; q += 2) z2[t][q >> 1] = pk_bf16(acc[t][0][q], acc[t][0][q + 1]);
                acc[t][0] = (f32x16){};
            }
            float ra[16];
#pragma unroll
            for (int k = 0; k < 8; ++k) {
                const auto a = __builtin_amdgcn_permlane16_swap(__float_as_uint(sm[2 * k]), __float_as_uint(sm[2 * k + 1]), false, false); ra[k] = add1(__uint_as_float(a[0]), __uint_as_float(a[1]));
                const auto b2 = __builtin_amdgcn_permlane16_swap(__float_as_uint(sq[2 * k]), __float_as_uint(sq[2 * k + 1]), false, false); ra[8 + k] = add1(__uint_as_float(b2[0]), __uint_as_float(b2[1])); }
            GM_ROR16(ra, 8); GM_ROR16(ra, 4); GM_ROR16(ra, 2); GM_ROR16(ra, 1);
            asm volatile("s_nop 1" ::: "memory");
            float va = ra[0], vb = ra[8];
#pragma unroll
            for (int k = 1; k < 8; ++k) { const bool sel = (lane & 7) == k; va = sel ? ra[k] : va; vb = sel ? ra[8 + k] : vb; }
            LAS f32x2* P = (LAS f32x2*)(lds + LDS_TAB);
            { const int q = 2 * (lane & 7) + ((lane >> 4) & 1), row = m0 + 8 * (q >> 2) + 4 * h + (q & 3);
              if ((lane & 8) == 0) P[row * 2 + (LD ? 0 : 1)] = (f32x2){va, vb}; }
            asm volatile("s_waitcnt lgkmcnt(0)" ::: "memory"); GM_BAR();
            if constexpr (!LD) {
                if (lane < 32) {
                    const int row = lid * 32 + lane;
                    const f32x2 a = P[row * 2], b = P[row * 2 + 1];
                    const float su = a.x + b.x, mt = su * (1.0f / 224.0f), m2 = fmaxf(a.y + b.y - su * mt, 0.f);
                    st16_wt(ws_rsrc(p.ws), (unsigned)WS_STAT + ((panel * 8 + j) * 128 + row) * 16, (u32x4){__float_as_uint(mt), p.tg(), __float_as_uint(m2), p.tg()});
                }
            }
        }
        STAMP(0, 3);
        LnHook<NT, LD> hl{p, lds, panel, j, wid, lane, n0, z2};
        pipe.template run<true>(s, DM / 64, 2 * DM / 64, acc, hl DBG_ARGS(2));
        pipe.end();
        STAMP(0, 4);
#pragma unroll
        for (int t = 0; t < NT; ++t)
#pragma unroll
            for (int q = 0; q < 16; q += 2) z1[t][q >> 1] = pk_bf16(acc[t][0][q], acc[t][0][q + 1]);
    }
    {
        using C = CfgS; using W = g32::Wv<1, NT, LD>; Src<C> s;
        src_x_raw<C>(s, p.wsp + (size_t)panel * 128 * SEQ, SEQ * 2, lid, lane);
        src_w_own<C>(s, p.ws + WS_Z + (size_t)j * 32 * 28672, lid, lane);
        g32::Pipe<C, W, SPD> pipe(lds, s, SEQ / 64, SEQ / 64, lid, m0, n0, lane);
        pipe.issue_x(s);
        if (wid == 4) wait_flags(p.ws, p.tg(), F_Z + j * 32, 16, false, 0, lane);
        GM_BAR();
        pipe.issue_w(s);
        STAMP(0, 5);
#pragma unroll
        for (int t = 0; t < NT; ++t) { gelu_pairs<8>(z1[t]);
#pragma unroll
            for (int q = 0; q < 8; ++q) asm volatile("" : "+v"(z1[t][q])); }
        STAMP(0, 6);
        pipe.landed();
        f32x16 acc[NT][1]; g32::acc_zero<W>(acc);
        SpHook hk{p, lds, panel, wid, lane};
        pipe.template run<true>(s, 0, SEQ / 64, acc, hk DBG_ARGS(3));
        pipe.end();
        STAMP(0, 7);
        const rsrc_t rs = ws_rsrc(p.ws);
        const f32x2 bg = ((const LAS f32x2*)(lds + LDS_TAB + 2048))[m0 + r]; const float b = bg.x, g1 = bg.y;
        const unsigned ub = (unsigned)WS_ACT + (panel * 352 + j * 28 + n0 * 4 + h) * 2048 + (m0 + r) * 16;
#pragma unroll
        for (int t = 0; t < NT; ++t)
#pragma unroll
            for (int i = 0; i < 4; i += 2) {
                unsigned w4[4];
#pragma unroll
                for (int e = 0; e < 4; ++e) { const int q = 4 * i + 2 * e; const unsigned z = z1[t][q >> 1];
                    w4[e] = pk_bf16((acc[t][0][q] + b) * g1 * bf_lo(z), (acc[t][0][q + 1] + b) * g1 * bf_hi(z)); }
                st16_wt(rs, ub + (t * 4 + i) * 2048, g32::pair32(w4[0], w4[1], w4[2], w4[3]));
            }
        asm volatile("s_waitcnt vmcnt(0)" ::: "memory");
        GM_BAR();
        if (threadIdx.x == 256) signal(p.ws, F_U + panel * 32 + j, p.tg());
    }
    STAMP(0, 8);
    out_part<LD, true>(p, lds, panel, j, wid, lane);
}

constexpr int AT_STAGE = 16384, AT_NS = 4;
#define MFMA32(a, b, c) __builtin_amdgcn_mfma_f32_32x32x16_bf16(a, b, c, 0, 0, 0)
#define MFMA16(a, b, c) __builtin_amdgcn_mfma_f32_16x16x32_bf16(a, b, c, 0, 0, 0)
#define PIN(x) asm volatile("" : "+v"(x))
__device__ __forceinline__ void gate_rows4(const Ctx& p, int row0, int lane);
__device__ __forceinline__ void attn_wg(const Ctx& p, ldsp lds, int head, int qb, int x, int a, int oj, int gpair, int gpanel, int wid, int lane) {
    if (wid == 4) wait_flags(p.ws, p.tg(), F_Q + (head >> 1) * 32, 8, false, 0, lane);
    if (wid < 4) { gate_rows4(p, gpanel * 256 + gpair * 16 + wid * 4, lane); asm volatile("s_waitcnt vmcnt(0)" ::: "memory"); }
    GM_BAR();
    if (threadIdx.x == 0) signal(p.ws, F_G + gpair * 32 + gpanel, p.tg());
    STAMP(0, 3);
    const unsigned char* Kg = p.ws + WS_K + (size_t)head * 131072; const unsigned char* Vg = p.ws + WS_V + (size_t)head * 131072;
    const unsigned ldb = (unsigned)(uintptr_t)lds;
    const unsigned voff = wid * 1024 + lane * 16;
    const char* kb = (const char*)Kg; const char* vb = (const char*)Vg;
    auto stage = [&](int st, int slot) {
        asm volatile("s_add_u32 m0, %2, 0\n\ts_nop 0\n\tglobal_load_lds_dwordx4 %0, %1 sc1" ::"v"(voff), "s"(kb + (size_t)st * 8192), "s"(ldb + slot * AT_STAGE + wid * 1024) : "memory", "scc");
        asm volatile("s_add_u32 m0, %2, 0\n\ts_nop 0\n\tglobal_load_lds_dwordx4 %0, %1 sc1" ::"v"(voff), "s"(vb + (size_t)st * 8192), "s"(ldb + slot * AT_STAGE + 8192 + wid * 1024) : "memory", "scc");
    };
    stage(0, 0);
    const int q0 = qb * 512 + wid * 64;
    bf16x8 qf[2][2];
#pragma unroll
    for (int a = 0; a < 2; ++a)
#pragma unroll
        for (int d = 0; d < 2; ++d) qf[a][d] = __builtin_bit_cast(bf16x8, ld16_dev(ws_rsrc(p.ws), (unsigned)WS_Q + (head * 64 + (q0 >> 5) + a) * 2048 + d * 1024 + lane * 16));
    bf16x8 sel;
    { const short one = ((lane & 15) == ((lane >> 4) & 1)) ? (short)0x3f80 : (short)0;
#pragma unroll
      for (int i = 0; i < 8; ++i) sel[i] = one; }
    GM_VM0();
    GM_BAR();
    stage(1, 1); stage(2, 2);
    f32x16 o[2], negm[2]; f32x4 l[2];
    const f32x16 zero16 = {};
    bf16x8 kA[2], kB[2], vA[2], vB[2];
    ldsp lb = lds + lane * 16;
    kA[0] = *(const LAS bf16x8*)(lb); kA[1] = *(const LAS bf16x8*)(lb + 1024);
#pragma unroll
    for (int a = 0; a < 2; ++a) {
        f32x16 s = MFMA32(kA[0], qf[a][0], zero16); s = MFMA32(kA[1], qf[a][1], s);
        float m = fmaxf(fmaxf(s[0], s[1]), fmaxf(s[2], s[3]));
#pragma unroll
        for (int r = 4; r < 16; r += 2) m = fmaxf(m, fmaxf(s[r], s[r + 1]));
        const auto rr = __builtin_amdgcn_permlane32_swap(__float_as_uint(m), __float_as_uint(m), false, false);
        m = -fmaxf(__uint_as_float(rr[0]), __uint_as_float(rr[1]));
#pragma unroll
        for (int r = 0; r < 16; ++r) negm[a][r] = m;
        PIN(negm[a]);
        o[a] = zero16; l[a] = (f32x4){0.f, 0.f, 0.f, 0.f};
    }
    f32x16 S0, S1; u32x4 p0l, p0h, p1l, p1h;
    S0 = MFMA32(kA[0], qf[0][0], negm[0]); S0 = MFMA32(kA[1], qf[0][1], S0);
    p1l = (u32x4){0u, 0u, 0u, 0u}; p1h = p1l;
    vB[0] = (bf16x8){0, 0, 0, 0, 0, 0, 0, 0}; vB[1] = vB[0];
    STAMP(0, 4);
#define EX4(S, i) do { S[i] = __builtin_amdgcn_exp2f(S[i]); S[i + 1] = __builtin_amdgcn_exp2f(S[i + 1]); S[i + 2] = __builtin_amdgcn_exp2f(S[i + 2]); S[i + 3] = __builtin_amdgcn_exp2f(S[i + 3]); } while (0)
#define PK4(P, S, i) do { P[0] = pk_bf16(S[i], S[i + 1]); P[1] = pk_bf16(S[i + 2], S[i + 3]); P[2] = pk_bf16(S[i + 4], S[i + 5]); P[3] = pk_bf16(S[i + 6], S[i + 7]); } while (0)
#define BC(x) __builtin_bit_cast(bf16x8, x)
#define AORD 1
#define ASTEP(SC, SN, PCl, PCh, PPl, PPh, KF, QN, VF, AP, LOADS) do { \
        SN = MFMA32(KF[0], qf[QN][0], negm[QN]); GM_SB(); EX4(SC, 0); PIN(SC); GM_SB(); \
        SN = MFMA32(KF[1], qf[QN][1], SN); GM_SB(); LOADS; EX4(SC, 4); PIN(SC); GM_SB(); \
        o[AP] = MFMA32(VF[0], BC(PPl), o[AP]); GM_SB(); EX4(SC, 8); PIN(SC); GM_SB(); \
        o[AP] = MFMA32(VF[1], BC(PPh), o[AP]); GM_SB(); EX4(SC, 12); PIN(SC); GM_SB(); \
        l[AP] = MFMA16(sel, BC(PPl), l[AP]); GM_SB(); PK4(PCl, SC, 0); PIN(PCl); GM_SB(); \
        l[AP] = MFMA16(sel, BC(PPh), l[AP]); GM_SB(); PK4(PCh, SC, 8); PIN(PCh); GM_SB(); } while (0)
#define LDK(KF, base, kt) do { KF[0] = *(const LAS bf16x8*)((base) + (kt) * 2048); KF[1] = *(const LAS bf16x8*)((base) + (kt) * 2048 + 1024); } while (0)
#define LDV(VF, base, kt) do { VF[0] = *(const LAS bf16x8*)((base) + 8192 + (kt) * 2048); VF[1] = *(const LAS bf16x8*)((base) + 8192 + (kt) * 2048 + 1024); } while (0)
    float gval = 0.f;
    for (int st = 0; st < 16; ++st) {
        ldsp cb = lb + (st & 3) * AT_STAGE, nb = lb + ((st + 1) & 3) * AT_STAGE;
        if (st < 5) {
            const int n = st * 8 + wid; const unsigned pfs = (unsigned)(uintptr_t)lds + LDS_PFS;
            const int p0 = 8 * (x & 1) + 4 * ((x >> 1) & 1), j0 = 4 * (x >> 2);
            if (n < 16) pf_lines(p.wsp + (size_t)(p0 * 128 + a * 32) * SEQ, (unsigned)(lane & 31) * (SEQ * 2) + (unsigned)(2 * n + (lane >> 5)) * 128u, pfs);
            else if (n < 30) pf_lines(p.wout + (size_t)(a * 112) * DM + j0 * 128, (unsigned)((n - 16) * 8 + (lane >> 3)) * (DM * 2) + (unsigned)(lane & 7) * 128u, pfs);
            else if (n < 38) pf_lines(p.wo + (size_t)(a * 64) * DM + j0 * 128, (unsigned)((n - 30) * 8 + (lane >> 3)) * (DM * 2) + (unsigned)(lane & 7) * 128u, pfs);
        }
        if (st == 8) {
            poll_flags(p.ws, p.tg(), F_G, 1, true, F_G, lane);
            gval = __uint_as_float(__hip_atomic_load((gu32*)(p.ws + WS_GATE) + q0 + lane, RLX_AGENT));
        }
        ASTEP(S0, S1, p0l, p0h, p1l, p1h, kA, 1, vB, 1, { LDK(kB, cb, 1); LDV(vA, cb, 0); });
        ASTEP(S1, S0, p1l, p1h, p0l, p0h, kB, 0, vA, 0, {});
        ASTEP(S0, S1, p0l, p0h, p1l, p1h, kB, 1, vA, 1, { LDK(kA, cb, 2); LDV(vB, cb, 1); });
        ASTEP(S1, S0, p1l, p1h, p0l, p0h, kA, 0, vB, 0, {});
        ASTEP(S0, S1, p0l, p0h, p1l, p1h, kA, 1, vB, 1, { LDK(kB, cb, 3); LDV(vA, cb, 2); });
        ASTEP(S1, S0, p1l, p1h, p0l, p0h, kB, 0, vA, 0, {});
        if (st + 1 < 16) {
            if (st + 2 < 16) asm volatile("s_waitcnt vmcnt(2)" ::: "memory"); else asm volatile("s_waitcnt vmcnt(0)" ::: "memory");
            GM_BAR();
            if (st + 3 < 16) stage(st + 3, (st + 3) & 3);
            if (st == 14) out_part_early_w(p, lds, oj, wid, lane);
        }
        ASTEP(S0, S1, p0l, p0h, p1l, p1h, kB, 1, vA, 1, { LDK(kA, nb, 0); LDV(vB, cb, 3); });
        ASTEP(S1, S0, p1l, p1h, p0l, p0h, kA, 0, vB, 0, {});
    }
    o[1] = MFMA32(vB[0], BC(p1l), o[1]); o[1] = MFMA32(vB[1], BC(p1h), o[1]);
    l[1] = MFMA16(sel, BC(p1l), l[1]); l[1] = MFMA16(sel, BC(p1h), l[1]);
#undef EX4
#undef PK4
#undef ASTEP
#undef LDK
#undef LDV
    STAMP(0, 5);
    float lsum[2];
    {   bool bad = false;
#pragma unroll
        for (int a = 0; a < 2; ++a) {
            const int q = lane & 31;
            const float l0 = __shfl(l[a][0], q & 15), l1 = __shfl(l[a][1], q & 15);
            lsum[a] = (q < 16) ? l0 : l1;
            float mx = 0.f;
#pragma unroll
            for (int r = 0; r < 16; ++r) mx = fmaxf(mx, fabsf(o[a][r]));
            float sm = 0.f;
#pragma unroll
            for (int r = 0; r < 16; ++r) sm += o[a][r];
            bad = bad || !(lsum[a] < 1.2676506e30f) || !(mx < 3.0e38f) || !(sm == sm);
        }
        if (__builtin_expect(__any(bad), 0)) {
            const rsrc_t rq = ws_rsrc(p.ws);
            float mrun[2] = {-INFINITY, -INFINITY}, lrun[2] = {0.f, 0.f};
            o[0] = zero16; o[1] = zero16;
            for (int kt = 0; kt < 64; ++kt) {
                const unsigned ko = (unsigned)WS_K + head * 131072 + kt * 2048 + lane * 16, vo = (unsigned)WS_V + head * 131072 + kt * 2048 + lane * 16;
                const bf16x8 k0 = __builtin_bit_cast(bf16x8, ld16_dev(rq, ko)), k1 = __builtin_bit_cast(bf16x8, ld16_dev(rq, ko + 1024));
                const bf16x8 v0 = __builtin_bit_cast(bf16x8, ld16_dev(rq, vo)), v1 = __builtin_bit_cast(bf16x8, ld16_dev(rq, vo + 1024));
#pragma unroll
                for (int a = 0; a < 2; ++a) {
                    f32x16 s = MFMA32(k0, qf[a][0], zero16); s = MFMA32(k1, qf[a][1], s);
                    float m = s[0];
#pragma unroll
                    for (int r = 1; r < 16; ++r) m = fmaxf(m, s[r]);
                    { const auto rr = __builtin_amdgcn_permlane32_swap(__float_as_uint(m), __float_as_uint(m), false, false); m = fmaxf(__uint_as_float(rr[0]), __uint_as_float(rr[1])); }
                    const float mn = fmaxf(mrun[a], m), alpha = __builtin_amdgcn_exp2f(mrun[a] - mn);
                    mrun[a] = mn;
                    u32x4 pl, ph; float ls = 0.f;
#pragma unroll
                    for (int i = 0; i < 4; ++i) {
                        pl[i] = pk_bf16(__builtin_amdgcn_exp2f(s[2 * i] - mn), __builtin_amdgcn_exp2f(s[2 * i + 1] - mn));
                        ph[i] = pk_bf16(__builtin_amdgcn_exp2f(s[8 + 2 * i] - mn), __builtin_amdgcn_exp2f(s[9 + 2 * i] - mn));
                        ls += (bf_lo(pl[i]) + bf_hi(pl[i])) + (bf_lo(ph[i]) + bf_hi(ph[i])); }
                    lrun[a] = lrun[a] * alpha + ls;
                    o[a] = o[a] * alpha;
                    o[a] = MFMA32(v0, BC(pl), o[a]); o[a] = MFMA32(v1, BC(ph), o[a]);
                }
            }
#pragma unroll
            for (int a = 0; a < 2; ++a) { const auto rr = __builtin_amdgcn_permlane32_swap(__float_as_uint(lrun[a]), __float_as_uint(lrun[a]), false, false); lsum[a] = __uint_as_float(rr[0]) + __uint_as_float(rr[1]); }
        }
    }
    const rsrc_t rs = ws_rsrc(p.ws);
    const float gt0 = __shfl(gval, lane & 31), gt1 = __shfl(gval, 32 + (lane & 31));
#pragma unroll
    for (int a = 0; a < 2; ++a) {
        const int q = lane & 31, hh = lane >> 5;
        const float sc = (a ? gt1 : gt0) * __builtin_amdgcn_rcpf(lsum[a]);
        const int row = q0 + a * 32 + q, panel = row >> 7;
#pragma unroll
        for (int i = 0; i < 4; i += 2) {
            const unsigned a0 = pk_bf16(o[a][4 * i] * sc, o[a][4 * i + 1] * sc), a1 = pk_bf16(o[a][4 * i + 2] * sc, o[a][4 * i + 3] * sc);
            const unsigned b0 = pk_bf16(o[a][4 * i + 4] * sc, o[a][4 * i + 5] * sc), b1 = pk_bf16(o[a][4 * i + 6] * sc, o[a][4 * i + 7] * sc);
            const auto r0 = __builtin_amdgcn_permlane32_swap(a0, b0, false, false); const auto r1 = __builtin_amdgcn_permlane32_swap(a1, b1, false, false);
            const int c = 224 + head * 4 + i + hh;
            st16_wt(rs, (unsigned)WS_ACT + (panel * 352 + c) * 2048 + (row & 127) * 16, (u32x4){r0[0], r1[0], r0[1], r1[1]});
        }
    }
    asm volatile("s_waitcnt vmcnt(0)" ::: "memory");
    GM_BAR();
    if (threadIdx.x == 0) signal(p.ws, F_O + qb * 32 + head, p.tg());
}

__device__ __forceinline__ void gate_rows4(const Ctx& p, int row0, int lane) {
    u32x4 g[2], xr[4][2];
#pragma unroll
    for (int i = 0; i < 2; ++i) g[i] = ((const u32x4*)p.gw)[lane * 2 + i];
#pragma unroll
    for (int r = 0; r < 4; ++r)
#pragma unroll
        for (int i = 0; i < 2; ++i) xr[r][i] = ((const u32x4*)(p.ax + (size_t)(row0 + r) * DM))[lane * 2 + i];
    float s[4];
#pragma unroll
    for (int r = 0; r < 4; ++r) { float a = 0.f, b = 0.f;
#pragma unroll
        for (int i = 0; i < 2; ++i)
#pragma unroll
            for (int e = 0; e < 4; ++e) { a += bf_lo(xr[r][i][e]) * bf_lo(g[i][e]); b += bf_hi(xr[r][i][e]) * bf_hi(g[i][e]); }
        s[r] = a + b; }
#pragma unroll
    for (int r = 0; r < 4; ++r) s[r] = dpp_add<0x128>(s[r]);
#pragma unroll
    for (int r = 0; r < 4; ++r) s[r] = dpp_add<0x124>(s[r]);
#pragma unroll
    for (int r = 0; r < 4; ++r) s[r] = dpp_add<0x122>(s[r]);
#pragma unroll
    for (int r = 0; r < 4; ++r) s[r] = dpp_add<0x121>(s[r]);
#pragma unroll
    for (int r = 0; r < 4; ++r) {
        const auto a = __builtin_amdgcn_permlane16_swap(__float_as_uint(s[r]), __float_as_uint(s[r]), false, false); s[r] = __uint_as_float(a[0]) + __uint_as_float(a[1]);
        const auto b = __builtin_amdgcn_permlane32_swap(__float_as_uint(s[r]), __float_as_uint(s[r]), false, false); s[r] = __uint_as_float(b[0]) + __uint_as_float(b[1]); }
    const float v = lane == 0 ? s[0] : lane == 1 ? s[1] : lane == 2 ? s[2] : s[3];
    if (lane < 4) __hip_atomic_store((gu32*)(p.ws + WS_GATE) + row0 + lane, __float_as_uint(__builtin_amdgcn_rcpf(1.0f + __builtin_amdgcn_exp2f(-1.4426950408889634f * v))), RLX_AGENT);
}
constexpr int LDS_BYTES = CfgO::LDS > CfgQ::LDS + 256 ? CfgO::LDS : CfgQ::LDS + 256;
static_assert(LDS_TAB + 3072 <= LDS_BYTES && CfgO::LDS <= LDS_BYTES && AT_NS * AT_STAGE <= LDS_BYTES, "LDS map");
__global__ __launch_bounds__(512, 2) void fused(Params pp) {
    extern __shared__ __attribute__((aligned(16))) unsigned char smem[]; ldsp lds = (ldsp)smem;
    const int lane = threadIdx.x & 63, wid = __builtin_amdgcn_readfirstlane(threadIdx.x >> 6);
    const int b = blockIdx.x, x = b & 7, role = (b >> 3) & 1, idx = b >> 4;
    Ctx p; static_cast<Params&>(p) = pp;
    p.epv = __hip_atomic_load((gu32*)(pp.ws + WS_CTL) + C_EPOCH, RLX_AGENT);
    STAMP(0, 0);
    if (role == 0) {
        const int pair = 4 * (x >> 1) + (idx >> 2), panel = 4 * (x & 1) + (idx & 3);
        if (wid < 4) qkv_wave<0>(p, lds, pair, panel, wid, lane); else qkv_wave<1>(p, lds, pair, panel, wid, lane);
        STAMP(0, 6);
        const int opanel = 8 * (x & 1) + 4 * ((x >> 1) & 1) + (idx >> 2), oj = 4 * (x >> 2) + (idx & 3);
        attn_wg(p, lds, 4 * x + (idx >> 2), idx & 3, x, idx, oj, pair, panel, wid, lane);
        STAMP(0, 9);
        {
            if (wid < 4) out_part<true, false>(p, lds, opanel, oj, wid, lane); else out_part<false, false>(p, lds, opanel, oj, wid, lane); }
    } else {
        const int panel = 8 * (x & 1) + 4 * ((x >> 1) & 1) + (idx >> 2), j = 4 * (x >> 2) + (idx & 3);
        if (wid < 4) b_wave<true>(p, lds, panel, j, wid, lane); else b_wave<false>(p, lds, panel, j, wid, lane);
        if (panel == 0 && j == 0 && threadIdx.x == 0) __hip_atomic_store((gu32*)(pp.ws + WS_CTL) + C_EPOCH, p.tg(), RLX_AGENT);
    }
    STAMP(0, 15);
}
}

extern "C" void kernel_launch(void* const* d_in, const int* in_sizes, int n_in, void* d_out, int out_size, void* d_ws, size_t ws_size, hipStream_t stream) {
    using namespace gm;
    Params p;
    p.ax = (const u16*)d_in[0]; p.wqkv = (const u16*)d_in[1]; p.wo = (const u16*)d_in[2]; p.bx = (const u16*)d_in[3]; p.win = (const u16*)d_in[4];
    p.wsp = (const u16*)d_in[5]; p.bsp = (const u16*)d_in[6]; p.wout = (const u16*)d_in[7]; p.gw = (const u16*)d_in[8];
    p.out = (u16*)d_out; p.ws = (unsigned char*)d_ws;
    (void)hipFuncSetAttribute((const void*)fused, hipFuncAttributeMaxDynamicSharedMemorySize, LDS_BYTES);
    fused<<<256, 512, LDS_BYTES, stream>>>(p);
}
```

```cpp
#include <hip/hip_runtime.h>
#include <hip/hip_bf16.h>
#include <stdint.h>

#define LAS __attribute__((address_space(3)))
typedef short bf16x8 __attribute__((ext_vector_type(8)));
typedef short s16x4 __attribute__((ext_vector_type(4)));
typedef float f32x2 __attribute__((ext_vector_type(2)));
typedef float f32x4 __attribute__((ext_vector_type(4)));
typedef float f32x16 __attribute__((ext_vector_type(16)));
typedef unsigned u32x4 __attribute__((ext_vector_type(4)));
typedef unsigned u32x2 __attribute__((ext_vector_type(2)));
typedef LAS unsigned char* ldsp;
typedef unsigned short u16;

#define NA 16
#define TF 5
#define PFD 5
#define SPD 1
namespace gm {
constexpr int SEQ = 2048, DM = 1024, NH = 32, FH = 1792;
constexpr size_t WS_CTL = 0;
constexpr size_t CTL_BYTES = 16384;
constexpr size_t WS_GATE = 16384;
constexpr size_t WS_STAT = 32768;
constexpr size_t WS_Q = 1u << 20;
constexpr size_t WS_K = WS_Q + (4u << 20);
constexpr size_t WS_V = WS_K + (4u << 20);
constexpr size_t WS_Z = WS_V + (4u << 20);
constexpr size_t WS_ACT = WS_Z + (8u << 20);
constexpr int LDS_PFS = 147456 - 0;
constexpr size_t WS_P = WS_ACT + (12u << 20);
constexpr float QSCALE = 0.17677669529663687f * 1.4426950408889634f;

#define TRACE(t) do {} while (0)
#define DBG_PARAMS
#define DBG_ARGS(id)
#define DBG_FWD
#define STAMP(k, i) do {} while (0)
constexpr int C_EPOCH = 0, C_TMO = 64, F_Q = 128  , F_S = F_Q + 512  , F_Z = F_S + 512  ,
              F_U = F_Z + 256  , F_O = F_U + 512  , F_P = F_O + 128  , F_G = F_P + 512  , C_END = F_G + 512;
static_assert(C_END * 4 <= (int)CTL_BYTES, "control block");
#define GM_VM0() __builtin_amdgcn_s_waitcnt(0x0F70)
#define RLX_AGENT __ATOMIC_RELAXED, __HIP_MEMORY_SCOPE_AGENT
typedef __attribute__((address_space(1))) unsigned gu32;
typedef __attribute__((address_space(1))) unsigned long long gu64;
typedef __amdgpu_buffer_rsrc_t rsrc_t;
__device__ __forceinline__ rsrc_t uni_rsrc(const void* base, unsigned bytes) {
    const unsigned long long a = (unsigned long long)(uintptr_t)base;
    const unsigned lo = __builtin_amdgcn_readfirstlane((unsigned)a), hi = __builtin_amdgcn_readfirstlane((unsigned)(a >> 32));
    return __builtin_amdgcn_make_buffer_rsrc((void*)(uintptr_t)(((unsigned long long)hi << 32) | lo), 0, bytes, 0x00020000);
}
__device__ __forceinline__ rsrc_t ws_rsrc(unsigned char* ws) { return uni_rsrc(ws, 64u << 20); }
__device__ __forceinline__ void st16_wt(rsrc_t r, unsigned off, u32x4 v) { __builtin_amdgcn_raw_buffer_store_b128(v, r, off, 0, 16); }
__device__ __forceinline__ void signal(unsigned char* ws, int word, unsigned tag) { __hip_atomic_store((gu32*)(ws + WS_CTL) + word, tag, RLX_AGENT); }
__device__ __forceinline__ void poll_flags(unsigned char* ws, unsigned tag, int w0, int n, bool all128, int wq, int lane) {
    gu32* ctl = (gu32*)(ws + WS_CTL);
    for (unsigned spins = 0;;) {
        bool ok = __hip_atomic_load(ctl + w0 + (lane < n ? lane : 0), RLX_AGENT) == tag;
        if (all128) { const unsigned q0 = __hip_atomic_load(ctl + wq + (lane >> 3) * 32 + (lane & 7), RLX_AGENT), q1 = __hip_atomic_load(ctl + wq + 256 + (lane >> 3) * 32 + (lane & 7), RLX_AGENT); ok = ok && q0 == tag && q1 == tag; }
        if (__all(ok)) break;
        __builtin_amdgcn_s_sleep(2);
        if (++spins > (1u << 19)) { __hip_atomic_store(ctl + C_TMO, tag, RLX_AGENT); break; }
        if ((spins & 255u) == 0u && __builtin_amdgcn_readfirstlane(__hip_atomic_load(ctl + C_TMO, RLX_AGENT)) == tag) break;
    }
}
__device__ __forceinline__ void wait_flags(unsigned char* ws, unsigned tag, int w0, int n, bool all128, int wq, int lane) {
    poll_flags(ws, tag, w0, n, all128, wq, lane);
    GM_VM0();
}
__device__ __forceinline__ u32x4 ld16_dev(rsrc_t r, unsigned off) { return __builtin_amdgcn_raw_buffer_load_b128(r, off, 0, 16); }
enum { X_RAW = 0, X_OWN = 1 };
enum { W_P384 = 0, W_P448 = 1, W_P256 = 2, W_OWN = 3 };

__device__ __forceinline__ unsigned pk_bf16(float lo, float hi) {
    typedef __bf16 bf16x2_t __attribute__((ext_vector_type(2)));
    f32x2 v = {lo, hi}; bf16x2_t b = __builtin_convertvector(v, bf16x2_t); return __builtin_bit_cast(unsigned, b);
}
__device__ __forceinline__ float bf_lo(unsigned w) { return __uint_as_float(w << 16); }
__device__ __forceinline__ float bf_hi(unsigned w) { return __uint_as_float(w & 0xffff0000u); }
__device__ __forceinline__ s16x4 tr_read(ldsp p) { typedef short v4s __attribute__((ext_vector_type(4))); return __builtin_amdgcn_ds_read_tr16_b64_v4i16((LAS v4s*)p); }
template <int I, bool SC = false> __device__ __forceinline__ void glds_run(const char* sbase, unsigned voff) {
    if constexpr (SC) asm volatile("global_load_lds_dwordx4 %0, %1 offset:%2 sc1" ::"v"(voff), "s"(sbase), "n"((I & 3) * 1024) : "memory");
    else asm volatile("global_load_lds_dwordx4 %0, %1 offset:%2" ::"v"(voff), "s"(sbase), "n"((I & 3) * 1024) : "memory");
}
template <int I, bool SC = false> __device__ __forceinline__ void glds_set(const char* sbase, unsigned voff, unsigned dst) {
    if constexpr (SC) asm volatile("s_add_u32 m0, %2, %3\n\ts_nop 0\n\tglobal_load_lds_dwordx4 %0, %1 offset:%4 sc1" ::"v"(voff), "s"(sbase), "s"(dst), "n"((I & ~3) * 1024), "n"((I & 3) * 1024) : "memory", "scc");
    else asm volatile("s_add_u32 m0, %2, %3\n\ts_nop 0\n\tglobal_load_lds_dwordx4 %0, %1 offset:%4" ::"v"(voff), "s"(sbase), "s"(dst), "n"((I & ~3) * 1024), "n"((I & 3) * 1024) : "memory", "scc");
}
__device__ __forceinline__ void pf_lines(const void* base, unsigned voff, unsigned ldsdst) {
    asm volatile("s_mov_b32 m0, %2\n\ts_nop 0\n\tglobal_load_lds_dword %0, %1" ::"v"(voff), "s"(base), "s"(ldsdst) : "memory");
}
#define GM_BAR() do { asm volatile("" ::: "memory"); __builtin_amdgcn_s_barrier(); asm volatile("" ::: "memory"); } while (0)
#define GM_SB() __builtin_amdgcn_sched_barrier(0)
__device__ __forceinline__ bool at_tile(int t, int k) { asm volatile("" : "+s"(t)); return t == k; }

template <int BM_, int BN_, int XI_, int WI_, int NSX_, int NSW_> struct Cfg {
    static constexpr int BM = BM_, BN = BN_, XI = XI_, WI = WI_, NSX = NSX_, NSW = NSW_;
    static constexpr int XB = BM * 128, WB = BN * 128, WOFF = NSX * XB, LDS = NSX * XB + NSW * WB;
    static constexpr int PX = BM / 32, PW = BN / 32;
    static constexpr int WPITCH = (WI == W_P384) ? 384 : (WI == W_P448) ? 448 : (WI == W_P256) ? 256 : 0;
    static constexpr bool XSC = false, WSC = false;
};
template <class C> struct Src { const char* x; const char* w; const char* w2; int ksplit; unsigned xstep, wstep; unsigned xv[C::PX]; unsigned wv[C::PW]; };
template <int WI> __device__ __forceinline__ int w_swz(int r, int x) {
    if (WI == W_P384) return x ^ (((((r >> 1) & 1) | (((r >> 3) & 1) << 1))) << 5);
    if (WI == W_P448) return x ^ (((r >> 3) & 1) << 5);
    if (WI == W_P256) return x ^ (((r & 3) | (((r >> 3) & 1) << 2)) << 5);
    return x;
}
template <class C> __device__ __forceinline__ const char* w_tile(const Src<C>& s, int kt) { return (kt < s.ksplit ? s.w : s.w2) + (unsigned)kt * s.wstep; }
template <class C> __device__ __forceinline__ void set_w2(Src<C>& s, const void* base, int ksplit) { s.ksplit = ksplit; s.w2 = (const char*)base - 4096 - (size_t)ksplit * s.wstep; }
template <class C> __device__ __forceinline__ void src_x_raw(Src<C>& s, const void* base, unsigned pitch, int lid, int lane) {
    s.x = (const char*)base - 4096; s.xstep = 128;
#pragma unroll
    for (int i = 0; i < C::PX; ++i) { const int row = 8 * (lid * C::PX + i) + (lane >> 3); s.xv[i] = row * pitch + (((lane & 7) ^ ((row >> 1) & 7)) * 16) + 4096 - (i & 3) * 1024; }
}
template <class C> __device__ __forceinline__ void src_x_own(Src<C>& s, const void* base, int lid, int lane) {
    s.x = (const char*)base - 4096; s.xstep = C::XB;
#pragma unroll
    for (int i = 0; i < C::PX; ++i) s.xv[i] = (lid * C::PX + i) * 1024 + lane * 16 + 4096 - (i & 3) * 1024;
}
template <class C> __device__ __forceinline__ void src_w_own(Src<C>& s, const void* base, int lid, int lane) {
    s.w = (const char*)base - 4096; s.w2 = s.w; s.ksplit = 1 << 20; s.wstep = C::WB;
#pragma unroll
    for (int i = 0; i < C::PW; ++i) s.wv[i] = (lid * C::PW + i) * 1024 + lane * 16 + 4096 - (i & 3) * 1024;
}
template <class C> __device__ __forceinline__ void src_w_raw(Src<C>& s, const void* base, unsigned pitch, unsigned segstride, int lid, int lane) {
    s.w = (const char*)base - 4096; s.w2 = s.w; s.ksplit = 1 << 20; s.wstep = 64 * pitch;
#pragma unroll
    for (int i = 0; i < C::PW; ++i) { const int o = 1024 * (lid * C::PW + i) + 16 * lane; const int r = o / C::WPITCH, x = w_swz<C::WI>(r, o % C::WPITCH);
        s.wv[i] = r * pitch + (x >> 7) * segstride + (x & 127) + 4096 - (i & 3) * 1024; }
}
template <class C, int I0, int I1, bool FIRST> __device__ __forceinline__ void stage_rng(unsigned dx, unsigned dw, const Src<C>& s, const char* xb, const char* wb) {
    if constexpr (I0 < I1) {
        if constexpr (I0 < C::PW) { if constexpr (FIRST || (I0 & 3) == 0) glds_set<I0, C::WSC>(wb, s.wv[I0], dw); else glds_run<I0, C::WSC>(wb, s.wv[I0]); }
        else { constexpr int j = I0 - C::PW; if constexpr (FIRST || (j & 3) == 0) glds_set<j, C::XSC>(xb, s.xv[j], dx); else glds_run<j, C::XSC>(xb, s.xv[j]); }
        stage_rng<C, I0 + 1, I1, false>(dx, dw, s, xb, wb);
    }
}
template <int MT_, int NT_, int N0_, unsigned SWAP_, bool LD_> struct Wv { static constexpr int MT = MT_, NT = NT_, N0 = N0_; static constexpr unsigned SWAP = SWAP_; static constexpr bool LD = LD_; };
template <class W> struct Frag { bf16x8 x[W::MT]; s16x4 wl[W::NT], wh[W::NT]; };
template <class C, class W> struct RdAddr { unsigned x[2]; unsigned w[(C::WI == W_P384) ? 4 : (C::WI == W_P256) ? W::NT : 2]; };
template <class C, class W> __device__ __forceinline__ void rd_init(RdAddr<C, W>& a, int m0, int n0, int lane) {
    const int fr = lane & 15, fq = lane >> 4, g = lane >> 4, q = (lane & 15) >> 2, p = lane & 3;
    if (C::XI == X_RAW) { for (int kk = 0; kk < 2; ++kk) a.x[kk] = (m0 + fr) * 128 + (((kk * 4 + fq) ^ (fr >> 1)) * 16); }
    else { for (int kk = 0; kk < 2; ++kk) a.x[kk] = (kk * 4 + fq) * (C::BM * 16) + (m0 + fr) * 16; }
    if (C::WI == W_P384) { const int s = ((q >> 1) & 1) | ((g & 1) << 1);
        for (int c = 0; c < 4; ++c) a.w[c] = C::WOFF + (8 * g + q) * 384 + ((c ^ s) * 32) + p * 8; }
    else if (C::WI == W_P448) { const int s = g & 1;
        for (int c = 0; c < 2; ++c) a.w[c] = C::WOFF + (8 * g + q) * 448 + (((c ^ s) - c) * 32) + p * 8; }
    else if (C::WI == W_P256) { const int s = (q & 3) | ((g & 1) << 2);
        for (int t = 0; t < W::NT; ++t) a.w[t] = C::WOFF + (8 * g + q) * 256 + (((n0 + t) ^ s) * 32) + p * 8; }
    else { for (int h = 0; h < 2; ++h) a.w[h] = C::WOFF + (p >> 1) * 1024 + (8 * g + 4 * (h ^ (p >> 1)) + q) * 16 + (p & 1) * 8 + W::N0 * 2048; }
}
template <class C, class W> __device__ __forceinline__ s16x4 rd_w(ldsp b, const RdAddr<C, W>& a, int t, int kk, int h) {
    const int c = W::N0 + t;
    if (C::WI == W_P384) return tr_read(b + a.w[c & 3] + (c >> 2) * 128 + kk * 32 * 384 + h * 4 * 384);
    else if (C::WI == W_P448) return tr_read(b + a.w[c & 1] + c * 32 + kk * 32 * 448 + h * 4 * 448);
    else if (C::WI == W_P256) return tr_read(b + a.w[t] + kk * 32 * 256 + h * 4 * 256);
    else return tr_read(b + a.w[h] + t * 2048 + kk * 512);
}
template <class C, class W> __device__ __forceinline__ void rd_one(Frag<W>& f, ldsp bx, ldsp bw, const RdAddr<C, W>& a, int kk, int r) {
    if (r < 2) { if (r == 0) f.wl[0] = rd_w<C, W>(bw, a, 0, kk, 0); else f.wh[0] = rd_w<C, W>(bw, a, 0, kk, 1); }
    else if (r < 2 + W::MT) { const int m = r - 2; f.x[m] = *(const LAS bf16x8*)(bx + a.x[kk] + m * ((C::XI == X_RAW) ? 2048 : 256)); }
    else { const int u = r - W::MT; const int t = u >> 1; if (u & 1) f.wh[t] = rd_w<C, W>(bw, a, t, kk, 1); else f.wl[t] = rd_w<C, W>(bw, a, t, kk, 0); }
}
template <class W> __device__ __forceinline__ void mma_one(f32x4 (&acc)[W::NT][W::MT], const Frag<W>& f, int i) {
    const int t = i / W::MT, m = i % W::MT;
    const bf16x8 w = __builtin_shufflevector(f.wl[t], f.wh[t], 0, 1, 2, 3, 4, 5, 6, 7);
    acc[t][m] = ((W::SWAP >> t) & 1) ? __builtin_amdgcn_mfma_f32_16x16x32_bf16(f.x[m], w, acc[t][m], 0, 0, 0)
                                     : __builtin_amdgcn_mfma_f32_16x16x32_bf16(w, f.x[m], acc[t][m], 0, 0, 0);
}
template <int RFRONT_, int VA_, int RF1_ = RFRONT_> struct Sch { static constexpr int RFRONT = RFRONT_, VA = VA_, RF1 = RF1_; };
template <class C, class W, class S, int RF, int V0, int V1, int I>
__device__ __forceinline__ void step_slot(f32x4 (&acc)[W::NT][W::MT], const Frag<W>& fc, Frag<W>& fn, ldsp bx, ldsp bw, const RdAddr<C, W>& a, int ksn,
                                          const Src<C>& s, const char* xb, const char* wb, unsigned dx, unsigned dw) {
    constexpr int NM = W::NT * W::MT, NR = 2 * W::NT + W::MT, NV = V1 - V0;
    constexpr int RS = (NM * RF / 16) < 1 ? 1 : (NM * RF / 16);
    if constexpr (I < NM) {
        mma_one<W>(acc, fc, I);
        if constexpr (I < RS) {
#pragma unroll
            for (int r = I * NR / RS; r < (I + 1) * NR / RS; ++r) rd_one<C, W>(fn, bx, bw, a, ksn, r);
        }
        if constexpr (W::LD) {
            constexpr int v0 = V0 + I * NV / NM, v1 = V0 + (I + 1) * NV / NM;
            stage_rng<C, v0, v1, v0 == V0>(dx, dw, s, xb, wb);
        }
        GM_SB();
        step_slot<C, W, S, RF, V0, V1, I + 1>(acc, fc, fn, bx, bw, a, ksn, s, xb, wb, dx, dw);
    }
}
struct NoHook { static constexpr bool PRE = false; __device__ __forceinline__ void pre_x() const {} __device__ __forceinline__ void at(int) const {} };
template <class C, class W, class S, class H>
__device__ __forceinline__ void gemm_loop(ldsp lds, const Src<C>& s, int nt, f32x4 (&acc)[W::NT][W::MT], int lid, int m0, int n0, int lane, H& hook DBG_PARAMS) {
    constexpr int PX = C::PX, PW = C::PW, NSX = C::NSX, NSW = C::NSW, NV = PX + PW;
    static_assert(S::VA >= PW && S::VA <= NV, "all W pieces go out in the step behind the barrier");
    static_assert(NSX >= NSW && NSW >= 2, "ring depths");
    RdAddr<C, W> a; rd_init<C, W>(a, m0, n0, lane);
    const unsigned ldb = (unsigned)(uintptr_t)lds;
    const unsigned lx0 = ldb + lid * PX * 1024, lw0 = ldb + C::WOFF + lid * PW * 1024;
    Frag<W> f0, f1;
    const char* wb = w_tile<C>(s, NSW - 1); const char* xb = s.x + (size_t)(NSX - 1) * s.xstep;
    unsigned dw = lw0 + (NSW - 1) * C::WB, dx = lx0 + (NSX - 1) * C::XB;
    if constexpr (H::PRE) {
        if constexpr (W::LD) {
#pragma unroll
            for (int i = 0; i < NSW; ++i) stage_rng<C, 0, PW, true>(0, lw0 + i * C::WB, s, nullptr, w_tile<C>(s, i));
        }
        hook.pre_x();
        if constexpr (W::LD) {
#pragma unroll
            for (int i = 0; i < NSX - 1; ++i) stage_rng<C, PW, NV, true>(lx0 + i * C::XB, 0, s, s.x + (size_t)i * s.xstep, nullptr);
            if constexpr (S::VA > PW) stage_rng<C, PW, S::VA, true>(dx, dw, s, xb, wb);
            asm volatile("s_waitcnt vmcnt(%0)" ::"n"(S::VA - PW) : "memory");
        }
    } else if constexpr (W::LD) {
#pragma unroll
        for (int i = 0; i < NSX - 1; ++i) {
            if (i < NSW - 1) stage_rng<C, 0, PW, true>(0, lw0 + i * C::WB, s, nullptr, w_tile<C>(s, i));
            stage_rng<C, PW, NV, true>(lx0 + i * C::XB, 0, s, s.x + (size_t)i * s.xstep, nullptr);
        }
        stage_rng<C, 0, S::VA, true>(dx, dw, s, xb, wb);
        constexpr int Y = (NSX - 2) * PX + (NSW - 2) * PW + S::VA;
        asm volatile("s_waitcnt vmcnt(%0)" ::"n"(Y) : "memory");
    }
    GM_BAR();
#pragma unroll
    for (int r = 0; r < 2 * W::NT + W::MT; ++r) rd_one<C, W>(f0, lds, lds, a, 0, r);
    int bx = 0, bw = 0, kx = NSX - 1, kw = NSW - 1;
    __builtin_amdgcn_s_waitcnt(0xC07F);
    for (int t = 0; t < nt; ++t) {
        ldsp cx = lds + bx * C::XB, cw = lds + bw * C::WB;
        const int nbx = (bx + 1 == NSX) ? 0 : bx + 1, nbw = (bw + 1 == NSW) ? 0 : bw + 1;
        ldsp nx = lds + nbx * C::XB, nw = lds + nbw * C::WB;
        TRACE(t);
        GM_SB();
#define GM_STEP(FC, FN, BX, BW, KSN, RF, V0, V1) step_slot<C, W, S, RF, V0, V1, 0>(acc, FC, FN, BX, BW, a, KSN, s, xb, wb, dx, dw)
        GM_STEP(f0, f1, cx, cw, 1, S::RFRONT, S::VA, NV);
        hook.at(t);
        asm volatile("s_waitcnt lgkmcnt(0)" ::: "memory");
        if constexpr (W::LD) {
            constexpr int pw = 1 - NSW, px = 1 - NSX, last = (pw > px) ? pw : px, nper = (-1) - last, Y = nper * NV + ((pw > px) ? PX : 0);
            asm volatile("s_waitcnt vmcnt(%0)" ::"n"(Y) : "memory");
        }
        GM_BAR();
        GM_SB();
        if constexpr (W::LD) {
            kx = (kx + 1 < nt) ? kx + 1 : kx; kw = (kw + 1 < nt) ? kw + 1 : kw;
            xb = s.x + (size_t)kx * s.xstep; wb = w_tile<C>(s, kw);
            dx = lx0 + bx * C::XB; dw = lw0 + bw * C::WB;
        }
        GM_STEP(f1, f0, nx, nw, 0, S::RF1, 0, S::VA);
#undef GM_STEP
        bx = nbx; bw = nbw;
    }
    GM_SB();
    asm volatile("s_waitcnt vmcnt(0) lgkmcnt(0)" ::: "memory");
    GM_BAR();
}
template <class W> __device__ __forceinline__ void acc_zero(f32x4 (&acc)[W::NT][W::MT]) {
#pragma unroll
    for (int t = 0; t < W::NT; ++t)
#pragma unroll
        for (int m = 0; m < W::MT; ++m) acc[t][m] = (f32x4){0.f, 0.f, 0.f, 0.f};
}
__device__ __forceinline__ u32x4 pair16(const f32x4& A, const f32x4& B) {
    const unsigned a0 = pk_bf16(A[0], A[1]), a1 = pk_bf16(A[2], A[3]), b0 = pk_bf16(B[0], B[1]), b1 = pk_bf16(B[2], B[3]);
    const auto r0 = __builtin_amdgcn_permlane16_swap(a0, b0, false, false); const auto r1 = __builtin_amdgcn_permlane16_swap(a1, b1, false, false);
    return (u32x4){r0[0], r1[0], r0[1], r1[1]};
}

namespace g32 {
enum { W_P448 = 1, W_P256 = 2, W_OWNT = 3 };
template <int BM_, int BN_, int XI_, int WI_, int NS_, int NL_ = 4> struct Cfg {
    static constexpr int BM = BM_, BN = BN_, XI = XI_, WI = WI_, NS = NS_, NL = NL_;
    static constexpr int XB = BM * 128, WB = BN * 128, WOFF = NS * XB, LDS = NS * (XB + WB);
    static constexpr int PX = BM / 8 / NL, PW = BN / 8 / NL;
    static constexpr int WPITCH = (WI == W_P448) ? 448 : (WI == W_P256) ? 256 : 16;
    static constexpr bool XSC = (XI == X_OWN), WSC = (WI == W_OWNT);
};
template <class C> __device__ __forceinline__ void src_w_raw(Src<C>& s, const void* base, unsigned pitch, int lid, int lane) {
    s.w = (const char*)base - 4096; s.w2 = s.w; s.ksplit = 1 << 20; s.wstep = 64 * pitch;
#pragma unroll
    for (int i = 0; i < C::PW; ++i) { const int o = 1024 * (lid * C::PW + i) + 16 * lane; const int r = o / C::WPITCH; int x = o % C::WPITCH;
        if (C::WI == W_P256) x ^= (r & 3) << 6;
        s.wv[i] = r * pitch + x + 4096 - (i & 3) * 1024; }
}
template <int MT_, int NT_, bool LD_> struct Wv { static constexpr int MT = MT_, NT = NT_; static constexpr bool LD = LD_; };
template <class C, class W> struct Frag { bf16x8 x[W::MT]; s16x4 wl[W::NT], wh[W::NT]; };
template <class C, class W> struct RdAddr { unsigned x[(C::XI == X_RAW) ? 4 : 1]; unsigned w[(C::WI == W_P256) ? W::NT : 1]; };
template <class C, class W> __device__ __forceinline__ void rd_init(RdAddr<C, W>& a, int m0, int n0  , int lane) {
    const int r = lane & 31, h = lane >> 5, g = lane >> 4, q = (lane & 15) >> 2, p = lane & 3;
    if (C::XI == X_RAW) { for (int ks = 0; ks < 4; ++ks) a.x[ks] = (m0 + r) * 128 + (((2 * ks + h) ^ ((r >> 1) & 7)) * 16); }
    else a.x[0] = h * (C::BM * 16) + (m0 + r) * 16;
    const int rowp = 8 * (g >> 1) + q, colp = (g & 1) * 32 + p * 8;
    if (C::WI == W_P448) a.w[0] = C::WOFF + rowp * 448 + colp + n0 * 64;
    else if (C::WI == W_P256) { for (int t = 0; t < W::NT; ++t) a.w[t] = C::WOFF + rowp * 256 + (((n0 + t) ^ q) * 64) + colp; }
    else a.w[0] = C::WOFF + h * (C::BN * 16) + (n0 * 32 + r) * 16;
}
template <class C, class W> constexpr int n_reads() { return W::MT + (C::WI == W_OWNT ? W::NT : 2 * W::NT); }
template <class C, class W> __device__ __forceinline__ void rd_one(Frag<C, W>& f, ldsp bx, ldsp bw, const RdAddr<C, W>& a, int ks, int r) {
    constexpr int PER = (C::WI == W_OWNT) ? 1 : 2;
    if (r >= PER && r < PER + W::MT) { const int m = r - PER;
        if (C::XI == X_RAW) f.x[m] = *(const LAS bf16x8*)(bx + a.x[ks] + m * 4096);
        else f.x[m] = *(const LAS bf16x8*)(bx + a.x[0] + ks * (2 * C::BM * 16) + m * 512);
        return; }
    const int u = r < PER ? r : r - W::MT, t = u / PER, hh = u % PER;
    if (C::WI == W_OWNT) { const bf16x8 v = *(const LAS bf16x8*)(bw + a.w[0] + ks * (2 * C::BN * 16) + t * 512); f.wl[t] = __builtin_shufflevector(v, v, 0, 1, 2, 3); f.wh[t] = __builtin_shufflevector(v, v, 4, 5, 6, 7); }
    else { s16x4 v;
        if (C::WI == W_P448) v = tr_read(bw + a.w[0] + t * 64 + (ks * 16 + hh * 4) * 448);
        else v = tr_read(bw + a.w[t] + (ks * 16 + hh * 4) * 256);
        if (hh) f.wh[t] = v; else f.wl[t] = v; }
}
template <class C, class W> __device__ __forceinline__ void rd_frags(Frag<C, W>& f, ldsp bx, ldsp bw, const RdAddr<C, W>& a, int ks) {
#pragma unroll
    for (int r = 0; r < n_reads<C, W>(); ++r) rd_one<C, W>(f, bx, bw, a, ks, r);
}
template <class C, class W, bool SWAP  > __device__ __forceinline__ void mma_one(f32x16 (&acc)[W::NT][W::MT], const Frag<C, W>& f, int i) {
    const int t = i / W::MT, m = i % W::MT;
    const bf16x8 w = __builtin_shufflevector(f.wl[t], f.wh[t], 0, 1, 2, 3, 4, 5, 6, 7);
    acc[t][m] = SWAP ? __builtin_amdgcn_mfma_f32_32x32x16_bf16(w, f.x[m], acc[t][m], 0, 0, 0) : __builtin_amdgcn_mfma_f32_32x32x16_bf16(f.x[m], w, acc[t][m], 0, 0, 0);
}
struct NoFill { template <int S> __device__ __forceinline__ void chunk() {} };
template <class C, class W, bool SWAP, int V0, int V1, int BASE, int I, class F>
__device__ __forceinline__ void step_slot(f32x16 (&acc)[W::NT][W::MT], const Frag<C, W>& fc, Frag<C, W>& fn, ldsp bx, ldsp bw, const RdAddr<C, W>& a, int ksn,
                                          const Src<C>& s, const char* xb, const char* wb, unsigned dx, unsigned dw, F& fill) {
    constexpr int NM = W::NT * W::MT, NR = n_reads<C, W>(), NV = V1 - V0;
    if constexpr (I < NM) {
        mma_one<C, W, SWAP>(acc, fc, I);
#pragma unroll
        for (int r = I * NR / NM; r < (I + 1) * NR / NM; ++r) rd_one<C, W>(fn, bx, bw, a, ksn, r);
        if constexpr (W::LD) { constexpr int v0 = V0 + I * NV / NM, v1 = V0 + (I + 1) * NV / NM; stage_rng<C, v0, v1, v0 == V0>(dx, dw, s, xb, wb); }
        fill.template chunk<BASE + I>();
        GM_SB();
        step_slot<C, W, SWAP, V0, V1, BASE, I + 1, F>(acc, fc, fn, bx, bw, a, ksn, s, xb, wb, dx, dw, fill);
    }
}
template <class C, class WF  , int D> struct Pipe {
    static constexpr int PX = C::PX, PW = C::PW, NS = C::NS, NV = PX + PW;
#define QSPLIT 0
    static constexpr int Q1 = QSPLIT == 0 ? (NV + 3) / 4 : QSPLIT == 1 ? (NV + 1) / 2 : PW, Q2 = QSPLIT == 0 ? (NV + 1) / 2 : QSPLIT == 1 ? NV : NV, Q3 = QSPLIT == 0 ? (3 * NV + 3) / 4 : NV;
    static constexpr int PRE = (D == 1) ? Q1 : Q2;
    ldsp lds; int nt, xmod;
    RdAddr<C, WF> a; unsigned lx0, lw0;
    Frag<C, WF> f0, f1, f2, f3;
    __device__ __forceinline__ Pipe(ldsp lds_, const Src<C>& s, int nt_, int xmod_, int lid, int m0, int n0, int lane) : lds(lds_), nt(nt_), xmod(xmod_) {
        rd_init<C, WF>(a, m0, n0, lane);
        const unsigned ldb = (unsigned)(uintptr_t)lds;
        lx0 = ldb + lid * PX * 1024; lw0 = ldb + C::WOFF + lid * PW * 1024;
    }
    __device__ __forceinline__ void issue_x(const Src<C>& s) {
        if constexpr (WF::LD) {
#pragma unroll
            for (int i = 0; i < NS - 1; ++i) stage_rng<C, PW, NV, true>(lx0 + i * C::XB, 0, s, s.x + (size_t)i * s.xstep, nullptr);
        }
    }
    __device__ __forceinline__ void issue_w(const Src<C>& s) {
        static_assert(PRE <= PW, "prologue split");
        if constexpr (WF::LD) {
#pragma unroll
            for (int i = 0; i < NS - 1; ++i) stage_rng<C, 0, PW, true>(0, lw0 + i * C::WB, s, nullptr, w_tile<C>(s, i));
            stage_rng<C, 0, PRE, true>(0, lw0 + (NS - 1) * C::WB, s, nullptr, w_tile<C>(s, NS - 1));
        }
    }
    __device__ __forceinline__ void landed() {
        if constexpr (WF::LD) asm volatile("s_waitcnt vmcnt(0)" ::: "memory");
        GM_BAR();
        rd_frags<C, WF>(f0, lds, lds, a, 0);
        if (D == 2) rd_frags<C, WF>(f1, lds, lds, a, 1);
    }
    __device__ __forceinline__ void begin(const Src<C>& s) {
        if constexpr (WF::LD) {
#pragma unroll
            for (int i = 0; i < NS - 1; ++i) stage_rng<C, 0, NV, true>(lx0 + i * C::XB, lw0 + i * C::WB, s, s.x + (size_t)i * s.xstep, w_tile<C>(s, i));
            stage_rng<C, 0, PRE, true>(0, lw0 + (NS - 1) * C::WB, s, nullptr, w_tile<C>(s, NS - 1));
            asm volatile("s_waitcnt vmcnt(%0)" ::"n"((NS - 2) * NV + PRE) : "memory");
        }
        GM_BAR();
        rd_frags<C, WF>(f0, lds, lds, a, 0);
        if (D == 2) rd_frags<C, WF>(f1, lds, lds, a, 1);
    }
    template <bool SWAP, class H> __device__ __forceinline__ void run(const Src<C>& s, int t0, int t1, f32x16 (&acc)[WF::NT][WF::MT], H& hook DBG_PARAMS) {
        NoFill nf; run_fill<SWAP>(s, t0, t1, t0, acc, hook, nf DBG_FWD);
    }
    template <bool SWAP, class H, class F> __device__ __forceinline__ void run_fill(const Src<C>& s, int t0, int t1, int tb, f32x16 (&acc)[WF::NT][WF::MT], H& hook, F& fill DBG_PARAMS) {
        using W = WF; constexpr int NM = WF::MT * WF::NT;
        int sl = t0 % NS, kn = t0 - 1 + NS; kn = kn < nt ? kn : nt - 1;
        const char* xb = s.x + (unsigned)(kn & (xmod - 1)) * s.xstep; const char* wb = w_tile<C>(s, kn);
        unsigned dx = lx0 + ((sl == 0) ? NS - 1 : sl - 1) * C::XB, dw = lw0 + ((sl == 0) ? NS - 1 : sl - 1) * C::WB;
        __builtin_amdgcn_s_waitcnt(0xC07F);
        for (int t = t0; t < t1; ++t) {
            const int ns = (sl + 1 == NS) ? 0 : sl + 1;
            ldsp cx = lds + sl * C::XB, cw = lds + sl * C::WB, nx = lds + ns * C::XB, nw = lds + ns * C::WB;
            TRACE(t - tb);
            hook.top(t - tb);
            GM_SB();
#define G32_STEP(FC, FN, BX, BW, KSN, V0, V1, ST) step_slot<C, W, SWAP, V0, V1, ST * NM, 0, F>(acc, FC, FN, BX, BW, a, KSN, s, xb, wb, dx, dw, fill)
            if constexpr (D == 1) { G32_STEP(f0, f1, cx, cw, 1, Q1, Q2, 0); G32_STEP(f1, f2, cx, cw, 2, Q2, Q3, 1); G32_STEP(f2, f3, cx, cw, 3, Q3, NV, 2); }
            else { G32_STEP(f0, f2, cx, cw, 2, Q2, Q3, 0); G32_STEP(f1, f3, cx, cw, 3, Q3, NV, 1); }
            hook.at(t - tb);
            asm volatile("s_waitcnt lgkmcnt(0)" ::: "memory");
            if constexpr (W::LD) asm volatile("s_waitcnt vmcnt(%0)" ::"n"((NS - 2) * NV) : "memory");
            GM_BAR();
            GM_SB();
            if constexpr (W::LD) { kn = (kn + 1 < nt) ? kn + 1 : kn; xb = s.x + (unsigned)(kn & (xmod - 1)) * s.xstep; wb = w_tile<C>(s, kn); dx = lx0 + sl * C::XB; dw = lw0 + sl * C::WB; }
            if constexpr (D == 1) { G32_STEP(f3, f0, nx, nw, 0, 0, Q1, 3); }
            else { G32_STEP(f2, f0, nx, nw, 0, 0, Q1, 2); G32_STEP(f3, f1, nx, nw, 1, Q1, Q2, 3); }
#undef G32_STEP
            sl = ns;
        }
        GM_SB();
    }
    __device__ __forceinline__ void end() {
        asm volatile("s_waitcnt vmcnt(0) lgkmcnt(0)" ::: "memory");
        GM_BAR();
    }
};
template <class C, class WF> struct PipeKS {
    static constexpr int PX = C::PX, PW = C::PW, NS = C::NS, NV = PX + PW, NM = WF::MT * WF::NT;
    ldsp lds; int nt, kh;
    RdAddr<C, WF> a; unsigned lx0, lw0;
    Frag<C, WF> a0, a1, b0, b1;
    __device__ __forceinline__ PipeKS(ldsp lds_, int nt_, int kh_, int lid, int m0, int n0, int lane) : lds(lds_), nt(nt_), kh(kh_) {
        rd_init<C, WF>(a, m0, n0, lane);
        static_assert(C::XI == X_OWN && C::WI == W_P256, "layouts of the output GEMM");
        a.x[0] += kh * 2 * (2 * C::BM * 16);
#pragma unroll
        for (int t = 0; t < WF::NT; ++t) a.w[t] += kh * 32 * 256;
        const unsigned ldb = (unsigned)(uintptr_t)lds;
        lx0 = ldb + lid * PX * 1024; lw0 = ldb + C::WOFF + lid * PW * 1024;
    }
    __device__ __forceinline__ void issue_w(const Src<C>& s) {
        if constexpr (WF::LD) {
#pragma unroll
            for (int i = 0; i < NS; ++i) stage_rng<C, 0, PW, true>(0, lw0 + i * C::WB, s, nullptr, w_tile<C>(s, i)); }
    }
    __device__ __forceinline__ void issue_x(const Src<C>& s) {
        if constexpr (WF::LD) {
#pragma unroll
            for (int i = 0; i < NS; ++i) stage_rng<C, PW, NV, true>(lx0 + i * C::XB, 0, s, s.x + (size_t)i * s.xstep, nullptr); }
    }
    __device__ __forceinline__ void landed() {
        if constexpr (WF::LD) asm volatile("s_waitcnt vmcnt(0)" ::: "memory");
        GM_BAR();
        rd_frags<C, WF>(a0, lds, lds, a, 0); rd_frags<C, WF>(a1, lds, lds, a, 1);
    }
    template <class H> __device__ __forceinline__ void run(const Src<C>& s, f32x16 (&acc)[WF::NT][WF::MT], H& hook DBG_PARAMS) {
        using W = WF; NoFill fill; using F = NoFill;
        int sl = 0, kn = NS;
        __builtin_amdgcn_s_waitcnt(0xC07F);
#define KS_PERIOD(T, C0, C1, N0, N1) do { \
            TRACE(T); hook.at(T); \
            asm volatile("s_waitcnt lgkmcnt(0)" ::: "memory"); \
            if constexpr (W::LD) asm volatile("s_waitcnt vmcnt(%0)" ::"n"((NS - 2) * NV) : "memory"); \
            GM_BAR(); GM_SB(); \
            const int ns = (sl + 1 == NS) ? 0 : sl + 1, kc = kn < nt ? kn : nt - 1; \
            const char* xb = s.x + (unsigned)kc * s.xstep; const char* wb = w_tile<C>(s, kc); \
            const unsigned dx = lx0 + sl * C::XB, dw = lw0 + sl * C::WB; \
            ldsp nx = lds + ns * C::XB, nw = lds + ns * C::WB; \
            step_slot<C, W, true, 0, NV / 2, 0, 0, F>(acc, C0, N0, nx, nw, a, 0, s, xb, wb, dx, dw, fill); \
            step_slot<C, W, true, NV / 2, NV, NM, 0, F>(acc, C1, N1, nx, nw, a, 1, s, xb, wb, dx, dw, fill); \
            sl = ns; ++kn; } while (0)
        for (int t = 0; t < nt; t += 2) { KS_PERIOD(t, a0, a1, b0, b1); if (t + 1 >= nt) break; KS_PERIOD(t + 1, b0, b1, a0, a1); }
#undef KS_PERIOD
        GM_SB();
        asm volatile("s_waitcnt vmcnt(0) lgkmcnt(0)" ::: "memory");
        GM_BAR();
    }
};
template <class W> __device__ __forceinline__ void acc_zero(f32x16 (&acc)[W::NT][W::MT]) {
#pragma unroll
    for (int t = 0; t < W::NT; ++t)
#pragma unroll
        for (int m = 0; m < W::MT; ++m) acc[t][m] = (f32x16){};
}
__device__ __forceinline__ u32x4 pair32(unsigned a0, unsigned a1, unsigned b0, unsigned b1) {
    const auto r0 = __builtin_amdgcn_permlane32_swap(a0, b0, false, false); const auto r1 = __builtin_amdgcn_permlane32_swap(a1, b1, false, false);
    return (u32x4){r0[0], r1[0], r0[1], r1[1]};
}
}

struct Params {
    const u16* ax; const u16* wqkv; const u16* wo; const u16* bx; const u16* win; const u16* wsp; const u16* bsp; const u16* wout; const u16* gw;
    u16* out; unsigned char* ws;
};
struct Ctx : Params { unsigned epv; __device__ __forceinline__ unsigned tg() const { return (unsigned)__builtin_amdgcn_readfirstlane(epv) + 1u; } };

using CfgQ = Cfg<256, 192, X_RAW, W_P384, 3, 2>;
struct Warm {
    const char* base; unsigned pitch; int lpr_log2; int cnt; unsigned scratch;
    __device__ __forceinline__ unsigned mask(int t0, int wid) const {
        if (wid < 4 || cnt <= 0) return 0u;
        return ((0x11111111u << (wid - 4)) & (cnt >= 32 ? ~0u : ((1u << cnt) - 1u))) << t0;
    }
    __device__ __forceinline__ void issue(int n, int wid, int lane) const {
        if (n >= 0 && n < cnt && wid == 4 + (n & 3)) { const int idx = n * 64 + lane; pf_lines(base, (unsigned)(idx >> lpr_log2) * pitch + (unsigned)(idx & ((1 << lpr_log2) - 1)) * 128u, scratch); }
    }
};

struct SigHook {
    static constexpr bool PRE = false; int wid, lane; Warm w0, w1; unsigned act;
    __device__ __forceinline__ void pre_x() const {}
    __device__ __forceinline__ void top(int) const {}
    __device__ __forceinline__ void at(int t) const { if ((act >> t) & 1u) { w0.issue(t - 1, wid, lane); w1.issue(t - 1, wid, lane); } }
};
struct AheadHook {
    static constexpr bool PRE = false;
    int wid, lane, nt; unsigned scratch;
    const char* b0; unsigned v0, step0;
    const char* b1; unsigned v1, step1;
    __device__ __forceinline__ void pre_x() const {}
    __device__ __forceinline__ void top(int) const {}
    __device__ __forceinline__ void first() const {
        if (wid >= 4) for (int k = 3 + ((wid - 3) & 3); k < PFD; k += 4) { pf_lines(b0, v0 + k * step0, scratch); if (b1) pf_lines(b1, v1 + k * step1, scratch); }
    }
    __device__ __forceinline__ void at(int t) const {
        if (wid == 4 + (t & 3) && t + PFD < nt) { pf_lines(b0, v0 + (t + PFD) * step0, scratch); if (b1) pf_lines(b1, v1 + (t + PFD) * step1, scratch); }
    }
};
template <int WN> __device__ __forceinline__ void qkv_wave(const Ctx& p, ldsp lds, int pair, int panel, int wid, int lane) {
    using C = CfgQ; using W = Wv<4, 6, WN * 6, WN ? 0x3Cu : 0u, WN == 0>;
    const int lid = wid & 3, m0 = (wid & 3) * 64;
    Src<C> s;
    src_x_raw<C>(s, p.ax + (size_t)panel * 256 * DM, DM * 2, lid, lane);
    src_w_raw<C>(s, p.wqkv + pair * 64, 3 * DM * 2, DM * 2, lid, lane);
    f32x4 acc[6][4]; acc_zero<W>(acc);
    STAMP(0, 1);
    const int wl = lane < 48 ? lane : 47;
    AheadHook hk{wid, lane, DM / 64, (unsigned)(uintptr_t)lds + LDS_PFS,
                 (const char*)(p.wqkv + pair * 64), (unsigned)(((panel & 3) * 16 + wl / 3) * (3 * DM * 2) + (wl % 3) * (DM * 2)), 64u * 3 * DM * 2,
                 nullptr, 0u, 0u};
    hk.first();
    gemm_loop<C, W, Sch<12, 7>>(lds, s, DM / 64, acc, lid, m0, 0, lane, hk DBG_ARGS(0));
    STAMP(0, 2);
    const int fr = lane & 15, fq = lane >> 4;
    const int row0 = panel * 256 + m0;
    const rsrc_t rs = ws_rsrc(p.ws);
#pragma unroll
    for (int t = 0; t < 6; t += 2) {
        const int T = WN * 6 + t, kind = T >> 2, head = pair * 2 + ((T >> 1) & 1);
        if (kind < 2) {
            const unsigned base = (unsigned)(kind == 0 ? WS_Q : WS_K);
            const int c = 2 * (fq & 1) + (fq >> 1);
            const float sc = kind == 0 ? QSCALE : 1.0f;
#pragma unroll
            for (int m = 0; m < 4; ++m) { const int row = row0 + m * 16 + fr;
                st16_wt(rs, base + ((head * 64 + (row >> 5)) * 4 + c) * 512 + (row & 31) * 16, pair16(acc[t][m] * sc, acc[t + 1][m] * sc)); }
        } else {
#pragma unroll
            for (int tt = 0; tt < 2; ++tt)
#pragma unroll
                for (int m = 0; m < 4; m += 2) {
                    const f32x4 A = acc[t + tt][m], B = acc[t + tt][m + 1];
                    const unsigned a0 = pk_bf16(A[0], A[1]), a1 = pk_bf16(A[2], A[3]), b0 = pk_bf16(B[0], B[1]), b1 = pk_bf16(B[2], B[3]);
                    const auto r0 = __builtin_amdgcn_permlane32_swap(a0, b0, false, false); const auto r1 = __builtin_amdgcn_permlane32_swap(a1, b1, false, false);
                    const int kb = (row0 >> 4) + m + (fq >> 1), h = fq & 1, d = tt * 16 + fr;
                    st16_wt(rs, (unsigned)WS_V + ((head * 128 + kb) * 2 + h) * 512 + d * 16, (u32x4){r0[0], r1[0], r0[1], r1[1]});
                }
        }
    }
    asm volatile("s_waitcnt vmcnt(0)" ::: "memory");
    GM_BAR();
    if (threadIdx.x == 0) signal(p.ws, F_Q + pair * 32 + panel, p.tg());
}

using CfgZ = g32::Cfg<128, 224, X_RAW, g32::W_P448, 3>;
using CfgS = g32::Cfg<128, 224, X_RAW, g32::W_OWNT, 3>;
#define ONS 5
#define ONL 4
using CfgO = g32::Cfg<128, 128, X_OWN, g32::W_P256, ONS, ONL>;
__device__ __forceinline__ float gelu_tanh(float x) {
    const float t = x * x * (-0.044715f * 2.0f * 0.7978845608028654f * 1.4426950408889634f) + (-2.0f * 0.7978845608028654f * 1.4426950408889634f);
    return x * __builtin_amdgcn_rcpf(1.0f + __builtin_amdgcn_exp2f(x * t));
}
template <int N> __device__ __forceinline__ void gelu_pairs(unsigned* z) {
    constexpr float C1 = -0.044715f * 2.0f * 0.7978845608028654f * 1.4426950408889634f, C2 = -2.0f * 0.7978845608028654f * 1.4426950408889634f;
    f32x2 x[N], e[N];
#pragma unroll
    for (int i = 0; i < N; ++i) x[i] = (f32x2){bf_lo(z[i]), bf_hi(z[i])};
#pragma unroll
    for (int i = 0; i < N; ++i) e[i] = x[i] * (x[i] * x[i] * C1 + C2);
    GM_SB();
#pragma unroll
    for (int i = 0; i < N; ++i) { e[i].x = __builtin_amdgcn_exp2f(e[i].x); e[i].y = __builtin_amdgcn_exp2f(e[i].y); }
    GM_SB();
#pragma unroll
    for (int i = 0; i < N; ++i) e[i] = e[i] + 1.0f;
#pragma unroll
    for (int i = 0; i < N; ++i) { e[i].x = __builtin_amdgcn_rcpf(e[i].x); e[i].y = __builtin_amdgcn_rcpf(e[i].y); }
    GM_SB();
#pragma unroll
    for (int i = 0; i < N; ++i) { const f32x2 y = x[i] * e[i]; z[i] = pk_bf16(y.x, y.y); }
    GM_SB();
}
__device__ __forceinline__ void gelu16(f32x16& a) {
    constexpr float C1 = -0.044715f * 2.0f * 0.7978845608028654f * 1.4426950408889634f, C2 = -2.0f * 0.7978845608028654f * 1.4426950408889634f;
    f32x16 e = a * (a * a * C1 + C2);
    GM_SB();
#pragma unroll
    for (int i = 0; i < 16; ++i) e[i] = __builtin_amdgcn_exp2f(e[i]);
    GM_SB();
    e = e + 1.0f;
#pragma unroll
    for (int i = 0; i < 16; ++i) e[i] = __builtin_amdgcn_rcpf(e[i]);
    GM_SB();
    a = a * e;
}
__device__ __forceinline__ float add1(float a, float b) { float r; asm("v_add_f32_e32 %0, %1, %2" : "=v"(r) : "v"(a), "v"(b)); return r; }
__device__ __forceinline__ float fma1(float a, float b, float c) { float r; asm("v_fma_f32 %0, %1, %2, %3" : "=v"(r) : "v"(a), "v"(b), "v"(c)); return r; }
#define GM_ROR16(x, N) do { asm volatile("s_nop 1" ::: "memory"); _Pragma("unroll") for (int k_ = 0; k_ < 16; ++k_) \
        asm volatile("v_add_f32_dpp %0, %0, %0 row_ror:" #N " row_mask:0xf bank_mask:0xf" : "+v"(x[k_])); } while (0)
template <int CTRL> __device__ __forceinline__ float dpp_add(float x) { return x + __int_as_float(__builtin_amdgcn_update_dpp(0, __float_as_int(x), CTRL, 0xf, 0xf, true)); }
__device__ __forceinline__ float red32_pair(float a, float b) {
    const auto r = __builtin_amdgcn_permlane16_swap(__float_as_uint(a), __float_as_uint(b), false, false);
    float x = __uint_as_float(r[0]) + __uint_as_float(r[1]);
    x = dpp_add<0x128>(x); x = dpp_add<0x124>(x); x = dpp_add<0x122>(x); x = dpp_add<0x121>(x);
    return x;
}
constexpr int LDS_TAB = CfgZ::LDS;
template <int NT> struct GeluFill {
    static constexpr int NSLOT = 4 * NT, STRIDE = NSLOT / 4;
    unsigned (&z)[NT][8];
    f32x2 x, e;
    template <int S> __device__ __forceinline__ void chunk() {
        constexpr float C1 = -0.044715f * 2.0f * 0.7978845608028654f * 1.4426950408889634f, C2 = -2.0f * 0.7978845608028654f * 1.4426950408889634f;
        if constexpr (S % STRIDE == 0) { constexpr int pa = S / STRIDE;
            x = (f32x2){bf_lo(z[0][pa]), bf_hi(z[0][pa])};
            const f32x2 w = x * (x * x * C1 + C2);
            e.x = __builtin_amdgcn_exp2f(w.x); e.y = __builtin_amdgcn_exp2f(w.y);
#pragma unroll
            for (int g = 0; g + 1 < 2 * NT; ++g) z[g >> 1][(g & 1) * 4 + pa] = z[(g + 1) >> 1][((g + 1) & 1) * 4 + pa];
            asm volatile("" : "+v"(x), "+v"(e));
        }
        if constexpr (S % STRIDE == 2) { constexpr int pb = S / STRIDE;
            const f32x2 d = e + 1.0f;
            const f32x2 y = x * (f32x2){__builtin_amdgcn_rcpf(d.x), __builtin_amdgcn_rcpf(d.y)};
            z[NT - 1][4 + pb] = pk_bf16(y.x, y.y);
            asm volatile("" : "+v"(z[NT - 1][4 + pb]));
        }
    }
};
struct ZHook {
    int wid, lane; unsigned scratch; const char* b2; const char* b1; unsigned v;
    __device__ __forceinline__ void touch(int k) const { pf_lines(k < 16 ? b2 : b1, v + (unsigned)(k & 15) * (64u * 2 * FH * 2), scratch); }
    __device__ __forceinline__ void top(int) const {}
    __device__ __forceinline__ void first() const { if (wid >= 4) for (int k = 3 + ((wid - 3) & 3); k < PFD; k += 4) touch(k); }
    __device__ __forceinline__ void at(int t) const { if (wid == 4 + (t & 3)) { touch(t + PFD); if (t >= PFD) touch(t + 16); } }
};
template <int NT, bool LD> struct LnHook {
    const Ctx& p; ldsp lds; int panel, j, wid, lane, n0; unsigned (&z2)[NT][8];
    u32x4 w[4];
    __device__ __forceinline__ void normalise(int tt) {
        LAS f32x2* R = (LAS f32x2*)(lds + LDS_TAB + 2048);
        const int r = lane & 31, h = lane >> 5, m0 = (wid & 3) * 32;
        const rsrc_t rs = ws_rsrc(p.ws);
        const int srow = panel * 128 + m0;
#pragma unroll
        for (int i = 0; i < 4; i += 2) {
            const f32x4 ra = *(const LAS f32x4*)(R + m0 + 8 * i + 4 * h), rb = *(const LAS f32x4*)(R + m0 + 8 * i + 4 * h + 2);
            const f32x4 rc = *(const LAS f32x4*)(R + m0 + 8 * i + 8 + 4 * h), rd = *(const LAS f32x4*)(R + m0 + 8 * i + 8 + 4 * h + 2);
            const int k = srow + 8 * (i + h);
            const unsigned base = (unsigned)WS_Z + ((j * 32 + (k >> 6)) * 8 + ((k & 63) >> 3)) * 3584 + (n0 * 32 + r) * 16;
            const unsigned a0 = z2[tt][2 * i], a1 = z2[tt][2 * i + 1], b0 = z2[tt][2 * i + 2], b1 = z2[tt][2 * i + 3];
            st16_wt(rs, base + tt * 512, g32::pair32(pk_bf16(fma1(bf_lo(a0), ra[1], ra[0]), fma1(bf_hi(a0), ra[3], ra[2])), pk_bf16(fma1(bf_lo(a1), rb[1], rb[0]), fma1(bf_hi(a1), rb[3], rb[2])),
                                                      pk_bf16(fma1(bf_lo(b0), rc[1], rc[0]), fma1(bf_hi(b0), rc[3], rc[2])), pk_bf16(fma1(bf_lo(b1), rd[1], rd[0]), fma1(bf_hi(b1), rd[3], rd[2]))));
        }
    }
    __device__ __forceinline__ void normalise_at(int t) {
        if (t >= TF + 1 && t < TF + 5) { if (at_tile(t, TF + 1)) normalise(0); if (at_tile(t, TF + 2)) normalise(1); if (at_tile(t, TF + 3)) normalise(2); if constexpr (NT == 4) { if (at_tile(t, TF + 4)) normalise(3); } }
    }
    __device__ __forceinline__ void fetch() {
        const unsigned off = (unsigned)WS_STAT + ((panel * 8 + (lane >> 5) * 4) * 128 + (wid - 4) * 32 + (lane & 31)) * 16;
#pragma unroll
        for (int v = 0; v < 4; ++v) w[v] = ld16_dev(ws_rsrc(p.ws), off + v * 2048);
    }
    __device__ __forceinline__ bool fetched() const { bool ok = true;
#pragma unroll
        for (int v = 0; v < 4; ++v) ok = ok && w[v][1] == p.tg() && w[v][3] == p.tg();
        return __all(ok); }
    __device__ __forceinline__ void top(int t) {
        if constexpr (LD) normalise_at(t);
        else { if (t == TF) fetch(); }
    }
    __device__ __forceinline__ void at(int t) {
        if (t < TF || t > TF + 7) return;
        if constexpr (!LD) {
            if (at_tile(t, TF)) {
                for (unsigned spins = 0; !fetched();) {
                    __builtin_amdgcn_s_sleep(2); fetch();
                    if (++spins > (1u << 19)) { __hip_atomic_store((gu32*)(p.ws + WS_CTL) + C_TMO, p.tg(), RLX_AGENT); break; }
                }
                LAS f32x2* R = (LAS f32x2*)(lds + LDS_TAB + 2048);
                float ms = 0.f;
#pragma unroll
                for (int v = 0; v < 4; ++v) ms += __uint_as_float(w[v][0]);
                { const auto r = __builtin_amdgcn_permlane32_swap(__float_as_uint(ms), __float_as_uint(ms), false, false); ms = __uint_as_float(r[0]) + __uint_as_float(r[1]); }
                const float mean = ms * 0.125f; float q = 0.f;
#pragma unroll
                for (int v = 0; v < 4; ++v) { const float dm = __uint_as_float(w[v][0]) - mean; q += __uint_as_float(w[v][2]) + 224.0f * dm * dm; }
                { const auto r = __builtin_amdgcn_permlane32_swap(__float_as_uint(q), __float_as_uint(q), false, false); q = __uint_as_float(r[0]) + __uint_as_float(r[1]); }
                { const float rs_ = __builtin_amdgcn_rsqf(q * (1.0f / 1792.0f) + 1e-5f); if (lane < 32) R[(wid - 4) * 32 + lane] = (f32x2){-mean * rs_, rs_}; }
                GM_VM0();
            }
            normalise_at(t);
        }
        if (at_tile(t, TF + 6)) asm volatile("s_waitcnt vmcnt(0)" ::: "memory");
        if constexpr (!LD) { if (at_tile(t, TF + 7)) { if (threadIdx.x == 256) signal(p.ws, F_Z + j * 32 + panel, p.tg()); } }
    }
};
struct SpHook {
    static constexpr bool PRE = false; const Ctx& p; ldsp lds; int panel, wid, lane;
    __device__ __forceinline__ void pre_x() const {}
    __device__ __forceinline__ void top(int) const {}
    __device__ __forceinline__ void at(int t) {
        if (t == 2 && (wid == 4 || wid == 5)) {
            poll_flags(p.ws, p.tg(), F_G, 1, true, F_G, lane);
            const int row = (wid - 4) * 64 + lane, trow = panel * 128 + row;
            ((LAS f32x2*)(lds + LDS_TAB + 2048))[row] = (f32x2){__uint_as_float((unsigned)p.bsp[trow] << 16), 1.0f - __uint_as_float(__hip_atomic_load((gu32*)(p.ws + WS_GATE) + trow, RLX_AGENT))};
            GM_VM0();
        }
    }
};
struct WaitHook {
    unsigned char* ws; unsigned tag; int wid, lane, tlate, wl, nl;
    __device__ __forceinline__ void top(int) const {}
    __device__ __forceinline__ void at(int t) const { if (t == tlate && wid == 4) wait_flags(ws, tag, wl, nl, false, 0, lane); }
};
template <class C> __device__ __forceinline__ void out_src_w(Src<C>& s, const Ctx& p, int k0, int j, int lid, int lane) {
    if (k0 < FH / 64) { g32::src_w_raw<C>(s, p.wout + (size_t)k0 * 64 * DM + j * 128, DM * 2, lid, lane); set_w2<C>(s, p.wo + j * 128, FH / 64 - k0); }
    else g32::src_w_raw<C>(s, p.wo + (size_t)(k0 - FH / 64) * 64 * DM + j * 128, DM * 2, lid, lane);
}
__device__ __forceinline__ void out_part_early_w(const Ctx& p, ldsp lds, int j, int wid, int lane) {
    using C = CfgO; using W = g32::Wv<2, 2, true>;
    static_assert(C::WOFF >= 65536, "W slots behind the attention ring");
    if (wid < 4) {
        Src<C> s; out_src_w<C>(s, p, 44 - NA, j, wid, lane);
        g32::PipeKS<C, W> pipe(lds, NA, 0, wid, 0, 0, lane);
        pipe.issue_w(s);
    }
}
template <bool LD, bool FINAL> __device__ __forceinline__ void out_part(const Ctx& p, ldsp lds, int panel, int j, int wid, int lane) {
    using C = CfgO; using W = g32::Wv<2, 2, LD>;
    const int lid = wid & 3, r = lane & 31, h = lane >> 5;
    const int kh = wid >> 2, mo = ((wid >> 1) & 1) * 64, no = (wid & 1) * 2;
    Src<C> s;
    constexpr int K0 = FINAL ? 0 : 44 - NA, NKT = FINAL ? 44 - NA : NA, NU = FH / 64 - K0 < NKT ? FH / 64 - K0 : NKT;
    src_x_own<C>(s, p.ws + WS_ACT + (size_t)panel * 352 * 2048 + K0 * C::XB, lid, lane);
    out_src_w<C>(s, p, K0, j, lid, lane);
    f32x16 acc[2][2]; g32::acc_zero<W>(acc);
    constexpr bool LATE = NU >= C::NS && NU < NKT;
    WaitHook hk{p.ws, p.tg(), wid, lane, LATE ? NU - C::NS : -1, F_O + (panel >> 2) * 32, 32};
    g32::PipeKS<C, W> pipe(lds, NKT, kh, lid, mo, no, lane);
    if (FINAL) pipe.issue_w(s);
    if (wid == 4) {
        if (NU > 0) wait_flags(p.ws, p.tg(), F_U + panel * 32, 8, false, 0, lane);
        if (NU < NKT && !LATE) wait_flags(p.ws, p.tg(), F_O + (panel >> 2) * 32, 32, false, 0, lane);
    }
    GM_BAR();
    pipe.issue_x(s);
    pipe.landed();
    STAMP(0, FINAL ? 10 : 7);
    pipe.run(s, acc, hk DBG_ARGS(4));
    STAMP(0, FINAL ? 9 : 8);
    ldsp mine = lds + wid * 8192 + lane * 16, theirs = lds + (wid ^ 4) * 8192 + lane * 16;
#pragma unroll
    for (int t = 0; t < 2; ++t)
#pragma unroll
        for (int i = 0; i < 4; ++i) { const f32x16& A = LD ? acc[t][1] : acc[t][0]; *(LAS f32x4*)(mine + (t * 4 + i) * 1024) = (f32x4){A[4 * i], A[4 * i + 1], A[4 * i + 2], A[4 * i + 3]}; }
    asm volatile("s_waitcnt lgkmcnt(0)" ::: "memory");
    if (FINAL && wid == 4) wait_flags(p.ws, p.tg(), F_P + panel * 32 + j, 1, false, 0, lane);
    GM_BAR();
    const rsrc_t rs = ws_rsrc(p.ws);
    const unsigned pb = (unsigned)WS_P + ((panel * 8 + j) * 8 + wid) * 8192 + lane * 16;
    f32x16 o[2];
    u32x4 pv[8];
    if (FINAL) {
#pragma unroll
        for (int q = 0; q < 8; ++q) pv[q] = ld16_dev(rs, pb + q * 1024);
    }
#pragma unroll
    for (int t = 0; t < 2; ++t) { o[t] = LD ? acc[t][0] : acc[t][1];
#pragma unroll
        for (int i = 0; i < 4; ++i) { const f32x4 v = *(const LAS f32x4*)(theirs + (t * 4 + i) * 1024); o[t][4 * i] += v[0]; o[t][4 * i + 1] += v[1]; o[t][4 * i + 2] += v[2]; o[t][4 * i + 3] += v[3]; } }
    if (!FINAL) {
#pragma unroll
        for (int t = 0; t < 2; ++t)
#pragma unroll
            for (int i = 0; i < 4; ++i) st16_wt(rs, pb + (t * 4 + i) * 1024, (u32x4){__float_as_uint(o[t][4 * i]), __float_as_uint(o[t][4 * i + 1]), __float_as_uint(o[t][4 * i + 2]), __float_as_uint(o[t][4 * i + 3])});
        asm volatile("s_waitcnt vmcnt(0)" ::: "memory");
        GM_BAR();
        if (threadIdx.x == 0) signal(p.ws, F_P + panel * 32 + j, p.tg());
    } else {
#pragma unroll
        for (int t = 0; t < 2; ++t)
#pragma unroll
            for (int i = 0; i < 4; ++i)
#pragma unroll
                for (int e = 0; e < 4; ++e) o[t][4 * i + e] += __uint_as_float(pv[t * 4 + i][e]);
        ldsp st = lds + 65536;
#pragma unroll
        for (int t = 0; t < 2; ++t)
#pragma unroll
            for (int i = 0; i < 4; i += 2) { const f32x16& A = o[t];
                *(LAS u32x4*)(st + (mo + kh * 32 + r) * 272 + (no + t) * 64 + (i + h) * 16) =
                    g32::pair32(pk_bf16(A[4 * i], A[4 * i + 1]), pk_bf16(A[4 * i + 2], A[4 * i + 3]), pk_bf16(A[4 * i + 4], A[4 * i + 5]), pk_bf16(A[4 * i + 6], A[4 * i + 7])); }
        asm volatile("s_waitcnt lgkmcnt(0)" ::: "memory"); GM_BAR();
#pragma unroll
        for (int i = 0; i < 4; ++i) { const int row = wid * 16 + i * 4 + (lane >> 4);
            __builtin_amdgcn_raw_buffer_store_b128(*(const LAS u32x4*)(st + row * 272 + (lane & 15) * 16), uni_rsrc(p.out, SEQ * DM * 2), (unsigned)(((panel * 128 + row) * DM + j * 128 + (lane & 15) * 8) * 2), 0, 16); }
    }
}
template <bool LD> __device__ __forceinline__ void b_wave(const Ctx& p, ldsp lds, int panel, int j, int wid, int lane) {
    constexpr int NT = LD ? 3 : 4;
    const int lid = wid & 3, m0 = lid * 32, n0 = LD ? 0 : 3;
    const int r = lane & 31, h = lane >> 5;
    unsigned z2[NT][8], z1[NT][8];
    const unsigned pfs = (unsigned)(uintptr_t)lds + LDS_PFS;
    {
        using C = CfgZ; using W = g32::Wv<1, NT, LD>; Src<C> s;
        src_x_raw<C>(s, p.ax + (size_t)panel * 128 * DM, DM * 2, lid, lane);
        g32::src_w_raw<C>(s, p.win + FH + j * 224, 2 * FH * 2, lid, lane);
        set_w2<C>(s, p.win + j * 224, DM / 64);
        g32::Pipe<C, W, 1> pipe(lds, s, 2 * DM / 64, DM / 64, lid, m0, n0, lane);
        f32x16 acc[NT][1]; g32::acc_zero<W>(acc);
        STAMP(0, 1);
        const unsigned wv = (unsigned)(((panel & 3) * 16 + (lane >> 2)) * (2 * FH * 2) + (lane & 3) * 128);
        ZHook hk{wid, lane, pfs, (const char*)p.win + (((FH + j * 224) * 2) & ~127), (const char*)p.win + ((j * 224 * 2) & ~127), wv};
        hk.first();
        pipe.begin(s);
        pipe.template run<false>(s, 0, DM / 64, acc, hk DBG_ARGS(1));
        STAMP(0, 2);
        {
            float sm[16], sq[16];
#pragma unroll
            for (int t = 0; t < NT; ++t) {
                gelu16(acc[t][0]);
#pragma unroll
                for (int q = 0; q < 16; ++q) { const float v = acc[t][0][q]; sm[q] = t ? add1(sm[q], v) : v; sq[q] = t ? fma1(v, v, sq[q]) : v * v; }
#pragma unroll
                for (int q = 0; q < 16; q += 2) z2[t][q >> 1] = pk_bf16(acc[t][0][q], acc[t][0][q + 1]);
                acc[t][0] = (f32x16){};
            }
            float ra[16];
#pragma unroll
            for (int k = 0; k < 8; ++k) {
                const auto a = __builtin_amdgcn_permlane16_swap(__float_as_uint(sm[2 * k]), __float_as_uint(sm[2 * k + 1]), false, false); ra[k] = add1(__uint_as_float(a[0]), __uint_as_float(a[1]));
                const auto b2 = __builtin_amdgcn_permlane16_swap(__float_as_uint(sq[2 * k]), __float_as_uint(sq[2 * k + 1]), false, false); ra[8 + k] = add1(__uint_as_float(b2[0]), __uint_as_float(b2[1])); }
            GM_ROR16(ra, 8); GM_ROR16(ra, 4); GM_ROR16(ra, 2); GM_ROR16(ra, 1);
            asm volatile("s_nop 1" ::: "memory");
            float va = ra[0], vb = ra[8];
#pragma unroll
            for (int k = 1; k < 8; ++k) { const bool sel = (lane & 7) == k; va = sel ? ra[k] : va; vb = sel ? ra[8 + k] : vb; }
            LAS f32x2* P = (LAS f32x2*)(lds + LDS_TAB);
            { const int q = 2 * (lane & 7) + ((lane >> 4) & 1), row = m0 + 8 * (q >> 2) + 4 * h + (q & 3);
              if ((lane & 8) == 0) P[row * 2 + (LD ? 0 : 1)] = (f32x2){va, vb}; }
            asm volatile("s_waitcnt lgkmcnt(0)" ::: "memory"); GM_BAR();
            if constexpr (!LD) {
                if (lane < 32) {
                    const int row = lid * 32 + lane;
                    const f32x2 a = P[row * 2], b = P[row * 2 + 1];
                    const float su = a.x + b.x, mt = su * (1.0f / 224.0f), m2 = fmaxf(a.y + b.y - su * mt, 0.f);
                    st16_wt(ws_rsrc(p.ws), (unsigned)WS_STAT + ((panel * 8 + j) * 128 + row) * 16, (u32x4){__float_as_uint(mt), p.tg(), __float_as_uint(m2), p.tg()});
                }
            }
        }
        STAMP(0, 3);
        LnHook<NT, LD> hl{p, lds, panel, j, wid, lane, n0, z2};
        pipe.template run<true>(s, DM / 64, 2 * DM / 64, acc, hl DBG_ARGS(2));
        pipe.end();
        STAMP(0, 4);
#pragma unroll
        for (int t = 0; t < NT; ++t)
#pragma unroll
            for (int q = 0; q < 16; q += 2) z1[t][q >> 1] = pk_bf16(acc[t][0][q], acc[t][0][q + 1]);
    }
    {
        using C = CfgS; using W = g32::Wv<1, NT, LD>; Src<C> s;
        src_x_raw<C>(s, p.wsp + (size_t)panel * 128 * SEQ, SEQ * 2, lid, lane);
        src_w_own<C>(s, p.ws + WS_Z + (size_t)j * 32 * 28672, lid, lane);
        g32::Pipe<C, W, SPD> pipe(lds, s, SEQ / 64, SEQ / 64, lid, m0, n0, lane);
        pipe.issue_x(s);
        if (wid == 4) wait_flags(p.ws, p.tg(), F_Z + j * 32, 16, false, 0, lane);
        GM_BAR();
        pipe.issue_w(s);
        STAMP(0, 5);
#pragma unroll
        for (int t = 0; t < NT; ++t) { gelu_pairs<8>(z1[t]);
#pragma unroll
            for (int q = 0; q < 8; ++q) asm volatile("" : "+v"(z1[t][q])); }
        STAMP(0, 6);
        pipe.landed();
        f32x16 acc[NT][1]; g32::acc_zero<W>(acc);
        SpHook hk{p, lds, panel, wid, lane};
        pipe.template run<true>(s, 0, SEQ / 64, acc, hk DBG_ARGS(3));
        pipe.end();
        STAMP(0, 7);
        const rsrc_t rs = ws_rsrc(p.ws);
        const f32x2 bg = ((const LAS f32x2*)(lds + LDS_TAB + 2048))[m0 + r]; const float b = bg.x, g1 = bg.y;
        const unsigned ub = (unsigned)WS_ACT + (panel * 352 + j * 28 + n0 * 4 + h) * 2048 + (m0 + r) * 16;
#pragma unroll
        for (int t = 0; t < NT; ++t)
#pragma unroll
            for (int i = 0; i < 4; i += 2) {
                unsigned w4[4];
#pragma unroll
                for (int e = 0; e < 4; ++e) { const int q = 4 * i + 2 * e; const unsigned z = z1[t][q >> 1];
                    w4[e] = pk_bf16((acc[t][0][q] + b) * g1 * bf_lo(z), (acc[t][0][q + 1] + b) * g1 * bf_hi(z)); }
                st16_wt(rs, ub + (t * 4 + i) * 2048, g32::pair32(w4[0], w4[1], w4[2], w4[3]));
            }
        asm volatile("s_waitcnt vmcnt(0)" ::: "memory");
        GM_BAR();
        if (threadIdx.x == 256) signal(p.ws, F_U + panel * 32 + j, p.tg());
    }
    STAMP(0, 8);
    out_part<LD, true>(p, lds, panel, j, wid, lane);
}

constexpr int AT_STAGE = 16384, AT_NS = 4;
#define MFMA32(a, b, c) __builtin_amdgcn_mfma_f32_32x32x16_bf16(a, b, c, 0, 0, 0)
#define MFMA16(a, b, c) __builtin_amdgcn_mfma_f32_16x16x32_bf16(a, b, c, 0, 0, 0)
#define PIN(x) asm volatile("" : "+v"(x))
__device__ __forceinline__ void gate_rows4(const Ctx& p, int row0, int lane);
__device__ __forceinline__ void attn_wg(const Ctx& p, ldsp lds, int head, int qb, int x, int a, int oj, int gpair, int gpanel, int wid, int lane) {
    if (wid == 4) wait_flags(p.ws, p.tg(), F_Q + (head >> 1) * 32, 8, false, 0, lane);
    if (wid < 4) { gate_rows4(p, gpanel * 256 + gpair * 16 + wid * 4, lane); asm volatile("s_waitcnt vmcnt(0)" ::: "memory"); }
    GM_BAR();
    if (threadIdx.x == 0) signal(p.ws, F_G + gpair * 32 + gpanel, p.tg());
    STAMP(0, 3);
    const unsigned char* Kg = p.ws + WS_K + (size_t)head * 131072; const unsigned char* Vg = p.ws + WS_V + (size_t)head * 131072;
    const unsigned ldb = (unsigned)(uintptr_t)lds;
    const unsigned voff = wid * 1024 + lane * 16;
    const char* kb = (const char*)Kg; const char* vb = (const char*)Vg;
    auto stage = [&](int st, int slot) {
        asm volatile("s_add_u32 m0, %2, 0\n\ts_nop 0\n\tglobal_load_lds_dwordx4 %0, %1 sc1" ::"v"(voff), "s"(kb + (size_t)st * 8192), "s"(ldb + slot * AT_STAGE + wid * 1024) : "memory", "scc");
        asm volatile("s_add_u32 m0, %2, 0\n\ts_nop 0\n\tglobal_load_lds_dwordx4 %0, %1 sc1" ::"v"(voff), "s"(vb + (size_t)st * 8192), "s"(ldb + slot * AT_STAGE + 8192 + wid * 1024) : "memory", "scc");
    };
    stage(0, 0);
    const int q0 = qb * 512 + wid * 64;
    bf16x8 qf[2][2];
#pragma unroll
    for (int a = 0; a < 2; ++a)
#pragma unroll
        for (int d = 0; d < 2; ++d) qf[a][d] = __builtin_bit_cast(bf16x8, ld16_dev(ws_rsrc(p.ws), (unsigned)WS_Q + (head * 64 + (q0 >> 5) + a) * 2048 + d * 1024 + lane * 16));
    bf16x8 sel;
    { const short one = ((lane & 15) == ((lane >> 4) & 1)) ? (short)0x3f80 : (short)0;
#pragma unroll
      for (int i = 0; i < 8; ++i) sel[i] = one; }
    GM_VM0();
    GM_BAR();
    stage(1, 1); stage(2, 2);
    f32x16 o[2], negm[2]; f32x4 l[2];
    const f32x16 zero16 = {};
    bf16x8 kA[2], kB[2], vA[2], vB[2];
    ldsp lb = lds + lane * 16;
    kA[0] = *(const LAS bf16x8*)(lb); kA[1] = *(const LAS bf16x8*)(lb + 1024);
#pragma unroll
    for (int a = 0; a < 2; ++a) {
        f32x16 s = MFMA32(kA[0], qf[a][0], zero16); s = MFMA32(kA[1], qf[a][1], s);
        float m = fmaxf(fmaxf(s[0], s[1]), fmaxf(s[2], s[3]));
#pragma unroll
        for (int r = 4; r < 16; r += 2) m = fmaxf(m, fmaxf(s[r], s[r + 1]));
        const auto rr = __builtin_amdgcn_permlane32_swap(__float_as_uint(m), __float_as_uint(m), false, false);
        m = -fmaxf(__uint_as_float(rr[0]), __uint_as_float(rr[1]));
#pragma unroll
        for (int r = 0; r < 16; ++r) negm[a][r] = m;
        PIN(negm[a]);
        o[a] = zero16; l[a] = (f32x4){0.f, 0.f, 0.f, 0.f};
    }
    f32x16 S0, S1; u32x4 p0l, p0h, p1l, p1h;
    S0 = MFMA32(kA[0], qf[0][0], negm[0]); S0 = MFMA32(kA[1], qf[0][1], S0);
    p1l = (u32x4){0u, 0u, 0u, 0u}; p1h = p1l;
    vB[0] = (bf16x8){0, 0, 0, 0, 0, 0, 0, 0}; vB[1] = vB[0];
    STAMP(0, 4);
#define EX4(S, i) do { S[i] = __builtin_amdgcn_exp2f(S[i]); S[i + 1] = __builtin_amdgcn_exp2f(S[i + 1]); S[i + 2] = __builtin_amdgcn_exp2f(S[i + 2]); S[i + 3] = __builtin_amdgcn_exp2f(S[i + 3]); } while (0)
#define PK4(P, S, i) do { P[0] = pk_bf16(S[i], S[i + 1]); P[1] = pk_bf16(S[i + 2], S[i + 3]); P[2] = pk_bf16(S[i + 4], S[i + 5]); P[3] = pk_bf16(S[i + 6], S[i + 7]); } while (0)
#define BC(x) __builtin_bit_cast(bf16x8, x)
#define AORD 1
#define ASTEP(SC, SN, PCl, PCh, PPl, PPh, KF, QN, VF, AP, LOADS) do { \
        SN = MFMA32(KF[0], qf[QN][0], negm[QN]); GM_SB(); EX4(SC, 0); PIN(SC); GM_SB(); \
        SN = MFMA32(KF[1], qf[QN][1], SN); GM_SB(); LOADS; EX4(SC, 4); PIN(SC); GM_SB(); \
        o[AP] = MFMA32(VF[0], BC(PPl), o[AP]); GM_SB(); EX4(SC, 8); PIN(SC); GM_SB(); \
        o[AP] = MFMA32(VF[1], BC(PPh), o[AP]); GM_SB(); EX4(SC, 12); PIN(SC); GM_SB(); \
        l[AP] = MFMA16(sel, BC(PPl), l[AP]); GM_SB(); PK4(PCl, SC, 0); PIN(PCl); GM_SB(); \
        l[AP] = MFMA16(sel, BC(PPh), l[AP]); GM_SB(); PK4(PCh, SC, 8); PIN(PCh); GM_SB(); } while (0)
#define LDK(KF, base, kt) do { KF[0] = *(const LAS bf16x8*)((base) + (kt) * 2048); KF[1] = *(const LAS bf16x8*)((base) + (kt) * 2048 + 1024); } while (0)
#define LDV(VF, base, kt) do { VF[0] = *(const LAS bf16x8*)((base) + 8192 + (kt) * 2048); VF[1] = *(const LAS bf16x8*)((base) + 8192 + (kt) * 2048 + 1024); } while (0)
    float gval = 0.f;
    for (int st = 0; st < 16; ++st) {
        ldsp cb = lb + (st & 3) * AT_STAGE, nb = lb + ((st + 1) & 3) * AT_STAGE;
        if (st < 5) {
            const int n = st * 8 + wid; const unsigned pfs = (unsigned)(uintptr_t)lds + LDS_PFS;
            const int p0 = 8 * (x & 1) + 4 * ((x >> 1) & 1), j0 = 4 * (x >> 2);
            if (n < 16) pf_lines(p.wsp + (size_t)(p0 * 128 + a * 32) * SEQ, (unsigned)(lane & 31) * (SEQ * 2) + (unsigned)(2 * n + (lane >> 5)) * 128u, pfs);
            else if (n < 30) pf_lines(p.wout + (size_t)(a * 112) * DM + j0 * 128, (unsigned)((n - 16) * 8 + (lane >> 3)) * (DM * 2) + (unsigned)(lane & 7) * 128u, pfs);
            else if (n < 38) pf_lines(p.wo + (size_t)(a * 64) * DM + j0 * 128, (unsigned)((n - 30) * 8 + (lane >> 3)) * (DM * 2) + (unsigned)(lane & 7) * 128u, pfs);
        }
        if (st == 8) {
            poll_flags(p.ws, p.tg(), F_G, 1, true, F_G, lane);
            gval = __uint_as_float(__hip_atomic_load((gu32*)(p.ws + WS_GATE) + q0 + lane, RLX_AGENT));
        }
        ASTEP(S0, S1, p0l, p0h, p1l, p1h, kA, 1, vB, 1, { LDK(kB, cb, 1); LDV(vA, cb, 0); });
        ASTEP(S1, S0, p1l, p1h, p0l, p0h, kB, 0, vA, 0, {});
        ASTEP(S0, S1, p0l, p0h, p1l, p1h, kB, 1, vA, 1, { LDK(kA, cb, 2); LDV(vB, cb, 1); });
        ASTEP(S1, S0, p1l, p1h, p0l, p0h, kA, 0, vB, 0, {});
        ASTEP(S0, S1, p0l, p0h, p1l, p1h, kA, 1, vB, 1, { LDK(kB, cb, 3); LDV(vA, cb, 2); });
        ASTEP(S1, S0, p1l, p1h, p0l, p0h, kB, 0, vA, 0, {});
        if (st + 1 < 16) {
            if (st + 2 < 16) asm volatile("s_waitcnt vmcnt(2)" ::: "memory"); else asm volatile("s_waitcnt vmcnt(0)" ::: "memory");
            GM_BAR();
            if (st + 3 < 16) stage(st + 3, (st + 3) & 3);
            if (st == 14) out_part_early_w(p, lds, oj, wid, lane);
        }
        ASTEP(S0, S1, p0l, p0h, p1l, p1h, kB, 1, vA, 1, { LDK(kA, nb, 0); LDV(vB, cb, 3); });
        ASTEP(S1, S0, p1l, p1h, p0l, p0h, kA, 0, vB, 0, {});
    }
    o[1] = MFMA32(vB[0], BC(p1l), o[1]); o[1] = MFMA32(vB[1], BC(p1h), o[1]);
    l[1] = MFMA16(sel, BC(p1l), l[1]); l[1] = MFMA16(sel, BC(p1h), l[1]);
#undef EX4
#undef PK4
#undef ASTEP
#undef LDK
#undef LDV
    STAMP(0, 5);
    float lsum[2];
    {   bool bad = false;
#pragma unroll
        for (int a = 0; a < 2; ++a) {
            const int q = lane & 31;
            const float l0 = __shfl(l[a][0], q & 15), l1 = __shfl(l[a][1], q & 15);
            lsum[a] = (q < 16) ? l0 : l1;
            float mx = 0.f;
#pragma unroll
            for (int r = 0; r < 16; ++r) mx = fmaxf(mx, fabsf(o[a][r]));
            float sm = 0.f;
#pragma unroll
            for (int r = 0; r < 16; ++r) sm += o[a][r];
            bad = bad || !(lsum[a] < 1.2676506e30f) || !(mx < 3.0e38f) || !(sm == sm);
        }
        if (__builtin_expect(__any(bad), 0)) {
            const rsrc_t rq = ws_rsrc(p.ws);
            float mrun[2] = {-INFINITY, -INFINITY}, lrun[2] = {0.f, 0.f};
            o[0] = zero16; o[1] = zero16;
            for (int kt = 0; kt < 64; ++kt) {
                const unsigned ko = (unsigned)WS_K + head * 131072 + kt * 2048 + lane * 16, vo = (unsigned)WS_V + head * 131072 + kt * 2048 + lane * 16;
                const bf16x8 k0 = __builtin_bit_cast(bf16x8, ld16_dev(rq, ko)), k1 = __builtin_bit_cast(bf16x8, ld16_dev(rq, ko + 1024));
                const bf16x8 v0 = __builtin_bit_cast(bf16x8, ld16_dev(rq, vo)), v1 = __builtin_bit_cast(bf16x8, ld16_dev(rq, vo + 1024));
#pragma unroll
                for (int a = 0; a < 2; ++a) {
                    f32x16 s = MFMA32(k0, qf[a][0], zero16); s = MFMA32(k1, qf[a][1], s);
                    float m = s[0];
#pragma unroll
                    for (int r = 1; r < 16; ++r) m = fmaxf(m, s[r]);
                    { const auto rr = __builtin_amdgcn_permlane32_swap(__float_as_uint(m), __float_as_uint(m), false, false); m = fmaxf(__uint_as_float(rr[0]), __uint_as_float(rr[1])); }
                    const float mn = fmaxf(mrun[a], m), alpha = __builtin_amdgcn_exp2f(mrun[a] - mn);
                    mrun[a] = mn;
                    u32x4 pl, ph; float ls = 0.f;
#pragma unroll
                    for (int i = 0; i < 4; ++i) {
                        pl[i] = pk_bf16(__builtin_amdgcn_exp2f(s[2 * i] - mn), __builtin_amdgcn_exp2f(s[2 * i + 1] - mn));
                        ph[i] = pk_bf16(__builtin_amdgcn_exp2f(s[8 + 2 * i] - mn), __builtin_amdgcn_exp2f(s[9 + 2 * i] - mn));
                        ls += (bf_lo(pl[i]) + bf_hi(pl[i])) + (bf_lo(ph[i]) + bf_hi(ph[i])); }
                    lrun[a] = lrun[a] * alpha + ls;
                    o[a] = o[a] * alpha;
                    o[a] = MFMA32(v0, BC(pl), o[a]); o[a] = MFMA32(v1, BC(ph), o[a]);
                }
            }
#pragma unroll
            for (int a = 0; a < 2; ++a) { const auto rr = __builtin_amdgcn_permlane32_swap(__float_as_uint(lrun[a]), __float_as_uint(lrun[a]), false, false); lsum[a] = __uint_as_float(rr[0]) + __uint_as_float(rr[1]); }
        }
    }
    const rsrc_t rs = ws_rsrc(p.ws);
    const float gt0 = __shfl(gval, lane & 31), gt1 = __shfl(gval, 32 + (lane & 31));
#pragma unroll
    for (int a = 0; a < 2; ++a) {
        const int q = lane & 31, hh = lane >> 5;
        const float sc = (a ? gt1 : gt0) * __builtin_amdgcn_rcpf(lsum[a]);
        const int row = q0 + a * 32 + q, panel = row >> 7;
#pragma unroll
        for (int i = 0; i < 4; i += 2) {
            const unsigned a0 = pk_bf16(o[a][4 * i] * sc, o[a][4 * i + 1] * sc), a1 = pk_bf16(o[a][4 * i + 2] * sc, o[a][4 * i + 3] * sc);
            const unsigned b0 = pk_bf16(o[a][4 * i + 4] * sc, o[a][4 * i + 5] * sc), b1 = pk_bf16(o[a][4 * i + 6] * sc, o[a][4 * i + 7] * sc);
            const auto r0 = __builtin_amdgcn_permlane32_swap(a0, b0, false, false); const auto r1 = __builtin_amdgcn_permlane32_swap(a1, b1, false, false);
            const int c = 224 + head * 4 + i + hh;
            st16_wt(rs, (unsigned)WS_ACT + (panel * 352 + c) * 2048 + (row & 127) * 16, (u32x4){r0[0], r1[0], r0[1], r1[1]});
        }
    }
    asm volatile("s_waitcnt vmcnt(0)" ::: "memory");
    GM_BAR();
    if (threadIdx.x == 0) signal(p.ws, F_O + qb * 32 + head, p.tg());
}

__device__ __forceinline__ void gate_rows4(const Ctx& p, int row0, int lane) {
    u32x4 g[2], xr[4][2];
#pragma unroll
    for (int i = 0; i < 2; ++i) g[i] = ((const u32x4*)p.gw)[lane * 2 + i];
#pragma unroll
    for (int r = 0; r < 4; ++r)
#pragma unroll
        for (int i = 0; i < 2; ++i) xr[r][i] = ((const u32x4*)(p.ax + (size_t)(row0 + r) * DM))[lane * 2 + i];
    float s[4];
#pragma unroll
    for (int r = 0; r < 4; ++r) { float a = 0.f, b = 0.f;
#pragma unroll
        for (int i = 0; i < 2; ++i)
#pragma unroll
            for (int e = 0; e < 4; ++e) { a += bf_lo(xr[r][i][e]) * bf_lo(g[i][e]); b += bf_hi(xr[r][i][e]) * bf_hi(g[i][e]); }
        s[r] = a + b; }
#pragma unroll
    for (int r = 0; r < 4; ++r) s[r] = dpp_add<0x128>(s[r]);
#pragma unroll
    for (int r = 0; r < 4; ++r) s[r] = dpp_add<0x124>(s[r]);
#pragma unroll
    for (int r = 0; r < 4; ++r) s[r] = dpp_add<0x122>(s[r]);
#pragma unroll
    for (int r = 0; r < 4; ++r) s[r] = dpp_add<0x121>(s[r]);
#pragma unroll
    for (int r = 0; r < 4; ++r) {
        const auto a = __builtin_amdgcn_permlane16_swap(__float_as_uint(s[r]), __float_as_uint(s[r]), false, false); s[r] = __uint_as_float(a[0]) + __uint_as_float(a[1]);
        const auto b = __builtin_amdgcn_permlane32_swap(__float_as_uint(s[r]), __float_as_uint(s[r]), false, false); s[r] = __uint_as_float(b[0]) + __uint_as_float(b[1]); }
    const float v = lane == 0 ? s[0] : lane == 1 ? s[1] : lane == 2 ? s[2] : s[3];
    if (lane < 4) __hip_atomic_store((gu32*)(p.ws + WS_GATE) + row0 + lane, __float_as_uint(__builtin_amdgcn_rcpf(1.0f + __builtin_amdgcn_exp2f(-1.4426950408889634f * v))), RLX_AGENT);
}
constexpr int LDS_BYTES = CfgO::LDS > CfgQ::LDS + 256 ? CfgO::LDS : CfgQ::LDS + 256;
static_assert(LDS_TAB + 3072 <= LDS_BYTES && CfgO::LDS <= LDS_BYTES && AT_NS * AT_STAGE <= LDS_BYTES, "LDS map");
__global__ __launch_bounds__(512, 2) void fused(Params pp) {
    extern __shared__ __attribute__((aligned(16))) unsigned char smem[]; ldsp lds = (ldsp)smem;
    const int lane = threadIdx.x & 63, wid = __builtin_amdgcn_readfirstlane(threadIdx.x >> 6);
    const int b = blockIdx.x, x = b & 7, role = (b >> 3) & 1, idx = b >> 4;
    Ctx p; static_cast<Params&>(p) = pp;
    p.epv = __hip_atomic_load((gu32*)(pp.ws + WS_CTL) + C_EPOCH, RLX_AGENT);
    STAMP(0, 0);
    if (role == 0) {
        const int pair = 4 * (x >> 1) + (idx >> 2), panel = 4 * (x & 1) + (idx & 3);
        if (wid < 4) qkv_wave<0>(p, lds, pair, panel, wid, lane); else qkv_wave<1>(p, lds, pair, panel, wid, lane);
        STAMP(0, 6);
        const int opanel = 8 * (x & 1) + 4 * ((x >> 1) & 1) + (idx >> 2), oj = 4 * (x >> 2) + (idx & 3);
        attn_wg(p, lds, 4 * x + (idx >> 2), idx & 3, x, idx, oj, pair, panel, wid, lane);
        STAMP(0, 9);
        {
            if (wid < 4) out_part<true, false>(p, lds, opanel, oj, wid, lane); else out_part<false, false>(p, lds, opanel, oj, wid, lane); }
    } else {
        const int panel = 8 * (x & 1) + 4 * ((x >> 1) & 1) + (idx >> 2), j = 4 * (x >> 2) + (idx & 3);
        if (wid < 4) b_wave<true>(p, lds, panel, j, wid, lane); else b_wave<false>(p, lds, panel, j, wid, lane);
        if (panel == 0 && j == 0 && threadIdx.x == 0) __hip_atomic_store((gu32*)(pp.ws + WS_CTL) + C_EPOCH, p.tg(), RLX_AGENT);
    }
    STAMP(0, 15);
}
}

extern "C" void kernel_launch(void* const* d_in, const int* in_sizes, int n_in, void* d_out, int out_size, void* d_ws, size_t ws_size, hipStream_t stream) {
    using namespace gm;
    Params p;
    p.ax = (const u16*)d_in[0]; p.wqkv = (const u16*)d_in[1]; p.wo = (const u16*)d_in[2]; p.bx = (const u16*)d_in[3]; p.win = (const u16*)d_in[4];
    p.wsp = (const u16*)d_in[5]; p.bsp = (const u16*)d_in[6]; p.wout = (const u16*)d_in[7]; p.gw = (const u16*)d_in[8];
    p.out = (u16*)d_out; p.ws = (unsigned char*)d_ws;
    (void)hipFuncSetAttribute((const void*)fused, hipFuncAttributeMaxDynamicSharedMemorySize, LDS_BYTES);
    fused<<<256, 512, LDS_BYTES, stream>>>(p);
}
```

```cpp
#include <hip/hip_runtime.h>
#include <hip/hip_bf16.h>
#include <stdint.h>

#define LAS __attribute__((address_space(3)))
typedef short bf16x8 __attribute__((ext_vector_type(8)));
typedef short s16x4 __attribute__((ext_vector_type(4)));
typedef float f32x2 __attribute__((ext_vector_type(2)));
typedef float f32x4 __attribute__((ext_vector_type(4)));
typedef float f32x16 __attribute__((ext_vector_type(16)));
typedef unsigned u32x4 __attribute__((ext_vector_type(4)));
typedef unsigned u32x2 __attribute__((ext_vector_type(2)));
typedef LAS unsigned char* ldsp;
typedef unsigned short u16;

namespace gm {
constexpr int NA = 16;
constexpr int TF = 5;
constexpr int PFD = 5;
constexpr int SEQ = 2048, DM = 1024, NH = 32, FH = 1792;
constexpr size_t WS_CTL = 0;
constexpr size_t CTL_BYTES = 16384;
constexpr size_t WS_GATE = 16384;
constexpr size_t WS_STAT = 32768;
constexpr size_t WS_Q = 1u << 20;
constexpr size_t WS_K = WS_Q + (4u << 20);
constexpr size_t WS_V = WS_K + (4u << 20);
constexpr size_t WS_Z = WS_V + (4u << 20);
constexpr size_t WS_ACT = WS_Z + (8u << 20);
constexpr int LDS_PFS = 147456 - 0;
constexpr size_t WS_P = WS_ACT + (12u << 20);
constexpr float QSCALE = 0.17677669529663687f * 1.4426950408889634f;

constexpr int C_EPOCH = 0, C_TMO = 64, F_Q = 128  , F_S = F_Q + 512  , F_Z = F_S + 512  ,
              F_U = F_Z + 256  , F_O = F_U + 512  , F_P = F_O + 128  , F_G = F_P + 512  , C_END = F_G + 512;
static_assert(C_END * 4 <= (int)CTL_BYTES, "control block");
#define GM_VM0() __builtin_amdgcn_s_waitcnt(0x0F70)
#define RLX_AGENT __ATOMIC_RELAXED, __HIP_MEMORY_SCOPE_AGENT
typedef __attribute__((address_space(1))) unsigned gu32;
typedef __attribute__((address_space(1))) unsigned long long gu64;
typedef __amdgpu_buffer_rsrc_t rsrc_t;
__device__ __forceinline__ rsrc_t uni_rsrc(const void* base, unsigned bytes) {
    const unsigned long long a = (unsigned long long)(uintptr_t)base;
    const unsigned lo = __builtin_amdgcn_readfirstlane((unsigned)a), hi = __builtin_amdgcn_readfirstlane((unsigned)(a >> 32));
    return __builtin_amdgcn_make_buffer_rsrc((void*)(uintptr_t)(((unsigned long long)hi << 32) | lo), 0, bytes, 0x00020000);
}
__device__ __forceinline__ rsrc_t ws_rsrc(unsigned char* ws) { return uni_rsrc(ws, 64u << 20); }
__device__ __forceinline__ void st16_wt(rsrc_t r, unsigned off, u32x4 v) { __builtin_amdgcn_raw_buffer_store_b128(v, r, off, 0, 16); }
__device__ __forceinline__ void signal(unsigned char* ws, int word, unsigned tag) { __hip_atomic_store((gu32*)(ws + WS_CTL) + word, tag, RLX_AGENT); }
__device__ __forceinline__ void poll_flags(unsigned char* ws, unsigned tag, int w0, int n, bool all128, int wq, int lane) {
    gu32* ctl = (gu32*)(ws + WS_CTL);
    for (unsigned spins = 0;;) {
        bool ok = __hip_atomic_load(ctl + w0 + (lane < n ? lane : 0), RLX_AGENT) == tag;
        if (all128) { const unsigned q0 = __hip_atomic_load(ctl + wq + (lane >> 3) * 32 + (lane & 7), RLX_AGENT), q1 = __hip_atomic_load(ctl + wq + 256 + (lane >> 3) * 32 + (lane & 7), RLX_AGENT); ok = ok && q0 == tag && q1 == tag; }
        if (__all(ok)) break;
        __builtin_amdgcn_s_sleep(2);
        if (++spins > (1u << 19)) { __hip_atomic_store(ctl + C_TMO, tag, RLX_AGENT); break; }
        if ((spins & 255u) == 0u && __builtin_amdgcn_readfirstlane(__hip_atomic_load(ctl + C_TMO, RLX_AGENT)) == tag) break;
    }
}
__device__ __forceinline__ void wait_flags(unsigned char* ws, unsigned tag, int w0, int n, bool all128, int wq, int lane) {
    poll_flags(ws, tag, w0, n, all128, wq, lane);
    GM_VM0();
}
__device__ __forceinline__ u32x4 ld16_dev(rsrc_t r, unsigned off) { return __builtin_amdgcn_raw_buffer_load_b128(r, off, 0, 16); }
enum { X_RAW = 0, X_OWN = 1 };
enum { W_P384 = 0, W_P448 = 1, W_P256 = 2, W_OWN = 3 };

__device__ __forceinline__ unsigned pk_bf16(float lo, float hi) {
    typedef __bf16 bf16x2_t __attribute__((ext_vector_type(2)));
    f32x2 v = {lo, hi}; bf16x2_t b = __builtin_convertvector(v, bf16x2_t); return __builtin_bit_cast(unsigned, b);
}
__device__ __forceinline__ float bf_lo(unsigned w) { return __uint_as_float(w << 16); }
__device__ __forceinline__ float bf_hi(unsigned w) { return __uint_as_float(w & 0xffff0000u); }
__device__ __forceinline__ s16x4 tr_read(ldsp p) { typedef short v4s __attribute__((ext_vector_type(4))); return __builtin_amdgcn_ds_read_tr16_b64_v4i16((LAS v4s*)p); }
template <int I, bool SC = false> __device__ __forceinline__ void glds_run(const char* sbase, unsigned voff) {
    if constexpr (SC) asm volatile("global_load_lds_dwordx4 %0, %1 offset:%2 sc1" ::"v"(voff), "s"(sbase), "n"((I & 3) * 1024) : "memory");
    else asm volatile("global_load_lds_dwordx4 %0, %1 offset:%2" ::"v"(voff), "s"(sbase), "n"((I & 3) * 1024) : "memory");
}
template <int I, bool SC = false> __device__ __forceinline__ void glds_set(const char* sbase, unsigned voff, unsigned dst) {
    if constexpr (SC) asm volatile("s_add_u32 m0, %2, %3\n\ts_nop 0\n\tglobal_load_lds_dwordx4 %0, %1 offset:%4 sc1" ::"v"(voff), "s"(sbase), "s"(dst), "n"((I & ~3) * 1024), "n"((I & 3) * 1024) : "memory", "scc");
    else asm volatile("s_add_u32 m0, %2, %3\n\ts_nop 0\n\tglobal_load_lds_dwordx4 %0, %1 offset:%4" ::"v"(voff), "s"(sbase), "s"(dst), "n"((I & ~3) * 1024), "n"((I & 3) * 1024) : "memory", "scc");
}
__device__ __forceinline__ void pf_lines(const void* base, unsigned voff, unsigned ldsdst) {
    asm volatile("s_mov_b32 m0, %2\n\ts_nop 0\n\tglobal_load_lds_dword %0, %1" ::"v"(voff), "s"(base), "s"(ldsdst) : "memory");
}
#define GM_BAR() do { asm volatile("" ::: "memory"); __builtin_amdgcn_s_barrier(); asm volatile("" ::: "memory"); } while (0)
#define GM_SB() __builtin_amdgcn_sched_barrier(0)
__device__ __forceinline__ bool at_tile(int t, int k) { asm volatile("" : "+s"(t)); return t == k; }

template <int BM_, int BN_, int XI_, int WI_, int NSX_, int NSW_> struct Cfg {
    static constexpr int BM = BM_, BN = BN_, XI = XI_, WI = WI_, NSX = NSX_, NSW = NSW_;
    static constexpr int XB = BM * 128, WB = BN * 128, WOFF = NSX * XB, LDS = NSX * XB + NSW * WB;
    static constexpr int PX = BM / 32, PW = BN / 32;
    static constexpr int WPITCH = (WI == W_P384) ? 384 : (WI == W_P448) ? 448 : (WI == W_P256) ? 256 : 0;
    static constexpr bool XSC = false, WSC = false;
};
template <class C> struct Src { const char* x; const char* w; const char* w2; int ksplit; unsigned xstep, wstep; unsigned xv[C::PX]; unsigned wv[C::PW]; };
template <int WI> __device__ __forceinline__ int w_swz(int r, int x) {
    if (WI == W_P384) return x ^ (((((r >> 1) & 1) | (((r >> 3) & 1) << 1))) << 5);
    if (WI == W_P448) return x ^ (((r >> 3) & 1) << 5);
    if (WI == W_P256) return x ^ (((r & 3) | (((r >> 3) & 1) << 2)) << 5);
    return x;
}
template <class C> __device__ __forceinline__ const char* w_tile(const Src<C>& s, int kt) { return (kt < s.ksplit ? s.w : s.w2) + (unsigned)kt * s.wstep; }
template <class C> __device__ __forceinline__ void set_w2(Src<C>& s, const void* base, int ksplit) { s.ksplit = ksplit; s.w2 = (const char*)base - 4096 - (size_t)ksplit * s.wstep; }
template <class C> __device__ __forceinline__ void src_x_raw(Src<C>& s, const void* base, unsigned pitch, int lid, int lane) {
    s.x = (const char*)base - 4096; s.xstep = 128;
#pragma unroll
    for (int i = 0; i < C::PX; ++i) { const int row = 8 * (lid * C::PX + i) + (lane >> 3); s.xv[i] = row * pitch + (((lane & 7) ^ ((row >> 1) & 7)) * 16) + 4096 - (i & 3) * 1024; }
}
template <class C> __device__ __forceinline__ void src_x_own(Src<C>& s, const void* base, int lid, int lane) {
    s.x = (const char*)base - 4096; s.xstep = C::XB;
#pragma unroll
    for (int i = 0; i < C::PX; ++i) s.xv[i] = (lid * C::PX + i) * 1024 + lane * 16 + 4096 - (i & 3) * 1024;
}
template <class C> __device__ __forceinline__ void src_w_own(Src<C>& s, const void* base, int lid, int lane) {
    s.w = (const char*)base - 4096; s.w2 = s.w; s.ksplit = 1 << 20; s.wstep = C::WB;
#pragma unroll
    for (int i = 0; i < C::PW; ++i) s.wv[i] = (lid * C::PW + i) * 1024 + lane * 16 + 4096 - (i & 3) * 1024;
}
template <class C> __device__ __forceinline__ void src_w_raw(Src<C>& s, const void* base, unsigned pitch, unsigned segstride, int lid, int lane) {
    s.w = (const char*)base - 4096; s.w2 = s.w; s.ksplit = 1 << 20; s.wstep = 64 * pitch;
#pragma unroll
    for (int i = 0; i < C::PW; ++i) { const int o = 1024 * (lid * C::PW + i) + 16 * lane; const int r = o / C::WPITCH, x = w_swz<C::WI>(r, o % C::WPITCH);
        s.wv[i] = r * pitch + (x >> 7) * segstride + (x & 127) + 4096 - (i & 3) * 1024; }
}
template <class C, int I0, int I1, bool FIRST> __device__ __forceinline__ void stage_rng(unsigned dx, unsigned dw, const Src<C>& s, const char* xb, const char* wb) {
    if constexpr (I0 < I1) {
        if constexpr (I0 < C::PW) { if constexpr (FIRST || (I0 & 3) == 0) glds_set<I0, C::WSC>(wb, s.wv[I0], dw); else glds_run<I0, C::WSC>(wb, s.wv[I0]); }
        else { constexpr int j = I0 - C::PW; if constexpr (FIRST || (j & 3) == 0) glds_set<j, C::XSC>(xb, s.xv[j], dx); else glds_run<j, C::XSC>(xb, s.xv[j]); }
        stage_rng<C, I0 + 1, I1, false>(dx, dw, s, xb, wb);
    }
}
template <int MT_, int NT_, int N0_, unsigned SWAP_, bool LD_> struct Wv { static constexpr int MT = MT_, NT = NT_, N0 = N0_; static constexpr unsigned SWAP = SWAP_; static constexpr bool LD = LD_; };
template <class W> struct Frag { bf16x8 x[W::MT]; s16x4 wl[W::NT], wh[W::NT]; };
template <class C, class W> struct RdAddr { unsigned x[2]; unsigned w[(C::WI == W_P384) ? 4 : (C::WI == W_P256) ? W::NT : 2]; };
template <class C, class W> __device__ __forceinline__ void rd_init(RdAddr<C, W>& a, int m0, int n0, int lane) {
    const int fr = lane & 15, fq = lane >> 4, g = lane >> 4, q = (lane & 15) >> 2, p = lane & 3;
    if (C::XI == X_RAW) { for (int kk = 0; kk < 2; ++kk) a.x[kk] = (m0 + fr) * 128 + (((kk * 4 + fq) ^ (fr >> 1)) * 16); }
    else { for (int kk = 0; kk < 2; ++kk) a.x[kk] = (kk * 4 + fq) * (C::BM * 16) + (m0 + fr) * 16; }
    if (C::WI == W_P384) { const int s = ((q >> 1) & 1) | ((g & 1) << 1);
        for (int c = 0; c < 4; ++c) a.w[c] = C::WOFF + (8 * g + q) * 384 + ((c ^ s) * 32) + p * 8; }
    else if (C::WI == W_P448) { const int s = g & 1;
        for (int c = 0; c < 2; ++c) a.w[c] = C::WOFF + (8 * g + q) * 448 + (((c ^ s) - c) * 32) + p * 8; }
    else if (C::WI == W_P256) { const int s = (q & 3) | ((g & 1) << 2);
        for (int t = 0; t < W::NT; ++t) a.w[t] = C::WOFF + (8 * g + q) * 256 + (((n0 + t) ^ s) * 32) + p * 8; }
    else { for (int h = 0; h < 2; ++h) a.w[h] = C::WOFF + (p >> 1) * 1024 + (8 * g + 4 * (h ^ (p >> 1)) + q) * 16 + (p & 1) * 8 + W::N0 * 2048; }
}
template <class C, class W> __device__ __forceinline__ s16x4 rd_w(ldsp b, const RdAddr<C, W>& a, int t, int kk, int h) {
    const int c = W::N0 + t;
    if (C::WI == W_P384) return tr_read(b + a.w[c & 3] + (c >> 2) * 128 + kk * 32 * 384 + h * 4 * 384);
    else if (C::WI == W_P448) return tr_read(b + a.w[c & 1] + c * 32 + kk * 32 * 448 + h * 4 * 448);
    else if (C::WI == W_P256) return tr_read(b + a.w[t] + kk * 32 * 256 + h * 4 * 256);
    else return tr_read(b + a.w[h] + t * 2048 + kk * 512);
}
template <class C, class W> __device__ __forceinline__ void rd_one(Frag<W>& f, ldsp bx, ldsp bw, const RdAddr<C, W>& a, int kk, int r) {
    if (r < 2) { if (r == 0) f.wl[0] = rd_w<C, W>(bw, a, 0, kk, 0); else f.wh[0] = rd_w<C, W>(bw, a, 0, kk, 1); }
    else if (r < 2 + W::MT) { const int m = r - 2; f.x[m] = *(const LAS bf16x8*)(bx + a.x[kk] + m * ((C::XI == X_RAW) ? 2048 : 256)); }
    else { const int u = r - W::MT; const int t = u >> 1; if (u & 1) f.wh[t] = rd_w<C, W>(bw, a, t, kk, 1); else f.wl[t] = rd_w<C, W>(bw, a, t, kk, 0); }
}
template <class W> __device__ __forceinline__ void mma_one(f32x4 (&acc)[W::NT][W::MT], const Frag<W>& f, int i) {
    const int t = i / W::MT, m = i % W::MT;
    const bf16x8 w = __builtin_shufflevector(f.wl[t], f.wh[t], 0, 1, 2, 3, 4, 5, 6, 7);
    acc[t][m] = ((W::SWAP >> t) & 1) ? __builtin_amdgcn_mfma_f32_16x16x32_bf16(f.x[m], w, acc[t][m], 0, 0, 0)
                                     : __builtin_amdgcn_mfma_f32_16x16x32_bf16(w, f.x[m], acc[t][m], 0, 0, 0);
}
template <int RFRONT_, int VA_, int RF1_ = RFRONT_> struct Sch { static constexpr int RFRONT = RFRONT_, VA = VA_, RF1 = RF1_; };
template <class C, class W, class S, int RF, int V0, int V1, int I>
__device__ __forceinline__ void step_slot(f32x4 (&acc)[W::NT][W::MT], const Frag<W>& fc, Frag<W>& fn, ldsp bx, ldsp bw, const RdAddr<C, W>& a, int ksn,
                                          const Src<C>& s, const char* xb, const char* wb, unsigned dx, unsigned dw) {
    constexpr int NM = W::NT * W::MT, NR = 2 * W::NT + W::MT, NV = V1 - V0;
    constexpr int RS = (NM * RF / 16) < 1 ? 1 : (NM * RF / 16);
    if constexpr (I < NM) {
        mma_one<W>(acc, fc, I);
        if constexpr (I < RS) {
#pragma unroll
            for (int r = I * NR / RS; r < (I + 1) * NR / RS; ++r) rd_one<C, W>(fn, bx, bw, a, ksn, r);
        }
        if constexpr (W::LD) {
            constexpr int v0 = V0 + I * NV / NM, v1 = V0 + (I + 1) * NV / NM;
            stage_rng<C, v0, v1, v0 == V0>(dx, dw, s, xb, wb);
        }
        GM_SB();
        step_slot<C, W, S, RF, V0, V1, I + 1>(acc, fc, fn, bx, bw, a, ksn, s, xb, wb, dx, dw);
    }
}
template <class C, class W, class S, class H>
__device__ __forceinline__ void gemm_loop(ldsp lds, const Src<C>& s, int nt, f32x4 (&acc)[W::NT][W::MT], int lid, int m0, int n0, int lane, H& hook) {
    constexpr int PX = C::PX, PW = C::PW, NSX = C::NSX, NSW = C::NSW, NV = PX + PW;
    static_assert(S::VA >= PW && S::VA <= NV, "all W pieces go out in the step behind the barrier");
    static_assert(NSX >= NSW && NSW >= 2, "ring depths");
    RdAddr<C, W> a; rd_init<C, W>(a, m0, n0, lane);
    const unsigned ldb = (unsigned)(uintptr_t)lds;
    const unsigned lx0 = ldb + lid * PX * 1024, lw0 = ldb + C::WOFF + lid * PW * 1024;
    Frag<W> f0, f1;
    const char* wb = w_tile<C>(s, NSW - 1); const char* xb = s.x + (size_t)(NSX - 1) * s.xstep;
    unsigned dw = lw0 + (NSW - 1) * C::WB, dx = lx0 + (NSX - 1) * C::XB;
    if constexpr (H::PRE) {
        if constexpr (W::LD) {
#pragma unroll
            for (int i = 0; i < NSW; ++i) stage_rng<C, 0, PW, true>(0, lw0 + i * C::WB, s, nullptr, w_tile<C>(s, i));
        }
        hook.pre_x();
        if constexpr (W::LD) {
#pragma unroll
            for (int i = 0; i < NSX - 1; ++i) stage_rng<C, PW, NV, true>(lx0 + i * C::XB, 0, s, s.x + (size_t)i * s.xstep, nullptr);
            if constexpr (S::VA > PW) stage_rng<C, PW, S::VA, true>(dx, dw, s, xb, wb);
            asm volatile("s_waitcnt vmcnt(%0)" ::"n"(S::VA - PW) : "memory");
        }
    } else if constexpr (W::LD) {
#pragma unroll
        for (int i = 0; i < NSX - 1; ++i) {
            if (i < NSW - 1) stage_rng<C, 0, PW, true>(0, lw0 + i * C::WB, s, nullptr, w_tile<C>(s, i));
            stage_rng<C, PW, NV, true>(lx0 + i * C::XB, 0, s, s.x + (size_t)i * s.xstep, nullptr);
        }
        stage_rng<C, 0, S::VA, true>(dx, dw, s, xb, wb);
        constexpr int Y = (NSX - 2) * PX + (NSW - 2) * PW + S::VA;
        asm volatile("s_waitcnt vmcnt(%0)" ::"n"(Y) : "memory");
    }
    GM_BAR();
#pragma unroll
    for (int r = 0; r < 2 * W::NT + W::MT; ++r) rd_one<C, W>(f0, lds, lds, a, 0, r);
    int bx = 0, bw = 0, kx = NSX - 1, kw = NSW - 1;
    __builtin_amdgcn_s_waitcnt(0xC07F);
    for (int t = 0; t < nt; ++t) {
        ldsp cx = lds + bx * C::XB, cw = lds + bw * C::WB;
        const int nbx = (bx + 1 == NSX) ? 0 : bx + 1, nbw = (bw + 1 == NSW) ? 0 : bw + 1;
        ldsp nx = lds + nbx * C::XB, nw = lds + nbw * C::WB;
        GM_SB();
#define GM_STEP(FC, FN, BX, BW, KSN, RF, V0, V1) step_slot<C, W, S, RF, V0, V1, 0>(acc, FC, FN, BX, BW, a, KSN, s, xb, wb, dx, dw)
        GM_STEP(f0, f1, cx, cw, 1, S::RFRONT, S::VA, NV);
        hook.at(t);
        asm volatile("s_waitcnt lgkmcnt(0)" ::: "memory");
        if constexpr (W::LD) {
            constexpr int pw = 1 - NSW, px = 1 - NSX, last = (pw > px) ? pw : px, nper = (-1) - last, Y = nper * NV + ((pw > px) ? PX : 0);
            asm volatile("s_waitcnt vmcnt(%0)" ::"n"(Y) : "memory");
        }
        GM_BAR();
        GM_SB();
        if constexpr (W::LD) {
            kx = (kx + 1 < nt) ? kx + 1 : kx; kw = (kw + 1 < nt) ? kw + 1 : kw;
            xb = s.x + (size_t)kx * s.xstep; wb = w_tile<C>(s, kw);
            dx = lx0 + bx * C::XB; dw = lw0 + bw * C::WB;
        }
        GM_STEP(f1, f0, nx, nw, 0, S::RF1, 0, S::VA);
#undef GM_STEP
        bx = nbx; bw = nbw;
    }
    GM_SB();
    asm volatile("s_waitcnt vmcnt(0) lgkmcnt(0)" ::: "memory");
    GM_BAR();
}
template <class W> __device__ __forceinline__ void acc_zero(f32x4 (&acc)[W::NT][W::MT]) {
#pragma unroll
    for (int t = 0; t < W::NT; ++t)
#pragma unroll
        for (int m = 0; m < W::MT; ++m) acc[t][m] = (f32x4){0.f, 0.f, 0.f, 0.f};
}
__device__ __forceinline__ u32x4 pair16(const f32x4& A, const f32x4& B) {
    const unsigned a0 = pk_bf16(A[0], A[1]), a1 = pk_bf16(A[2], A[3]), b0 = pk_bf16(B[0], B[1]), b1 = pk_bf16(B[2], B[3]);
    const auto r0 = __builtin_amdgcn_permlane16_swap(a0, b0, false, false); const auto r1 = __builtin_amdgcn_permlane16_swap(a1, b1, false, false);
    return (u32x4){r0[0], r1[0], r0[1], r1[1]};
}

namespace g32 {
enum { W_P448 = 1, W_P256 = 2, W_OWNT = 3 };
template <int BM_, int BN_, int XI_, int WI_, int NS_, int NL_ = 4> struct Cfg {
    static constexpr int BM = BM_, BN = BN_, XI = XI_, WI = WI_, NS = NS_, NL = NL_;
    static constexpr int XB = BM * 128, WB = BN * 128, WOFF = NS * XB, LDS = NS * (XB + WB);
    static constexpr int PX = BM / 8 / NL, PW = BN / 8 / NL;
    static constexpr int WPITCH = (WI == W_P448) ? 448 : (WI == W_P256) ? 256 : 16;
    static constexpr bool XSC = (XI == X_OWN), WSC = (WI == W_OWNT);
};
template <class C> __device__ __forceinline__ void src_w_raw(Src<C>& s, const void* base, unsigned pitch, int lid, int lane) {
    s.w = (const char*)base - 4096; s.w2 = s.w; s.ksplit = 1 << 20; s.wstep = 64 * pitch;
#pragma unroll
    for (int i = 0; i < C::PW; ++i) { const int o = 1024 * (lid * C::PW + i) + 16 * lane; const int r = o / C::WPITCH; int x = o % C::WPITCH;
        if (C::WI == W_P256) x ^= (r & 3) << 6;
        s.wv[i] = r * pitch + x + 4096 - (i & 3) * 1024; }
}
template <int MT_, int NT_, bool LD_> struct Wv { static constexpr int MT = MT_, NT = NT_; static constexpr bool LD = LD_; };
template <class C, class W> struct Frag { bf16x8 x[W::MT]; s16x4 wl[W::NT], wh[W::NT]; };
template <class C, class W> struct RdAddr { unsigned x[(C::XI == X_RAW) ? 4 : 1]; unsigned w[(C::WI == W_P256) ? W::NT : 1]; };
template <class C, class W> __device__ __forceinline__ void rd_init(RdAddr<C, W>& a, int m0, int n0  , int lane) {
    const int r = lane & 31, h = lane >> 5, g = lane >> 4, q = (lane & 15) >> 2, p = lane & 3;
    if (C::XI == X_RAW) { for (int ks = 0; ks < 4; ++ks) a.x[ks] = (m0 + r) * 128 + (((2 * ks + h) ^ ((r >> 1) & 7)) * 16); }
    else a.x[0] = h * (C::BM * 16) + (m0 + r) * 16;
    const int rowp = 8 * (g >> 1) + q, colp = (g & 1) * 32 + p * 8;
    if (C::WI == W_P448) a.w[0] = C::WOFF + rowp * 448 + colp + n0 * 64;
    else if (C::WI == W_P256) { for (int t = 0; t < W::NT; ++t) a.w[t] = C::WOFF + rowp * 256 + (((n0 + t) ^ q) * 64) + colp; }
    else a.w[0] = C::WOFF + h * (C::BN * 16) + (n0 * 32 + r) * 16;
}
template <class C, class W> constexpr int n_reads() { return W::MT + (C::WI == W_OWNT ? W::NT : 2 * W::NT); }
template <class C, class W> __device__ __forceinline__ void rd_one(Frag<C, W>& f, ldsp bx, ldsp bw, const RdAddr<C, W>& a, int ks, int r) {
    constexpr int PER = (C::WI == W_OWNT) ? 1 : 2;
    if (r >= PER && r < PER + W::MT) { const int m = r - PER;
        if (C::XI == X_RAW) f.x[m] = *(const LAS bf16x8*)(bx + a.x[ks] + m * 4096);
        else f.x[m] = *(const LAS bf16x8*)(bx + a.x[0] + ks * (2 * C::BM * 16) + m * 512);
        return; }
    const int u = r < PER ? r : r - W::MT, t = u / PER, hh = u % PER;
    if (C::WI == W_OWNT) { const bf16x8 v = *(const LAS bf16x8*)(bw + a.w[0] + ks * (2 * C::BN * 16) + t * 512); f.wl[t] = __builtin_shufflevector(v, v, 0, 1, 2, 3); f.wh[t] = __builtin_shufflevector(v, v, 4, 5, 6, 7); }
    else { s16x4 v;
        if (C::WI == W_P448) v = tr_read(bw + a.w[0] + t * 64 + (ks * 16 + hh * 4) * 448);
        else v = tr_read(bw + a.w[t] + (ks * 16 + hh * 4) * 256);
        if (hh) f.wh[t] = v; else f.wl[t] = v; }
}
template <class C, class W> __device__ __forceinline__ void rd_frags(Frag<C, W>& f, ldsp bx, ldsp bw, const RdAddr<C, W>& a, int ks) {
#pragma unroll
    for (int r = 0; r < n_reads<C, W>(); ++r) rd_one<C, W>(f, bx, bw, a, ks, r);
}
template <class C, class W, bool SWAP  > __device__ __forceinline__ void mma_one(f32x16 (&acc)[W::NT][W::MT], const Frag<C, W>& f, int i) {
    const int t = i / W::MT, m = i % W::MT;
    const bf16x8 w = __builtin_shufflevector(f.wl[t], f.wh[t], 0, 1, 2, 3, 4, 5, 6, 7);
    acc[t][m] = SWAP ? __builtin_amdgcn_mfma_f32_32x32x16_bf16(w, f.x[m], acc[t][m], 0, 0, 0) : __builtin_amdgcn_mfma_f32_32x32x16_bf16(f.x[m], w, acc[t][m], 0, 0, 0);
}
struct NoFill { template <int S> __device__ __forceinline__ void chunk() {} };
template <class C, class W, bool SWAP, int V0, int V1, int BASE, int I, class F>
__device__ __forceinline__ void step_slot(f32x16 (&acc)[W::NT][W::MT], const Frag<C, W>& fc, Frag<C, W>& fn, ldsp bx, ldsp bw, const RdAddr<C, W>& a, int ksn,
                                          const Src<C>& s, const char* xb, const char* wb, unsigned dx, unsigned dw, F& fill) {
    constexpr int NM = W::NT * W::MT, NR = n_reads<C, W>(), NV = V1 - V0;
    if constexpr (I < NM) {
        mma_one<C, W, SWAP>(acc, fc, I);
#pragma unroll
        for (int r = I * NR / NM; r < (I + 1) * NR / NM; ++r) rd_one<C, W>(fn, bx, bw, a, ksn, r);
        if constexpr (W::LD) { constexpr int v0 = V0 + I * NV / NM, v1 = V0 + (I + 1) * NV / NM; stage_rng<C, v0, v1, v0 == V0>(dx, dw, s, xb, wb); }
        fill.template chunk<BASE + I>();
        GM_SB();
        step_slot<C, W, SWAP, V0, V1, BASE, I + 1, F>(acc, fc, fn, bx, bw, a, ksn, s, xb, wb, dx, dw, fill);
    }
}
template <class C, class WF  , int D> struct Pipe {
    static constexpr int PX = C::PX, PW = C::PW, NS = C::NS, NV = PX + PW;
    static constexpr int Q1 = (NV + 3) / 4, Q2 = (NV + 1) / 2, Q3 = (3 * NV + 3) / 4;
    static constexpr int PRE = (D == 1) ? Q1 : Q2;
    ldsp lds; int nt, xmod;
    RdAddr<C, WF> a; unsigned lx0, lw0;
    Frag<C, WF> f0, f1, f2, f3;
    __device__ __forceinline__ Pipe(ldsp lds_, const Src<C>& s, int nt_, int xmod_, int lid, int m0, int n0, int lane) : lds(lds_), nt(nt_), xmod(xmod_) {
        rd_init<C, WF>(a, m0, n0, lane);
        const unsigned ldb = (unsigned)(uintptr_t)lds;
        lx0 = ldb + lid * PX * 1024; lw0 = ldb + C::WOFF + lid * PW * 1024;
    }
    __device__ __forceinline__ void issue_x(const Src<C>& s) {
        if constexpr (WF::LD) {
#pragma unroll
            for (int i = 0; i < NS - 1; ++i) stage_rng<C, PW, NV, true>(lx0 + i * C::XB, 0, s, s.x + (size_t)i * s.xstep, nullptr);
        }
    }
    __device__ __forceinline__ void issue_w(const Src<C>& s) {
        static_assert(PRE <= PW, "prologue split");
        if constexpr (WF::LD) {
#pragma unroll
            for (int i = 0; i < NS - 1; ++i) stage_rng<C, 0, PW, true>(0, lw0 + i * C::WB, s, nullptr, w_tile<C>(s, i));
            stage_rng<C, 0, PRE, true>(0, lw0 + (NS - 1) * C::WB, s, nullptr, w_tile<C>(s, NS - 1));
        }
    }
    __device__ __forceinline__ void landed() {
        if constexpr (WF::LD) asm volatile("s_waitcnt vmcnt(0)" ::: "memory");
        GM_BAR();
        rd_frags<C, WF>(f0, lds, lds, a, 0);
        if (D == 2) rd_frags<C, WF>(f1, lds, lds, a, 1);
    }
    __device__ __forceinline__ void begin(const Src<C>& s) {
        if constexpr (WF::LD) {
#pragma unroll
            for (int i = 0; i < NS - 1; ++i) stage_rng<C, 0, NV, true>(lx0 + i * C::XB, lw0 + i * C::WB, s, s.x + (size_t)i * s.xstep, w_tile<C>(s, i));
            stage_rng<C, 0, PRE, true>(0, lw0 + (NS - 1) * C::WB, s, nullptr, w_tile<C>(s, NS - 1));
            asm volatile("s_waitcnt vmcnt(%0)" ::"n"((NS - 2) * NV + PRE) : "memory");
        }
        GM_BAR();
        rd_frags<C, WF>(f0, lds, lds, a, 0);
        if (D == 2) rd_frags<C, WF>(f1, lds, lds, a, 1);
    }
    template <bool SWAP, class H> __device__ __forceinline__ void run(const Src<C>& s, int t0, int t1, f32x16 (&acc)[WF::NT][WF::MT], H& hook) {
        NoFill nf; run_fill<SWAP>(s, t0, t1, t0, acc, hook, nf);
    }
    template <bool SWAP, class H, class F> __device__ __forceinline__ void run_fill(const Src<C>& s, int t0, int t1, int tb, f32x16 (&acc)[WF::NT][WF::MT], H& hook, F& fill) {
        using W = WF; constexpr int NM = WF::MT * WF::NT;
        int sl = t0 % NS, kn = t0 - 1 + NS; kn = kn < nt ? kn : nt - 1;
        const char* xb = s.x + (unsigned)(kn & (xmod - 1)) * s.xstep; const char* wb = w_tile<C>(s, kn);
        unsigned dx = lx0 + ((sl == 0) ? NS - 1 : sl - 1) * C::XB, dw = lw0 + ((sl == 0) ? NS - 1 : sl - 1) * C::WB;
        __builtin_amdgcn_s_waitcnt(0xC07F);
        for (int t = t0; t < t1; ++t) {
            const int ns = (sl + 1 == NS) ? 0 : sl + 1;
            ldsp cx = lds + sl * C::XB, cw = lds + sl * C::WB, nx = lds + ns * C::XB, nw = lds + ns * C::WB;
            hook.top(t - tb);
            GM_SB();
#define G32_STEP(FC, FN, BX, BW, KSN, V0, V1, ST) step_slot<C, W, SWAP, V0, V1, ST * NM, 0, F>(acc, FC, FN, BX, BW, a, KSN, s, xb, wb, dx, dw, fill)
            if constexpr (D == 1) { G32_STEP(f0, f1, cx, cw, 1, Q1, Q2, 0); G32_STEP(f1, f2, cx, cw, 2, Q2, Q3, 1); G32_STEP(f2, f3, cx, cw, 3, Q3, NV, 2); }
            else { G32_STEP(f0, f2, cx, cw, 2, Q2, Q3, 0); G32_STEP(f1, f3, cx, cw, 3, Q3, NV, 1); }
            hook.at(t - tb);
            asm volatile("s_waitcnt lgkmcnt(0)" ::: "memory");
            if constexpr (W::LD) asm volatile("s_waitcnt vmcnt(%0)" ::"n"((NS - 2) * NV) : "memory");
            GM_BAR();
            GM_SB();
            if constexpr (W::LD) { kn = (kn + 1 < nt) ? kn + 1 : kn; xb = s.x + (unsigned)(kn & (xmod - 1)) * s.xstep; wb = w_tile<C>(s, kn); dx = lx0 + sl * C::XB; dw = lw0 + sl * C::WB; }
            if constexpr (D == 1) { G32_STEP(f3, f0, nx, nw, 0, 0, Q1, 3); }
            else { G32_STEP(f2, f0, nx, nw, 0, 0, Q1, 2); G32_STEP(f3, f1, nx, nw, 1, Q1, Q2, 3); }
#undef G32_STEP
            sl = ns;
        }
        GM_SB();
    }
    __device__ __forceinline__ void end() {
        asm volatile("s_waitcnt vmcnt(0) lgkmcnt(0)" ::: "memory");
        GM_BAR();
    }
};
template <class C, class WF> struct PipeKS {
    static constexpr int PX = C::PX, PW = C::PW, NS = C::NS, NV = PX + PW, NM = WF::MT * WF::NT;
    ldsp lds; int nt, kh;
    RdAddr<C, WF> a; unsigned lx0, lw0;
    Frag<C, WF> a0, a1, b0, b1;
    __device__ __forceinline__ PipeKS(ldsp lds_, int nt_, int kh_, int lid, int m0, int n0, int lane) : lds(lds_), nt(nt_), kh(kh_) {
        rd_init<C, WF>(a, m0, n0, lane);
        static_assert(C::XI == X_OWN && C::WI == W_P256, "layouts of the output GEMM");
        a.x[0] += kh * 2 * (2 * C::BM * 16);
#pragma unroll
        for (int t = 0; t < WF::NT; ++t) a.w[t] += kh * 32 * 256;
        const unsigned ldb = (unsigned)(uintptr_t)lds;
        lx0 = ldb + lid * PX * 1024; lw0 = ldb + C::WOFF + lid * PW * 1024;
    }
    __device__ __forceinline__ void issue_w(const Src<C>& s) {
        if constexpr (WF::LD) {
#pragma unroll
            for (int i = 0; i < NS; ++i) stage_rng<C, 0, PW, true>(0, lw0 + i * C::WB, s, nullptr, w_tile<C>(s, i)); }
    }
    __device__ __forceinline__ void issue_x(const Src<C>& s) {
        if constexpr (WF::LD) {
#pragma unroll
            for (int i = 0; i < NS; ++i) stage_rng<C, PW, NV, true>(lx0 + i * C::XB, 0, s, s.x + (size_t)i * s.xstep, nullptr); }
    }
    __device__ __forceinline__ void landed() {
        if constexpr (WF::LD) asm volatile("s_waitcnt vmcnt(0)" ::: "memory");
        GM_BAR();
        rd_frags<C, WF>(a0, lds, lds, a, 0); rd_frags<C, WF>(a1, lds, lds, a, 1);
    }
    template <class H> __device__ __forceinline__ void run(const Src<C>& s, f32x16 (&acc)[WF::NT][WF::MT], H& hook) {
        using W = WF; NoFill fill; using F = NoFill;
        int sl = 0, kn = NS;
        __builtin_amdgcn_s_waitcnt(0xC07F);
#define KS_PERIOD(T, C0, C1, N0, N1) do { \
            hook.at(T); \
            asm volatile("s_waitcnt lgkmcnt(0)" ::: "memory"); \
            if constexpr (W::LD) asm volatile("s_waitcnt vmcnt(%0)" ::"n"((NS - 2) * NV) : "memory"); \
            GM_BAR(); GM_SB(); \
            const int ns = (sl + 1 == NS) ? 0 : sl + 1, kc = kn < nt ? kn : nt - 1; \
            const char* xb = s.x + (unsigned)kc * s.xstep; const char* wb = w_tile<C>(s, kc); \
            const unsigned dx = lx0 + sl * C::XB, dw = lw0 + sl * C::WB; \
            ldsp nx = lds + ns * C::XB, nw = lds + ns * C::WB; \
            step_slot<C, W, true, 0, NV / 2, 0, 0, F>(acc, C0, N0, nx, nw, a, 0, s, xb, wb, dx, dw, fill); \
            step_slot<C, W, true, NV / 2, NV, NM, 0, F>(acc, C1, N1, nx, nw, a, 1, s, xb, wb, dx, dw, fill); \
            sl = ns; ++kn; } while (0)
        for (int t = 0; t < nt; t += 2) { KS_PERIOD(t, a0, a1, b0, b1); if (t + 1 >= nt) break; KS_PERIOD(t + 1, b0, b1, a0, a1); }
#undef KS_PERIOD
        GM_SB();
        asm volatile("s_waitcnt vmcnt(0) lgkmcnt(0)" ::: "memory");
        GM_BAR();
    }
};
template <class W> __device__ __forceinline__ void acc_zero(f32x16 (&acc)[W::NT][W::MT]) {
#pragma unroll
    for (int t = 0; t < W::NT; ++t)
#pragma unroll
        for (int m = 0; m < W::MT; ++m) acc[t][m] = (f32x16){};
}
__device__ __forceinline__ u32x4 pair32(unsigned a0, unsigned a1, unsigned b0, unsigned b1) {
    const auto r0 = __builtin_amdgcn_permlane32_swap(a0, b0, false, false); const auto r1 = __builtin_amdgcn_permlane32_swap(a1, b1, false, false);
    return (u32x4){r0[0], r1[0], r0[1], r1[1]};
}
}

struct Params {
    const u16* ax; const u16* wqkv; const u16* wo; const u16* bx; const u16* win; const u16* wsp; const u16* bsp; const u16* wout; const u16* gw;
    u16* out; unsigned char* ws;
};
struct Ctx : Params { unsigned epv; __device__ __forceinline__ unsigned tg() const { return (unsigned)__builtin_amdgcn_readfirstlane(epv) + 1u; } };

using CfgQ = Cfg<256, 192, X_RAW, W_P384, 3, 2>;
struct AheadHook {
    static constexpr bool PRE = false;
    int wid, lane, nt; unsigned scratch;
    const char* b0; unsigned v0, step0;
    const char* b1; unsigned v1, step1;
    __device__ __forceinline__ void pre_x() const {}
    __device__ __forceinline__ void top(int) const {}
    __device__ __forceinline__ void first() const {
        if (wid >= 4) for (int k = 3 + ((wid - 3) & 3); k < PFD; k += 4) { pf_lines(b0, v0 + k * step0, scratch); if (b1) pf_lines(b1, v1 + k * step1, scratch); }
    }
    __device__ __forceinline__ void at(int t) const {
        if (wid == 4 + (t & 3) && t + PFD < nt) { pf_lines(b0, v0 + (t + PFD) * step0, scratch); if (b1) pf_lines(b1, v1 + (t + PFD) * step1, scratch); }
    }
};
template <int WN> __device__ __forceinline__ void qkv_wave(const Ctx& p, ldsp lds, int pair, int panel, int wid, int lane) {
    using C = CfgQ; using W = Wv<4, 6, WN * 6, WN ? 0x3Cu : 0u, WN == 0>;
    const int lid = wid & 3, m0 = (wid & 3) * 64;
    Src<C> s;
    src_x_raw<C>(s, p.ax + (size_t)panel * 256 * DM, DM * 2, lid, lane);
    src_w_raw<C>(s, p.wqkv + pair * 64, 3 * DM * 2, DM * 2, lid, lane);
    f32x4 acc[6][4]; acc_zero<W>(acc);
    const int wl = lane < 48 ? lane : 47;
    AheadHook hk{wid, lane, DM / 64, (unsigned)(uintptr_t)lds + LDS_PFS,
                 (const char*)(p.wqkv + pair * 64), (unsigned)(((panel & 3) * 16 + wl / 3) * (3 * DM * 2) + (wl % 3) * (DM * 2)), 64u * 3 * DM * 2,
                 nullptr, 0u, 0u};
    hk.first();
    gemm_loop<C, W, Sch<12, 7>>(lds, s, DM / 64, acc, lid, m0, 0, lane, hk);
    const int fr = lane & 15, fq = lane >> 4;
    const int row0 = panel * 256 + m0;
    const rsrc_t rs = ws_rsrc(p.ws);
#pragma unroll
    for (int t = 0; t < 6; t += 2) {
        const int T = WN * 6 + t, kind = T >> 2, head = pair * 2 + ((T >> 1) & 1);
        if (kind < 2) {
            const unsigned base = (unsigned)(kind == 0 ? WS_Q : WS_K);
            const int c = 2 * (fq & 1) + (fq >> 1);
            const float sc = kind == 0 ? QSCALE : 1.0f;
#pragma unroll
            for (int m = 0; m < 4; ++m) { const int row = row0 + m * 16 + fr;
                st16_wt(rs, base + ((head * 64 + (row >> 5)) * 4 + c) * 512 + (row & 31) * 16, pair16(acc[t][m] * sc, acc[t + 1][m] * sc)); }
        } else {
#pragma unroll
            for (int tt = 0; tt < 2; ++tt)
#pragma unroll
                for (int m = 0; m < 4; m += 2) {
                    const f32x4 A = acc[t + tt][m], B = acc[t + tt][m + 1];
                    const unsigned a0 = pk_bf16(A[0], A[1]), a1 = pk_bf16(A[2], A[3]), b0 = pk_bf16(B[0], B[1]), b1 = pk_bf16(B[2], B[3]);
                    const auto r0 = __builtin_amdgcn_permlane32_swap(a0, b0, false, false); const auto r1 = __builtin_amdgcn_permlane32_swap(a1, b1, false, false);
                    const int kb = (row0 >> 4) + m + (fq >> 1), h = fq & 1, d = tt * 16 + fr;
                    st16_wt(rs, (unsigned)WS_V + ((head * 128 + kb) * 2 + h) * 512 + d * 16, (u32x4){r0[0], r1[0], r0[1], r1[1]});
                }
        }
    }
    asm volatile("s_waitcnt vmcnt(0)" ::: "memory");
    GM_BAR();
    if (threadIdx.x == 0) signal(p.ws, F_Q + pair * 32 + panel, p.tg());
}

using CfgZ = g32::Cfg<128, 224, X_RAW, g32::W_P448, 3>;
using CfgS = g32::Cfg<128, 224, X_RAW, g32::W_OWNT, 3>;
using CfgO = g32::Cfg<128, 128, X_OWN, g32::W_P256, 5>;
template <int N> __device__ __forceinline__ void gelu_pairs(unsigned* z) {
    constexpr float C1 = -0.044715f * 2.0f * 0.7978845608028654f * 1.4426950408889634f, C2 = -2.0f * 0.7978845608028654f * 1.4426950408889634f;
    f32x2 x[N], e[N];
#pragma unroll
    for (int i = 0; i < N; ++i) x[i] = (f32x2){bf_lo(z[i]), bf_hi(z[i])};
#pragma unroll
    for (int i = 0; i < N; ++i) e[i] = x[i] * (x[i] * x[i] * C1 + C2);
    GM_SB();
#pragma unroll
    for (int i = 0; i < N; ++i) { e[i].x = __builtin_amdgcn_exp2f(e[i].x); e[i].y = __builtin_amdgcn_exp2f(e[i].y); }
    GM_SB();
#pragma unroll
    for (int i = 0; i < N; ++i) e[i] = e[i] + 1.0f;
#pragma unroll
    for (int i = 0; i < N; ++i) { e[i].x = __builtin_amdgcn_rcpf(e[i].x); e[i].y = __builtin_amdgcn_rcpf(e[i].y); }
    GM_SB();
#pragma unroll
    for (int i = 0; i < N; ++i) { const f32x2 y = x[i] * e[i]; z[i] = pk_bf16(y.x, y.y); }
    GM_SB();
}
__device__ __forceinline__ void gelu16(f32x16& a) {
    constexpr float C1 = -0.044715f * 2.0f * 0.7978845608028654f * 1.4426950408889634f, C2 = -2.0f * 0.7978845608028654f * 1.4426950408889634f;
    f32x16 e = a * (a * a * C1 + C2);
    GM_SB();
#pragma unroll
    for (int i = 0; i < 16; ++i) e[i] = __builtin_amdgcn_exp2f(e[i]);
    GM_SB();
    e = e + 1.0f;
#pragma unroll
    for (int i = 0; i < 16; ++i) e[i] = __builtin_amdgcn_rcpf(e[i]);
    GM_SB();
    a = a * e;
}
__device__ __forceinline__ float add1(float a, float b) { float r; asm("v_add_f32_e32 %0, %1, %2" : "=v"(r) : "v"(a), "v"(b)); return r; }
__device__ __forceinline__ float fma1(float a, float b, float c) { float r; asm("v_fma_f32 %0, %1, %2, %3" : "=v"(r) : "v"(a), "v"(b), "v"(c)); return r; }
#define GM_ROR16(x, N) do { asm volatile("s_nop 1" ::: "memory"); _Pragma("unroll") for (int k_ = 0; k_ < 16; ++k_) \
        asm volatile("v_add_f32_dpp %0, %0, %0 row_ror:" #N " row_mask:0xf bank_mask:0xf" : "+v"(x[k_])); } while (0)
template <int CTRL> __device__ __forceinline__ float dpp_add(float x) { return x + __int_as_float(__builtin_amdgcn_update_dpp(0, __float_as_int(x), CTRL, 0xf, 0xf, true)); }
constexpr int LDS_TAB = CfgZ::LDS;
struct ZHook {
    int wid, lane; unsigned scratch; const char* b2; const char* b1; unsigned v;
    __device__ __forceinline__ void touch(int k) const { pf_lines(k < 16 ? b2 : b1, v + (unsigned)(k & 15) * (64u * 2 * FH * 2), scratch); }
    __device__ __forceinline__ void top(int) const {}
    __device__ __forceinline__ void first() const { if (wid >= 4) for (int k = 3 + ((wid - 3) & 3); k < PFD; k += 4) touch(k); }
    __device__ __forceinline__ void at(int t) const { if (wid == 4 + (t & 3)) { touch(t + PFD); if (t >= PFD) touch(t + 16); } }
};
template <int NT, bool LD> struct LnHook {
    const Ctx& p; ldsp lds; int panel, j, wid, lane, n0; unsigned (&z2)[NT][8];
    u32x4 w[4];
    __device__ __forceinline__ void normalise(int tt) {
        LAS f32x2* R = (LAS f32x2*)(lds + LDS_TAB + 2048);
        const int r = lane & 31, h = lane >> 5, m0 = (wid & 3) * 32;
        const rsrc_t rs = ws_rsrc(p.ws);
        const int srow = panel * 128 + m0;
#pragma unroll
        for (int i = 0; i < 4; i += 2) {
            const f32x4 ra = *(const LAS f32x4*)(R + m0 + 8 * i + 4 * h), rb = *(const LAS f32x4*)(R + m0 + 8 * i + 4 * h + 2);
            const f32x4 rc = *(const LAS f32x4*)(R + m0 + 8 * i + 8 + 4 * h), rd = *(const LAS f32x4*)(R + m0 + 8 * i + 8 + 4 * h + 2);
            const int k = srow + 8 * (i + h);
            const unsigned base = (unsigned)WS_Z + ((j * 32 + (k >> 6)) * 8 + ((k & 63) >> 3)) * 3584 + (n0 * 32 + r) * 16;
            const unsigned a0 = z2[tt][2 * i], a1 = z2[tt][2 * i + 1], b0 = z2[tt][2 * i + 2], b1 = z2[tt][2 * i + 3];
            st16_wt(rs, base + tt * 512, g32::pair32(pk_bf16(fma1(bf_lo(a0), ra[1], ra[0]), fma1(bf_hi(a0), ra[3], ra[2])), pk_bf16(fma1(bf_lo(a1), rb[1], rb[0]), fma1(bf_hi(a1), rb[3], rb[2])),
                                                      pk_bf16(fma1(bf_lo(b0), rc[1], rc[0]), fma1(bf_hi(b0), rc[3], rc[2])), pk_bf16(fma1(bf_lo(b1), rd[1], rd[0]), fma1(bf_hi(b1), rd[3], rd[2]))));
        }
    }
    __device__ __forceinline__ void normalise_at(int t) {
        if (t >= TF + 1 && t < TF + 5) { if (at_tile(t, TF + 1)) normalise(0); if (at_tile(t, TF + 2)) normalise(1); if (at_tile(t, TF + 3)) normalise(2); if constexpr (NT == 4) { if (at_tile(t, TF + 4)) normalise(3); } }
    }
    __device__ __forceinline__ void fetch() {
        const unsigned off = (unsigned)WS_STAT + ((panel * 8 + (lane >> 5) * 4) * 128 + (wid - 4) * 32 + (lane & 31)) * 16;
#pragma unroll
        for (int v = 0; v < 4; ++v) w[v] = ld16_dev(ws_rsrc(p.ws), off + v * 2048);
    }
    __device__ __forceinline__ bool fetched() const { bool ok = true;
#pragma unroll
        for (int v = 0; v < 4; ++v) ok = ok && w[v][1] == p.tg() && w[v][3] == p.tg();
        return __all(ok); }
    __device__ __forceinline__ void top(int t) {
        if constexpr (LD) normalise_at(t);
        else { if (t == TF) fetch(); }
    }
    __device__ __forceinline__ void at(int t) {
        if (t < TF || t > TF + 7) return;
        if constexpr (!LD) {
            if (at_tile(t, TF)) {
                for (unsigned spins = 0; !fetched();) {
                    __builtin_amdgcn_s_sleep(2); fetch();
                    if (++spins > (1u << 19)) { __hip_atomic_store((gu32*)(p.ws + WS_CTL) + C_TMO, p.tg(), RLX_AGENT); break; }
                }
                LAS f32x2* R = (LAS f32x2*)(lds + LDS_TAB + 2048);
                float ms = 0.f;
#pragma unroll
                for (int v = 0; v < 4; ++v) ms += __uint_as_float(w[v][0]);
                { const auto r = __builtin_amdgcn_permlane32_swap(__float_as_uint(ms), __float_as_uint(ms), false, false); ms = __uint_as_float(r[0]) + __uint_as_float(r[1]); }
                const float mean = ms * 0.125f; float q = 0.f;
#pragma unroll
                for (int v = 0; v < 4; ++v) { const float dm = __uint_as_float(w[v][0]) - mean; q += __uint_as_float(w[v][2]) + 224.0f * dm * dm; }
                { const auto r = __builtin_amdgcn_permlane32_swap(__float_as_uint(q), __float_as_uint(q), false, false); q = __uint_as_float(r[0]) + __uint_as_float(r[1]); }
                { const float rs_ = __builtin_amdgcn_rsqf(q * (1.0f / 1792.0f) + 1e-5f); if (lane < 32) R[(wid - 4) * 32 + lane] = (f32x2){-mean * rs_, rs_}; }
                GM_VM0();
            }
            normalise_at(t);
        }
        if (at_tile(t, TF + 6)) asm volatile("s_waitcnt vmcnt(0)" ::: "memory");
        if constexpr (!LD) { if (at_tile(t, TF + 7)) { if (threadIdx.x == 256) signal(p.ws, F_Z + j * 32 + panel, p.tg()); } }
    }
};
struct SpHook {
    static constexpr bool PRE = false; const Ctx& p; ldsp lds; int panel, wid, lane;
    __device__ __forceinline__ void pre_x() const {}
    __device__ __forceinline__ void top(int) const {}
    __device__ __forceinline__ void at(int t) {
        if (t == 2 && (wid == 4 || wid == 5)) {
            poll_flags(p.ws, p.tg(), F_G, 1, true, F_G, lane);
            const int row = (wid - 4) * 64 + lane, trow = panel * 128 + row;
            ((LAS f32x2*)(lds + LDS_TAB + 2048))[row] = (f32x2){__uint_as_float((unsigned)p.bsp[trow] << 16), 1.0f - __uint_as_float(__hip_atomic_load((gu32*)(p.ws + WS_GATE) + trow, RLX_AGENT))};
            GM_VM0();
        }
    }
};
struct WaitHook {
    unsigned char* ws; unsigned tag; int wid, lane, tlate, wl, nl;
    __device__ __forceinline__ void top(int) const {}
    __device__ __forceinline__ void at(int t) const { if (t == tlate && wid == 4) wait_flags(ws, tag, wl, nl, false, 0, lane); }
};
template <class C> __device__ __forceinline__ void out_src_w(Src<C>& s, const Ctx& p, int k0, int j, int lid, int lane) {
    if (k0 < FH / 64) { g32::src_w_raw<C>(s, p.wout + (size_t)k0 * 64 * DM + j * 128, DM * 2, lid, lane); set_w2<C>(s, p.wo + j * 128, FH / 64 - k0); }
    else g32::src_w_raw<C>(s, p.wo + (size_t)(k0 - FH / 64) * 64 * DM + j * 128, DM * 2, lid, lane);
}
__device__ __forceinline__ void out_part_early_w(const Ctx& p, ldsp lds, int j, int wid, int lane) {
    using C = CfgO; using W = g32::Wv<2, 2, true>;
    static_assert(C::WOFF >= 65536, "W slots behind the attention ring");
    if (wid < 4) {
        Src<C> s; out_src_w<C>(s, p, 44 - NA, j, wid, lane);
        g32::PipeKS<C, W> pipe(lds, NA, 0, wid, 0, 0, lane);
        pipe.issue_w(s);
    }
}
template <bool LD, bool FINAL> __device__ __forceinline__ void out_part(const Ctx& p, ldsp lds, int panel, int j, int wid, int lane) {
    using C = CfgO; using W = g32::Wv<2, 2, LD>;
    const int lid = wid & 3, r = lane & 31, h = lane >> 5;
    const int kh = wid >> 2, mo = ((wid >> 1) & 1) * 64, no = (wid & 1) * 2;
    Src<C> s;
    constexpr int K0 = FINAL ? 0 : 44 - NA, NKT = FINAL ? 44 - NA : NA, NU = FH / 64 - K0 < NKT ? FH / 64 - K0 : NKT;
    src_x_own<C>(s, p.ws + WS_ACT + (size_t)panel * 352 * 2048 + K0 * C::XB, lid, lane);
    out_src_w<C>(s, p, K0, j, lid, lane);
    f32x16 acc[2][2]; g32::acc_zero<W>(acc);
    constexpr bool LATE = NU >= C::NS && NU < NKT;
    WaitHook hk{p.ws, p.tg(), wid, lane, LATE ? NU - C::NS : -1, F_O + (panel >> 2) * 32, 32};
    g32::PipeKS<C, W> pipe(lds, NKT, kh, lid, mo, no, lane);
    if (FINAL) pipe.issue_w(s);
    if (wid == 4) {
        if (NU > 0) wait_flags(p.ws, p.tg(), F_U + panel * 32, 8, false, 0, lane);
        if (NU < NKT && !LATE) wait_flags(p.ws, p.tg(), F_O + (panel >> 2) * 32, 32, false, 0, lane);
    }
    GM_BAR();
    pipe.issue_x(s);
    pipe.landed();
    pipe.run(s, acc, hk);
    ldsp mine = lds + wid * 8192 + lane * 16, theirs = lds + (wid ^ 4) * 8192 + lane * 16;
#pragma unroll
    for (int t = 0; t < 2; ++t)
#pragma unroll
        for (int i = 0; i < 4; ++i) { const f32x16& A = LD ? acc[t][1] : acc[t][0]; *(LAS f32x4*)(mine + (t * 4 + i) * 1024) = (f32x4){A[4 * i], A[4 * i + 1], A[4 * i + 2], A[4 * i + 3]}; }
    asm volatile("s_waitcnt lgkmcnt(0)" ::: "memory");
    if (FINAL && wid == 4) wait_flags(p.ws, p.tg(), F_P + panel * 32 + j, 1, false, 0, lane);
    GM_BAR();
    const rsrc_t rs = ws_rsrc(p.ws);
    const unsigned pb = (unsigned)WS_P + ((panel * 8 + j) * 8 + wid) * 8192 + lane * 16;
    f32x16 o[2];
    u32x4 pv[8];
    if (FINAL) {
#pragma unroll
        for (int q = 0; q < 8; ++q) pv[q] = ld16_dev(rs, pb + q * 1024);
    }
#pragma unroll
    for (int t = 0; t < 2; ++t) { o[t] = LD ? acc[t][0] : acc[t][1];
#pragma unroll
        for (int i = 0; i < 4; ++i) { const f32x4 v = *(const LAS f32x4*)(theirs + (t * 4 + i) * 1024); o[t][4 * i] += v[0]; o[t][4 * i + 1] += v[1]; o[t][4 * i + 2] += v[2]; o[t][4 * i + 3] += v[3]; } }
    if (!FINAL) {
#pragma unroll
        for (int t = 0; t < 2; ++t)
#pragma unroll
            for (int i = 0; i < 4; ++i) st16_wt(rs, pb + (t * 4 + i) * 1024, (u32x4){__float_as_uint(o[t][4 * i]), __float_as_uint(o[t][4 * i + 1]), __float_as_uint(o[t][4 * i + 2]), __float_as_uint(o[t][4 * i + 3])});
        asm volatile("s_waitcnt vmcnt(0)" ::: "memory");
        GM_BAR();
        if (threadIdx.x == 0) signal(p.ws, F_P + panel * 32 + j, p.tg());
    } else {
#pragma unroll
        for (int t = 0; t < 2; ++t)
#pragma unroll
            for (int i = 0; i < 4; ++i)
#pragma unroll
                for (int e = 0; e < 4; ++e) o[t][4 * i + e] += __uint_as_float(pv[t * 4 + i][e]);
        ldsp st = lds + 65536;
#pragma unroll
        for (int t = 0; t < 2; ++t)
#pragma unroll
            for (int i = 0; i < 4; i += 2) { const f32x16& A = o[t];
                *(LAS u32x4*)(st + (mo + kh * 32 + r) * 272 + (no + t) * 64 + (i + h) * 16) =
                    g32::pair32(pk_bf16(A[4 * i], A[4 * i + 1]), pk_bf16(A[4 * i + 2], A[4 * i + 3]), pk_bf16(A[4 * i + 4], A[4 * i + 5]), pk_bf16(A[4 * i + 6], A[4 * i + 7])); }
        asm volatile("s_waitcnt lgkmcnt(0)" ::: "memory"); GM_BAR();
#pragma unroll
        for (int i = 0; i < 4; ++i) { const int row = wid * 16 + i * 4 + (lane >> 4);
            __builtin_amdgcn_raw_buffer_store_b128(*(const LAS u32x4*)(st + row * 272 + (lane & 15) * 16), uni_rsrc(p.out, SEQ * DM * 2), (unsigned)(((panel * 128 + row) * DM + j * 128 + (lane & 15) * 8) * 2), 0, 16); }
    }
}
template <bool LD> __device__ __forceinline__ void b_wave(const Ctx& p, ldsp lds, int panel, int j, int wid, int lane) {
    constexpr int NT = LD ? 3 : 4;
    const int lid = wid & 3, m0 = lid * 32, n0 = LD ? 0 : 3;
    const int r = lane & 31, h = lane >> 5;
    unsigned z2[NT][8], z1[NT][8];
    const unsigned pfs = (unsigned)(uintptr_t)lds + LDS_PFS;
    {
        using C = CfgZ; using W = g32::Wv<1, NT, LD>; Src<C> s;
        src_x_raw<C>(s, p.ax + (size_t)panel * 128 * DM, DM * 2, lid, lane);
        g32::src_w_raw<C>(s, p.win + FH + j * 224, 2 * FH * 2, lid, lane);
        set_w2<C>(s, p.win + j * 224, DM / 64);
        g32::Pipe<C, W, 1> pipe(lds, s, 2 * DM / 64, DM / 64, lid, m0, n0, lane);
        f32x16 acc[NT][1]; g32::acc_zero<W>(acc);
        const unsigned wv = (unsigned)(((panel & 3) * 16 + (lane >> 2)) * (2 * FH * 2) + (lane & 3) * 128);
        ZHook hk{wid, lane, pfs, (const char*)p.win + (((FH + j * 224) * 2) & ~127), (const char*)p.win + ((j * 224 * 2) & ~127), wv};
        hk.first();
        pipe.begin(s);
        pipe.template run<false>(s, 0, DM / 64, acc, hk);
        {
            float sm[16], sq[16];
#pragma unroll
            for (int t = 0; t < NT; ++t) {
                gelu16(acc[t][0]);
#pragma unroll
                for (int q = 0; q < 16; ++q) { const float v = acc[t][0][q]; sm[q] = t ? add1(sm[q], v) : v; sq[q] = t ? fma1(v, v, sq[q]) : v * v; }
#pragma unroll
                for (int q = 0; q < 16; q += 2) z2[t][q >> 1] = pk_bf16(acc[t][0][q], acc[t][0][q + 1]);
                acc[t][0] = (f32x16){};
            }
            float ra[16];
#pragma unroll
            for (int k = 0; k < 8; ++k) {
                const auto a = __builtin_amdgcn_permlane16_swap(__float_as_uint(sm[2 * k]), __float_as_uint(sm[2 * k + 1]), false, false); ra[k] = add1(__uint_as_float(a[0]), __uint_as_float(a[1]));
                const auto b2 = __builtin_amdgcn_permlane16_swap(__float_as_uint(sq[2 * k]), __float_as_uint(sq[2 * k + 1]), false, false); ra[8 + k] = add1(__uint_as_float(b2[0]), __uint_as_float(b2[1])); }
            GM_ROR16(ra, 8); GM_ROR16(ra, 4); GM_ROR16(ra, 2); GM_ROR16(ra, 1);
            asm volatile("s_nop 1" ::: "memory");
            float va = ra[0], vb = ra[8];
#pragma unroll
            for (int k = 1; k < 8; ++k) { const bool sel = (lane & 7) == k; va = sel ? ra[k] : va; vb = sel ? ra[8 + k] : vb; }
            LAS f32x2* P = (LAS f32x2*)(lds + LDS_TAB);
            { const int q = 2 * (lane & 7) + ((lane >> 4) & 1), row = m0 + 8 * (q >> 2) + 4 * h + (q & 3);
              if ((lane & 8) == 0) P[row * 2 + (LD ? 0 : 1)] = (f32x2){va, vb}; }
            asm volatile("s_waitcnt lgkmcnt(0)" ::: "memory"); GM_BAR();
            if constexpr (!LD) {
                if (lane < 32) {
                    const int row = lid * 32 + lane;
                    const f32x2 a = P[row * 2], b = P[row * 2 + 1];
                    const float su = a.x + b.x, mt = su * (1.0f / 224.0f), m2 = fmaxf(a.y + b.y - su * mt, 0.f);
                    st16_wt(ws_rsrc(p.ws), (unsigned)WS_STAT + ((panel * 8 + j) * 128 + row) * 16, (u32x4){__float_as_uint(mt), p.tg(), __float_as_uint(m2), p.tg()});
                }
            }
        }
        LnHook<NT, LD> hl{p, lds, panel, j, wid, lane, n0, z2};
        pipe.template run<true>(s, DM / 64, 2 * DM / 64, acc, hl);
        pipe.end();
#pragma unroll
        for (int t = 0; t < NT; ++t)
#pragma unroll
            for (int q = 0; q < 16; q += 2) z1[t][q >> 1] = pk_bf16(acc[t][0][q], acc[t][0][q + 1]);
    }
    {
        using C = CfgS; using W = g32::Wv<1, NT, LD>; Src<C> s;
        src_x_raw<C>(s, p.wsp + (size_t)panel * 128 * SEQ, SEQ * 2, lid, lane);
        src_w_own<C>(s, p.ws + WS_Z + (size_t)j * 32 * 28672, lid, lane);
        g32::Pipe<C, W, 1> pipe(lds, s, SEQ / 64, SEQ / 64, lid, m0, n0, lane);
        pipe.issue_x(s);
        if (wid == 4) wait_flags(p.ws, p.tg(), F_Z + j * 32, 16, false, 0, lane);
        GM_BAR();
        pipe.issue_w(s);
#pragma unroll
        for (int t = 0; t < NT; ++t) { gelu_pairs<8>(z1[t]);
#pragma unroll
            for (int q = 0; q < 8; ++q) asm volatile("" : "+v"(z1[t][q])); }
        pipe.landed();
        f32x16 acc[NT][1]; g32::acc_zero<W>(acc);
        SpHook hk{p, lds, panel, wid, lane};
        pipe.template run<true>(s, 0, SEQ / 64, acc, hk);
        pipe.end();
        const rsrc_t rs = ws_rsrc(p.ws);
        const f32x2 bg = ((const LAS f32x2*)(lds + LDS_TAB + 2048))[m0 + r]; const float b = bg.x, g1 = bg.y;
        const unsigned ub = (unsigned)WS_ACT + (panel * 352 + j * 28 + n0 * 4 + h) * 2048 + (m0 + r) * 16;
#pragma unroll
        for (int t = 0; t < NT; ++t)
#pragma unroll
            for (int i = 0; i < 4; i += 2) {
                unsigned w4[4];
#pragma unroll
                for (int e = 0; e < 4; ++e) { const int q = 4 * i + 2 * e; const unsigned z = z1[t][q >> 1];
                    w4[e] = pk_bf16((acc[t][0][q] + b) * g1 * bf_lo(z), (acc[t][0][q + 1] + b) * g1 * bf_hi(z)); }
                st16_wt(rs, ub + (t * 4 + i) * 2048, g32::pair32(w4[0], w4[1], w4[2], w4[3]));
            }
        asm volatile("s_waitcnt vmcnt(0)" ::: "memory");
        GM_BAR();
        if (threadIdx.x == 256) signal(p.ws, F_U + panel * 32 + j, p.tg());
    }
    out_part<LD, true>(p, lds, panel, j, wid, lane);
}

constexpr int AT_STAGE = 16384, AT_NS = 4;
#define MFMA32(a, b, c) __builtin_amdgcn_mfma_f32_32x32x16_bf16(a, b, c, 0, 0, 0)
#define MFMA16(a, b, c) __builtin_amdgcn_mfma_f32_16x16x32_bf16(a, b, c, 0, 0, 0)
#define PIN(x) asm volatile("" : "+v"(x))
__device__ __forceinline__ void gate_rows4(const Ctx& p, int row0, int lane);
__device__ __forceinline__ void attn_wg(const Ctx& p, ldsp lds, int head, int qb, int x, int a, int oj, int gpair, int gpanel, int wid, int lane) {
    if (wid == 4) wait_flags(p.ws, p.tg(), F_Q + (head >> 1) * 32, 8, false, 0, lane);
    if (wid < 4) { gate_rows4(p, gpanel * 256 + gpair * 16 + wid * 4, lane); asm volatile("s_waitcnt vmcnt(0)" ::: "memory"); }
    GM_BAR();
    if (threadIdx.x == 0) signal(p.ws, F_G + gpair * 32 + gpanel, p.tg());
    const unsigned char* Kg = p.ws + WS_K + (size_t)head * 131072; const unsigned char* Vg = p.ws + WS_V + (size_t)head * 131072;
    const unsigned ldb = (unsigned)(uintptr_t)lds;
    const unsigned voff = wid * 1024 + lane * 16;
    const char* kb = (const char*)Kg; const char* vb = (const char*)Vg;
    auto stage = [&](int st, int slot) {
        asm volatile("s_add_u32 m0, %2, 0\n\ts_nop 0\n\tglobal_load_lds_dwordx4 %0, %1 sc1" ::"v"(voff), "s"(kb + (size_t)st * 8192), "s"(ldb + slot * AT_STAGE + wid * 1024) : "memory", "scc");
        asm volatile("s_add_u32 m0, %2, 0\n\ts_nop 0\n\tglobal_load_lds_dwordx4 %0, %1 sc1" ::"v"(voff), "s"(vb + (size_t)st * 8192), "s"(ldb + slot * AT_STAGE + 8192 + wid * 1024) : "memory", "scc");
    };
    stage(0, 0);
    const int q0 = qb * 512 + wid * 64;
    bf16x8 qf[2][2];
#pragma unroll
    for (int a = 0; a < 2; ++a)
#pragma unroll
        for (int d = 0; d < 2; ++d) qf[a][d] = __builtin_bit_cast(bf16x8, ld16_dev(ws_rsrc(p.ws), (unsigned)WS_Q + (head * 64 + (q0 >> 5) + a) * 2048 + d * 1024 + lane * 16));
    bf16x8 sel;
    { const short one = ((lane & 15) == ((lane >> 4) & 1)) ? (short)0x3f80 : (short)0;
#pragma unroll
      for (int i = 0; i < 8; ++i) sel[i] = one; }
    GM_VM0();
    GM_BAR();
    stage(1, 1); stage(2, 2);
    f32x16 o[2], negm[2]; f32x4 l[2];
    const f32x16 zero16 = {};
    bf16x8 kA[2], kB[2], vA[2], vB[2];
    ldsp lb = lds + lane * 16;
    kA[0] = *(const LAS bf16x8*)(lb); kA[1] = *(const LAS bf16x8*)(lb + 1024);
#pragma unroll
    for (int a = 0; a < 2; ++a) {
        f32x16 s = MFMA32(kA[0], qf[a][0], zero16); s = MFMA32(kA[1], qf[a][1], s);
        float m = fmaxf(fmaxf(s[0], s[1]), fmaxf(s[2], s[3]));
#pragma unroll
        for (int r = 4; r < 16; r += 2) m = fmaxf(m, fmaxf(s[r], s[r + 1]));
        const auto rr = __builtin_amdgcn_permlane32_swap(__float_as_uint(m), __float_as_uint(m), false, false);
        m = -fmaxf(__uint_as_float(rr[0]), __uint_as_float(rr[1]));
#pragma unroll
        for (int r = 0; r < 16; ++r) negm[a][r] = m;
        PIN(negm[a]);
        o[a] = zero16; l[a] = (f32x4){0.f, 0.f, 0.f, 0.f};
    }
    f32x16 S0, S1; u32x4 p0l, p0h, p1l, p1h;
    S0 = MFMA32(kA[0], qf[0][0], negm[0]); S0 = MFMA32(kA[1], qf[0][1], S0);
    p1l = (u32x4){0u, 0u, 0u, 0u}; p1h = p1l;
    vB[0] = (bf16x8){0, 0, 0, 0, 0, 0, 0, 0}; vB[1] = vB[0];
#define EX4(S, i) do { S[i] = __builtin_amdgcn_exp2f(S[i]); S[i + 1] = __builtin_amdgcn_exp2f(S[i + 1]); S[i + 2] = __builtin_amdgcn_exp2f(S[i + 2]); S[i + 3] = __builtin_amdgcn_exp2f(S[i + 3]); } while (0)
#define PK4(P, S, i) do { P[0] = pk_bf16(S[i], S[i + 1]); P[1] = pk_bf16(S[i + 2], S[i + 3]); P[2] = pk_bf16(S[i + 4], S[i + 5]); P[3] = pk_bf16(S[i + 6], S[i + 7]); } while (0)
#define BC(x) __builtin_bit_cast(bf16x8, x)
#define ASTEP(SC, SN, PCl, PCh, PPl, PPh, KF, QN, VF, AP, LOADS) do { \
        SN = MFMA32(KF[0], qf[QN][0], negm[QN]); GM_SB(); EX4(SC, 0); PIN(SC); GM_SB(); \
        SN = MFMA32(KF[1], qf[QN][1], SN); GM_SB(); LOADS; EX4(SC, 4); PIN(SC); GM_SB(); \
        o[AP] = MFMA32(VF[0], BC(PPl), o[AP]); GM_SB(); EX4(SC, 8); PIN(SC); GM_SB(); \
        o[AP] = MFMA32(VF[1], BC(PPh), o[AP]); GM_SB(); EX4(SC, 12); PIN(SC); GM_SB(); \
        l[AP] = MFMA16(sel, BC(PPl), l[AP]); GM_SB(); PK4(PCl, SC, 0); PIN(PCl); GM_SB(); \
        l[AP] = MFMA16(sel, BC(PPh), l[AP]); GM_SB(); PK4(PCh, SC, 8); PIN(PCh); GM_SB(); } while (0)
#define LDK(KF, base, kt) do { KF[0] = *(const LAS bf16x8*)((base) + (kt) * 2048); KF[1] = *(const LAS bf16x8*)((base) + (kt) * 2048 + 1024); } while (0)
#define LDV(VF, base, kt) do { VF[0] = *(const LAS bf16x8*)((base) + 8192 + (kt) * 2048); VF[1] = *(const LAS bf16x8*)((base) + 8192 + (kt) * 2048 + 1024); } while (0)
    float gval = 0.f;
    for (int st = 0; st < 16; ++st) {
        ldsp cb = lb + (st & 3) * AT_STAGE, nb = lb + ((st + 1) & 3) * AT_STAGE;
        if (st < 5) {
            const int n = st * 8 + wid; const unsigned pfs = (unsigned)(uintptr_t)lds + LDS_PFS;
            const int p0 = 8 * (x & 1) + 4 * ((x >> 1) & 1), j0 = 4 * (x >> 2);
            if (n < 16) pf_lines(p.wsp + (size_t)(p0 * 128 + a * 32) * SEQ, (unsigned)(lane & 31) * (SEQ * 2) + (unsigned)(2 * n + (lane >> 5)) * 128u, pfs);
            else if (n < 30) pf_lines(p.wout + (size_t)(a * 112) * DM + j0 * 128, (unsigned)((n - 16) * 8 + (lane >> 3)) * (DM * 2) + (unsigned)(lane & 7) * 128u, pfs);
            else if (n < 38) pf_lines(p.wo + (size_t)(a * 64) * DM + j0 * 128, (unsigned)((n - 30) * 8 + (lane >> 3)) * (DM * 2) + (unsigned)(lane & 7) * 128u, pfs);
        }
        if (st == 8) {
            poll_flags(p.ws, p.tg(), F_G, 1, true, F_G, lane);
            gval = __uint_as_float(__hip_atomic_load((gu32*)(p.ws + WS_GATE) + q0 + lane, RLX_AGENT));
        }
        ASTEP(S0, S1, p0l, p0h, p1l, p1h, kA, 1, vB, 1, { LDK(kB, cb, 1); LDV(vA, cb, 0); });
        ASTEP(S1, S0, p1l, p1h, p0l, p0h, kB, 0, vA, 0, {});
        ASTEP(S0, S1, p0l, p0h, p1l, p1h, kB, 1, vA, 1, { LDK(kA, cb, 2); LDV(vB, cb, 1); });
        ASTEP(S1, S0, p1l, p1h, p0l, p0h, kA, 0, vB, 0, {});
        ASTEP(S0, S1, p0l, p0h, p1l, p1h, kA, 1, vB, 1, { LDK(kB, cb, 3); LDV(vA, cb, 2); });
        ASTEP(S1, S0, p1l, p1h, p0l, p0h, kB, 0, vA, 0, {});
        if (st + 1 < 16) {
            if (st + 2 < 16) asm volatile("s_waitcnt vmcnt(2)" ::: "memory"); else asm volatile("s_waitcnt vmcnt(0)" ::: "memory");
            GM_BAR();
            if (st + 3 < 16) stage(st + 3, (st + 3) & 3);
            if (st == 14) out_part_early_w(p, lds, oj, wid, lane);
        }
        ASTEP(S0, S1, p0l, p0h, p1l, p1h, kB, 1, vA, 1, { LDK(kA, nb, 0); LDV(vB, cb, 3); });
        ASTEP(S1, S0, p1l, p1h, p0l, p0h, kA, 0, vB, 0, {});
    }
    o[1] = MFMA32(vB[0], BC(p1l), o[1]); o[1] = MFMA32(vB[1], BC(p1h), o[1]);
    l[1] = MFMA16(sel, BC(p1l), l[1]); l[1] = MFMA16(sel, BC(p1h), l[1]);
#undef EX4
#undef PK4
#undef ASTEP
#undef LDK
#undef LDV
    float lsum[2];
    {   bool bad = false;
#pragma unroll
        for (int a = 0; a < 2; ++a) {
            const int q = lane & 31;
            const float l0 = __shfl(l[a][0], q & 15), l1 = __shfl(l[a][1], q & 15);
            lsum[a] = (q < 16) ? l0 : l1;
            float mx = 0.f;
#pragma unroll
            for (int r = 0; r < 16; ++r) mx = fmaxf(mx, fabsf(o[a][r]));
            float sm = 0.f;
#pragma unroll
            for (int r = 0; r < 16; ++r) sm += o[a][r];
            bad = bad || !(lsum[a] < 1.2676506e30f) || !(mx < 3.0e38f) || !(sm == sm);
        }
        if (__builtin_expect(__any(bad), 0)) {
            const rsrc_t rq = ws_rsrc(p.ws);
            float mrun[2] = {-INFINITY, -INFINITY}, lrun[2] = {0.f, 0.f};
            o[0] = zero16; o[1] = zero16;
            for (int kt = 0; kt < 64; ++kt) {
                const unsigned ko = (unsigned)WS_K + head * 131072 + kt * 2048 + lane * 16, vo = (unsigned)WS_V + head * 131072 + kt * 2048 + lane * 16;
                const bf16x8 k0 = __builtin_bit_cast(bf16x8, ld16_dev(rq, ko)), k1 = __builtin_bit_cast(bf16x8, ld16_dev(rq, ko + 1024));
                const bf16x8 v0 = __builtin_bit_cast(bf16x8, ld16_dev(rq, vo)), v1 = __builtin_bit_cast(bf16x8, ld16_dev(rq, vo + 1024));
#pragma unroll
                for (int a = 0; a < 2; ++a) {
                    f32x16 s = MFMA32(k0, qf[a][0], zero16); s = MFMA32(k1, qf[a][1], s);
                    float m = s[0];
#pragma unroll
                    for (int r = 1; r < 16; ++r) m = fmaxf(m, s[r]);
                    { const auto rr = __builtin_amdgcn_permlane32_swap(__float_as_uint(m), __float_as_uint(m), false, false); m = fmaxf(__uint_as_float(rr[0]), __uint_as_float(rr[1])); }
                    const float mn = fmaxf(mrun[a], m), alpha = __builtin_amdgcn_exp2f(mrun[a] - mn);
                    mrun[a] = mn;
                    u32x4 pl, ph; float ls = 0.f;
#pragma unroll
                    for (int i = 0; i < 4; ++i) {
                        pl[i] = pk_bf16(__builtin_amdgcn_exp2f(s[2 * i] - mn), __builtin_amdgcn_exp2f(s[2 * i + 1] - mn));
                        ph[i] = pk_bf16(__builtin_amdgcn_exp2f(s[8 + 2 * i] - mn), __builtin_amdgcn_exp2f(s[9 + 2 * i] - mn));
                        ls += (bf_lo(pl[i]) + bf_hi(pl[i])) + (bf_lo(ph[i]) + bf_hi(ph[i])); }
                    lrun[a] = lrun[a] * alpha + ls;
                    o[a] = o[a] * alpha;
                    o[a] = MFMA32(v0, BC(pl), o[a]); o[a] = MFMA32(v1, BC(ph), o[a]);
                }
            }
#pragma unroll
            for (int a = 0; a < 2; ++a) { const auto rr = __builtin_amdgcn_permlane32_swap(__float_as_uint(lrun[a]), __float_as_uint(lrun[a]), false, false); lsum[a] = __uint_as_float(rr[0]) + __uint_as_float(rr[1]); }
        }
    }
    const rsrc_t rs = ws_rsrc(p.ws);
    const float gt0 = __shfl(gval, lane & 31), gt1 = __shfl(gval, 32 + (lane & 31));
#pragma unroll
    for (int a = 0; a < 2; ++a) {
        const int q = lane & 31, hh = lane >> 5;
        const float sc = (a ? gt1 : gt0) * __builtin_amdgcn_rcpf(lsum[a]);
        const int row = q0 + a * 32 + q, panel = row >> 7;
#pragma unroll
        for (int i = 0; i < 4; i += 2) {
            const unsigned a0 = pk_bf16(o[a][4 * i] * sc, o[a][4 * i + 1] * sc), a1 = pk_bf16(o[a][4 * i + 2] * sc, o[a][4 * i + 3] * sc);
            const unsigned b0 = pk_bf16(o[a][4 * i + 4] * sc, o[a][4 * i + 5] * sc), b1 = pk_bf16(o[a][4 * i + 6] * sc, o[a][4 * i + 7] * sc);
            const auto r0 = __builtin_amdgcn_permlane32_swap(a0, b0, false, false); const auto r1 = __builtin_amdgcn_permlane32_swap(a1, b1, false, false);
            const int c = 224 + head * 4 + i + hh;
            st16_wt(rs, (unsigned)WS_ACT + (panel * 352 + c) * 2048 + (row & 127) * 16, (u32x4){r0[0], r1[0], r0[1], r1[1]});
        }
    }
    asm volatile("s_waitcnt vmcnt(0)" ::: "memory");
    GM_BAR();
    if (threadIdx.x == 0) signal(p.ws, F_O + qb * 32 + head, p.tg());
}

__device__ __forceinline__ void gate_rows4(const Ctx& p, int row0, int lane) {
    u32x4 g[2], xr[4][2];
#pragma unroll
    for (int i = 0; i < 2; ++i) g[i] = ((const u32x4*)p.gw)[lane * 2 + i];
#pragma unroll
    for (int r = 0; r < 4; ++r)
#pragma unroll
        for (int i = 0; i < 2; ++i) xr[r][i] = ((const u32x4*)(p.ax + (size_t)(row0 + r) * DM))[lane * 2 + i];
    float s[4];
#pragma unroll
    for (int r = 0; r < 4; ++r) { float a = 0.f, b = 0.f;
#pragma unroll
        for (int i = 0; i < 2; ++i)
#pragma unroll
            for (int e = 0; e < 4; ++e) { a += bf_lo(xr[r][i][e]) * bf_lo(g[i][e]); b += bf_hi(xr[r][i][e]) * bf_hi(g[i][e]); }
        s[r] = a + b; }
#pragma unroll
    for (int r = 0; r < 4; ++r) s[r] = dpp_add<0x128>(s[r]);
#pragma unroll
    for (int r = 0; r < 4; ++r) s[r] = dpp_add<0x124>(s[r]);
#pragma unroll
    for (int r = 0; r < 4; ++r) s[r] = dpp_add<0x122>(s[r]);
#pragma unroll
    for (int r = 0; r < 4; ++r) s[r] = dpp_add<0x121>(s[r]);
#pragma unroll
    for (int r = 0; r < 4; ++r) {
        const auto a = __builtin_amdgcn_permlane16_swap(__float_as_uint(s[r]), __float_as_uint(s[r]), false, false); s[r] = __uint_as_float(a[0]) + __uint_as_float(a[1]);
        const auto b = __builtin_amdgcn_permlane32_swap(__float_as_uint(s[r]), __float_as_uint(s[r]), false, false); s[r] = __uint_as_float(b[0]) + __uint_as_float(b[1]); }
    const float v = lane == 0 ? s[0] : lane == 1 ? s[1] : lane == 2 ? s[2] : s[3];
    if (lane < 4) __hip_atomic_store((gu32*)(p.ws + WS_GATE) + row0 + lane, __float_as_uint(__builtin_amdgcn_rcpf(1.0f + __builtin_amdgcn_exp2f(-1.4426950408889634f * v))), RLX_AGENT);
}
constexpr int LDS_BYTES = CfgO::LDS > CfgQ::LDS + 256 ? CfgO::LDS : CfgQ::LDS + 256;
static_assert(LDS_TAB + 3072 <= LDS_BYTES && CfgO::LDS <= LDS_BYTES && AT_NS * AT_STAGE <= LDS_BYTES, "LDS map");
__global__ __launch_bounds__(512, 2) void fused(Params pp) {
    extern __shared__ __attribute__((aligned(16))) unsigned char smem[]; ldsp lds = (ldsp)smem;
    const int lane = threadIdx.x & 63, wid = __builtin_amdgcn_readfirstlane(threadIdx.x >> 6);
    const int b = blockIdx.x, x = b & 7, role = (b >> 3) & 1, idx = b >> 4;
    Ctx p; static_cast<Params&>(p) = pp;
    p.epv = __hip_atomic_load((gu32*)(pp.ws + WS_CTL) + C_EPOCH, RLX_AGENT);
    if (role == 0) {
        const int pair = 4 * (x >> 1) + (idx >> 2), panel = 4 * (x & 1) + (idx & 3);
        if (wid < 4) qkv_wave<0>(p, lds, pair, panel, wid, lane); else qkv_wave<1>(p, lds, pair, panel, wid, lane);
        const int opanel = 8 * (x & 1) + 4 * ((x >> 1) & 1) + (idx >> 2), oj = 4 * (x >> 2) + (idx & 3);
        attn_wg(p, lds, 4 * x + (idx >> 2), idx & 3, x, idx, oj, pair, panel, wid, lane);
        {
            if (wid < 4) out_part<true, false>(p, lds, opanel, oj, wid, lane); else out_part<false, false>(p, lds, opanel, oj, wid, lane); }
    } else {
        const int panel = 8 * (x & 1) + 4 * ((x >> 1) & 1) + (idx >> 2), j = 4 * (x >> 2) + (idx & 3);
        if (wid < 4) b_wave<true>(p, lds, panel, j, wid, lane); else b_wave<false>(p, lds, panel, j, wid, lane);
        if (panel == 0 && j == 0 && threadIdx.x == 0) __hip_atomic_store((gu32*)(pp.ws + WS_CTL) + C_EPOCH, p.tg(), RLX_AGENT);
    }
}
}

extern "C" void kernel_launch(void* const* d_in, const int* in_sizes, int n_in, void* d_out, int out_size, void* d_ws, size_t ws_size, hipStream_t stream) {
    using namespace gm;
    Params p;
    p.ax = (const u16*)d_in[0]; p.wqkv = (const u16*)d_in[1]; p.wo = (const u16*)d_in[2]; p.bx = (const u16*)d_in[3]; p.win = (const u16*)d_in[4];
    p.wsp = (const u16*)d_in[5]; p.bsp = (const u16*)d_in[6]; p.wout = (const u16*)d_in[7]; p.gw = (const u16*)d_in[8];
    p.out = (u16*)d_out; p.ws = (unsigned char*)d_ws;
    (void)hipFuncSetAttribute((const void*)fused, hipFuncAttributeMaxDynamicSharedMemorySize, LDS_BYTES);
    fused<<<256, 512, LDS_BYTES, stream>>>(p);
}
```
